```python
import torch

def _softmax(x, axis=-1):
    return torch.softmax(x, dim=axis)
def _gelu(x):
    return torch.nn.functional.gelu(x, approximate="tanh")
def _silu(x):
    return torch.nn.functional.silu(x)
def _sigmoid(x):
    return torch.sigmoid(x)
def _layernorm(x, axis=-1, eps=1e-5):
    m = x.mean(dim=axis, keepdim=True)
    v = x.var(dim=axis, keepdim=True, unbiased=False)
    return (x - m) / torch.sqrt(v + eps)
def _scatter_add(tgt, idx, src, axis=0):
    return tgt.clone().index_add_(axis, idx, src)
def _segment_sum(src, seg, n):
    out = torch.zeros((n,) + src.shape[1:], dtype=src.dtype)
    return out.index_add_(0, seg, src)
def _pad(x, pw):
    flat = tuple(v for pair in reversed(pw) for v in pair)
    return torch.nn.functional.pad(x, flat)

kernel_name = 'interleave_layers__mha_block__mixer_channel_mlp__prompt'

def setup_inputs():

    _w = (lambda a: (lambda t: t.to(torch.bfloat16) if t.is_floating_point() else t)(torch.as_tensor(a)))
    def _A_setup():
        B, S, D, H = 1, 2048, 1024, 32
        return {
            "x": _w((lambda *s: torch.randn(*s, dtype=torch.bfloat16) * 0.05)(B, S, D)),
            "wqkv": _w((lambda *s: torch.randn(*s, dtype=torch.bfloat16) * 0.05)(D, 3 * D)),
            "wo": _w((lambda *s: torch.randn(*s, dtype=torch.bfloat16) * 0.05)(D, D)),
        }
    _A_in = _A_setup()

    def _B_setup():
        B, S, D, F = 1, 2048, 1024, 3584
        return {
            "x": _w((lambda *s: torch.randn(*s, dtype=torch.bfloat16) * 0.05)(B, S, D)),
            "w1": _w((lambda *s: torch.randn(*s, dtype=torch.bfloat16) * 3.2)(D, F) * (D ** -0.5)),
            "b1": _w((lambda *s: torch.randn(*s, dtype=torch.bfloat16) * 0.8)(F)),
            "w2": _w((lambda *s: torch.randn(*s, dtype=torch.bfloat16) * 0.4)(F, D) * (F ** -0.5)),
            "b2": _w((lambda *s: torch.randn(*s, dtype=torch.bfloat16) * 0.4)(D)),
        }
    _B_in = _B_setup()

    return {
        "A_x": _A_in["x"],
        "A_wqkv": _A_in["wqkv"],
        "A_wo": _A_in["wo"],
        "B_x": _B_in["x"],
        "B_w1": _B_in["w1"],
        "B_b1": _B_in["b1"],
        "B_w2": _B_in["w2"],
        "B_b2": _B_in["b2"],
    }

def reference(A_x, A_wqkv, A_wo, B_x, B_w1, B_b1, B_w2, B_b2):
    def _A_ref(x, wqkv, wo):
        B, S, D = x.shape
        H = 32
        Dh = D // H
        qkv = (lambda x, *d: x.permute(*d))((x @ wqkv).reshape(B, S, 3, H, Dh), 2, 0, 3, 1, 4)
        q, k, v = qkv[0], qkv[1], qkv[2]
        scores = torch.einsum("bhqd,bhkd->bhqk", q, k) * (Dh ** -0.5)
        p = _softmax(scores, axis=-1)
        o = torch.einsum("bhqk,bhkd->bhqd", p, v)
        return (lambda x, *d: x.permute(*d))(o, 0, 2, 1, 3).reshape(B, S, D) @ wo

    def _B_ref(x, w1, b1, w2, b2):
        y = _layernorm(x, axis=-1)
        h = _gelu(torch.einsum("bsd,df->bsf", y, w1) + b1)
        return torch.einsum("bsf,fd->bsd", h, w2) + b2

    x = A_x
    x = x + _A_ref(_layernorm(x, axis=-1), A_wqkv, A_wo)
    x = x + _B_ref(_layernorm(x, axis=-1), B_w1, B_b1, B_w2, B_b2)
    x = x + _A_ref(_layernorm(x, axis=-1), A_wqkv, A_wo)
    x = x + _B_ref(_layernorm(x, axis=-1), B_w1, B_b1, B_w2, B_b2)
    return x
```

```cpp
#include <hip/hip_runtime.h>
#include <stdint.h>

typedef __attribute__((ext_vector_type(8))) __bf16 bf16x8;
typedef __attribute__((ext_vector_type(16))) __bf16 bf16x16;
typedef __attribute__((ext_vector_type(2))) __bf16 bf16x2;
typedef __attribute__((ext_vector_type(2))) float f32x2;
typedef __attribute__((ext_vector_type(4))) float f32x4;
typedef __attribute__((ext_vector_type(8))) float f32x8;
typedef __attribute__((ext_vector_type(16))) float f32x16;
typedef __attribute__((ext_vector_type(4))) unsigned u32x4;
typedef __attribute__((ext_vector_type(2))) unsigned u32x2;
typedef __attribute__((ext_vector_type(4))) short s16x4;
typedef __attribute__((address_space(3))) char lds_char;
typedef __attribute__((address_space(3))) s16x4 lds_s16x4;
typedef __attribute__((address_space(3))) void lds_void;

#define DEV __device__ __forceinline__

constexpr int SEQ = 2048, DM = 1024, FF = 3584;
constexpr float LN_EPS = 1e-5f;

constexpr size_t MB = 1024 * 1024;
constexpr size_t WS_WQKV = 0;
constexpr size_t WS_WO = WS_WQKV + 6 * MB;
constexpr size_t WS_W1 = WS_WO + 2 * MB;
constexpr size_t WS_W2 = WS_W1 + 7 * MB;
constexpr size_t WS_X = WS_W2 + 7 * MB;
constexpr size_t WS_Q = WS_X + 4 * MB;
constexpr size_t WS_KV = WS_Q + 4 * MB;
constexpr size_t WS_O = WS_KV + 8 * MB;
constexpr size_t WS_H = WS_O + 4 * MB;
constexpr size_t WS_ST = WS_H + 14 * MB;
constexpr size_t WS_CSQ = WS_ST + 256 * 1024;
constexpr size_t WS_CS1 = WS_CSQ + 128 * 1024;

DEV unsigned pack2(float a, float b) {
  f32x2 v = {a, b};
  return __builtin_bit_cast(unsigned, __builtin_convertvector(v, bf16x2));
}
DEV float bf_lo(unsigned u) { return __builtin_bit_cast(float, u << 16); }
DEV float bf_hi(unsigned u) { return __builtin_bit_cast(float, u & 0xffff0000u); }
DEV float xsum16(float x) {
  auto r = __builtin_amdgcn_permlane16_swap(__builtin_bit_cast(unsigned, x), __builtin_bit_cast(unsigned, x), false, false);
  const unsigned a = r[0], b = r[1];
  return __builtin_bit_cast(float, a) + __builtin_bit_cast(float, b);
}
DEV float xsum32(float x) {
  auto r = __builtin_amdgcn_permlane32_swap(__builtin_bit_cast(unsigned, x), __builtin_bit_cast(unsigned, x), false, false);
  const unsigned a = r[0], b = r[1];
  return __builtin_bit_cast(float, a) + __builtin_bit_cast(float, b);
}
DEV float xmax32(float x) {
  auto r = __builtin_amdgcn_permlane32_swap(__builtin_bit_cast(unsigned, x), __builtin_bit_cast(unsigned, x), false, false);
  const unsigned a = r[0], b = r[1];
  return fmaxf(__builtin_bit_cast(float, a), __builtin_bit_cast(float, b));
}
DEV f32x2 row_stats64(u32x4 c0, u32x4 c1) {
  float v[16];
#pragma unroll
  for (int i = 0; i < 4; ++i) { v[2 * i] = bf_lo(c0[i]); v[2 * i + 1] = bf_hi(c0[i]); v[8 + 2 * i] = bf_lo(c1[i]); v[9 + 2 * i] = bf_hi(c1[i]); }
  float s = 0.f;
#pragma unroll
  for (int i = 0; i < 16; ++i) s += v[i];
  s = xsum32(xsum16(s));
  float mean = s * (1.f / 64.f), q = 0.f;
#pragma unroll
  for (int i = 0; i < 16; ++i) { float d = v[i] - mean; q = fmaf(d, d, q); }
  q = xsum32(xsum16(q));
  f32x2 r = {mean, q};
  return r;
}
template <int N> DEV void wait_vm() { asm volatile("s_waitcnt vmcnt(%0)" ::"i"(N) : "memory"); }
DEV void bar() { asm volatile("s_barrier" ::: "memory"); }
DEV void lgkm_bar() { asm volatile("s_waitcnt lgkmcnt(0)\n\ts_barrier" ::: "memory"); }
template <int N> DEV void glds4(unsigned l, const char* g, unsigned voff) {
  if constexpr (N == 1)
    asm volatile("s_mov_b32 m0, %0\n\ts_nop 0\n\tglobal_load_lds_dwordx4 %1, %2" ::"s"(l), "v"(voff), "s"(g) : "memory");
  else if constexpr (N == 2)
    asm volatile("s_mov_b32 m0, %0\n\ts_nop 0\n\tglobal_load_lds_dwordx4 %1, %2\n\tglobal_load_lds_dwordx4 %1, %2 offset:1024" ::"s"(l), "v"(voff), "s"(g) : "memory");
  else if constexpr (N == 3)
    asm volatile("s_mov_b32 m0, %0\n\ts_nop 0\n\tglobal_load_lds_dwordx4 %1, %2\n\tglobal_load_lds_dwordx4 %1, %2 offset:1024\n\tglobal_load_lds_dwordx4 %1, %2 offset:2048" ::"s"(l), "v"(voff), "s"(g) : "memory");
  else
    asm volatile("s_mov_b32 m0, %0\n\ts_nop 0\n\tglobal_load_lds_dwordx4 %1, %2\n\tglobal_load_lds_dwordx4 %1, %2 offset:1024\n\tglobal_load_lds_dwordx4 %1, %2 offset:2048\n\tglobal_load_lds_dwordx4 %1, %2 offset:3072" ::"s"(l), "v"(voff), "s"(g) : "memory");
}
template <int N> DEV void glds(unsigned l, const char* g, unsigned voff) {
  if constexpr (N <= 4) glds4<N>(l, g, voff);
  else { glds4<4>(l, g, voff); glds<N - 4>(l + 4096, g + 4096, voff); }
}
DEV f32x4 mfma16(bf16x8 a, bf16x8 b, f32x4 c) { return __builtin_amdgcn_mfma_f32_16x16x32_bf16(a, b, c, 0, 0, 0); }
DEV f32x16 mfma32(bf16x8 a, bf16x8 b, f32x16 c) { return __builtin_amdgcn_mfma_f32_32x32x16_bf16(a, b, c, 0, 0, 0); }
#define SCHED0() __builtin_amdgcn_sched_barrier(0)
#define STAMP(kid, i)

__global__ __launch_bounds__(256) void prologue_kernel(const char* __restrict__ x, const char* __restrict__ wqkv, const char* __restrict__ wo,
                                                       const char* __restrict__ w1, const char* __restrict__ b1, const char* __restrict__ w2,
                                                       char* __restrict__ ws) {
  __shared__ __attribute__((aligned(16))) char lds[32768];
  const int u = blockIdx.x, lane = threadIdx.x & 63, w = __builtin_amdgcn_readfirstlane(threadIdx.x >> 6);
  const int fq = lane >> 4, lr = lane & 15;
  if (u < 128) {
    const int rb = u >> 3, cb = u & 7;
    u32x4 c[2][4];
#pragma unroll
    for (int mf = 0; mf < 2; ++mf)
#pragma unroll
      for (int k = 0; k < 4; ++k)
        c[mf][k] = *(const u32x4*)(x + ((size_t)(rb * 128 + w * 32 + mf * 16 + lr) * DM + cb * 128 + k * 32 + fq * 8) * 2);
#pragma unroll
    for (int mf = 0; mf < 2; ++mf) {
#pragma unroll
      for (int k = 0; k < 4; ++k)
        *(u32x4*)(ws + WS_X + ((size_t)((rb * 32 + cb * 4 + k) * 8 + w * 2 + mf) * 64 + lane) * 16) = c[mf][k];
#pragma unroll
      for (int h = 0; h < 2; ++h) {
        f32x2 s = row_stats64(c[mf][2 * h], c[mf][2 * h + 1]);
        if (fq == 0) *(f32x2*)(ws + WS_ST + ((size_t)((rb * 4 + w) * 16 + cb * 2 + h) * 32 + mf * 16 + lr) * 8) = s;
      }
    }
    return;
  }
  int kind, ku, nb, N;
  const char* src;
  if (u < 320) { kind = 0; ku = (u - 128) & 7; nb = (u - 128) >> 3; N = 3072; src = wqkv; }
  else if (u < 384) { kind = 1; ku = (u - 320) & 7; nb = (u - 320) >> 3; N = 1024; src = wo; }
  else if (u < 608) { kind = 2; ku = (u - 384) & 7; nb = (u - 384) >> 3; N = FF; src = w1; }
  else { kind = 3; nb = (u - 608) & 7; ku = (u - 608) >> 3; N = 1024; src = w2; }
  src += ((size_t)ku * 128 * N + nb * 128) * 2;
#pragma unroll
  for (int i = 0; i < 8; ++i) {
    const int p = w * 8 + i, row = p * 4 + fq;
    const int ch = lr ^ (((row & 3) << 2) | ((row >> 2) & 3));
    __builtin_amdgcn_global_load_lds((const void*)(src + (size_t)row * N * 2 + ch * 16), (lds_void*)(lds + p * 1024), 16, 0, 0);
  }
  const int G = nb * 4 + w;
  int tile, slot0, slot1, nslot, kbt, cscol = 0;
  bool vperm = false;
  char* dst;
  float* cs = nullptr;
  if (kind == 0) {
    const int part = G >> 5, head = G & 31;
    tile = head >> 1; slot0 = (head & 1) * 6 + part * 2; slot1 = slot0 + 1; nslot = 12; kbt = 32; vperm = part == 2;
    dst = ws + WS_WQKV; cs = (float*)(ws + WS_CSQ) + tile * 2048 + ku * 192; cscol = (head & 1) * 96 + part * 32;
  } else if (kind == 2) {
    const int gg = G % 7;
    tile = G / 7; nslot = 14; kbt = 32;
    if (gg < 3) { slot0 = 2 * gg; slot1 = slot0 + 1; } else if (gg == 3) { slot0 = 6; slot1 = 13; } else { slot0 = 7 + 2 * (gg - 4); slot1 = slot0 + 1; }
    dst = ws + WS_W1; cs = (float*)(ws + WS_CS1) + tile * 2048 + ku * 224; cscol = gg * 32;
    if (ku == 0 && lane < 32) cs[8 * 224 + cscol + lane] = bf_lo(*(const unsigned short*)(b1 + (G * 32 + lane) * 2));
  } else {
    tile = G >> 1; slot0 = (G & 1) * 2; slot1 = slot0 + 1; nslot = 4; kbt = kind == 1 ? 32 : 112;
    dst = ws + (kind == 1 ? WS_WO : WS_W2);
  }
  __syncthreads();
  const int q = (lane >> 2) & 3, p = lane & 3;
  bf16x8 ones;
#pragma unroll
  for (int i = 0; i < 8; ++i) ones[i] = (__bf16)1.0f;
#pragma unroll
  for (int j = 0; j < 2; ++j) {
    const int cbase = vperm ? 16 * j + 8 * (p & 1) + 4 * (p >> 1) : 8 * p + 4 * j;
    const int col = w * 32 + cbase, chunk = col >> 3, half = (col >> 2) & 1;
    f32x4 acc = {0.f, 0.f, 0.f, 0.f};
#pragma unroll
    for (int kbl = 0; kbl < 4; ++kbl) {
      const int r0 = kbl * 32 + fq * 8 + q, r1 = r0 + 4;
      const int a0 = 256 * r0 + 16 * (chunk ^ (((r0 & 3) << 2) | ((r0 >> 2) & 3))) + 8 * half;
      const int a1 = 256 * r1 + 16 * (chunk ^ (((r1 & 3) << 2) | ((r1 >> 2) & 3))) + 8 * half;
      s16x4 v0 = __builtin_amdgcn_ds_read_tr16_b64_v4i16((lds_s16x4*)(lds + a0));
      s16x4 v1 = __builtin_amdgcn_ds_read_tr16_b64_v4i16((lds_s16x4*)(lds + a1));
      u32x2 w0 = __builtin_bit_cast(u32x2, v0), w1v = __builtin_bit_cast(u32x2, v1);
      u32x4 f = {w0[0], w0[1], w1v[0], w1v[1]};
      *(u32x4*)(dst + ((size_t)((tile * kbt + ku * 4 + kbl) * nslot + (j ? slot1 : slot0)) * 64 + lane) * 16) = f;
      if (cs) acc = mfma16(__builtin_bit_cast(bf16x8, f), ones, acc);
    }
    if (cs && lr == 0) {
      const int cb2 = vperm ? 16 * j + 8 * (fq & 1) + 4 * (fq >> 1) : 8 * fq + 4 * j;
      *(f32x4*)(cs + cscol + cb2) = acc;
    }
  }
}

enum { G_QKV = 0, G_WO = 1, G_W1 = 2, G_W2 = 3, G_W2L = 4 };
template <int MODE> struct Cfg;
template <> struct Cfg<G_QKV> { static constexpr int NSLOT = 12, WN = 2, MF = 4, NF = 6, KB = 32, KS = 2, NS = 3, XM = 8, XN = 4; static constexpr bool AUX = true; };
template <> struct Cfg<G_W1> { static constexpr int NSLOT = 14, WN = 2, MF = 4, NF = 7, KB = 32, KS = 2, NS = 3, XM = 8, XN = 4; static constexpr bool AUX = true; };
template <> struct Cfg<G_WO> { static constexpr int NSLOT = 4, WN = 1, MF = 2, NF = 4, KB = 32, KS = 4, NS = 3, XM = 4, XN = 8; static constexpr bool AUX = false; };
template <> struct Cfg<G_W2> { static constexpr int NSLOT = 4, WN = 1, MF = 2, NF = 4, KB = 112, KS = 4, NS = 3, XM = 4, XN = 8; static constexpr bool AUX = false; };
template <> struct Cfg<G_W2L> : Cfg<G_W2> {};
template <int MODE> constexpr int gemm_lds_bytes() {
  using C = Cfg<MODE>;
  return C::NS * C::KS * (8 + C::NSLOT) * 1024 + (C::AUX ? 26 * 1024 : 0);
}

template <int MODE>
__global__ __launch_bounds__(512, 2) void gemm_kernel(const char* __restrict__ A, const char* __restrict__ W, char* __restrict__ ws,
                                                      const char* __restrict__ bias, char* __restrict__ out) {
  using C = Cfg<MODE>;
  constexpr int NSLOT = C::NSLOT, WN = C::WN, MF = C::MF, NF = C::NF, KB = C::KB, KS = C::KS, NS = C::NS;
  constexpr int ASTG = KS * 8 * 1024, BSTG = KS * NSLOT * 1024, SLOTB = ASTG + BSTG, NT = KB / KS;
  constexpr int PA = KS * 2, PB = KS * NSLOT / 4, P = PA + PB;
  constexpr int AUXO = NS * SLOTB;
  constexpr int CSRAW = AUXO + 16384, FIN = AUXO + 24576;
  constexpr int BN = NSLOT * 16;
  static_assert(KS * NSLOT % 4 == 0 && KS % 2 == 0 && NT >= 5, "cfg");
  extern __shared__ __attribute__((aligned(16))) char lds[];
  const int lane = threadIdx.x & 63, wid = __builtin_amdgcn_readfirstlane(threadIdx.x >> 6);
  const int xcd = blockIdx.x & 7, idx = blockIdx.x >> 3;
  constexpr int GX = 16 / C::XN;
  const int rb = (xcd / GX) * C::XM + idx / C::XN, tn = (xcd % GX) * C::XN + idx % C::XN;
  if (wid < 4) STAMP(MODE, 0);

  if (wid >= 4) {
    const int w = wid - 4;
    const unsigned lbase = (unsigned)(uintptr_t)(lds_char*)lds;
    const unsigned voff = lane * 16;
    const char* ga = A + (size_t)rb * KB * 8192 + w * PA * 1024;
    const char* gb = W + (size_t)tn * KB * NSLOT * 1024 + w * PB * 1024;
    const unsigned la = lbase + w * PA * 1024, lb = lbase + ASTG + w * PB * 1024;
#pragma unroll
    for (int s = 0; s < NS; ++s) {
      glds<PA>(la + s * SLOTB, ga, voff); glds<PB>(lb + s * SLOTB, gb, voff);
      ga += ASTG; gb += BSTG;
    }
    constexpr int NA = C::AUX ? 6 : 0;
    if constexpr (C::AUX) {
      glds<4>(lbase + AUXO + w * 4096, ws + WS_ST + (size_t)(rb * 4 + w) * 4096, voff);
      glds<2>(lbase + CSRAW + w * 2048, ws + (MODE == G_QKV ? WS_CSQ : WS_CS1) + (size_t)tn * 8192 + w * 2048, voff);
    }
    wait_vm<(NS - 1) * P + NA>();
    bar();
    int slot = 0;
    for (int t = 0; t < NT - 1; ++t) {
      if (t + NS - 1 < NT) {
        if (t < NS - 1) wait_vm<(NS - 2) * P + NA>(); else wait_vm<(NS - 2) * P>();
      } else wait_vm<0>();
      bar();
      if (t + NS < NT) {
        glds<PA>(la + slot * SLOTB, ga, voff); glds<PB>(lb + slot * SLOTB, gb, voff);
        ga += ASTG; gb += BSTG;
      }
      slot = slot == NS - 1 ? 0 : slot + 1;
      if constexpr (C::AUX) {
        if (t == NS - 1) {
          const int row = lane & 31, hf = lane >> 5;
          f32x2 pt[8];
#pragma unroll
          for (int j = 0; j < 8; ++j) pt[j] = *(const f32x2*)(lds + AUXO + w * 4096 + (hf * 8 + j) * 256 + row * 8);
          float s = 0.f;
#pragma unroll
          for (int j = 0; j < 8; ++j) s += pt[j][0];
          const float mu = xsum32(s) * (1.f / 16.f);
          float q = 0.f;
#pragma unroll
          for (int j = 0; j < 8; ++j) { float d = pt[j][0] - mu; q += fmaf(64.f * d, d, pt[j][1]); }
          const float var = xsum32(q) * (1.f / 1024.f);
          float r = __builtin_amdgcn_rsqf(var + LN_EPS);
          if constexpr (MODE == G_W1) r *= __builtin_amdgcn_rsqf(var * r * r + LN_EPS);
          if (lane < 32) {
            *(float*)(lds + FIN + (w * 32 + row) * 4) = r;
            *(float*)(lds + FIN + 512 + (w * 32 + row) * 4) = -mu * r;
          }
          if (lane < BN / 4) {
            const int col = w * (BN / 4) + lane;
            float c = 0.f;
#pragma unroll
            for (int j = 0; j < 8; ++j) c += *(const float*)(lds + CSRAW + (j * BN + col) * 4);
            *(float*)(lds + FIN + 1024 + col * 4) = c;
          }
          asm volatile("s_waitcnt lgkmcnt(0)" ::: "memory");
        }
      }
    }
    return;
  }

  const int wm = wid / WN, wn = wid % WN;
  const int fq = lane >> 4, lr = lane & 15;
  u32x4 res[2][2], bia[2];
  if constexpr (MODE == G_WO || MODE == G_W2 || MODE == G_W2L) {
#pragma unroll
    for (int mf = 0; mf < 2; ++mf)
#pragma unroll
      for (int g = 0; g < 2; ++g)
        res[mf][g] = *(const u32x4*)(ws + WS_X + ((size_t)((rb * 32 + tn * 2 + g) * 8 + wm * 2 + mf) * 64 + lane) * 16);
    if constexpr (MODE != G_WO) {
#pragma unroll
      for (int g = 0; g < 2; ++g) bia[g] = *(const u32x4*)(bias + (tn * 64 + g * 32 + fq * 8) * 2);
    }
  }
  f32x4 acc[MF][NF];
#pragma unroll
  for (int m = 0; m < MF; ++m)
#pragma unroll
    for (int n = 0; n < NF; ++n) acc[m][n] = f32x4{0.f, 0.f, 0.f, 0.f};
  bf16x8 fa[2][MF], fb[2][NF];
  const char* pa = lds + wm * MF * 1024 + lane * 16;
  const char* pb = lds + ASTG + wn * NF * 1024 + lane * 16;
  auto rd = [&](int buf, int so, int ks) {
#pragma unroll
    for (int m = 0; m < MF; ++m) fa[buf][m] = *(const bf16x8*)(pa + so + (ks * 8 + m) * 1024);
#pragma unroll
    for (int n = 0; n < NF; ++n) fb[buf][n] = *(const bf16x8*)(pb + so + (ks * NSLOT + n) * 1024);
  };
  auto mm = [&](int buf) {
#pragma unroll
    for (int n = 0; n < NF; ++n)
#pragma unroll
      for (int m = 0; m < MF; ++m) {
        if (MODE == G_QKV && n >= 4) acc[m][n] = mfma16(fa[buf][m], fb[buf][n], acc[m][n]);
        else acc[m][n] = mfma16(fb[buf][n], fa[buf][m], acc[m][n]);
      }
  };
  auto inter = [&]() {
    constexpr int NR = MF + NF, NM = MF * NF, PER = NM / NR;
#pragma unroll
    for (int i = 0; i < NR; ++i) {
      __builtin_amdgcn_sched_group_barrier(0x008, PER, 0);
      __builtin_amdgcn_sched_group_barrier(0x100, 1, 0);
    }
    __builtin_amdgcn_sched_group_barrier(0x008, NM - PER * NR, 0);
  };
  bar();
  STAMP(MODE, 1);
  rd(0, 0, 0);
  int so = 0;
  for (int t = 0; t < NT - 1; ++t) {
#pragma unroll
    for (int ks = 0; ks < KS - 1; ++ks) {
      rd((ks + 1) & 1, so, ks + 1);
      mm(ks & 1);
      inter();
      SCHED0();
    }
    lgkm_bar();
    so = so == (NS - 1) * SLOTB ? 0 : so + SLOTB;
    rd(0, so, 0);
    mm(1);
    inter();
    SCHED0();
  }
#pragma unroll
  for (int ks = 0; ks < KS - 1; ++ks) {
    rd((ks + 1) & 1, so, ks + 1);
    mm(ks & 1);
    inter();
    SCHED0();
  }
  mm(1);
  STAMP(MODE, 2);

  if constexpr (MODE == G_QKV) {
    const float* RS = (const float*)(lds + FIN);
    const float* CM = RS + 128;
    const float* CS = RS + 256 + wn * 96;
    const int h = tn * 2 + wn;
    constexpr float QS = 0.17677669529663687f * 1.4426950408889634f;
    f32x4 cq0 = *(const f32x4*)(CS + fq * 8), cq1 = *(const f32x4*)(CS + fq * 8 + 4);
    f32x4 ck0 = *(const f32x4*)(CS + 32 + fq * 8), ck1 = *(const f32x4*)(CS + 32 + fq * 8 + 4);
#pragma unroll
    for (int mf = 0; mf < 4; ++mf) {
      const int row = wm * 64 + mf * 16 + lr;
      const float r = RS[row], cm = CM[row], rq = r * QS, cmq = cm * QS;
      const int qb = rb * 4 + wm * 2 + (mf >> 1), b = mf & 1;
      u32x4 o;
      o[0] = pack2(fmaf(acc[mf][0][0], rq, cmq * cq0[0]), fmaf(acc[mf][0][1], rq, cmq * cq0[1]));
      o[1] = pack2(fmaf(acc[mf][0][2], rq, cmq * cq0[2]), fmaf(acc[mf][0][3], rq, cmq * cq0[3]));
      o[2] = pack2(fmaf(acc[mf][1][0], rq, cmq * cq1[0]), fmaf(acc[mf][1][1], rq, cmq * cq1[1]));
      o[3] = pack2(fmaf(acc[mf][1][2], rq, cmq * cq1[2]), fmaf(acc[mf][1][3], rq, cmq * cq1[3]));
      *(u32x4*)(ws + WS_Q + (size_t)((h * 64 + qb) * 2 + (fq >> 1)) * 1024 + (16 * b + lr + 32 * (fq & 1)) * 16) = o;
      o[0] = pack2(fmaf(acc[mf][2][0], r, cm * ck0[0]), fmaf(acc[mf][2][1], r, cm * ck0[1]));
      o[1] = pack2(fmaf(acc[mf][2][2], r, cm * ck0[2]), fmaf(acc[mf][2][3], r, cm * ck0[3]));
      o[2] = pack2(fmaf(acc[mf][3][0], r, cm * ck1[0]), fmaf(acc[mf][3][1], r, cm * ck1[1]));
      o[3] = pack2(fmaf(acc[mf][3][2], r, cm * ck1[2]), fmaf(acc[mf][3][3], r, cm * ck1[3]));
      const int rho = 8 * (lr >> 2) + 4 * b + (lr & 3);
      *(u32x4*)(ws + WS_KV + (size_t)((h * 64 + qb) * 4 + (fq >> 1)) * 1024 + (rho + 32 * (fq & 1)) * 16) = o;
    }
    const int dv = 8 * ((lr >> 2) & 1) + 4 * (lr >> 3) + (lr & 3);
    const float cv0 = CS[64 + dv], cv1 = CS[64 + 16 + dv];
#pragma unroll
    for (int a = 0; a < 2; ++a) {
      const int row = wm * 64 + a * 32 + fq * 4;
      const f32x4 r0 = *(const f32x4*)(RS + row), r1 = *(const f32x4*)(RS + row + 16);
      const f32x4 c0 = *(const f32x4*)(CM + row), c1 = *(const f32x4*)(CM + row + 16);
      const int qb = rb * 4 + wm * 2 + a;
#pragma unroll
      for (int j = 0; j < 2; ++j) {
        const float cv = j ? cv1 : cv0;
        const f32x4 x0 = acc[2 * a][4 + j], x1 = acc[2 * a + 1][4 + j];
        u32x4 o;
        o[0] = pack2(fmaf(x0[0], r0[0], c0[0] * cv), fmaf(x0[1], r0[1], c0[1] * cv));
        o[1] = pack2(fmaf(x0[2], r0[2], c0[2] * cv), fmaf(x0[3], r0[3], c0[3] * cv));
        o[2] = pack2(fmaf(x1[0], r1[0], c1[0] * cv), fmaf(x1[1], r1[1], c1[1] * cv));
        o[3] = pack2(fmaf(x1[2], r1[2], c1[2] * cv), fmaf(x1[3], r1[3], c1[3] * cv));
        *(u32x4*)(ws + WS_KV + (size_t)((h * 64 + qb) * 4 + 2 + (fq >> 1)) * 1024 + (16 * j + lr + 32 * (fq & 1)) * 16) = o;
      }
    }
  } else if constexpr (MODE == G_W1) {
    const float* RS = (const float*)(lds + FIN);
    const float* CM = RS + 128;
    const float* CS = RS + 256;
    const float* B1 = (const float*)(lds + CSRAW) + 8 * 224;
    constexpr float K1 = -2.f * 0.7978845608028654f * 1.4426950408889634f, K2 = K1 * 0.044715f;
    float r[4], cm[4];
#pragma unroll
    for (int mf = 0; mf < 4; ++mf) { r[mf] = RS[wm * 64 + mf * 16 + lr]; cm[mf] = CM[wm * 64 + mf * 16 + lr]; }
    auto gelu4 = [&](f32x4 a, float rr, float cc, f32x4 c, f32x4 b, unsigned& o0, unsigned& o1) {
      float g[4];
#pragma unroll
      for (int i = 0; i < 4; ++i) {
        const float v = fmaf(a[i], rr, fmaf(cc, c[i], b[i]));
        const float e = __builtin_amdgcn_exp2f(v * fmaf(v * v, K2, K1));
        g[i] = v * __builtin_amdgcn_rcpf(1.f + e);
      }
      o0 = pack2(g[0], g[1]); o1 = pack2(g[2], g[3]);
    };
#pragma unroll
    for (int gp = 0; gp < 3; ++gp) {
      const int grp = wn * 4 + gp, col = grp * 32 + fq * 8;
      const f32x4 c0 = *(const f32x4*)(CS + col), c1 = *(const f32x4*)(CS + col + 4);
      const f32x4 b0 = *(const f32x4*)(B1 + col), b1v = *(const f32x4*)(B1 + col + 4);
#pragma unroll
      for (int mf = 0; mf < 4; ++mf) {
        unsigned o0, o1, o2, o3;
        gelu4(acc[mf][2 * gp], r[mf], cm[mf], c0, b0, o0, o1);
        gelu4(acc[mf][2 * gp + 1], r[mf], cm[mf], c1, b1v, o2, o3);
        u32x4 o = {o0, o1, o2, o3};
        *(u32x4*)(ws + WS_H + ((size_t)((rb * 112 + tn * 7 + grp) * 8 + wm * 4 + mf) * 64 + lane) * 16) = o;
      }
    }
    {
      const int col = 96 + fq * 8 + wn * 4;
      const f32x4 c0 = *(const f32x4*)(CS + col), b0 = *(const f32x4*)(B1 + col);
#pragma unroll
      for (int mf = 0; mf < 4; ++mf) {
        unsigned o0, o1;
        gelu4(acc[mf][6], r[mf], cm[mf], c0, b0, o0, o1);
        u32x2 o = {o0, o1};
        *(u32x2*)(ws + WS_H + ((size_t)((rb * 112 + tn * 7 + 3) * 8 + wm * 4 + mf) * 64 + lane) * 16 + wn * 8) = o;
      }
    }
  } else {
#pragma unroll
    for (int mf = 0; mf < 2; ++mf) {
      u32x4 o[2];
#pragma unroll
      for (int g = 0; g < 2; ++g) {
        float v[8];
#pragma unroll
        for (int i = 0; i < 4; ++i) { v[i] = acc[mf][2 * g][i]; v[4 + i] = acc[mf][2 * g + 1][i]; }
#pragma unroll
        for (int i = 0; i < 4; ++i) {
          v[2 * i] += bf_lo(res[mf][g][i]); v[2 * i + 1] += bf_hi(res[mf][g][i]);
          if constexpr (MODE != G_WO) { v[2 * i] += bf_lo(bia[g][i]); v[2 * i + 1] += bf_hi(bia[g][i]); }
          o[g][i] = pack2(v[2 * i], v[2 * i + 1]);
        }
        if constexpr (MODE == G_W2L)
          *(u32x4*)(out + ((size_t)(rb * 128 + wm * 32 + mf * 16 + lr) * DM + tn * 64 + g * 32 + fq * 8) * 2) = o[g];
        else
          *(u32x4*)(ws + WS_X + ((size_t)((rb * 32 + tn * 2 + g) * 8 + wm * 2 + mf) * 64 + lane) * 16) = o[g];
      }
      if constexpr (MODE != G_W2L) {
        f32x2 s = row_stats64(o[0], o[1]);
        if (fq == 0) *(f32x2*)(ws + WS_ST + ((size_t)((rb * 4 + wm) * 16 + tn) * 32 + mf * 16 + lr) * 8) = s;
      }
    }
  }
  STAMP(MODE, 3);
}

constexpr int ATT_TPS = 4;
constexpr int ATT_STG = ATT_TPS * 4096, ATT_RING = 4, ATT_NST = 64 / ATT_TPS, ATT_PP = ATT_STG / 8192;
constexpr int ATT_LDS = ATT_STG * ATT_RING;

DEV f32x16 smf32(bf16x8 a, f32x8 b, f32x16 c, int idx) {
  return __builtin_amdgcn_smfmac_f32_32x32x32_bf16(a, __builtin_bit_cast(bf16x16, b), c, idx, 0, 0);
}
DEV f32x4 smf16(bf16x8 a, f32x8 b, f32x4 c, int idx) {
  return __builtin_amdgcn_smfmac_f32_16x16x64_bf16(a, __builtin_bit_cast(bf16x16, b), c, idx, 0, 0);
}
DEV f32x8 lo8(f32x16 v) { return __builtin_shufflevector(v, v, 0, 1, 2, 3, 4, 5, 6, 7); }
DEV f32x8 hi8(f32x16 v) { return __builtin_shufflevector(v, v, 8, 9, 10, 11, 12, 13, 14, 15); }
DEV float max16(f32x16 s) {
  float a = fmaxf(fmaxf(s[0], s[1]), s[2]), b = fmaxf(fmaxf(s[3], s[4]), s[5]), c = fmaxf(fmaxf(s[6], s[7]), s[8]);
  float d = fmaxf(fmaxf(s[9], s[10]), s[11]), e = fmaxf(fmaxf(s[12], s[13]), s[14]);
  return fmaxf(fmaxf(fmaxf(a, b), c), fmaxf(fmaxf(d, e), s[15]));
}

__global__ __launch_bounds__(512, 2) void attn_kernel(char* __restrict__ ws) {
  extern __shared__ __attribute__((aligned(16))) char lds[];
  const int lane = threadIdx.x & 63, wid = __builtin_amdgcn_readfirstlane(threadIdx.x >> 6);
  const int xcd = blockIdx.x & 7, idx = blockIdx.x >> 3;
  const int h = xcd * 4 + (idx >> 3), qblk = idx & 7;
  const unsigned lbase = (unsigned)(uintptr_t)(lds_char*)lds;
  const unsigned voff = lane * 16;
  const char* kvh = ws + WS_KV + (size_t)h * 64 * 4096;
  const char* gsrc = kvh + wid * ATT_PP * 1024;
  const unsigned ldst = lbase + wid * ATT_PP * 1024;
  const char* qsrc = ws + WS_Q + (size_t)(h * 64 + qblk * 8 + wid) * 2048;
  bf16x8 q0, q1;
  STAMP(5, 0);
  asm volatile("global_load_dwordx4 %0, %2, %3\n\tglobal_load_dwordx4 %1, %2, %3 offset:1024" : "=&v"(q0), "=&v"(q1) : "v"(voff), "s"(qsrc) : "memory");
#pragma unroll
  for (int s = 0; s < ATT_RING - 1; ++s) { glds<ATT_PP>(ldst + s * ATT_STG, gsrc, voff); gsrc += ATT_STG; }
  bf16x8 sel;
  {
    const bool odd = (lane >> 2) & 1;
#pragma unroll
    for (int e = 0; e < 8; ++e) sel[e] = ((e >= 4) == odd) ? (__bf16)1.0f : (__bf16)0.0f;
  }
  const int sidx = 0xDDDD;
  asm volatile("s_waitcnt vmcnt(%2)" : "+v"(q0), "+v"(q1) : "i"((ATT_RING - 2) * ATT_PP) : "memory");
  bar();
  STAMP(5, 1);
  const char* lp = lds + lane * 16;
#define KF(off, s) (*(const bf16x8*)(lp + (off) + (s) * 1024))
#define VF(off, s) (*(const bf16x8*)(lp + (off) + 2048 + (s) * 1024))
  const f32x16 zero16 = {};
  f32x16 negm;
  f32x16 sa, sb;
  bf16x8 k0 = KF(0, 0), k1 = KF(0, 1);
  {
    f32x16 s = mfma32(k1, q1, mfma32(k0, q0, zero16));
    const float m = xmax32(max16(s));
#pragma unroll
    for (int i = 0; i < 16; ++i) { negm[i] = -m; sa[i] = s[i] - m; }
  }
  f32x16 o = zero16;
  f32x4 l = {0.f, 0.f, 0.f, 0.f};
  bf16x8 v0, v1 = {}, vp = {};
  sb = zero16;
  k0 = KF(4096, 0); k1 = KF(4096, 1);
  v0 = VF(0, 0); v1 = VF(0, 1);
#define EXP(v, a, b) _Pragma("unroll") for (int e_ = (a); e_ < (b); ++e_) v[e_] = __builtin_amdgcn_exp2f(v[e_]);
#define STEP(cur, nxt, koff, voff2, MID)                         \
  {                                                              \
    o = smf32(vp, hi8(nxt), o, sidx); SCHED0();                  \
    EXP(cur, 0, 3) SCHED0();                                     \
    l = smf16(sel, hi8(nxt), l, sidx); SCHED0();                 \
    EXP(cur, 3, 5) SCHED0();                                     \
    nxt = mfma32(k0, q0, negm); SCHED0();                        \
    EXP(cur, 5, 8) SCHED0();                                     \
    nxt = mfma32(k1, q1, nxt); SCHED0();                         \
    MID                                                          \
    k0 = KF(koff, 0); k1 = KF(koff, 1);                          \
    EXP(cur, 8, 11) SCHED0();                                    \
    o = smf32(v0, lo8(cur), o, sidx); SCHED0();                  \
    vp = v1;                                                     \
    v0 = VF(voff2, 0); v1 = VF(voff2, 1);                        \
    EXP(cur, 11, 13) SCHED0();                                   \
    l = smf16(sel, lo8(cur), l, sidx); SCHED0();                 \
    EXP(cur, 13, 16) SCHED0();                                   \
  }
  int so = 0;
  for (int t = 0; t < ATT_NST; ++t) {
    const int sn = so == (ATT_RING - 1) * ATT_STG ? 0 : so + ATT_STG;
    const int sp = so == 0 ? (ATT_RING - 1) * ATT_STG : so - ATT_STG;
#pragma unroll
    for (int i = 0; i < ATT_TPS - 2; i += 2) {
      STEP(sa, sb, so + (i + 2) * 4096, so + (i + 1) * 4096, )
      STEP(sb, sa, so + (i + 3) * 4096, so + (i + 2) * 4096, )
    }
    STEP(sa, sb, sn, so + (ATT_TPS - 1) * 4096,
         if (t + 2 < ATT_NST) wait_vm<(ATT_RING - 3) * ATT_PP>(); else wait_vm<0>();
         bar();
         if (t + ATT_RING - 1 < ATT_NST) { glds<ATT_PP>(ldst + sp, gsrc, voff); gsrc += ATT_STG; })
    STEP(sb, sa, sn + 4096, sn, )
    so = sn;
  }
  o = smf32(vp, hi8(sb), o, sidx);
  l = smf16(sel, hi8(sb), l, sidx);
  STAMP(5, 2);
  bool bad = !(l[0] < 1.8446744e19f);
#pragma unroll
  for (int i = 0; i < 16; ++i) bad |= !(fabsf(o[i]) < 3.0e38f);
  if (__builtin_amdgcn_ballot_w64(bad) != 0) {
    o = zero16; l = f32x4{0.f, 0.f, 0.f, 0.f};
    float m = -__builtin_inff();
    for (int t = 0; t < 64; ++t) {
      const char* g = kvh + t * 4096 + lane * 16;
      const bf16x8 a0 = *(const bf16x8*)g, a1 = *(const bf16x8*)(g + 1024), b0 = *(const bf16x8*)(g + 2048), b1 = *(const bf16x8*)(g + 3072);
      f32x16 s = mfma32(a1, q1, mfma32(a0, q0, zero16));
      const float mn = fmaxf(m, xmax32(max16(s)));
      const float al = __builtin_amdgcn_exp2f(m - mn);
      m = mn;
#pragma unroll
      for (int i = 0; i < 16; ++i) { s[i] = __builtin_amdgcn_exp2f(s[i] - mn); o[i] *= al; }
#pragma unroll
      for (int i = 0; i < 4; ++i) l[i] *= al;
      o = smf32(b0, lo8(s), o, sidx); o = smf32(b1, hi8(s), o, sidx);
      l = smf16(sel, lo8(s), l, sidx); l = smf16(sel, hi8(s), l, sidx);
    }
  }
  const float inv = 1.f / l[0];
  const int q0r = qblk * 256 + wid * 32;
  char* od = ws + WS_O + ((size_t)(((q0r >> 7) * 32 + h) * 8 + ((q0r & 127) >> 4) + ((lane >> 4) & 1)) * 64 + (lane & 15) + 16 * (lane >> 5)) * 16;
#pragma unroll
  for (int c = 0; c < 2; ++c) {
    u32x4 w;
#pragma unroll
    for (int i = 0; i < 4; ++i) w[i] = pack2(o[8 * c + 2 * i] * inv, o[8 * c + 2 * i + 1] * inv);
    *(u32x4*)(od + c * 512) = w;
  }
  STAMP(5, 3);
}

#define STOP_AFTER 100
template <int MODE>
static void launch_gemm(const char* A, const char* W, char* ws, const char* bias, char* out, hipStream_t stream) {
  constexpr int L = gemm_lds_bytes<MODE>();
  (void)hipFuncSetAttribute((const void*)gemm_kernel<MODE>, hipFuncAttributeMaxDynamicSharedMemorySize, L);
  gemm_kernel<MODE><<<256, 512, L, stream>>>(A, W, ws, bias, out);
}

extern "C" void kernel_launch(void* const* d_in, const int* in_sizes, int n_in, void* d_out, int out_size, void* d_ws, size_t ws_size,
                              hipStream_t stream) {
  const char* x = (const char*)d_in[0];
  const char* wqkv = (const char*)d_in[1];
  const char* wo = (const char*)d_in[2];
  const char* w1 = (const char*)d_in[4];
  const char* b1 = (const char*)d_in[5];
  const char* w2 = (const char*)d_in[6];
  const char* b2 = (const char*)d_in[7];
  char* ws = (char*)d_ws;
  char* out = (char*)d_out;
  int n = 0;
  prologue_kernel<<<832, 256, 0, stream>>>(x, wqkv, wo, w1, b1, w2, ws);
  (void)hipFuncSetAttribute((const void*)attn_kernel, hipFuncAttributeMaxDynamicSharedMemorySize, ATT_LDS);
  for (int layer = 0; layer < 2; ++layer) {
    if (++n > STOP_AFTER) return;
    launch_gemm<G_QKV>(ws + WS_X, ws + WS_WQKV, ws, b2, out, stream);
    if (++n > STOP_AFTER) return;
    attn_kernel<<<256, 512, ATT_LDS, stream>>>(ws);
    if (++n > STOP_AFTER) return;
    launch_gemm<G_WO>(ws + WS_O, ws + WS_WO, ws, b2, out, stream);
    if (++n > STOP_AFTER) return;
    launch_gemm<G_W1>(ws + WS_X, ws + WS_W1, ws, b2, out, stream);
    if (++n > STOP_AFTER) return;
    if (layer == 0) launch_gemm<G_W2>(ws + WS_H, ws + WS_W2, ws, b2, out, stream);
    else launch_gemm<G_W2L>(ws + WS_H, ws + WS_W2, ws, b2, out, stream);
  }
}
```

```cpp
#include <hip/hip_runtime.h>
#include <stdint.h>

typedef __attribute__((ext_vector_type(8))) __bf16 bf16x8;
typedef __attribute__((ext_vector_type(16))) __bf16 bf16x16;
typedef __attribute__((ext_vector_type(2))) __bf16 bf16x2;
typedef __attribute__((ext_vector_type(2))) float f32x2;
typedef __attribute__((ext_vector_type(4))) float f32x4;
typedef __attribute__((ext_vector_type(8))) float f32x8;
typedef __attribute__((ext_vector_type(16))) float f32x16;
typedef __attribute__((ext_vector_type(4))) unsigned u32x4;
typedef __attribute__((ext_vector_type(2))) unsigned u32x2;
typedef __attribute__((ext_vector_type(4))) short s16x4;
typedef __attribute__((address_space(3))) char lds_char;
typedef __attribute__((address_space(3))) s16x4 lds_s16x4;
typedef __attribute__((address_space(3))) void lds_void;

#define DEV __device__ __forceinline__

constexpr int SEQ = 2048, DM = 1024, FF = 3584;
constexpr float LN_EPS = 1e-5f;

constexpr size_t MB = 1024 * 1024;
constexpr size_t WS_WQKV = 0;
constexpr size_t WS_WO = WS_WQKV + 6 * MB;
constexpr size_t WS_W1 = WS_WO + 2 * MB;
constexpr size_t WS_W2 = WS_W1 + 7 * MB;
constexpr size_t WS_X = WS_W2 + 7 * MB;
constexpr size_t WS_Q = WS_X + 4 * MB;
constexpr size_t WS_KV = WS_Q + 4 * MB;
constexpr size_t WS_O = WS_KV + 8 * MB;
constexpr size_t WS_H = WS_O + 4 * MB;
constexpr size_t WS_ST = WS_H + 14 * MB;
constexpr size_t WS_CSQ = WS_ST + 256 * 1024;
constexpr size_t WS_CS1 = WS_CSQ + 128 * 1024;

DEV unsigned pack2(float a, float b) {
  f32x2 v = {a, b};
  return __builtin_bit_cast(unsigned, __builtin_convertvector(v, bf16x2));
}
DEV float bf_lo(unsigned u) { return __builtin_bit_cast(float, u << 16); }
DEV float bf_hi(unsigned u) { return __builtin_bit_cast(float, u & 0xffff0000u); }
DEV float xsum16(float x) {
  auto r = __builtin_amdgcn_permlane16_swap(__builtin_bit_cast(unsigned, x), __builtin_bit_cast(unsigned, x), false, false);
  const unsigned a = r[0], b = r[1];
  return __builtin_bit_cast(float, a) + __builtin_bit_cast(float, b);
}
DEV float xsum32(float x) {
  auto r = __builtin_amdgcn_permlane32_swap(__builtin_bit_cast(unsigned, x), __builtin_bit_cast(unsigned, x), false, false);
  const unsigned a = r[0], b = r[1];
  return __builtin_bit_cast(float, a) + __builtin_bit_cast(float, b);
}
DEV float xmax32(float x) {
  auto r = __builtin_amdgcn_permlane32_swap(__builtin_bit_cast(unsigned, x), __builtin_bit_cast(unsigned, x), false, false);
  const unsigned a = r[0], b = r[1];
  return fmaxf(__builtin_bit_cast(float, a), __builtin_bit_cast(float, b));
}
DEV f32x2 row_stats64(u32x4 c0, u32x4 c1) {
  float v[16];
#pragma unroll
  for (int i = 0; i < 4; ++i) { v[2 * i] = bf_lo(c0[i]); v[2 * i + 1] = bf_hi(c0[i]); v[8 + 2 * i] = bf_lo(c1[i]); v[9 + 2 * i] = bf_hi(c1[i]); }
  float s = 0.f;
#pragma unroll
  for (int i = 0; i < 16; ++i) s += v[i];
  s = xsum32(xsum16(s));
  float mean = s * (1.f / 64.f), q = 0.f;
#pragma unroll
  for (int i = 0; i < 16; ++i) { float d = v[i] - mean; q = fmaf(d, d, q); }
  q = xsum32(xsum16(q));
  f32x2 r = {mean, q};
  return r;
}
template <int N> DEV void wait_vm() { asm volatile("s_waitcnt vmcnt(%0)" ::"i"(N) : "memory"); }
DEV void bar() { asm volatile("s_barrier" ::: "memory"); }
DEV void lgkm_bar() { asm volatile("s_waitcnt lgkmcnt(0)\n\ts_barrier" ::: "memory"); }
template <int N> DEV void glds4(unsigned l, const char* g, unsigned voff) {
  if constexpr (N == 1)
    asm volatile("s_mov_b32 m0, %0\n\ts_nop 0\n\tglobal_load_lds_dwordx4 %1, %2" ::"s"(l), "v"(voff), "s"(g) : "memory");
  else if constexpr (N == 2)
    asm volatile("s_mov_b32 m0, %0\n\ts_nop 0\n\tglobal_load_lds_dwordx4 %1, %2\n\tglobal_load_lds_dwordx4 %1, %2 offset:1024" ::"s"(l), "v"(voff), "s"(g) : "memory");
  else if constexpr (N == 3)
    asm volatile("s_mov_b32 m0, %0\n\ts_nop 0\n\tglobal_load_lds_dwordx4 %1, %2\n\tglobal_load_lds_dwordx4 %1, %2 offset:1024\n\tglobal_load_lds_dwordx4 %1, %2 offset:2048" ::"s"(l), "v"(voff), "s"(g) : "memory");
  else
    asm volatile("s_mov_b32 m0, %0\n\ts_nop 0\n\tglobal_load_lds_dwordx4 %1, %2\n\tglobal_load_lds_dwordx4 %1, %2 offset:1024\n\tglobal_load_lds_dwordx4 %1, %2 offset:2048\n\tglobal_load_lds_dwordx4 %1, %2 offset:3072" ::"s"(l), "v"(voff), "s"(g) : "memory");
}
template <int N> DEV void glds(unsigned l, const char* g, unsigned voff) {
  if constexpr (N <= 4) glds4<N>(l, g, voff);
  else { glds4<4>(l, g, voff); glds<N - 4>(l + 4096, g + 4096, voff); }
}
DEV f32x4 mfma16(bf16x8 a, bf16x8 b, f32x4 c) { return __builtin_amdgcn_mfma_f32_16x16x32_bf16(a, b, c, 0, 0, 0); }
DEV f32x16 mfma32(bf16x8 a, bf16x8 b, f32x16 c) { return __builtin_amdgcn_mfma_f32_32x32x16_bf16(a, b, c, 0, 0, 0); }
#define SCHED0() __builtin_amdgcn_sched_barrier(0)
#define STAMP(kid, i)

__global__ __launch_bounds__(256) void prologue_kernel(const char* __restrict__ x, const char* __restrict__ wqkv, const char* __restrict__ wo,
                                                       const char* __restrict__ w1, const char* __restrict__ b1, const char* __restrict__ w2,
                                                       char* __restrict__ ws) {
  __shared__ __attribute__((aligned(16))) char lds[32768];
  const int u = blockIdx.x, lane = threadIdx.x & 63, w = __builtin_amdgcn_readfirstlane(threadIdx.x >> 6);
  const int fq = lane >> 4, lr = lane & 15;
  if (u < 128) {
    const int rb = u >> 3, cb = u & 7;
    u32x4 c[2][4];
#pragma unroll
    for (int mf = 0; mf < 2; ++mf)
#pragma unroll
      for (int k = 0; k < 4; ++k)
        c[mf][k] = *(const u32x4*)(x + ((size_t)(rb * 128 + w * 32 + mf * 16 + lr) * DM + cb * 128 + k * 32 + fq * 8) * 2);
#pragma unroll
    for (int mf = 0; mf < 2; ++mf) {
#pragma unroll
      for (int k = 0; k < 4; ++k)
        *(u32x4*)(ws + WS_X + ((size_t)((rb * 32 + cb * 4 + k) * 8 + w * 2 + mf) * 64 + lane) * 16) = c[mf][k];
#pragma unroll
      for (int h = 0; h < 2; ++h) {
        f32x2 s = row_stats64(c[mf][2 * h], c[mf][2 * h + 1]);
        if (fq == 0) *(f32x2*)(ws + WS_ST + ((size_t)((rb * 4 + w) * 16 + cb * 2 + h) * 32 + mf * 16 + lr) * 8) = s;
      }
    }
    return;
  }
  int kind, ku, nb, N;
  const char* src;
  if (u < 320) { kind = 0; ku = (u - 128) & 7; nb = (u - 128) >> 3; N = 3072; src = wqkv; }
  else if (u < 384) { kind = 1; ku = (u - 320) & 7; nb = (u - 320) >> 3; N = 1024; src = wo; }
  else if (u < 608) { kind = 2; ku = (u - 384) & 7; nb = (u - 384) >> 3; N = FF; src = w1; }
  else { kind = 3; nb = (u - 608) & 7; ku = (u - 608) >> 3; N = 1024; src = w2; }
  src += ((size_t)ku * 128 * N + nb * 128) * 2;
#pragma unroll
  for (int i = 0; i < 8; ++i) {
    const int p = w * 8 + i, row = p * 4 + fq;
    const int ch = lr ^ (((row & 3) << 2) | ((row >> 2) & 3));
    __builtin_amdgcn_global_load_lds((const void*)(src + (size_t)row * N * 2 + ch * 16), (lds_void*)(lds + p * 1024), 16, 0, 0);
  }
  const int G = nb * 4 + w;
  int tile, slot0, slot1, nslot, kbt, cscol = 0;
  bool vperm = false;
  char* dst;
  float* cs = nullptr;
  if (kind == 0) {
    const int part = G >> 5, head = G & 31;
    tile = head >> 1; slot0 = (head & 1) * 6 + part * 2; slot1 = slot0 + 1; nslot = 12; kbt = 32; vperm = part == 2;
    dst = ws + WS_WQKV; cs = (float*)(ws + WS_CSQ) + tile * 2048 + ku * 192; cscol = (head & 1) * 96 + part * 32;
  } else if (kind == 2) {
    const int gg = G % 7;
    tile = G / 7; nslot = 14; kbt = 32;
    if (gg < 3) { slot0 = 2 * gg; slot1 = slot0 + 1; } else if (gg == 3) { slot0 = 6; slot1 = 13; } else { slot0 = 7 + 2 * (gg - 4); slot1 = slot0 + 1; }
    dst = ws + WS_W1; cs = (float*)(ws + WS_CS1) + tile * 2048 + ku * 224; cscol = gg * 32;
    if (ku == 0 && lane < 32) cs[8 * 224 + cscol + lane] = bf_lo(*(const unsigned short*)(b1 + (G * 32 + lane) * 2));
  } else {
    tile = G >> 1; slot0 = (G & 1) * 2; slot1 = slot0 + 1; nslot = 4; kbt = kind == 1 ? 32 : 112;
    dst = ws + (kind == 1 ? WS_WO : WS_W2);
  }
  __syncthreads();
  const int q = (lane >> 2) & 3, p = lane & 3;
  bf16x8 ones;
#pragma unroll
  for (int i = 0; i < 8; ++i) ones[i] = (__bf16)1.0f;
#pragma unroll
  for (int j = 0; j < 2; ++j) {
    const int cbase = vperm ? 16 * j + 8 * (p & 1) + 4 * (p >> 1) : 8 * p + 4 * j;
    const int col = w * 32 + cbase, chunk = col >> 3, half = (col >> 2) & 1;
    f32x4 acc = {0.f, 0.f, 0.f, 0.f};
#pragma unroll
    for (int kbl = 0; kbl < 4; ++kbl) {
      const int r0 = kbl * 32 + fq * 8 + q, r1 = r0 + 4;
      const int a0 = 256 * r0 + 16 * (chunk ^ (((r0 & 3) << 2) | ((r0 >> 2) & 3))) + 8 * half;
      const int a1 = 256 * r1 + 16 * (chunk ^ (((r1 & 3) << 2) | ((r1 >> 2) & 3))) + 8 * half;
      s16x4 v0 = __builtin_amdgcn_ds_read_tr16_b64_v4i16((lds_s16x4*)(lds + a0));
      s16x4 v1 = __builtin_amdgcn_ds_read_tr16_b64_v4i16((lds_s16x4*)(lds + a1));
      u32x2 w0 = __builtin_bit_cast(u32x2, v0), w1v = __builtin_bit_cast(u32x2, v1);
      u32x4 f = {w0[0], w0[1], w1v[0], w1v[1]};
      *(u32x4*)(dst + ((size_t)((tile * kbt + ku * 4 + kbl) * nslot + (j ? slot1 : slot0)) * 64 + lane) * 16) = f;
      if (cs) acc = mfma16(__builtin_bit_cast(bf16x8, f), ones, acc);
    }
    if (cs && lr == 0) {
      const int cb2 = vperm ? 16 * j + 8 * (fq & 1) + 4 * (fq >> 1) : 8 * fq + 4 * j;
      *(f32x4*)(cs + cscol + cb2) = acc;
    }
  }
}

enum { G_QKV = 0, G_WO = 1, G_W1 = 2, G_W2 = 3, G_W2L = 4 };
template <int MODE> struct Cfg;
template <> struct Cfg<G_QKV> { static constexpr int NSLOT = 12, WN = 2, MF = 4, NF = 6, KB = 32, KS = 2, NS = 3, XM = 8, XN = 4; static constexpr bool AUX = true; };
template <> struct Cfg<G_W1> { static constexpr int NSLOT = 14, WN = 2, MF = 4, NF = 7, KB = 32, KS = 2, NS = 3, XM = 8, XN = 4; static constexpr bool AUX = true; };
template <> struct Cfg<G_WO> { static constexpr int NSLOT = 4, WN = 1, MF = 2, NF = 4, KB = 32, KS = 4, NS = 3, XM = 4, XN = 8; static constexpr bool AUX = false; };
template <> struct Cfg<G_W2> { static constexpr int NSLOT = 4, WN = 1, MF = 2, NF = 4, KB = 112, KS = 4, NS = 3, XM = 4, XN = 8; static constexpr bool AUX = false; };
template <> struct Cfg<G_W2L> : Cfg<G_W2> {};
template <int MODE> constexpr int gemm_lds_bytes() {
  using C = Cfg<MODE>;
  return C::NS * C::KS * (8 + C::NSLOT) * 1024 + (C::AUX ? 26 * 1024 : 0);
}

template <int MODE>
__global__ __launch_bounds__(512, 2) void gemm_kernel(const char* __restrict__ A, const char* __restrict__ W, char* __restrict__ ws,
                                                      const char* __restrict__ bias, char* __restrict__ out) {
  using C = Cfg<MODE>;
  constexpr int NSLOT = C::NSLOT, WN = C::WN, MF = C::MF, NF = C::NF, KB = C::KB, KS = C::KS, NS = C::NS;
  constexpr int ASTG = KS * 8 * 1024, BSTG = KS * NSLOT * 1024, SLOTB = ASTG + BSTG, NT = KB / KS;
  constexpr int PA = KS * 2, PB = KS * NSLOT / 4, P = PA + PB;
  constexpr int AUXO = NS * SLOTB;
  constexpr int CSRAW = AUXO + 16384, FIN = AUXO + 24576;
  constexpr int BN = NSLOT * 16;
  static_assert(KS * NSLOT % 4 == 0 && KS % 2 == 0 && NT >= 5, "cfg");
  extern __shared__ __attribute__((aligned(16))) char lds[];
  const int lane = threadIdx.x & 63, wid = __builtin_amdgcn_readfirstlane(threadIdx.x >> 6);
  const int xcd = blockIdx.x & 7, idx = blockIdx.x >> 3;
  constexpr int GX = 16 / C::XN;
  const int rb = (xcd / GX) * C::XM + idx / C::XN, tn = (xcd % GX) * C::XN + idx % C::XN;
  if (wid < 4) STAMP(MODE, 0);

  if (wid >= 4) {
    const int w = wid - 4;
    const unsigned lbase = (unsigned)(uintptr_t)(lds_char*)lds;
    const unsigned voff = lane * 16;
    const char* ga = A + (size_t)rb * KB * 8192 + w * PA * 1024;
    const char* gb = W + (size_t)tn * KB * NSLOT * 1024 + w * PB * 1024;
    const unsigned la = lbase + w * PA * 1024, lb = lbase + ASTG + w * PB * 1024;
#pragma unroll
    for (int s = 0; s < NS; ++s) {
      glds<PA>(la + s * SLOTB, ga, voff); glds<PB>(lb + s * SLOTB, gb, voff);
      ga += ASTG; gb += BSTG;
    }
    constexpr int NA = C::AUX ? 6 : 0;
    if constexpr (C::AUX) {
      glds<4>(lbase + AUXO + w * 4096, ws + WS_ST + (size_t)(rb * 4 + w) * 4096, voff);
      glds<2>(lbase + CSRAW + w * 2048, ws + (MODE == G_QKV ? WS_CSQ : WS_CS1) + (size_t)tn * 8192 + w * 2048, voff);
    }
    wait_vm<(NS - 1) * P + NA>();
    bar();
    int slot = 0;
    for (int t = 0; t < NT - 1; ++t) {
      if (t + NS - 1 < NT) {
        if (t < NS - 1) wait_vm<(NS - 2) * P + NA>(); else wait_vm<(NS - 2) * P>();
      } else wait_vm<0>();
      bar();
      if (t + NS < NT) {
        glds<PA>(la + slot * SLOTB, ga, voff); glds<PB>(lb + slot * SLOTB, gb, voff);
        ga += ASTG; gb += BSTG;
      }
      slot = slot == NS - 1 ? 0 : slot + 1;
      if constexpr (C::AUX) {
        if (t == NS - 1) {
          const int row = lane & 31, hf = lane >> 5;
          f32x2 pt[8];
#pragma unroll
          for (int j = 0; j < 8; ++j) pt[j] = *(const f32x2*)(lds + AUXO + w * 4096 + (hf * 8 + j) * 256 + row * 8);
          float s = 0.f;
#pragma unroll
          for (int j = 0; j < 8; ++j) s += pt[j][0];
          const float mu = xsum32(s) * (1.f / 16.f);
          float q = 0.f;
#pragma unroll
          for (int j = 0; j < 8; ++j) { float d = pt[j][0] - mu; q += fmaf(64.f * d, d, pt[j][1]); }
          const float var = xsum32(q) * (1.f / 1024.f);
          float r = __builtin_amdgcn_rsqf(var + LN_EPS);
          if constexpr (MODE == G_W1) r *= __builtin_amdgcn_rsqf(var * r * r + LN_EPS);
          if (lane < 32) {
            *(float*)(lds + FIN + (w * 32 + row) * 4) = r;
            *(float*)(lds + FIN + 512 + (w * 32 + row) * 4) = -mu * r;
          }
          if (lane < BN / 4) {
            const int col = w * (BN / 4) + lane;
            float c = 0.f;
#pragma unroll
            for (int j = 0; j < 8; ++j) c += *(const float*)(lds + CSRAW + (j * BN + col) * 4);
            *(float*)(lds + FIN + 1024 + col * 4) = c;
          }
          asm volatile("s_waitcnt lgkmcnt(0)" ::: "memory");
        }
      }
    }
    return;
  }

  const int wm = wid / WN, wn = wid % WN;
  const int fq = lane >> 4, lr = lane & 15;
  u32x4 res[2][2], bia[2];
  if constexpr (MODE == G_WO || MODE == G_W2 || MODE == G_W2L) {
#pragma unroll
    for (int mf = 0; mf < 2; ++mf)
#pragma unroll
      for (int g = 0; g < 2; ++g)
        res[mf][g] = *(const u32x4*)(ws + WS_X + ((size_t)((rb * 32 + tn * 2 + g) * 8 + wm * 2 + mf) * 64 + lane) * 16);
    if constexpr (MODE != G_WO) {
#pragma unroll
      for (int g = 0; g < 2; ++g) bia[g] = *(const u32x4*)(bias + (tn * 64 + g * 32 + fq * 8) * 2);
    }
  }
  f32x4 acc[MF][NF];
#pragma unroll
  for (int m = 0; m < MF; ++m)
#pragma unroll
    for (int n = 0; n < NF; ++n) acc[m][n] = f32x4{0.f, 0.f, 0.f, 0.f};
  bf16x8 fa[2][MF], fb[2][NF];
  const char* pa = lds + wm * MF * 1024 + lane * 16;
  const char* pb = lds + ASTG + wn * NF * 1024 + lane * 16;
  auto rd = [&](int buf, int so, int ks) {
#pragma unroll
    for (int m = 0; m < MF; ++m) fa[buf][m] = *(const bf16x8*)(pa + so + (ks * 8 + m) * 1024);
#pragma unroll
    for (int n = 0; n < NF; ++n) fb[buf][n] = *(const bf16x8*)(pb + so + (ks * NSLOT + n) * 1024);
  };
  auto mm = [&](int buf) {
#pragma unroll
    for (int n = 0; n < NF; ++n)
#pragma unroll
      for (int m = 0; m < MF; ++m) {
        if (MODE == G_QKV && n >= 4) acc[m][n] = mfma16(fa[buf][m], fb[buf][n], acc[m][n]);
        else acc[m][n] = mfma16(fb[buf][n], fa[buf][m], acc[m][n]);
      }
  };
  auto inter = [&]() {
    constexpr int NR = MF + NF, NM = MF * NF, PER = NM / NR;
#pragma unroll
    for (int i = 0; i < NR; ++i) {
      __builtin_amdgcn_sched_group_barrier(0x008, PER, 0);
      __builtin_amdgcn_sched_group_barrier(0x100, 1, 0);
    }
    __builtin_amdgcn_sched_group_barrier(0x008, NM - PER * NR, 0);
  };
  bar();
  STAMP(MODE, 1);
  rd(0, 0, 0);
  int so = 0;
  for (int t = 0; t < NT - 1; ++t) {
#pragma unroll
    for (int ks = 0; ks < KS - 1; ++ks) {
      rd((ks + 1) & 1, so, ks + 1);
      mm(ks & 1);
      inter();
      SCHED0();
    }
    lgkm_bar();
    so = so == (NS - 1) * SLOTB ? 0 : so + SLOTB;
    rd(0, so, 0);
    mm(1);
    inter();
    SCHED0();
  }
#pragma unroll
  for (int ks = 0; ks < KS - 1; ++ks) {
    rd((ks + 1) & 1, so, ks + 1);
    mm(ks & 1);
    inter();
    SCHED0();
  }
  mm(1);
  STAMP(MODE, 2);

  if constexpr (MODE == G_QKV) {
    const float* RS = (const float*)(lds + FIN);
    const float* CM = RS + 128;
    const float* CS = RS + 256 + wn * 96;
    const int h = tn * 2 + wn;
    constexpr float QS = 0.17677669529663687f * 1.4426950408889634f;
    f32x4 cq0 = *(const f32x4*)(CS + fq * 8), cq1 = *(const f32x4*)(CS + fq * 8 + 4);
    f32x4 ck0 = *(const f32x4*)(CS + 32 + fq * 8), ck1 = *(const f32x4*)(CS + 32 + fq * 8 + 4);
#pragma unroll
    for (int mf = 0; mf < 4; ++mf) {
      const int row = wm * 64 + mf * 16 + lr;
      const float r = RS[row], cm = CM[row], rq = r * QS, cmq = cm * QS;
      const int qb = rb * 4 + wm * 2 + (mf >> 1), b = mf & 1;
      u32x4 o;
      o[0] = pack2(fmaf(acc[mf][0][0], rq, cmq * cq0[0]), fmaf(acc[mf][0][1], rq, cmq * cq0[1]));
      o[1] = pack2(fmaf(acc[mf][0][2], rq, cmq * cq0[2]), fmaf(acc[mf][0][3], rq, cmq * cq0[3]));
      o[2] = pack2(fmaf(acc[mf][1][0], rq, cmq * cq1[0]), fmaf(acc[mf][1][1], rq, cmq * cq1[1]));
      o[3] = pack2(fmaf(acc[mf][1][2], rq, cmq * cq1[2]), fmaf(acc[mf][1][3], rq, cmq * cq1[3]));
      *(u32x4*)(ws + WS_Q + (size_t)((h * 64 + qb) * 2 + (fq >> 1)) * 1024 + (16 * b + lr + 32 * (fq & 1)) * 16) = o;
      o[0] = pack2(fmaf(acc[mf][2][0], r, cm * ck0[0]), fmaf(acc[mf][2][1], r, cm * ck0[1]));
      o[1] = pack2(fmaf(acc[mf][2][2], r, cm * ck0[2]), fmaf(acc[mf][2][3], r, cm * ck0[3]));
      o[2] = pack2(fmaf(acc[mf][3][0], r, cm * ck1[0]), fmaf(acc[mf][3][1], r, cm * ck1[1]));
      o[3] = pack2(fmaf(acc[mf][3][2], r, cm * ck1[2]), fmaf(acc[mf][3][3], r, cm * ck1[3]));
      const int rho = 8 * (lr >> 2) + 4 * b + (lr & 3);
      *(u32x4*)(ws + WS_KV + (size_t)((h * 64 + qb) * 4 + (fq >> 1)) * 1024 + (rho + 32 * (fq & 1)) * 16) = o;
    }
    const int dv = 8 * ((lr >> 2) & 1) + 4 * (lr >> 3) + (lr & 3);
    const float cv0 = CS[64 + dv], cv1 = CS[64 + 16 + dv];
#pragma unroll
    for (int a = 0; a < 2; ++a) {
      const int row = wm * 64 + a * 32 + fq * 4;
      const f32x4 r0 = *(const f32x4*)(RS + row), r1 = *(const f32x4*)(RS + row + 16);
      const f32x4 c0 = *(const f32x4*)(CM + row), c1 = *(const f32x4*)(CM + row + 16);
      const int qb = rb * 4 + wm * 2 + a;
#pragma unroll
      for (int j = 0; j < 2; ++j) {
        const float cv = j ? cv1 : cv0;
        const f32x4 x0 = acc[2 * a][4 + j], x1 = acc[2 * a + 1][4 + j];
        u32x4 o;
        o[0] = pack2(fmaf(x0[0], r0[0], c0[0] * cv), fmaf(x0[1], r0[1], c0[1] * cv));
        o[1] = pack2(fmaf(x0[2], r0[2], c0[2] * cv), fmaf(x0[3], r0[3], c0[3] * cv));
        o[2] = pack2(fmaf(x1[0], r1[0], c1[0] * cv), fmaf(x1[1], r1[1], c1[1] * cv));
        o[3] = pack2(fmaf(x1[2], r1[2], c1[2] * cv), fmaf(x1[3], r1[3], c1[3] * cv));
        *(u32x4*)(ws + WS_KV + (size_t)((h * 64 + qb) * 4 + 2 + (fq >> 1)) * 1024 + (16 * j + lr + 32 * (fq & 1)) * 16) = o;
      }
    }
  } else if constexpr (MODE == G_W1) {
    const float* RS = (const float*)(lds + FIN);
    const float* CM = RS + 128;
    const float* CS = RS + 256;
    const float* B1 = (const float*)(lds + CSRAW) + 8 * 224;
    constexpr float K1 = -2.f * 0.7978845608028654f * 1.4426950408889634f, K2 = K1 * 0.044715f;
    float r[4], cm[4];
#pragma unroll
    for (int mf = 0; mf < 4; ++mf) { r[mf] = RS[wm * 64 + mf * 16 + lr]; cm[mf] = CM[wm * 64 + mf * 16 + lr]; }
    auto gelu4 = [&](f32x4 a, float rr, float cc, f32x4 c, f32x4 b, unsigned& o0, unsigned& o1) {
      float g[4];
#pragma unroll
      for (int i = 0; i < 4; ++i) {
        const float v = fmaf(a[i], rr, fmaf(cc, c[i], b[i]));
        const float e = __builtin_amdgcn_exp2f(v * fmaf(v * v, K2, K1));
        g[i] = v * __builtin_amdgcn_rcpf(1.f + e);
      }
      o0 = pack2(g[0], g[1]); o1 = pack2(g[2], g[3]);
    };
#pragma unroll
    for (int gp = 0; gp < 3; ++gp) {
      const int grp = wn * 4 + gp, col = grp * 32 + fq * 8;
      const f32x4 c0 = *(const f32x4*)(CS + col), c1 = *(const f32x4*)(CS + col + 4);
      const f32x4 b0 = *(const f32x4*)(B1 + col), b1v = *(const f32x4*)(B1 + col + 4);
#pragma unroll
      for (int mf = 0; mf < 4; ++mf) {
        unsigned o0, o1, o2, o3;
        gelu4(acc[mf][2 * gp], r[mf], cm[mf], c0, b0, o0, o1);
        gelu4(acc[mf][2 * gp + 1], r[mf], cm[mf], c1, b1v, o2, o3);
        u32x4 o = {o0, o1, o2, o3};
        *(u32x4*)(ws + WS_H + ((size_t)((rb * 112 + tn * 7 + grp) * 8 + wm * 4 + mf) * 64 + lane) * 16) = o;
      }
    }
    {
      const int col = 96 + fq * 8 + wn * 4;
      const f32x4 c0 = *(const f32x4*)(CS + col), b0 = *(const f32x4*)(B1 + col);
#pragma unroll
      for (int mf = 0; mf < 4; ++mf) {
        unsigned o0, o1;
        gelu4(acc[mf][6], r[mf], cm[mf], c0, b0, o0, o1);
        u32x2 o = {o0, o1};
        *(u32x2*)(ws + WS_H + ((size_t)((rb * 112 + tn * 7 + 3) * 8 + wm * 4 + mf) * 64 + lane) * 16 + wn * 8) = o;
      }
    }
  } else {
#pragma unroll
    for (int mf = 0; mf < 2; ++mf) {
      u32x4 o[2];
#pragma unroll
      for (int g = 0; g < 2; ++g) {
        float v[8];
#pragma unroll
        for (int i = 0; i < 4; ++i) { v[i] = acc[mf][2 * g][i]; v[4 + i] = acc[mf][2 * g + 1][i]; }
#pragma unroll
        for (int i = 0; i < 4; ++i) {
          v[2 * i] += bf_lo(res[mf][g][i]); v[2 * i + 1] += bf_hi(res[mf][g][i]);
          if constexpr (MODE != G_WO) { v[2 * i] += bf_lo(bia[g][i]); v[2 * i + 1] += bf_hi(bia[g][i]); }
          o[g][i] = pack2(v[2 * i], v[2 * i + 1]);
        }
        if constexpr (MODE == G_W2L)
          *(u32x4*)(out + ((size_t)(rb * 128 + wm * 32 + mf * 16 + lr) * DM + tn * 64 + g * 32 + fq * 8) * 2) = o[g];
        else
          *(u32x4*)(ws + WS_X + ((size_t)((rb * 32 + tn * 2 + g) * 8 + wm * 2 + mf) * 64 + lane) * 16) = o[g];
      }
      if constexpr (MODE != G_W2L) {
        f32x2 s = row_stats64(o[0], o[1]);
        if (fq == 0) *(f32x2*)(ws + WS_ST + ((size_t)((rb * 4 + wm) * 16 + tn) * 32 + mf * 16 + lr) * 8) = s;
      }
    }
  }
  STAMP(MODE, 3);
}

constexpr int ATT_TPS = 4;
constexpr int ATT_STG = ATT_TPS * 4096, ATT_RING = 4, ATT_NST = 64 / ATT_TPS, ATT_PP = ATT_STG / 8192;
constexpr int ATT_LDS = ATT_STG * ATT_RING;

DEV f32x16 smf32(bf16x8 a, f32x8 b, f32x16 c, int idx) {
  return __builtin_amdgcn_smfmac_f32_32x32x32_bf16(a, __builtin_bit_cast(bf16x16, b), c, idx, 0, 0);
}
DEV f32x4 smf16(bf16x8 a, f32x8 b, f32x4 c, int idx) {
  return __builtin_amdgcn_smfmac_f32_16x16x64_bf16(a, __builtin_bit_cast(bf16x16, b), c, idx, 0, 0);
}
DEV f32x8 lo8(f32x16 v) { return __builtin_shufflevector(v, v, 0, 1, 2, 3, 4, 5, 6, 7); }
DEV f32x8 hi8(f32x16 v) { return __builtin_shufflevector(v, v, 8, 9, 10, 11, 12, 13, 14, 15); }
DEV float max16(f32x16 s) {
  float a = fmaxf(fmaxf(s[0], s[1]), s[2]), b = fmaxf(fmaxf(s[3], s[4]), s[5]), c = fmaxf(fmaxf(s[6], s[7]), s[8]);
  float d = fmaxf(fmaxf(s[9], s[10]), s[11]), e = fmaxf(fmaxf(s[12], s[13]), s[14]);
  return fmaxf(fmaxf(fmaxf(a, b), c), fmaxf(fmaxf(d, e), s[15]));
}

__global__ __launch_bounds__(512, 2) void attn_kernel(char* __restrict__ ws) {
  extern __shared__ __attribute__((aligned(16))) char lds[];
  const int lane = threadIdx.x & 63, wid = __builtin_amdgcn_readfirstlane(threadIdx.x >> 6);
  const int xcd = blockIdx.x & 7, idx = blockIdx.x >> 3;
  const int h = xcd * 4 + (idx >> 3), qblk = idx & 7;
  const unsigned lbase = (unsigned)(uintptr_t)(lds_char*)lds;
  const unsigned voff = lane * 16;
  const char* kvh = ws + WS_KV + (size_t)h * 64 * 4096;
  const char* gsrc = kvh + wid * ATT_PP * 1024;
  const unsigned ldst = lbase + wid * ATT_PP * 1024;
  const char* qsrc = ws + WS_Q + (size_t)(h * 64 + qblk * 8 + wid) * 2048;
  bf16x8 q0, q1;
  STAMP(5, 0);
  asm volatile("global_load_dwordx4 %0, %2, %3\n\tglobal_load_dwordx4 %1, %2, %3 offset:1024" : "=&v"(q0), "=&v"(q1) : "v"(voff), "s"(qsrc) : "memory");
#pragma unroll
  for (int s = 0; s < ATT_RING - 1; ++s) { glds<ATT_PP>(ldst + s * ATT_STG, gsrc, voff); gsrc += ATT_STG; }
  bf16x8 sel;
  {
    const bool odd = (lane >> 2) & 1;
#pragma unroll
    for (int e = 0; e < 8; ++e) sel[e] = ((e >= 4) == odd) ? (__bf16)1.0f : (__bf16)0.0f;
  }
  const int sidx = 0xDDDD;
  asm volatile("s_waitcnt vmcnt(%2)" : "+v"(q0), "+v"(q1) : "i"((ATT_RING - 2) * ATT_PP) : "memory");
  bar();
  STAMP(5, 1);
  const char* lp = lds + lane * 16;
#define KF(off, s) (*(const bf16x8*)(lp + (off) + (s) * 1024))
#define VF(off, s) (*(const bf16x8*)(lp + (off) + 2048 + (s) * 1024))
  const f32x16 zero16 = {};
  f32x16 negm;
  f32x16 sa, sb;
  bf16x8 k0 = KF(0, 0), k1 = KF(0, 1);
  {
    f32x16 s = mfma32(k1, q1, mfma32(k0, q0, zero16));
    const float m = xmax32(max16(s));
#pragma unroll
    for (int i = 0; i < 16; ++i) { negm[i] = -m; sa[i] = s[i] - m; }
  }
  f32x16 o = zero16;
  f32x4 l = {0.f, 0.f, 0.f, 0.f};
  sb = zero16;
  bf16x8 v0, v1a, v1b = {};
  const unsigned laddr = lbase + lane * 16;
#define E4(a, b, c, d) "v_exp_f32 v" #a ", v" #a "\n\tv_exp_f32 v" #b ", v" #b "\n\tv_exp_f32 v" #c ", v" #c "\n\tv_exp_f32 v" #d ", v" #d "\n\t"
#define STEP_(N, NHI, CLO, V1P, EA, EB, EC, ED, K0O, K1O, V0O, V1O, MID)                \
  "v_smfmac_f32_32x32x32_bf16 v[64:79], " V1P ", " NHI ", v116\n\t" EA                 \
  "v_smfmac_f32_16x16x64_bf16 v[80:83], v[84:87], " NHI ", v116\n\t"                   \
  "s_waitcnt lgkmcnt(2)\n\t"                                                            \
  "v_mfma_f32_32x32x16_bf16 " N ", v[96:99], v[88:91], v[48:63]\n\t" EB                \
  "v_mfma_f32_32x32x16_bf16 " N ", v[100:103], v[92:95], " N "\n\t" MID                \
  "ds_read_b128 v[96:99], v117 offset:" #K0O "\n\t"                                     \
  "ds_read_b128 v[100:103], v117 offset:" #K1O "\n\t" EC                                \
  "s_waitcnt lgkmcnt(2)\n\t"                                                            \
  "v_smfmac_f32_32x32x32_bf16 v[64:79], v[104:107], " CLO ", v116\n\t"                 \
  "ds_read_b128 v[104:107], v117 offset:" #V0O "\n\t" ED                                \
  "v_smfmac_f32_16x16x64_bf16 v[80:83], v[84:87], " CLO ", v116\n\t"                   \
  "ds_read_b128 " V1P ", v117 offset:" #V1O "\n\t"
#define STEPA(K0O, K1O, V0O, V1O, MID) STEP_("v[32:47]", "v[40:47]", "v[16:23]", "v[112:115]", E4(16, 17, 18, 19), E4(20, 21, 22, 23), E4(24, 25, 26, 27), E4(28, 29, 30, 31), K0O, K1O, V0O, V1O, MID)
#define STEPB(K0O, K1O, V0O, V1O, MID) STEP_("v[16:31]", "v[24:31]", "v[32:39]", "v[108:111]", E4(32, 33, 34, 35), E4(36, 37, 38, 39), E4(40, 41, 42, 43), E4(44, 45, 46, 47), K0O, K1O, V0O, V1O, MID)
#define MIDX(LBL, SLOTOFF)                                                              \
  "s_cmp_lt_u32 %[t], 14\n\ts_cbranch_scc1 1f\n\ts_waitcnt vmcnt(0)\n"                 \
  "1:\n\ts_waitcnt vmcnt(2)\n\ts_barrier\n\t"                                           \
  "s_cmp_lt_u32 %[t], 13\n\ts_cbranch_scc0 2f\n\t"                                      \
  "s_add_u32 m0, %[ld], " #SLOTOFF "\n\ts_nop 0\n\t"                                    \
  "global_load_lds_dwordx4 v118, %[g]\n\tglobal_load_lds_dwordx4 v118, %[g] offset:1024\n\t" \
  "s_add_u32 s20, s20, 0x4000\n\ts_addc_u32 s21, s21, 0\n"                  \
  "2:\n\ts_add_u32 %[t], %[t], 1\n\t"
  {
    unsigned t = 0;
    asm volatile(
        "ds_read_b128 v[96:99], v117 offset:4096\n\tds_read_b128 v[100:103], v117 offset:5120\n\t"
        "ds_read_b128 v[104:107], v117 offset:2048\n\tds_read_b128 v[108:111], v117 offset:3072\n\t"
        "s_nop 1\n"
        "3:\n\t"
        STEPA(8192, 9216, 6144, 7168, "") STEPB(12288, 13312, 10240, 11264, "")
        STEPA(16384, 17408, 14336, 15360, MIDX(a, 49152)) STEPB(20480, 21504, 18432, 19456, "")
        STEPA(24576, 25600, 22528, 23552, "") STEPB(28672, 29696, 26624, 27648, "")
        STEPA(32768, 33792, 30720, 31744, MIDX(b, 0)) STEPB(36864, 37888, 34816, 35840, "")
        STEPA(40960, 41984, 38912, 39936, "") STEPB(45056, 46080, 43008, 44032, "")
        STEPA(49152, 50176, 47104, 48128, MIDX(c, 16384)) STEPB(53248, 54272, 51200, 52224, "")
        STEPA(57344, 58368, 55296, 56320, "") STEPB(61440, 62464, 59392, 60416, "")
        STEPA(0, 1024, 63488, 64512, MIDX(d, 32768)) STEPB(4096, 5120, 2048, 3072, "")
        "s_cmp_lt_u32 %[t], 16\n\ts_cbranch_scc1 3b\n\t"
        "v_smfmac_f32_32x32x32_bf16 v[64:79], v[112:115], v[40:47], v116\n\t"
        "v_smfmac_f32_16x16x64_bf16 v[80:83], v[84:87], v[40:47], v116\n\t"
        "s_waitcnt lgkmcnt(0)\n\ts_nop 15\n\t"
        : "+{v[16:31]}"(sa), "+{v[32:47]}"(sb), "+{v[64:79]}"(o), "+{v[80:83]}"(l), "=&{v[96:99]}"(k0), "=&{v[100:103]}"(k1),
          "=&{v[104:107]}"(v0), "=&{v[108:111]}"(v1a), "+{v[112:115]}"(v1b), [t] "+s"(t), [g] "+{s[20:21]}"(gsrc)
        : "{v[48:63]}"(negm), "{v[84:87]}"(sel), "{v[88:91]}"(q0), "{v[92:95]}"(q1), "{v116}"(sidx), "{v117}"(laddr), "{v118}"(voff), [ld] "s"(ldst)
        : "memory", "scc");
  }
  STAMP(5, 2);
  bool bad = !(l[0] < 1.8446744e19f);
#pragma unroll
  for (int i = 0; i < 16; ++i) bad |= !(fabsf(o[i]) < 3.0e38f);
  if (__builtin_amdgcn_ballot_w64(bad) != 0) {
    o = zero16; l = f32x4{0.f, 0.f, 0.f, 0.f};
    float m = -__builtin_inff();
    for (int t = 0; t < 64; ++t) {
      const char* g = kvh + t * 4096 + lane * 16;
      const bf16x8 a0 = *(const bf16x8*)g, a1 = *(const bf16x8*)(g + 1024), b0 = *(const bf16x8*)(g + 2048), b1 = *(const bf16x8*)(g + 3072);
      f32x16 s = mfma32(a1, q1, mfma32(a0, q0, zero16));
      const float mn = fmaxf(m, xmax32(max16(s)));
      const float al = __builtin_amdgcn_exp2f(m - mn);
      m = mn;
#pragma unroll
      for (int i = 0; i < 16; ++i) { s[i] = __builtin_amdgcn_exp2f(s[i] - mn); o[i] *= al; }
#pragma unroll
      for (int i = 0; i < 4; ++i) l[i] *= al;
      o = smf32(b0, lo8(s), o, sidx); o = smf32(b1, hi8(s), o, sidx);
      l = smf16(sel, lo8(s), l, sidx); l = smf16(sel, hi8(s), l, sidx);
    }
  }
  const float inv = 1.f / l[0];
  const int q0r = qblk * 256 + wid * 32;
  char* od = ws + WS_O + ((size_t)(((q0r >> 7) * 32 + h) * 8 + ((q0r & 127) >> 4) + ((lane >> 4) & 1)) * 64 + (lane & 15) + 16 * (lane >> 5)) * 16;
#pragma unroll
  for (int c = 0; c < 2; ++c) {
    u32x4 w;
#pragma unroll
    for (int i = 0; i < 4; ++i) w[i] = pack2(o[8 * c + 2 * i] * inv, o[8 * c + 2 * i + 1] * inv);
    *(u32x4*)(od + c * 512) = w;
  }
  STAMP(5, 3);
}

#define STOP_AFTER 100
template <int MODE>
static void launch_gemm(const char* A, const char* W, char* ws, const char* bias, char* out, hipStream_t stream) {
  constexpr int L = gemm_lds_bytes<MODE>();
  (void)hipFuncSetAttribute((const void*)gemm_kernel<MODE>, hipFuncAttributeMaxDynamicSharedMemorySize, L);
  gemm_kernel<MODE><<<256, 512, L, stream>>>(A, W, ws, bias, out);
}

extern "C" void kernel_launch(void* const* d_in, const int* in_sizes, int n_in, void* d_out, int out_size, void* d_ws, size_t ws_size,
                              hipStream_t stream) {
  const char* x = (const char*)d_in[0];
  const char* wqkv = (const char*)d_in[1];
  const char* wo = (const char*)d_in[2];
  const char* w1 = (const char*)d_in[4];
  const char* b1 = (const char*)d_in[5];
  const char* w2 = (const char*)d_in[6];
  const char* b2 = (const char*)d_in[7];
  char* ws = (char*)d_ws;
  char* out = (char*)d_out;
  int n = 0;
  prologue_kernel<<<832, 256, 0, stream>>>(x, wqkv, wo, w1, b1, w2, ws);
  (void)hipFuncSetAttribute((const void*)attn_kernel, hipFuncAttributeMaxDynamicSharedMemorySize, ATT_LDS);
  for (int layer = 0; layer < 2; ++layer) {
    if (++n > STOP_AFTER) return;
    launch_gemm<G_QKV>(ws + WS_X, ws + WS_WQKV, ws, b2, out, stream);
    if (++n > STOP_AFTER) return;
    attn_kernel<<<256, 512, ATT_LDS, stream>>>(ws);
    if (++n > STOP_AFTER) return;
    launch_gemm<G_WO>(ws + WS_O, ws + WS_WO, ws, b2, out, stream);
    if (++n > STOP_AFTER) return;
    launch_gemm<G_W1>(ws + WS_X, ws + WS_W1, ws, b2, out, stream);
    if (++n > STOP_AFTER) return;
    if (layer == 0) launch_gemm<G_W2>(ws + WS_H, ws + WS_W2, ws, b2, out, stream);
    else launch_gemm<G_W2L>(ws + WS_H, ws + WS_W2, ws, b2, out, stream);
  }
}
```

```cpp
#include <hip/hip_runtime.h>
#include <stdint.h>

typedef __attribute__((ext_vector_type(8))) __bf16 bf16x8;
typedef __attribute__((ext_vector_type(16))) __bf16 bf16x16;
typedef __attribute__((ext_vector_type(2))) __bf16 bf16x2;
typedef __attribute__((ext_vector_type(2))) float f32x2;
typedef __attribute__((ext_vector_type(4))) float f32x4;
typedef __attribute__((ext_vector_type(8))) float f32x8;
typedef __attribute__((ext_vector_type(16))) float f32x16;
typedef __attribute__((ext_vector_type(4))) unsigned u32x4;
typedef __attribute__((ext_vector_type(2))) unsigned u32x2;
typedef __attribute__((ext_vector_type(8))) unsigned u32x8;
typedef __attribute__((ext_vector_type(16))) unsigned u32x16;
typedef __attribute__((ext_vector_type(32))) unsigned u32x32;
typedef __attribute__((ext_vector_type(4))) short s16x4;
typedef __attribute__((address_space(3))) char lds_char;
typedef __attribute__((address_space(3))) s16x4 lds_s16x4;
typedef __attribute__((address_space(3))) void lds_void;
typedef __attribute__((address_space(1))) unsigned gu32;

#define DEV __device__ __forceinline__

constexpr int SEQ = 2048, DM = 1024, FF = 3584;
constexpr float LN_EPS = 1e-5f;

constexpr unsigned MB = 1024 * 1024;
constexpr unsigned WS_WQKV = 0;
constexpr unsigned WS_WO = WS_WQKV + 6 * MB;
constexpr unsigned WS_W1 = WS_WO + 2 * MB;
constexpr unsigned WS_W2 = WS_W1 + 7 * MB;
constexpr unsigned WS_X = WS_W2 + 7 * MB;
constexpr unsigned WS_KV = WS_X + (16 >> 0) * MB;
constexpr unsigned WS_O = WS_KV + (16 >> 0) * MB;
constexpr unsigned WS_H = WS_O + (8 >> 1) * MB;
constexpr unsigned WS_XCH = WS_H + (28 >> 1) * MB;
constexpr unsigned WS_ST = WS_XCH + (32 >> 1) * MB;
constexpr unsigned WS_ST2 = WS_ST + MB;
constexpr unsigned WS_CS1 = WS_ST2 + MB / 2;
constexpr unsigned WS_CSQ = WS_CS1 + 512 * 1024;
constexpr unsigned FL_KV = WS_CSQ + 512 * 1024;
constexpr unsigned FL_ROW = FL_KV + 32768;
constexpr unsigned FL_WT = FL_ROW + 131072;
constexpr unsigned FL_WQ = FL_WT + 16384;
constexpr unsigned WS_EPOCH = FL_WQ + 16384;
constexpr unsigned WS_END = WS_EPOCH + 256;
#define AWS 3
#define PAUX 16
#define SPINL 18
constexpr int SPIN = 1 << SPINL;

DEV unsigned pack2(float a, float b) {
  f32x2 v = {a, b};
  return __builtin_bit_cast(unsigned, __builtin_convertvector(v, bf16x2));
}
DEV unsigned pack2(f32x2 v) { return __builtin_bit_cast(unsigned, __builtin_convertvector(v, bf16x2)); }
DEV float bf_lo(unsigned u) { return __builtin_bit_cast(float, u << 16); }
DEV float bf_hi(unsigned u) { return __builtin_bit_cast(float, u & 0xffff0000u); }
DEV float xsum16(float x) {
  auto r = __builtin_amdgcn_permlane16_swap(__builtin_bit_cast(unsigned, x), __builtin_bit_cast(unsigned, x), false, false);
  const unsigned a = r[0], b = r[1];
  return __builtin_bit_cast(float, a) + __builtin_bit_cast(float, b);
}
DEV float xsum32(float x) {
  auto r = __builtin_amdgcn_permlane32_swap(__builtin_bit_cast(unsigned, x), __builtin_bit_cast(unsigned, x), false, false);
  const unsigned a = r[0], b = r[1];
  return __builtin_bit_cast(float, a) + __builtin_bit_cast(float, b);
}
DEV float xmax32(float x) {
  auto r = __builtin_amdgcn_permlane32_swap(__builtin_bit_cast(unsigned, x), __builtin_bit_cast(unsigned, x), false, false);
  const unsigned a = r[0], b = r[1];
  return fmaxf(__builtin_bit_cast(float, a), __builtin_bit_cast(float, b));
}
DEV void sums8(u32x4 c, float& s, float& q) {
#pragma unroll
  for (int i = 0; i < 4; ++i) {
    const float a = bf_lo(c[i]), b = bf_hi(c[i]);
    s += a; q = fmaf(a, a, q); s += b; q = fmaf(b, b, q);
  }
}
DEV float fold32(float a, float b) {
  auto r = __builtin_amdgcn_permlane32_swap(__builtin_bit_cast(unsigned, a), __builtin_bit_cast(unsigned, b), false, false);
  const unsigned x = r[0], y = r[1];
  return __builtin_bit_cast(float, x) + __builtin_bit_cast(float, y);
}
DEV float fold16(float a, float b) {
  auto r = __builtin_amdgcn_permlane16_swap(__builtin_bit_cast(unsigned, a), __builtin_bit_cast(unsigned, b), false, false);
  const unsigned x = r[0], y = r[1];
  return __builtin_bit_cast(float, x) + __builtin_bit_cast(float, y);
}
template <int N> DEV void wait_vm() { asm volatile("s_waitcnt vmcnt(%0)" ::"i"(N) : "memory"); }
DEV void bar() { asm volatile("s_barrier" ::: "memory"); }
DEV void lgkm_bar() { asm volatile("s_waitcnt lgkmcnt(0)\n\ts_barrier" ::: "memory"); }
#define GSLP ""
#define GL1(A) "global_load_lds_dwordx4 %1, %2" A
#define GL2(A) GL1(A) GSLP "\n\tglobal_load_lds_dwordx4 %1, %2 offset:1024" A
#define GL3(A) GL2(A) GSLP "\n\tglobal_load_lds_dwordx4 %1, %2 offset:2048" A
#define GL4(A) GL3(A) GSLP "\n\tglobal_load_lds_dwordx4 %1, %2 offset:3072" A
#define GLDS_(X) asm volatile("s_mov_b32 m0, %0\n\ts_nop 0\n\t" X GSLP ::"s"(l), "v"(voff), "s"(g) : "memory")
template <int N> DEV void glds(unsigned l, const char* g, unsigned voff) {
  if constexpr (N == 1) GLDS_(GL1(" sc1"));
  else if constexpr (N == 2) GLDS_(GL2(" sc1"));
  else if constexpr (N == 3) GLDS_(GL3(" sc1"));
  else if constexpr (N == 4) GLDS_(GL4(" sc1"));
  else { glds<4>(l, g, voff); glds<N - 4>(l + 4096, g + 4096, voff); }
}
typedef __amdgpu_buffer_rsrc_t rsrc_t;
DEV void st16(rsrc_t r, unsigned so, unsigned vo, u32x4 v) {
  __builtin_amdgcn_raw_buffer_store_b128(v, r, vo, so, 16);
  asm volatile("s_nop 1" ::"v"(v));
}
DEV void st4(rsrc_t r, unsigned so, unsigned vo, float v) { __builtin_amdgcn_raw_buffer_store_b32(__builtin_bit_cast(unsigned, v), r, vo, so, 16); }
typedef unsigned long long u64;
DEV void signal(u32x4 rs4, unsigned so, u64 tok) { asm volatile("buffer_store_dwordx2 %[one], off, %[rs], %[fl] sc1" ::[one] "v"(tok), [rs] "s"(rs4), [fl] "s"(so) : "memory"); }
DEV void epoch_ld(u64& e, const char* ws) { asm volatile("s_load_dwordx2 %0, %1, %2" : "=&s"(e) : "s"(ws), "s"(WS_EPOCH) : "memory"); }
DEV u64 token(u64 e) {
  asm volatile("s_waitcnt lgkmcnt(0)" : "+s"(e)::"memory");
  return e + 0x9e3779b97f4a7c15ull;
}
DEV void await(rsrc_t r, unsigned off, unsigned idx, u64 tok) {
#pragma unroll 1
  for (int spin = 0; spin < SPIN; ++spin) {
    const u64 f = __builtin_bit_cast(u64, __builtin_amdgcn_raw_buffer_load_b64(r, idx * 8, off, PAUX));
    if (__builtin_amdgcn_ballot_w64(f != tok) == 0) break;
    __builtin_amdgcn_s_sleep(AWS);
    asm volatile("" ::: "memory");
  }
}
DEV f32x4 mfma16(bf16x8 a, bf16x8 b, f32x4 c) { return __builtin_amdgcn_mfma_f32_16x16x32_bf16(a, b, c, 0, 0, 0); }
DEV f32x4 mfma16(u32x4 a, u32x4 b, f32x4 c) { return mfma16(__builtin_bit_cast(bf16x8, a), __builtin_bit_cast(bf16x8, b), c); }
DEV bool lanes(u64 m) { return __builtin_amdgcn_inverse_ballot_w64(m); }
constexpr u64 DIAG = 0x1000010000100001ull;
template <int N> DEV void row_stats(rsrc_t r, unsigned so, unsigned vo, const u32x4* c) {
  f32x4 s = {}, q = {};
#pragma unroll
  for (int i = 0; i < N; ++i) { s = mfma16(u32x4{0x3f803f80u, 0x3f803f80u, 0x3f803f80u, 0x3f803f80u}, c[i], s); q = mfma16(c[i], c[i], q); }
  const bool odd = lanes(0xaaaaaaaaaaaaaaaaull);
  const f32x2 v = {s[0], lanes(0xccccccccccccccccull) ? (odd ? q[3] : q[2]) : (odd ? q[1] : q[0])};
  __builtin_amdgcn_raw_buffer_store_b64(__builtin_bit_cast(u32x2, v), r, lanes(DIAG * 15) ? vo : WS_END, so, 16);
}
struct Sel { u32x4 lo, hi; };
DEV Sel make_sel() {
  const unsigned x = lanes(DIAG) ? 0x3f80u : lanes(DIAG * 2) ? 0x3f800000u : 0u, y = lanes(DIAG * 4) ? 0x3f80u : lanes(DIAG * 8) ? 0x3f800000u : 0u;
  Sel s = {u32x4{x, y, 0u, 0u}, u32x4{0u, 0u, x, y}};
  asm volatile("" : "+v"(s.lo), "+v"(s.hi));
  return s;
}
DEV f32x16 mfma32(bf16x8 a, bf16x8 b, f32x16 c) { return __builtin_amdgcn_mfma_f32_32x32x16_bf16(a, b, c, 0, 0, 0); }
DEV f32x4 sub4(const f32x16& v, int i) { return f32x4{v[4 * i], v[4 * i + 1], v[4 * i + 2], v[4 * i + 3]}; }
template <int I> struct IC { static constexpr int value = I; };
template <int I, int N, class F> DEV void static_for(F f) {
  if constexpr (I < N) { f(IC<I>{}); static_for<I + 1, N>(f); }
}
#define STAMP(i)
#define LSTAMP(i)
#define LSTAMP_OUT(l)
#define STAMP_DECL
#define RETRY()
#define RTC_ARG
#define RTC
#define STAMP_OUT()
#define PSTAMP(i)
#define LEND()

DEV float add1(float a, float b) { asm("v_add_f32 %0, %1, %2" : "=v"(a) : "v"(a), "v"(b)); return a; }
constexpr float GELU_A = 4.686686931e-01f;
template <bool TWICE> DEV f32x2 row_scale(float s, float q) {
  const float mu = s * (1.f / 1024.f);
  const float var = fmaxf(fmaf(-mu, mu, q * (1.f / 1024.f)), 0.f);
  float r = __builtin_amdgcn_rsqf(var + LN_EPS);
  if constexpr (TWICE) r *= __builtin_amdgcn_rsqf(var * r * r + LN_EPS) * GELU_A;
  return f32x2{r, -mu * r};
}
template <bool TWICE> DEV f32x2 finish_stats(const f32x4 (&p)[8]) {
  float v[4];
#pragma unroll
  for (int i = 0; i < 4; ++i) v[i] = add1(add1(p[0][i], p[1][i]), add1(p[2][i], p[3][i]));
  return row_scale<TWICE>(xsum16(fold32(v[0], v[2])), xsum16(fold32(v[1], v[3])));
}
DEV float sum8(const float* c) { return add1(add1(add1(c[0], c[1]), add1(c[2], c[3])), add1(add1(c[4], c[5]), add1(c[6], c[7]))); }
DEV f32x2 finish_stats2(const f32x4 (&p)[8]) {
  float v[4];
#pragma unroll
  for (int i = 0; i < 4; ++i) v[i] = add1(add1(add1(p[0][i], p[1][i]), add1(p[2][i], p[3][i])), add1(add1(p[4][i], p[5][i]), add1(p[6][i], p[7][i])));
  return row_scale<false>(xsum32(add1(v[0], v[2])), xsum32(add1(v[1], v[3])));
}
#define LD8X4 "global_load_dwordx4 %0, %8, %9 sc1\n\tglobal_load_dwordx4 %1, %8, %9 offset:512 sc1\n\tglobal_load_dwordx4 %2, %8, %9 offset:1024 sc1\n\t" \
  "global_load_dwordx4 %3, %8, %9 offset:1536 sc1\n\tglobal_load_dwordx4 %4, %8, %9 offset:2048 sc1\n\tglobal_load_dwordx4 %5, %8, %9 offset:2560 sc1\n\t" \
  "global_load_dwordx4 %6, %8, %9 offset:3072 sc1\n\tglobal_load_dwordx4 %7, %8, %9 offset:3584 sc1"
#define LD8X1(o, s) "global_load_dword %0, %8, %9 offset:" #o "+0*" #s " sc1\n\tglobal_load_dword %1, %8, %9 offset:" #o "+1*" #s " sc1\n\tglobal_load_dword %2, %8, %9 offset:" #o "+2*" #s " sc1\n\t" \
  "global_load_dword %3, %8, %9 offset:" #o "+3*" #s " sc1\n\tglobal_load_dword %4, %8, %9 offset:" #o "+4*" #s " sc1\n\tglobal_load_dword %5, %8, %9 offset:" #o "+5*" #s " sc1\n\t" \
  "global_load_dword %6, %8, %9 offset:" #o "+6*" #s " sc1\n\tglobal_load_dword %7, %8, %9 offset:" #o "+7*" #s " sc1"
#define OUT8(a, i) "=&v"(a[i]), "=&v"(a[i + 1]), "=&v"(a[i + 2]), "=&v"(a[i + 3]), "=&v"(a[i + 4]), "=&v"(a[i + 5]), "=&v"(a[i + 6]), "=&v"(a[i + 7])
#define PIN8(a, i) asm volatile("" : "+v"(a[i]), "+v"(a[i + 1]), "+v"(a[i + 2]), "+v"(a[i + 3]), "+v"(a[i + 4]), "+v"(a[i + 5]), "+v"(a[i + 6]), "+v"(a[i + 7]))

DEV f32x16 smf32(bf16x8 a, f32x8 b, f32x16 c, int idx) {
  return __builtin_amdgcn_smfmac_f32_32x32x32_bf16(a, __builtin_bit_cast(bf16x16, b), c, idx, 0, 0);
}
DEV f32x4 smf16(bf16x8 a, f32x8 b, f32x4 c, int idx) {
  return __builtin_amdgcn_smfmac_f32_16x16x64_bf16(a, __builtin_bit_cast(bf16x16, b), c, idx, 0, 0);
}
DEV f32x8 lo8(f32x16 v) { return __builtin_shufflevector(v, v, 0, 1, 2, 3, 4, 5, 6, 7); }
DEV f32x8 hi8(f32x16 v) { return __builtin_shufflevector(v, v, 8, 9, 10, 11, 12, 13, 14, 15); }
DEV float max16(f32x16 s) {
  float a = fmaxf(fmaxf(s[0], s[1]), s[2]), b = fmaxf(fmaxf(s[3], s[4]), s[5]), c = fmaxf(fmaxf(s[6], s[7]), s[8]);
  float d = fmaxf(fmaxf(s[9], s[10]), s[11]), e = fmaxf(fmaxf(s[12], s[13]), s[14]);
  return fmaxf(fmaxf(fmaxf(a, b), c), fmaxf(fmaxf(d, e), s[15]));
}
DEV u32x4 scale8(f32x4 a, f32x4 b, float r, float cm, f32x4 c0, f32x4 c1) {
  return u32x4{pack2(fmaf(a[0], r, cm * c0[0]), fmaf(a[1], r, cm * c0[1])), pack2(fmaf(a[2], r, cm * c0[2]), fmaf(a[3], r, cm * c0[3])),
               pack2(fmaf(b[0], r, cm * c1[0]), fmaf(b[1], r, cm * c1[1])), pack2(fmaf(b[2], r, cm * c1[2]), fmaf(b[3], r, cm * c1[3]))};
}

#define PIN4(a) asm volatile("" : "+v"(a[0]), "+v"(a[1]), "+v"(a[2]), "+v"(a[3]))
template <int OFF, class T> DEV void ldf(T& x, const char* g, unsigned vo) {
  asm volatile("global_load_dwordx4 %0, %1, %2 offset:%3 sc1" : "=&v"(x) : "v"(vo), "s"(g), "i"(OFF) : "memory");
}
template <int OFF> DEV void ldin(u32x4& x, const char* g, unsigned vo) {
  asm volatile("global_load_dwordx4 %0, %1, %2 offset:%3 sc0 sc1 nt" : "=&v"(x) : "v"(vo), "s"(g), "i"(OFF) : "memory");
}
template <int OFF> DEV void stl(unsigned l, const u32x4& x) { asm volatile("ds_write_b128 %0, %1 offset:%2" ::"v"(l), "v"(x), "i"(OFF) : "memory"); }
DEV void poll(u64& f, u32x4 rs4, unsigned off, unsigned vo) {
  asm volatile("buffer_load_dwordx2 %0, %1, %2, %3 offen sc1" : "+v"(f) : "v"(vo), "s"(rs4), "s"(off) : "memory");
}
DEV void res_pack(const f32x4 (&a)[4], u32x4 r0, u32x4 r1, u32x4 (&o)[2]) {
#pragma unroll
  for (int g = 0; g < 2; ++g)
#pragma unroll
    for (int i = 0; i < 4; ++i) {
      const unsigned rr = (g ? r1 : r0)[i];
      o[g][i] = pack2(a[2 * g + (i >> 1)][2 * (i & 1)] + bf_lo(rr), a[2 * g + (i >> 1)][2 * (i & 1) + 1] + bf_hi(rr));
    }
}
template <int OFF, class T> DEV void ldl(T& x, unsigned l) { asm volatile("ds_read_b128 %0, %1 offset:%2" : "=&v"(x) : "v"(l), "i"(OFF) : "memory"); }
DEV f32x4 unpk(const u32x4& v, int j) { return f32x4{bf_lo(v[j]), bf_hi(v[j]), bf_lo(v[j + 1]), bf_hi(v[j + 1])}; }
DEV f32x4 add4(f32x4 a, f32x4 b) { return f32x4{add1(a[0], b[0]), add1(a[1], b[1]), add1(a[2], b[2]), add1(a[3], b[3])}; }
DEV void fin2_pre(f32x4 (&ps)[4], u32x4 (&pr)[3]) {
  asm volatile("" : "+v"(pr[0]), "+v"(pr[1]), "+v"(pr[2]));
#pragma unroll
  for (int k = 0; k < 4; ++k) ps[k] = add4(unpk(pr[2], k & 2), unpk(pr[k & 1], k & 2));
  PIN4(ps);
}
DEV void fin2_add(f32x4 (&pp)[4], const f32x4 (&ps)[4]) {
  asm volatile("s_waitcnt lgkmcnt(0)" : "+v"(pp[0]), "+v"(pp[1]), "+v"(pp[2]), "+v"(pp[3])::"memory");
#pragma unroll
  for (int k = 0; k < 4; ++k) pp[k] = add4(pp[k], ps[k]);
  PIN4(pp);
}
template <int SITE> DEV void fin2(f32x4 (&pp)[4], u32x4 (&pi)[6], const Sel& sel, const char* gi, rsrc_t rs, unsigned xo, unsigned so, char* out, int lane RTC_ARG) {
  f32x4 a[4];
  auto sum = [&]() {
    asm volatile("" : "+v"(pi[0]), "+v"(pi[1]), "+v"(pi[2]), "+v"(pi[3]), "+v"(pi[4]), "+v"(pi[5]));
#pragma unroll
    for (int i = 0; i < 3; ++i)
#pragma unroll
      for (int k = 0; k < 4; ++k) a[k] = mfma16(k & 2 ? sel.hi : sel.lo, pi[2 * i + (k & 1)], i ? a[k] : pp[k]);
    return __builtin_amdgcn_ballot_w64(__builtin_isunordered(a[0][0], a[1][0])) != 0;
  };
  if (__builtin_expect(sum(), 0)) {
#pragma unroll 1
    for (int spin = 0; spin < SPIN; ++spin) {
      u32x4 m = pi[0];
#pragma unroll
      for (int i = 1; i < 6; ++i) m = __builtin_elementwise_max(m, pi[i]);
      if (__builtin_amdgcn_ballot_w64(max(max(m[0], m[1]), max(m[2], m[3])) == ~0u) == 0) break;
      RETRY();
      __builtin_amdgcn_s_sleep(AWS);
      asm volatile("global_load_dwordx4 %0, %6, %7 sc1\n\tglobal_load_dwordx4 %1, %6, %7 offset:1024 sc1\n\tglobal_load_dwordx4 %2, %6, %8 sc1\n\t"
                   "global_load_dwordx4 %3, %6, %8 offset:1024 sc1\n\tglobal_load_dwordx4 %4, %6, %9 sc1\n\tglobal_load_dwordx4 %5, %6, %9 offset:1024 sc1\n\ts_waitcnt vmcnt(0)"
                   : "+v"(pi[0]), "+v"(pi[1]), "+v"(pi[2]), "+v"(pi[3]), "+v"(pi[4]), "+v"(pi[5]) : "v"(lane * 16), "s"(gi), "s"(gi + 8192), "s"(gi + 16384) : "memory");
    }
    if (sum()) {
#pragma unroll
      for (int k = 0; k < 4; ++k) a[k] = add4(add4(add4(pp[k], unpk(pi[k & 1], k & 2)), unpk(pi[2 + (k & 1)], k & 2)), unpk(pi[4 + (k & 1)], k & 2));
    }
  }
  u32x4 o[2];
#pragma unroll
  for (int g = 0; g < 2; ++g) o[g] = u32x4{pack2(a[g][0], a[g][1]), pack2(a[g][2], a[g][3]), pack2(a[2 + g][0], a[2 + g][1]), pack2(a[2 + g][2], a[2 + g][3])};
  if constexpr (SITE >= 2) {
#pragma unroll
    for (int g = 0; g < 2; ++g) *(u32x4*)(out + ((g * 16 + (lane & 15)) * DM + (lane >> 4) * 8) * 2) = o[g];
  } else {
    st16(rs, xo, lane * 16, o[0]);
    st16(rs, xo, 1024 + lane * 16, o[1]);
    row_stats<1>(rs, so, (lane & 15) * 16, o);
    row_stats<1>(rs, so + 256, (lane & 15) * 16, o + 1);
  }
}
struct Rt { unsigned ra[9], ta[17], co; u32x4 oh[7]; };
DEV void rt_setup(Rt& r, unsigned l, int odd, int rot, int lane) {
  const int fq = lane >> 4, q = (lane >> 2) & 3, p = lane & 3, s0 = (fq * 2) & 3;
  const unsigned b0 = l + 256 * (fq * 8 + q) + 64 * q, bn = b0 + 16 * (p ^ s0), bs = b0 + 16 * ((p >> 1) ^ s0) + 8 * (p & 1);
#pragma unroll
  for (int i = 0; i < 4; ++i) {
    const unsigned c = ((lane & 15) ^ ((fq << 2) | i)) * 16;
    r.ra[i] = fq * FF * 2 + c; r.ra[4 + i] = fq * DM * 2 + c;
    r.ta[2 * i] = i < 3 ? bn ^ (64 * (i + odd)) : bs ^ (odd ? 32 : 192);
    r.ta[8 + 2 * i] = (bn + 8192) ^ (64 * ((i + rot) & 3));
    r.ta[2 * i + 1] = r.ta[2 * i] ^ 16; r.ta[9 + 2 * i] = r.ta[8 + 2 * i] ^ 16;
  }
  r.ra[8] = (lane >> 2) * DM * 2 + (lane & 3) * 16;
  r.ta[16] = l + 14336 + fq * 512 + (lane & 15) * 16;
  r.co = (lane & 15) < 7 ? (lane & 15) * 64 + fq * 16 : WS_END;
#pragma unroll
  for (int e = 0; e < 7; ++e) { const unsigned o = (lane & 15) == e ? 0x3f803f80u : 0u; r.oh[e] = u32x4{o, o, o, o}; asm volatile("" : "+v"(r.oh[e])); }
  PIN8(r.ra, 0); PIN8(r.ta, 0); PIN8(r.ta, 8);
  asm volatile("" : "+v"(r.ra[8]), "+v"(r.ta[16]), "+v"(r.co));
}
DEV void raw_rows(unsigned l, const char* src, unsigned vo) {
  asm volatile("s_mov_b32 m0, %0\n\ts_nop 0\n\tglobal_load_lds_dwordx4 %1, %2 sc0 sc1 nt" ::"s"(l), "v"(vo), "s"(src) : "memory");
}
DEV void raw_rows2(unsigned l, const char* src, unsigned vo) {
  asm volatile("s_mov_b32 m0, %0\n\ts_nop 0\n\tglobal_load_lds_dwordx4 %1, %2 sc0 sc1 nt" ::"s"(l), "v"(vo), "s"(src) : "memory");
}
template <int O0, int O1> DEV void trd(u32x2 (&f)[2], unsigned a0, unsigned a1) {
  asm volatile("ds_read_b64_tr_b16 %0, %2 offset:%4\n\tds_read_b64_tr_b16 %1, %3 offset:%5" : "=&v"(f[0]), "=&v"(f[1]) : "v"(a0), "v"(a1), "i"(O0), "i"(O1) : "memory");
}
template <int N> DEV u32x4 trw(u32x2 (&f)[2]) {
  asm volatile("s_waitcnt lgkmcnt(%2)" : "+v"(f[0]), "+v"(f[1]) : "i"(N) : "memory");
  return u32x4{f[0][0], f[0][1], f[1][0], f[1][1]};
}
template <int R, int E0, int N> DEV void retile(rsrc_t rs, const Rt& r, unsigned dst, unsigned vo, f32x4& csum) {
  u32x2 f[N][2];
  static_for<0, N>([&](auto ic) {
    constexpr int e = decltype(ic)::value, i = R * 8 + ((E0 + e) & ~1);
    if constexpr (R == 2) trd<8 * e, 8 * e + 256>(f[e], r.ta[16], r.ta[16]);
    else trd<8 * (e & 1), 8 * (e & 1) + 1024>(f[e], r.ta[i], r.ta[i + 1]);
  });
  static_for<0, N>([&](auto ic) {
    constexpr int e = decltype(ic)::value;
    const u32x4 v = trw<2 * (N - 1 - e)>(f[e]);
    st16(rs, dst + E0 * 1024, e * 1024 + vo, v);
    if constexpr (R == 0) csum = mfma16(__builtin_bit_cast(bf16x8, v), __builtin_bit_cast(bf16x8, r.oh[E0 + e]), csum);
  });
}

DEV void q_ld(unsigned l, const char* src, int lane) {
#pragma unroll
  for (int i = 0; i < 4; ++i) raw_rows(l + i * 1024, src + i * 8 * 6144, (lane >> 3) * 6144 + (lane & 7) * 16);
}
DEV void q_own(unsigned l, int part, int lane, rsrc_t rs, unsigned dst, unsigned cso) {
  const int lr = lane & 15, fq = lane >> 4, p = lane & 3;
  const unsigned a0 = l + fq * 1024 + (lr >> 2) * 128 + (part == 2 ? (p & 1) * 16 + (p >> 1) * 8 : p * 16), a1 = a0 + (part == 2 ? 32 : 8);
  u32x2 f[4][2];
  trd<0, 512>(f[0], a0, a0); trd<0, 512>(f[1], a1, a1); trd<64, 576>(f[2], a0, a0); trd<64, 576>(f[3], a1, a1);
  f32x4 cs = {0.f, 0.f, 0.f, 0.f};
  static_for<0, 4>([&](auto ic) {
    constexpr int e = decltype(ic)::value;
    const u32x4 v = trw<6 - 2 * e>(f[e]);
    st16(rs, dst + (e >> 1) * 196608, (e & 1) * 1024 + lane * 16, v);
    const unsigned o = lr == e ? 0x3f803f80u : 0u;
    cs = mfma16(__builtin_bit_cast(bf16x8, v), __builtin_bit_cast(bf16x8, u32x4{o, o, o, o}), cs);
  });
  st16(rs, cso, lr < 4 ? (lr >> 1) * 12288 + (lr & 1) * 64 + fq * 16 : WS_END, __builtin_bit_cast(u32x4, cs));
}
DEV void x_own(u32x4 c0, u32x4 c1, int r16, int cg, int lane, rsrc_t rs) {
  const unsigned d = WS_X + (((r16 >> 3) * 32 + cg * 2) * 8 + (r16 & 7)) * 1024;
  st16(rs, d, lane * 16, c0);
  st16(rs, d + 8192, lane * 16, c1);
  const u32x4 c[2] = {c0, c1};
  row_stats<2>(rs, WS_ST + ((r16 >> 1) * 16 + cg) * 256 + (r16 & 1) * 128, (lane & 15) * 8, c);
}

DEV void idle(unsigned t0, unsigned dt, unsigned lane) {
  while ((unsigned)__builtin_amdgcn_s_memrealtime() - t0 < dt) __builtin_amdgcn_s_sleep(1);
}
constexpr int L_OWN = 144 * 1024, L_FIN = 156 * 1024, L_BUF = 48 * 1024, L_TOTAL = 160 * 1024;
enum { K_O = 0, K_X1 = 1, K_H = 2, K_X2 = 3 };
constexpr unsigned FL_X0 = FL_ROW + 65536 + K_X2 * 16384;

#define IDS()                                                                                                              \
  int lane, wid = wid0 & 3, bid = blockIdx.x, layer = layer0;                                                              \
  char* ws = ws0;                                                                                                          \
  asm volatile("v_mbcnt_lo_u32_b32 %0, -1, 0\n\tv_mbcnt_hi_u32_b32 %0, -1, %0" : "=v"(lane));                              \
  asm volatile("" : "+s"(wid), "+s"(bid), "+s"(layer), "+s"(ws));                                                          \
  [[maybe_unused]] const int xcd = bid & 7, idx = bid >> 3, w = wid, fq = lane >> 4, lr = lane & 15;                       \
  [[maybe_unused]] const unsigned voff = lane * 16, pb = lbase + voff, sto = (lane >> 5) * 2048 + (lane & 31) * 8;         \
  [[maybe_unused]] const int h = (xcd >> 1) * 8 + (idx >> 2), blk = (xcd & 1) * 4 + (idx & 3);    \
  [[maybe_unused]] const int rb8 = (xcd & 1) * 4 + (idx >> 3), kq = xcd >> 1, ct = idx & 7;       \
  [[maybe_unused]] const int t1 = kq * 8 + ct, rb16 = rb8 * 2 + (ct & 1), tn = kq * 4 + (ct >> 1);   \
  [[maybe_unused]] const unsigned flr = FL_ROW + layer * 65536

struct Ln { unsigned pb0, pb1, pb2, e1, e2, e3, ak, av, w128, own; };
#define LN() asm volatile("" : "+{v250}"(n.pb0), "+{v251}"(n.pb1), "+{v252}"(n.pb2), "+{v253}"(n.e1), "+{v254}"(n.e2), "+{v255}"(n.e3), "+{v14}"(n.ak), "+{v15}"(n.av), "+{v[12:13]}"(tok))
template <class T> DEV T lds_at(unsigned a) { return *(__attribute__((address_space(3))) const T*)(uintptr_t)a; }
DEV bf16x8 rowsum_sel() {
  const bool odd = lanes(0xf0f0f0f0f0f0f0f0ull);
  const unsigned x = odd ? 0u : 0x3f803f80u, y = odd ? 0x3f803f80u : 0u;
  return __builtin_bit_cast(bf16x8, u32x4{x, x, y, y});
}
DEV unsigned k_lane(int lane) { return ((lane >> 2) & 1) * 1024 + (lane >> 5) * 512 + (((lane >> 3) & 3) * 4 + (lane & 3)) * 16; }
DEV unsigned v_lane(int lane) { return ((lane >> 4) & 1) * 1024 + (lane >> 5) * 256 + (lane & 15) * 16; }
DEV void attn_epilogue(f32x16 o, f32x4 l, bf16x8 q0, bf16x8 q1, const char* ws, unsigned kvo, rsrc_t rs, unsigned od, const Ln& n) {
  l[0] = xmax32(l[0]);
  auto put = [&]() {
    const float inv = __builtin_amdgcn_rcpf(l[0]);
#pragma unroll
    for (int c = 0; c < 2; ++c) {
      u32x4 wv;
#pragma unroll
      for (int i = 0; i < 4; ++i) wv[i] = pack2(o[8 * c + 2 * i] * inv, o[8 * c + 2 * i + 1] * inv);
      st16(rs, od, n.av + c * 512, wv);
    }
  };
  put();
  f32x2 mx[2] = {};
#pragma unroll
  for (int i = 0; i < 8; ++i) mx[i & 1] = __builtin_elementwise_fma(f32x2{o[2 * i], o[2 * i + 1]}, f32x2{0.f, 0.f}, mx[i & 1]);
  mx[0] += mx[1];
  bool bad = !(mx[0][0] + mx[0][1] + l[0] < 1.8446744e19f && l[0] > 2.3283064e-10f);
  if (__builtin_expect(__builtin_amdgcn_ballot_w64(bad) != 0, 0)) {
    const f32x16 zero16 = {};
    const bf16x8 sel = rowsum_sel();
    asm volatile("" : "+s"(kvo));
    const char* kvh = ws + (kvo & ~262143u);
    o = zero16; l = f32x4{0.f, 0.f, 0.f, 0.f};
    float m = -__builtin_inff();
#pragma unroll 1
    for (int t = 0; t < 64; ++t) {
      bf16x8 a0, a1, b0, b1v;
      asm volatile("global_load_dwordx4 %0, %4, %6 sc1\n\tglobal_load_dwordx4 %1, %4, %6 offset:256 sc1\n\t"
                   "global_load_dwordx4 %2, %5, %6 offset:2048 sc1\n\tglobal_load_dwordx4 %3, %5, %6 offset:2560 sc1\n\ts_waitcnt vmcnt(0)"
                   : "=&v"(a0), "=&v"(a1), "=&v"(b0), "=&v"(b1v) : "v"(n.ak + t * 4096), "v"(n.av + t * 4096), "s"(kvh) : "memory");
      f32x16 s = mfma32(a1, q1, mfma32(a0, q0, zero16));
      const float mn = fmaxf(m, xmax32(max16(s)));
      const float al = __builtin_amdgcn_exp2f(m - mn);
      m = mn;
#pragma unroll
      for (int i = 0; i < 16; ++i) { s[i] = __builtin_amdgcn_exp2f(s[i] - mn); o[i] *= al; }
#pragma unroll
      for (int i = 0; i < 4; ++i) l[i] *= al;
      o = smf32(b0, lo8(s), o, 0xDDDD); o = smf32(b1v, hi8(s), o, 0xDDDD);
      l = smf16(sel, lo8(s), l, 0xDDDD); l = smf16(sel, hi8(s), l, 0xDDDD);
    }
    put();
  }
}
DEV void wo_epilogue(const f32x16& acc, const u32x8& res, rsrc_t rs, rsrc_t rt, unsigned xo, unsigned so, const Ln& n) {
  const f32x4 a[4] = {sub4(acc, 0), sub4(acc, 1), sub4(acc, 2), sub4(acc, 3)};
  u32x4 o[2];
  res_pack(a, u32x4{res[0], res[1], res[2], res[3]}, u32x4{res[4], res[5], res[6], res[7]}, o);
  st16(rt, xo, 0, o[0]);
  st16(rt, xo + 8192, 0, o[1]);
  row_stats<2>(rs, so, n.e1 * 2, o);
}
#define ATTN_Y(C) "+{v[88:91]}"(qb0), "+{v[92:95]}"(qb1), C "{v[16:31]}"(sx), C "{v[64:79]}"(ob), C "{v[80:83]}"(lb), C "{v[48:63]}"(negb), C "{v[84:87]}"(sel), C "{v112}"(six)
#define ATTN_IN [k0] "{v14}"(n.ak), [v0] "{v15}"(n.av), [rs] "s"(rs4), [one] "{v[12:13]}"(tok), [fl] "s"(fl), [pm] "s"(0x07060302u)
#define ATTN_CLOB "memory", "scc", "v113", "v114", "v32", "v33", "v34", "v35", "v36", "v37", "v38", "v39", "v40", "v41", "v42", "v43", "v44", "v45", "v46", "v47"

#define VISIT ::"s"(w) : "s96", "s97", "s98", "s99", "s100", "s101", "scc", "memory"
__global__ __launch_bounds__(512, 2) __attribute__((aligned(4096))) void main_kernel(char* __restrict__ ws0, const char* __restrict__ x, const char* __restrict__ wqkv, const char* __restrict__ wo, const char* __restrict__ w1, const char* __restrict__ w2, const char* __restrict__ b1,
                                                      const char* __restrict__ b2, char* __restrict__ out) {
  extern __shared__ __attribute__((aligned(16))) char lds[];
  const int wid0 = __builtin_amdgcn_readfirstlane(threadIdx.x >> 6);
  const unsigned lbase = (unsigned)(uintptr_t)(lds_char*)lds;
  const rsrc_t rs = __builtin_amdgcn_make_buffer_rsrc(ws0, 0, WS_END, 0x00020000);
  const u32x4 rs4 = {(unsigned)(uintptr_t)ws0, (unsigned)((uintptr_t)ws0 >> 32), WS_END, 0x00020000u};
  const u32x4 rt4 = {(unsigned)(uintptr_t)ws0, (unsigned)((uintptr_t)ws0 >> 32) | (16u << 16), WS_END, 0x00800000u};
  STAMP_DECL;
  u64 tok;
  const unsigned t0 = __builtin_amdgcn_s_memrealtime();
  const char* pc;
  int len;
  asm volatile("s_getpc_b64 %0\n.Lcode_pc:\n\ts_mov_b32 %1, .Lcode_end-.Lcode_pc" : "=s"(pc), "=s"(len));
  {
    const int layer0 = 0;
    IDS();
    STAMP(19);
    int wv = wid0;
    asm volatile("" : "+s"(wv));
    unsigned dummy = 0;
    if (wv == 7) {
      asm volatile("global_load_dword %0, %1, %2" : "=&v"(dummy) : "v"(max(min(idx * 4096 + lane * 64, len), 0)), "s"(pc) : "memory");
    }
    const int r16 = (wv >> 2) * 64 + (idx >> 2) * 8 + xcd, cg = (idx & 3) * 4 + wid;
    const int part = wv < 3 ? wv : wv - 3, kb = (wv < 3 ? 0 : 16) + (idx >> 4) * 8 + xcd, hp = idx & 15;
    unsigned li = lbase + 96 * 1024 + wv * 4096;
    asm volatile("" : "+s"(li));
    const char* xs = x + (r16 * 16 * DM + cg * 64) * 2;
    u32x4 c0, c1;
    if (wv < 4) { ldin<0>(c0, xs, lr * DM * 2 + fq * 16); ldin<64>(c1, xs, lr * DM * 2 + fq * 16); }
    else { raw_rows(lbase + L_OWN + wid * 3072, xs, lr * DM * 2 + fq * 16); raw_rows(lbase + L_OWN + wid * 3072 + 1024, xs + 64, lr * DM * 2 + fq * 16); }
    if (wv < 6) q_ld(li, wqkv + kb * 32 * 6144 + part * 2048 + hp * 128, lane);
    epoch_ld(tok, ws);
    tok = token(tok);
    idle(t0, 250u, lane);
    if (wv < 4) {
      wait_vm<0>();
      asm volatile("" : "+v"(c0), "+v"(c1));
      STAMP(20);
      x_own(c0, c1, r16, cg, lane, rs);
      wait_vm<3>();
    } else wait_vm<0>();
    STAMP(21);
    asm volatile("" : "+v"(dummy));
    if (__builtin_expect(wv < 6, 1)) {
      q_own(li, part, lane, rs, WS_WQKV + hp * 393216 + kb * 6144 + part * 2048, WS_CSQ + hp * 24576 + kb * 384 + part * 128);
      wait_vm<0>();
      signal(rs4, FL_WQ + (hp * 32 + kb) * 32 + part * 8, tok);
    }
    if (__builtin_expect(wv < 4, 1)) signal(rs4, FL_X0 + (r16 >> 1) * 256 + ((r16 & 1) * 16 + cg) * 8, tok);
    STAMP(22);
    PSTAMP(1);
  }

  if (__builtin_expect(wid0 >= 4, 0)) {
    const char* code = (const char*)((uintptr_t)pc & ~(uintptr_t)127) + max(min((int)((wid0 & 3) * 32 + (blockIdx.x >> 3)) * 1024, (len & ~127) - 1024), 0);
    f32x4 p4[8], pp[4], ps[4];
    u32x4 pr[3], pi[6];
    Sel sel;
    float cs[16], csq, csu;
    unsigned bb, cd = 0;
    u64 pq = 0;
    [[maybe_unused]] unsigned st_l = 0, rtc = 0;
    u32x4 u0_0_0, u0_0_1, u0_0_2, u0_0_3, u0_0_4, u0_0_5, u0_0_6, u0_1_0, u0_1_1, u0_1_2, u0_1_3, u0_1_4, u0_1_5, u0_1_6, u0_2_0, u0_2_1;
    u32x4 u0_2_2, u0_2_3, u0_2_4, u0_2_5, u0_2_6, u0_3_0, u0_3_1, u0_3_2, u0_3_3, u0_3_4, u0_3_5, u0_3_6, u0_4_0, u0_4_1, u0_4_2, u0_4_3;
    u32x4 u0_4_4, u0_4_5, u0_4_6, u0_5_0, u0_5_1, u0_5_2, u0_5_3, u0_5_4, u0_5_5, u0_5_6, u0_6_0, u0_6_1, u0_6_2, u0_6_3, u0_6_4, u0_6_5;
    u32x4 u0_6_6, u0_7_0, u0_7_1, u0_7_2, u0_7_3, u0_7_4, u0_7_5, u0_7_6, v0_0_0, v0_0_1, v0_0_2, v0_0_3, v0_1_0, v0_1_1, v0_1_2, v0_1_3;
    u32x4 v0_2_0, v0_2_1, v0_2_2, v0_2_3, v0_3_0, v0_3_1, v0_3_2, v0_3_3, v0_4_0, v0_4_1, v0_4_2, v0_4_3, v0_9_0, v0_9_1, v0_9_2, v0_9_3;
    u32x4 v0_10_0, v0_10_1, v0_10_2, v0_10_3, v0_11_0, v0_11_1, v0_11_2, v0_11_3, v0_12_0, v0_12_1, v0_12_2, v0_12_3, v0_13_0, v0_13_1, v0_13_2, v0_13_3;
    u32x4 q1_0_0, q1_0_1, q1_0_2, q1_0_3, q1_0_4, q1_0_5, q1_1_0, q1_1_1, q1_1_2, q1_1_3, q1_1_4, q1_1_5, q1_3_0, q1_3_1, q1_3_2, q1_3_3;
    u32x4 q1_3_4, q1_3_5, q1_4_0, q1_4_1, q1_4_2, q1_4_3, q1_4_4, q1_4_5, q1_5_0, q1_5_1, q1_5_2, q1_5_3, q1_5_4, q1_5_5, q1_6_0, q1_6_1;
    u32x4 q1_6_2, q1_6_3, q1_6_4, q1_6_5, q1_7_0, q1_7_1, q1_7_2, q1_7_3, q1_7_4, q1_7_5, u1_0_0, u1_0_1, u1_0_2, u1_0_3, u1_0_4, u1_0_5;
    u32x4 u1_0_6, u1_1_0, u1_1_1, u1_1_2, u1_1_3, u1_1_4, u1_1_5, u1_1_6, u1_2_0, u1_2_1, u1_2_2, u1_2_3, u1_2_4, u1_2_5, u1_2_6, u1_3_0;
    u32x4 u1_3_1, u1_3_2, u1_3_3, u1_3_4, u1_3_5, u1_3_6, u1_4_0, u1_4_1, u1_4_2, u1_4_3, u1_4_4, u1_4_5, u1_4_6, u1_5_0, u1_5_1, u1_5_2;
    u32x4 u1_5_3, u1_5_4, u1_5_5, u1_5_6, u1_6_0, u1_6_1, u1_6_2, u1_6_3, u1_6_4, u1_6_5, u1_6_6, u1_7_0, u1_7_1, u1_7_2, u1_7_3, u1_7_4;
    u32x4 u1_7_5, u1_7_6, v1_0_0, v1_0_1, v1_0_2, v1_0_3, v1_1_0, v1_1_1, v1_1_2, v1_1_3, v1_2_0, v1_2_1, v1_2_2, v1_2_3, v1_3_0, v1_3_1;
    u32x4 v1_3_2, v1_3_3, v1_4_0, v1_4_1, v1_4_2, v1_4_3, v1_9_0, v1_9_1, v1_9_2, v1_9_3, v1_10_0, v1_10_1, v1_10_2, v1_10_3, v1_11_0, v1_11_1;
    u32x4 v1_11_2, v1_11_3, v1_12_0, v1_12_1, v1_12_2, v1_12_3, v1_13_0, v1_13_1, v1_13_2, v1_13_3;
      asm volatile("; LOADER"); __builtin_amdgcn_s_setprio(0);
      { const int layer0 = 0; IDS();
      const char* gq = ws + WS_WQKV + h * 196608 + w * 6144;
      const int r16 = 64 + (idx >> 2) * 8 + xcd;
      x_own(*(const u32x4*)(lds + L_OWN + w * 3072 + voff), *(const u32x4*)(lds + L_OWN + w * 3072 + 1024 + voff), r16, (idx & 3) * 4 + w, lane, rs); wait_vm<0>();
      signal(rs4, FL_X0 + (r16 >> 1) * 256 + ((r16 & 1) * 16 + (idx & 3) * 4 + w) * 8, tok);
      asm volatile("s_getpc_b64 s[96:97]\n\t0:\n\ts_mov_b32 s100, .Lpg0-0b\n\ts_cmp_eq_u32 %0, 1\n\ts_cselect_b32 s100, .Lpg1-0b, s100\n\ts_cmp_eq_u32 %0, 2\n\ts_cselect_b32 s100, .Lpg2-0b, s100\n\ts_cmp_eq_u32 %0, 3\n\ts_cselect_b32 s100, .Lpg3-0b, s100\n\ts_ashr_i32 s101, s100, 31\n\ts_add_u32 s96, s96, s100\n\ts_addc_u32 s97, s97, s101\n\ts_mov_b64 exec, 0\n\ts_swappc_b64 s[98:99], s[96:97]\n\ts_mov_b64 exec, -1" VISIT);
      asm volatile("s_getpc_b64 s[96:97]\n\t0:\n\ts_mov_b32 s100, .Lpg4-0b\n\ts_cmp_eq_u32 %0, 1\n\ts_cselect_b32 s100, .Lpg5-0b, s100\n\ts_cmp_eq_u32 %0, 2\n\ts_cselect_b32 s100, .Lpg6-0b, s100\n\ts_ashr_i32 s101, s100, 31\n\ts_add_u32 s96, s96, s100\n\ts_addc_u32 s97, s97, s101\n\ts_mov_b64 exec, 0\n\ts_swappc_b64 s[98:99], s[96:97]\n\ts_mov_b64 exec, -1" VISIT);
      bar();
      glds<6>(lbase + 24576 + w * 6144, gq + 24576, voff); bar();
      poll(pq, rs4, FL_X0 + ((8 + blk) * 4 + w) * 256, (lane & 31) * 8); glds<6>(lbase + 49152 + w * 6144, gq + 49152, voff);
      glds<6>(lbase + 73728 + w * 6144, gq + 73728, voff); wait_vm<13>(); wait_vm<12>();
      { asm volatile("" : "+v"(pq)); if (__builtin_amdgcn_ballot_w64(pq != tok)) await(rs, FL_X0 + ((8 + blk) * 4 + w) * 256, lane & 31, tok); } bar();
      glds<6>(lbase + 98304 + w * 6144, gq + 98304, voff); wait_vm<12>(); bar();
      glds<6>(lbase + 122880 + w * 6144, gq + 122880, voff); wait_vm<12>(); bar();
      asm volatile("global_load_dword %0, %1, %2 sc1" : "+v"(cd) : "v"(voff), "s"(code) : "memory"); ldf<0>(p4[0], ws + WS_ST + 0 + (blk * 4 + w) * 4096, voff);
      ldf<1024>(p4[1], ws + WS_ST + 0 + (blk * 4 + w) * 4096, voff); ldf<2048>(p4[2], ws + WS_ST + 0 + (blk * 4 + w) * 4096, voff);
      ldf<3072>(p4[3], ws + WS_ST + 0 + (blk * 4 + w) * 4096, voff);
      glds<1>(lbase + L_OWN + w * 3072 + 0, ws + WS_CSQ + h * 12288 + w * 96, (0 + lane) / 6 * 384 + (0 + lane) % 6 * 16);
      glds<1>(lbase + L_OWN + w * 3072 + 1024, ws + WS_CSQ + h * 12288 + w * 96, (64 + lane) / 6 * 384 + (64 + lane) % 6 * 16);
      glds<1>(lbase + L_OWN + w * 3072 + 2048, ws + WS_CSQ + h * 12288 + w * 96, (128 + lane) / 6 * 384 + (128 + lane) % 6 * 16); wait_vm<14>(); wait_vm<3>();
      { PIN4(p4); const f32x2 f = finish_stats<false>(p4); *(float*)(lds + L_FIN + (fq & 1) * 1024 + 0 + w * 128 + lr * 8 + (lane >> 5) * 4) = (fq & 1) ? f[1] : f[0]; }
      lgkm_bar();
      asm volatile("global_load_dword %0, %1, %2 sc1" : "+v"(cd) : "v"(voff), "s"(code) : "memory"); glds<6>(lbase + 0 + w * 6144, gq + 147456, voff); wait_vm<7>();
      { const float* cp = (const float*)(lds + L_OWN + w * 3072 + (lane >> 5) * 1536) + ((lane & 31) < 24 ? (lane & 31) : 23); for (int i = 0; i < 16; ++i) cs[i] = cp[i * 24]; csq = xsum32(add1(sum8(cs), sum8(cs + 8))); }
      *(float*)(lds + L_FIN + 2048 + (w * 24 + ((lane & 31) < 24 ? (lane & 31) : 23)) * 4) = csq; lgkm_bar();
      glds<6>(lbase + 24576 + w * 6144, gq + 172032, voff); ldf<0>(p4[0], ws + WS_ST + 0 + ((8 + blk) * 4 + w) * 4096, voff);
      ldf<1024>(p4[1], ws + WS_ST + 0 + ((8 + blk) * 4 + w) * 4096, voff); ldf<2048>(p4[2], ws + WS_ST + 0 + ((8 + blk) * 4 + w) * 4096, voff);
      ldf<3072>(p4[3], ws + WS_ST + 0 + ((8 + blk) * 4 + w) * 4096, voff); wait_vm<10>(); bar();
      wait_vm<0>();
      { PIN4(p4); const f32x2 f = finish_stats<false>(p4); *(float*)(lds + L_FIN + (fq & 1) * 1024 + 512 + w * 128 + lr * 8 + (lane >> 5) * 4) = (fq & 1) ? f[1] : f[0]; }
      lgkm_bar();
      bar();
      asm volatile("global_load_dword %0, %1, %2 sc1" : "+v"(cd) : "v"(voff), "s"(code) : "memory");
      } { const int layer0 = 0; IDS();
      const char* gk = ws + WS_KV + layer * 8 * MB + h * 262144 + w * 4096;
      const unsigned fk = FL_KV + ((layer * 32 + h) * 4 + w) * 128;
      const char* go = ws + WS_WO + tn * 131072;
      const char* g1 = ws + WS_W1 + t1 * 229376 + w * 7168;
      const unsigned li = lbase + (w < 3 ? w * 16384 : 144 * 1024), fw = FL_WT + t1 * 256; f32x4 csum = {};
      const int kb = rb8 * 4 + w, k2 = kq * 28 + (kb < 28 ? kb : 27);
      const char* src0 = w1 + kb * 32 * FF * 2 + (t1 >> 1) * 448 + (t1 & 1) * 192;
      const char* src1 = w2 + k2 * 32 * DM * 2 + ct * 256; Rt rt; rt_setup(rt, li, t1 & 1, kq + 1, lane);
      const int cl = w * 28 + ((lane & 31) < 28 ? (lane & 31) : 27); unsigned cso = cl * 4 + (lane >> 5) * 7168, b1o;
      { const int sl = cl >> 4, i = cl & 15, col = sl == 6 ? ((t1 & 1) ? i : 96 + i) : (t1 & 1) * 16 + (sl >> 1) * 32 + (i >> 2) * 8 + (sl & 1) * 4 + (i & 3); b1o = (t1 * 112 + col) * 2; }
      asm volatile("" : "+v"(cso), "+v"(b1o)); bar();
      __builtin_amdgcn_s_sleep(10); await(rs, fk, 0 + (lane & 7), tok); wait_vm<0>(); glds<4>(lbase + 49152 + w * 4096, gk + 0, voff);
      glds<4>(lbase + 65536 + w * 4096, gk + 16384, voff); glds<4>(lbase + 81920 + w * 4096, gk + 32768, voff); glds<4>(lbase + 98304 + w * 4096, gk + 49152, voff);
      wait_vm<12>(); bar();
      glds<4>(lbase + 114688 + w * 4096, gk + 65536, voff); raw_rows2(li + 14336, wo + (kb * 32 + 0) * DM * 2 + tn * 128 + (ct & 1) * 64, rt.ra[8]); wait_vm<13>();
      bar();
      glds<4>(lbase + 131072 + w * 4096, gk + 81920, voff); raw_rows2(li + 15360, wo + (kb * 32 + 16) * DM * 2 + tn * 128 + (ct & 1) * 64, rt.ra[8]);
      raw_rows2(li + 0, src0 + 0 * FF, rt.ra[0]); wait_vm<15>(); bar();
      glds<4>(lbase + 49152 + w * 4096, gk + 98304, voff); raw_rows2(li + 1024, src0 + 8 * FF, rt.ra[1]); wait_vm<16>(); bar();
      poll(pq, rs4, fk, (8 + (lane & 7)) * 8); glds<4>(lbase + 65536 + w * 4096, gk + 114688, voff);
      asm volatile("global_load_ushort %0, %2, %3\n\tglobal_load_dword %1, %4, %5" : "=&v"(bb), "+v"(cd) : "v"(b1o), "s"(b1), "v"(voff), "s"(b2 + (w & 1) * 1024) : "memory");
      raw_rows2(li + 2048, src0 + 16 * FF, rt.ra[2]); wait_vm<20>(); bar();
      wait_vm<7>();
      { asm volatile("" : "+v"(pq)); if (__builtin_amdgcn_ballot_w64(pq != tok)) await(rs, fk, 8 + (lane & 7), tok); } glds<4>(lbase + 81920 + w * 4096, gk + 131072, voff);
      raw_rows2(li + 3072, src0 + 24 * FF, rt.ra[3]); raw_rows2(li + 4096, src0 + 32 * FF, rt.ra[0]); bar();
      asm volatile("global_load_dword %0, %1, %2 sc1" : "+v"(cd) : "v"(voff), "s"(code) : "memory"); glds<4>(lbase + 98304 + w * 4096, gk + 147456, voff);
      raw_rows2(li + 5120, src0 + 40 * FF, rt.ra[1]); raw_rows2(li + 6144, src0 + 48 * FF, rt.ra[2]); raw_rows2(li + 7168, src0 + 56 * FF, rt.ra[3]); bar();
      glds<4>(lbase + 114688 + w * 4096, gk + 163840, voff); retile<2, 0, 2>(rs, rt, WS_WO + (tn * 32 + kb) * 4096 + (ct & 1) * 2048, voff, csum);
      raw_rows2(li + 8192, src1 + 0 * DM, rt.ra[4]); raw_rows2(li + 9216, src1 + 8 * DM, rt.ra[5]); raw_rows2(li + 10240, src1 + 16 * DM, rt.ra[6]); wait_vm<26>();
      lgkm_bar();
      glds<4>(lbase + 131072 + w * 4096, gk + 180224, voff); wait_vm<13>(); retile<0, 0, 4>(rs, rt, WS_W1 + (t1 * 32 + kb) * 7168, voff, csum);
      raw_rows2(li + 11264, src1 + 24 * DM, rt.ra[7]); raw_rows2(li + 12288, src1 + 32 * DM, rt.ra[4]);
      asm volatile("s_getpc_b64 s[96:97]\n\t0:\n\ts_mov_b32 s100, .Lpg7-0b\n\ts_cmp_eq_u32 %0, 1\n\ts_cselect_b32 s100, .Lpg8-0b, s100\n\ts_cmp_eq_u32 %0, 2\n\ts_cselect_b32 s100, .Lpg9-0b, s100\n\ts_ashr_i32 s101, s100, 31\n\ts_add_u32 s96, s96, s100\n\ts_addc_u32 s97, s97, s101\n\ts_mov_b64 exec, 0\n\ts_swappc_b64 s[98:99], s[96:97]\n\ts_mov_b64 exec, -1" VISIT);
      bar();
      glds<4>(lbase + 49152 + w * 4096, gk + 196608, voff); retile<0, 4, 3>(rs, rt, WS_W1 + (t1 * 32 + kb) * 7168, voff, csum);
      st16(rs, WS_CS1 + (t1 * 32 + kb) * 448, rt.co, __builtin_bit_cast(u32x4, csum)); raw_rows2(li + 13312, src1 + 40 * DM, rt.ra[5]);
      raw_rows2(li + 14336, src1 + 48 * DM, rt.ra[6]); raw_rows2(li + 15360, src1 + 56 * DM, rt.ra[7]); lgkm_bar();
      glds<4>(lbase + 65536 + w * 4096, gk + 212992, voff); wait_vm<7>(); signal(rs4, fw + 0 + kb * 8, tok); bar();
      asm volatile("global_load_dword %0, %1, %2 sc1" : "+v"(cd) : "v"(voff), "s"(code) : "memory"); glds<4>(lbase + 81920 + w * 4096, gk + 229376, voff);
      for (int i = 0; i < 2; ++i) st16(rs, WS_XCH + 0 * MB + (idx * 8 + (xcd & 1)) * 32768 + ((kq + 1) & 3) * 65536 + w * 2048 + 0, voff + i * 1024, u32x4{~0u, ~0u, ~0u, ~0u});
      poll(pq, rs4, FL_WT + (t1 >> 1) * 512, ((lane & 15) * 4 + w) * 8); wait_vm<13>(); retile<1, 0, 4>(rs, rt, WS_W2 + (ct * 112 + k2) * 8192, voff, csum); bar();
      glds<4>(lbase + 98304 + w * 4096, gk + 245760, voff);
      for (int i = 0; i < 2; ++i) st16(rs, WS_XCH + 0 * MB + (idx * 8 + (xcd & 1)) * 32768 + ((kq + 2) & 3) * 65536 + w * 2048 + 8192, voff + i * 1024, u32x4{~0u, ~0u, ~0u, ~0u});
      wait_vm<10>();
      { asm volatile("" : "+v"(pq)); if (__builtin_amdgcn_ballot_w64(pq != tok)) await(rs, FL_WT + (t1 >> 1) * 512, (lane & 15) * 4 + w, tok); }
      retile<1, 4, 4>(rs, rt, WS_W2 + (ct * 112 + k2) * 8192, voff, csum); ldf<0>(u0_0_0, g1 + 0, voff); ldf<1024>(u0_0_1, g1 + 0, voff); ldf<2048>(u0_0_2, g1 + 0, voff);
      ldf<3072>(u0_0_3, g1 + 0, voff); ldf<-4096>(u0_0_4, g1 + 8192, voff); ldf<-3072>(u0_0_5, g1 + 8192, voff); ldf<-2048>(u0_0_6, g1 + 8192, voff);
      ldf<-4096>(u0_1_0, g1 + 32768, voff); ldf<-3072>(u0_1_1, g1 + 32768, voff); ldf<-2048>(u0_1_2, g1 + 32768, voff); lgkm_bar();
      glds<4>(lbase + 114688 + w * 4096, gk + 0, voff);
      for (int i = 0; i < 2; ++i) st16(rs, WS_XCH + 0 * MB + (idx * 8 + (xcd & 1)) * 32768 + ((kq + 3) & 3) * 65536 + w * 2048 + 16384, voff + i * 1024, u32x4{~0u, ~0u, ~0u, ~0u});
      glds<4>(lbase + 0 + w * 4096, go + w * 4096 + 0, voff); ldf<-1024>(u0_1_3, g1 + 32768, voff); ldf<0>(u0_1_4, g1 + 32768, voff); ldf<1024>(u0_1_5, g1 + 32768, voff);
      ldf<2048>(u0_1_6, g1 + 32768, voff); ldf<0>(u0_2_0, g1 + 57344, voff); ldf<1024>(u0_2_1, g1 + 57344, voff); ldf<2048>(u0_2_2, g1 + 57344, voff);
      ldf<3072>(u0_2_3, g1 + 57344, voff); ldf<-4096>(u0_2_4, g1 + 65536, voff); ldf<-3072>(u0_2_5, g1 + 65536, voff); bar();
      glds<4>(lbase + 131072 + w * 4096, gk + 16384, voff);
      for (int i = 0; i < 2; ++i) st16(rs, WS_XCH + 8 * MB + (idx * 8 + (xcd & 1)) * 32768 + ((kq + 1) & 3) * 65536 + w * 2048 + 0, voff + i * 1024, u32x4{~0u, ~0u, ~0u, ~0u});
      wait_vm<36>(); signal(rs4, fw + 8192 + kb * 8, tok); glds<4>(lbase + 16384 + w * 4096, go + w * 4096 + 16384, voff); ldf<-2048>(u0_2_6, g1 + 65536, voff);
      ldf<-4096>(u0_3_0, g1 + 90112, voff); ldf<-3072>(u0_3_1, g1 + 90112, voff); ldf<-2048>(u0_3_2, g1 + 90112, voff); ldf<-1024>(u0_3_3, g1 + 90112, voff);
      ldf<0>(u0_3_4, g1 + 90112, voff); ldf<1024>(u0_3_5, g1 + 90112, voff); ldf<2048>(u0_3_6, g1 + 90112, voff); ldf<0>(u0_4_0, g1 + 114688, voff);
      asm volatile("s_getpc_b64 s[96:97]\n\t0:\n\ts_mov_b32 s100, .Lpg10-0b\n\ts_cmp_eq_u32 %0, 1\n\ts_cselect_b32 s100, .Lpg11-0b, s100\n\ts_cmp_eq_u32 %0, 2\n\ts_cselect_b32 s100, .Lpg12-0b, s100\n\ts_cmp_eq_u32 %0, 3\n\ts_cselect_b32 s100, .Lpg13-0b, s100\n\ts_ashr_i32 s101, s100, 31\n\ts_add_u32 s96, s96, s100\n\ts_addc_u32 s97, s97, s101\n\ts_mov_b64 exec, 0\n\ts_swappc_b64 s[98:99], s[96:97]\n\ts_mov_b64 exec, -1" VISIT);
      bar();
      glds<4>(lbase + 49152 + w * 4096, gk + 32768, voff);
      for (int i = 0; i < 2; ++i) st16(rs, WS_XCH + 8 * MB + (idx * 8 + (xcd & 1)) * 32768 + ((kq + 2) & 3) * 65536 + w * 2048 + 8192, voff + i * 1024, u32x4{~0u, ~0u, ~0u, ~0u});
      wait_vm<55>(); glds<4>(lbase + 32768 + w * 4096, go + w * 4096 + 32768, voff); wait_vm<58>(); ldf<1024>(u0_4_1, g1 + 114688, voff); wait_vm<58>();
      ldf<2048>(u0_4_2, g1 + 114688, voff); wait_vm<58>(); ldf<3072>(u0_4_3, g1 + 114688, voff); wait_vm<58>(); ldf<-4096>(u0_4_4, g1 + 122880, voff); wait_vm<58>();
      ldf<-3072>(u0_4_5, g1 + 122880, voff); wait_vm<58>(); ldf<-2048>(u0_4_6, g1 + 122880, voff); wait_vm<58>(); ldf<-4096>(u0_5_0, g1 + 147456, voff); wait_vm<58>();
      ldf<-3072>(u0_5_1, g1 + 147456, voff); wait_vm<58>(); ldf<-2048>(u0_5_2, g1 + 147456, voff); bar();
      wait_vm<55>();
      asm volatile("global_load_dword %0, %1, %2 sc1" : "+v"(cd) : "v"(voff), "s"(code) : "memory"); wait_vm<14>(); glds<4>(lbase + 65536 + w * 4096, gk + 49152, voff);
      for (int i = 0; i < 2; ++i) st16(rs, WS_XCH + 8 * MB + (idx * 8 + (xcd & 1)) * 32768 + ((kq + 3) & 3) * 65536 + w * 2048 + 16384, voff + i * 1024, u32x4{~0u, ~0u, ~0u, ~0u});
      poll(pq, rs4, fw + 8192, ((lane < 14 ? lane : 13) * 2 + (w >> 1)) * 8); glds<4>(lbase + 147456 + w * 4096, go + w * 4096 + 49152, voff);
      ldf<-1024>(u0_5_3, g1 + 147456, voff); ldf<0>(u0_5_4, g1 + 147456, voff); ldf<1024>(u0_5_5, g1 + 147456, voff); ldf<2048>(u0_5_6, g1 + 147456, voff);
      ldf<0>(u0_6_0, g1 + 172032, voff); ldf<1024>(u0_6_1, g1 + 172032, voff); ldf<2048>(u0_6_2, g1 + 172032, voff); ldf<3072>(u0_6_3, g1 + 172032, voff);
      ldf<-4096>(u0_6_4, g1 + 180224, voff); bar();
      wait_vm<13>();
      { asm volatile("" : "+v"(pq)); if (__builtin_amdgcn_ballot_w64(pq != tok)) await(rs, fw + 8192, (lane < 14 ? lane : 13) * 2 + (w >> 1), tok); }
      glds<4>(lbase + 81920 + w * 4096, go + w * 4096 + 65536, voff); ldf<-3072>(u0_6_5, g1 + 180224, voff); ldf<-2048>(u0_6_6, g1 + 180224, voff);
      ldf<-4096>(u0_7_0, g1 + 204800, voff); ldf<-3072>(u0_7_1, g1 + 204800, voff); ldf<-2048>(u0_7_2, g1 + 204800, voff); ldf<-1024>(u0_7_3, g1 + 204800, voff);
      ldf<0>(u0_7_4, g1 + 204800, voff); ldf<1024>(u0_7_5, g1 + 204800, voff); ldf<2048>(u0_7_6, g1 + 204800, voff); bar();
      asm volatile(LD8X1(0, 448) : OUT8(cs, 0) : "v"(cso), "s"(ws + WS_CS1 + t1 * 14336 + 0) : "memory");
      asm volatile(LD8X1(0, 448) : OUT8(cs, 8) : "v"(cso), "s"(ws + WS_CS1 + t1 * 14336 + 3584) : "memory"); glds<4>(lbase + 98304 + w * 4096, go + w * 4096 + 81920, voff);
      bar();
      glds<4>(lbase + 114688 + w * 4096, go + w * 4096 + 98304, voff); poll(pq, rs4, flr + K_O * 16384 + (0 + rb16 * 2 + (w >> 1)) * 256, (lane & 31) * 8); wait_vm<0>();
      { asm volatile("" : "+v"(pq)); if (__builtin_amdgcn_ballot_w64(pq != tok)) await(rs, flr + K_O * 16384 + (0 + rb16 * 2 + (w >> 1)) * 256, lane & 31, tok); }
      bar();
      glds<4>(lbase + 131072 + w * 4096, go + w * 4096 + 114688, voff); bar();
      asm volatile("global_load_dword %0, %1, %2 sc1" : "+v"(cd) : "v"(voff), "s"(code) : "memory"); bar();
      wait_vm<1>(); bar();
      stl<53248>(lbase + voff + 0u + 0 + w * 7168, u0_0_0); stl<54272>(lbase + voff + 0u + 0 + w * 7168, u0_0_1); stl<55296>(lbase + voff + 0u + 0 + w * 7168, u0_0_2);
      stl<56320>(lbase + voff + 0u + 0 + w * 7168, u0_0_3); stl<57344>(lbase + voff + 0u + 0 + w * 7168, u0_0_4); stl<58368>(lbase + voff + 0u + 0 + w * 7168, u0_0_5);
      stl<59392>(lbase + voff + 0u + 0 + w * 7168, u0_0_6); poll(pq, rs4, flr + K_O * 16384 + (32 + rb16 * 2 + (w >> 1)) * 256, (lane & 31) * 8); wait_vm<0>();
      { asm volatile("" : "+v"(pq)); if (__builtin_amdgcn_ballot_w64(pq != tok)) await(rs, flr + K_O * 16384 + (32 + rb16 * 2 + (w >> 1)) * 256, lane & 31, tok); }
      lgkm_bar();
      { PIN8(cs, 0); PIN8(cs, 8); asm volatile("" : "+v"(bb)); const float c = xsum32(add1(sum8(cs), sum8(cs + 8))); csu = lane < 32 ? c : bf_lo(bb) * GELU_A; asm volatile("" : "+v"(csu)); }
      __builtin_amdgcn_s_sleep(28); await(rs, flr + K_X1 * 16384 + ((0 + rb8) * 4 + w) * 256, lane & 31, tok); wait_vm<0>(); bar();
      } { const int layer0 = 0; IDS();
      const char* g2 = ws + WS_W2 + (ct * 112 + kq * 28) * 8192 + w * 4096;
      const int cl = w * 28 + ((lane & 31) < 28 ? (lane & 31) : 27); stl<0>(lbase + voff + 0u + 0 + w * 7168, u0_1_0); stl<1024>(lbase + voff + 0u + 0 + w * 7168, u0_1_1);
      stl<2048>(lbase + voff + 0u + 0 + w * 7168, u0_1_2); stl<3072>(lbase + voff + 0u + 0 + w * 7168, u0_1_3); stl<4096>(lbase + voff + 0u + 0 + w * 7168, u0_1_4);
      stl<5120>(lbase + voff + 0u + 0 + w * 7168, u0_1_5); stl<6144>(lbase + voff + 0u + 0 + w * 7168, u0_1_6); stl<28672>(lbase + voff + 0u + 0 + w * 6144, u0_2_0);
      stl<29696>(lbase + voff + 0u + 0 + w * 6144, u0_2_1); stl<30720>(lbase + voff + 0u + 0 + w * 6144, u0_2_2); stl<31744>(lbase + voff + 0u + 0 + w * 6144, u0_2_3);
      stl<32768>(lbase + voff + 0u + 0 + w * 6144, u0_2_4); stl<33792>(lbase + voff + 0u + 0 + w * 6144, u0_2_5); stl<8192>(lbase + voff + 131072u + 0 + w * 1024, u0_2_6);
      lgkm_bar();
      asm volatile("global_load_dword %0, %1, %2 sc1" : "+v"(cd) : "v"(voff), "s"(code) : "memory"); stl<16384>(lbase + voff + 65536u + 0 + w * 7168, u0_3_0);
      stl<17408>(lbase + voff + 65536u + 0 + w * 7168, u0_3_1); stl<18432>(lbase + voff + 65536u + 0 + w * 7168, u0_3_2);
      stl<19456>(lbase + voff + 65536u + 0 + w * 7168, u0_3_3); stl<20480>(lbase + voff + 65536u + 0 + w * 7168, u0_3_4);
      stl<21504>(lbase + voff + 65536u + 0 + w * 7168, u0_3_5); stl<22528>(lbase + voff + 65536u + 0 + w * 7168, u0_3_6);
      ldf<0>(p4[0], ws + WS_ST + 262144 + ((0 + rb8) * 4 + w) * 4096, voff); ldf<1024>(p4[1], ws + WS_ST + 262144 + ((0 + rb8) * 4 + w) * 4096, voff);
      ldf<2048>(p4[2], ws + WS_ST + 262144 + ((0 + rb8) * 4 + w) * 4096, voff); ldf<3072>(p4[3], ws + WS_ST + 262144 + ((0 + rb8) * 4 + w) * 4096, voff);
      stl<45056>(lbase + voff + 65536u + 0 + w * 7168, u0_4_0); stl<46080>(lbase + voff + 65536u + 0 + w * 7168, u0_4_1);
      stl<47104>(lbase + voff + 65536u + 0 + w * 7168, u0_4_2); stl<48128>(lbase + voff + 65536u + 0 + w * 7168, u0_4_3);
      stl<49152>(lbase + voff + 65536u + 0 + w * 7168, u0_4_4); stl<50176>(lbase + voff + 65536u + 0 + w * 7168, u0_4_5);
      stl<51200>(lbase + voff + 65536u + 0 + w * 7168, u0_4_6); wait_vm<0>();
      { PIN4(p4); const f32x2 f = finish_stats<true>(p4); *(float*)(lds + L_FIN + (fq & 1) * 1024 + 0 + w * 128 + lr * 8 + (lane >> 5) * 4) = (fq & 1) ? f[1] : f[0]; }
      *(float*)(lds + L_FIN + 2048 + (lane >> 5) * 512 + cl * 4) = csu; lgkm_bar();
      stl<53248>(lbase + voff + 0u + 0 + w * 7168, u0_5_0); stl<54272>(lbase + voff + 0u + 0 + w * 7168, u0_5_1); stl<55296>(lbase + voff + 0u + 0 + w * 7168, u0_5_2);
      stl<56320>(lbase + voff + 0u + 0 + w * 7168, u0_5_3); stl<57344>(lbase + voff + 0u + 0 + w * 7168, u0_5_4); stl<58368>(lbase + voff + 0u + 0 + w * 7168, u0_5_5);
      stl<59392>(lbase + voff + 0u + 0 + w * 7168, u0_5_6); stl<0>(lbase + voff + 0u + 0 + w * 7168, u0_6_0); stl<1024>(lbase + voff + 0u + 0 + w * 7168, u0_6_1);
      stl<2048>(lbase + voff + 0u + 0 + w * 7168, u0_6_2); stl<3072>(lbase + voff + 0u + 0 + w * 7168, u0_6_3); stl<4096>(lbase + voff + 0u + 0 + w * 7168, u0_6_4);
      stl<5120>(lbase + voff + 0u + 0 + w * 7168, u0_6_5); stl<6144>(lbase + voff + 0u + 0 + w * 7168, u0_6_6); ldf<0>(v0_0_0, g2 + 0, voff); __builtin_amdgcn_s_sleep(2);
      ldf<1024>(v0_0_1, g2 + 0, voff); __builtin_amdgcn_s_sleep(2); ldf<2048>(v0_0_2, g2 + 0, voff); __builtin_amdgcn_s_sleep(2); ldf<3072>(v0_0_3, g2 + 0, voff);
      __builtin_amdgcn_s_sleep(2); lgkm_bar();
      stl<28672>(lbase + voff + 0u + 0 + w * 6144, u0_7_0); stl<29696>(lbase + voff + 0u + 0 + w * 6144, u0_7_1); stl<30720>(lbase + voff + 0u + 0 + w * 6144, u0_7_2);
      stl<31744>(lbase + voff + 0u + 0 + w * 6144, u0_7_3); stl<32768>(lbase + voff + 0u + 0 + w * 6144, u0_7_4); stl<33792>(lbase + voff + 0u + 0 + w * 6144, u0_7_5);
      stl<8192>(lbase + voff + 131072u + 0 + w * 1024, u0_7_6); await(rs, flr + K_X1 * 16384 + ((8 + rb8) * 4 + w) * 256, lane & 31, tok); wait_vm<0>(); lgkm_bar();
      ldf<0>(v0_1_0, g2 + 16384, voff); __builtin_amdgcn_s_sleep(3); ldf<1024>(v0_1_1, g2 + 16384, voff); __builtin_amdgcn_s_sleep(3); ldf<2048>(v0_1_2, g2 + 16384, voff);
      __builtin_amdgcn_s_sleep(3); ldf<3072>(v0_1_3, g2 + 16384, voff); __builtin_amdgcn_s_sleep(3); ldf<0>(v0_2_0, g2 + 32768, voff); __builtin_amdgcn_s_sleep(3);
      ldf<1024>(v0_2_1, g2 + 32768, voff); __builtin_amdgcn_s_sleep(3); ldf<2048>(v0_2_2, g2 + 32768, voff); __builtin_amdgcn_s_sleep(3); ldf<3072>(v0_2_3, g2 + 32768, voff);
      __builtin_amdgcn_s_sleep(3); bar();
      asm volatile("global_load_dword %0, %1, %2 sc1" : "+v"(cd) : "v"(voff), "s"(code) : "memory"); ldf<0>(v0_3_0, g2 + 49152, voff); ldf<1024>(v0_3_1, g2 + 49152, voff);
      ldf<2048>(v0_3_2, g2 + 49152, voff); ldf<3072>(v0_3_3, g2 + 49152, voff); ldf<0>(v0_4_0, g2 + 65536, voff); ldf<1024>(v0_4_1, g2 + 65536, voff);
      ldf<2048>(v0_4_2, g2 + 65536, voff); ldf<3072>(v0_4_3, g2 + 65536, voff); ldf<0>(v0_9_0, g2 + 147456, voff); ldf<1024>(v0_9_1, g2 + 147456, voff);
      ldf<2048>(v0_9_2, g2 + 147456, voff); ldf<3072>(v0_9_3, g2 + 147456, voff); ldf<0>(v0_10_0, g2 + 163840, voff); ldf<1024>(v0_10_1, g2 + 163840, voff);
      ldf<2048>(v0_10_2, g2 + 163840, voff); ldf<3072>(v0_10_3, g2 + 163840, voff); ldf<0>(p4[0], ws + WS_ST + 262144 + ((8 + rb8) * 4 + w) * 4096, voff);
      ldf<1024>(p4[1], ws + WS_ST + 262144 + ((8 + rb8) * 4 + w) * 4096, voff); ldf<2048>(p4[2], ws + WS_ST + 262144 + ((8 + rb8) * 4 + w) * 4096, voff);
      ldf<3072>(p4[3], ws + WS_ST + 262144 + ((8 + rb8) * 4 + w) * 4096, voff); wait_vm<0>();
      { PIN4(p4); const f32x2 f = finish_stats<true>(p4); *(float*)(lds + L_FIN + (fq & 1) * 1024 + 512 + w * 128 + lr * 8 + (lane >> 5) * 4) = (fq & 1) ? f[1] : f[0]; }
      lgkm_bar();
      stl<53248>(lbase + voff + 0u + 0 + w * 7168, u0_0_0); stl<54272>(lbase + voff + 0u + 0 + w * 7168, u0_0_1); stl<55296>(lbase + voff + 0u + 0 + w * 7168, u0_0_2);
      stl<56320>(lbase + voff + 0u + 0 + w * 7168, u0_0_3); stl<57344>(lbase + voff + 0u + 0 + w * 7168, u0_0_4); stl<58368>(lbase + voff + 0u + 0 + w * 7168, u0_0_5);
      stl<59392>(lbase + voff + 0u + 0 + w * 7168, u0_0_6); stl<0>(lbase + voff + 0u + 0 + w * 7168, u0_1_0); stl<1024>(lbase + voff + 0u + 0 + w * 7168, u0_1_1);
      stl<2048>(lbase + voff + 0u + 0 + w * 7168, u0_1_2); stl<3072>(lbase + voff + 0u + 0 + w * 7168, u0_1_3); stl<4096>(lbase + voff + 0u + 0 + w * 7168, u0_1_4);
      stl<5120>(lbase + voff + 0u + 0 + w * 7168, u0_1_5); stl<6144>(lbase + voff + 0u + 0 + w * 7168, u0_1_6); ldf<0>(v0_11_0, g2 + 180224, voff); __builtin_amdgcn_s_sleep(2);
      ldf<1024>(v0_11_1, g2 + 180224, voff); __builtin_amdgcn_s_sleep(2); ldf<2048>(v0_11_2, g2 + 180224, voff); __builtin_amdgcn_s_sleep(2);
      ldf<3072>(v0_11_3, g2 + 180224, voff); __builtin_amdgcn_s_sleep(2); lgkm_bar();
      stl<28672>(lbase + voff + 0u + 0 + w * 6144, u0_2_0); stl<29696>(lbase + voff + 0u + 0 + w * 6144, u0_2_1); stl<30720>(lbase + voff + 0u + 0 + w * 6144, u0_2_2);
      stl<31744>(lbase + voff + 0u + 0 + w * 6144, u0_2_3); stl<32768>(lbase + voff + 0u + 0 + w * 6144, u0_2_4); stl<33792>(lbase + voff + 0u + 0 + w * 6144, u0_2_5);
      stl<8192>(lbase + voff + 131072u + 0 + w * 1024, u0_2_6); await(rs, flr + K_H * 16384 + ((0 + rb8) * 4 + w) * 256, kq * 8 + (lane & 7), tok); wait_vm<0>();
      lgkm_bar();
      ldf<0>(v0_12_0, g2 + 196608, voff); __builtin_amdgcn_s_sleep(2); ldf<1024>(v0_12_1, g2 + 196608, voff); __builtin_amdgcn_s_sleep(2); ldf<2048>(v0_12_2, g2 + 196608, voff);
      __builtin_amdgcn_s_sleep(2); ldf<3072>(v0_12_3, g2 + 196608, voff); __builtin_amdgcn_s_sleep(2); ldf<0>(v0_13_0, g2 + 212992, voff); __builtin_amdgcn_s_sleep(2);
      ldf<1024>(v0_13_1, g2 + 212992, voff); __builtin_amdgcn_s_sleep(2); ldf<2048>(v0_13_2, g2 + 212992, voff); __builtin_amdgcn_s_sleep(2);
      ldf<3072>(v0_13_3, g2 + 212992, voff); __builtin_amdgcn_s_sleep(2);
      asm volatile("s_getpc_b64 s[96:97]\n\t0:\n\ts_mov_b32 s100, .Lpg14-0b\n\ts_cmp_eq_u32 %0, 1\n\ts_cselect_b32 s100, .Lpg15-0b, s100\n\ts_cmp_eq_u32 %0, 2\n\ts_cselect_b32 s100, .Lpg16-0b, s100\n\ts_ashr_i32 s101, s100, 31\n\ts_add_u32 s96, s96, s100\n\ts_addc_u32 s97, s97, s101\n\ts_mov_b64 exec, 0\n\ts_swappc_b64 s[98:99], s[96:97]\n\ts_mov_b64 exec, -1" VISIT);
      bar();
      asm volatile("global_load_dword %0, %1, %2 sc1" : "+v"(cd) : "v"(voff), "s"(code) : "memory"); glds<4>(lbase + 0 + w * 4096, g2 + 81920, voff);
      stl<0>(lbase + voff + 65536u + 0 + w * 4096, v0_0_0); stl<1024>(lbase + voff + 65536u + 0 + w * 4096, v0_0_1); stl<2048>(lbase + voff + 65536u + 0 + w * 4096, v0_0_2);
      stl<3072>(lbase + voff + 65536u + 0 + w * 4096, v0_0_3); glds<4>(lbase + 16384 + w * 4096, g2 + 98304, voff); stl<16384>(lbase + voff + 65536u + 0 + w * 4096, v0_1_0);
      stl<17408>(lbase + voff + 65536u + 0 + w * 4096, v0_1_1); stl<18432>(lbase + voff + 65536u + 0 + w * 4096, v0_1_2);
      stl<19456>(lbase + voff + 65536u + 0 + w * 4096, v0_1_3); glds<4>(lbase + 32768 + w * 4096, g2 + 114688, voff); stl<32768>(lbase + voff + 65536u + 0 + w * 4096, v0_2_0);
      stl<33792>(lbase + voff + 65536u + 0 + w * 4096, v0_2_1); stl<34816>(lbase + voff + 65536u + 0 + w * 4096, v0_2_2);
      stl<35840>(lbase + voff + 65536u + 0 + w * 4096, v0_2_3); glds<4>(lbase + 49152 + w * 4096, g2 + 131072, voff); stl<49152>(lbase + voff + 65536u + 0 + w * 4096, v0_3_0);
      stl<50176>(lbase + voff + 65536u + 0 + w * 4096, v0_3_1); stl<51200>(lbase + voff + 65536u + 0 + w * 4096, v0_3_2);
      stl<52224>(lbase + voff + 65536u + 0 + w * 4096, v0_3_3); stl<0>(lbase + voff + 131072u + 0 + w * 4096, v0_4_0); stl<1024>(lbase + voff + 131072u + 0 + w * 4096, v0_4_1);
      stl<2048>(lbase + voff + 131072u + 0 + w * 4096, v0_4_2); stl<3072>(lbase + voff + 131072u + 0 + w * 4096, v0_4_3);
      const char* gq = ws + WS_WQKV + h * 196608 + w * 6144; ldf<0>(q1_0_0, gq + 0, voff); ldf<1024>(q1_0_1, gq + 0, voff); ldf<2048>(q1_0_2, gq + 0, voff);
      ldf<3072>(q1_0_3, gq + 0, voff); ldf<-4096>(q1_0_4, gq + 8192, voff); ldf<-3072>(q1_0_5, gq + 8192, voff);
      } { const int layer0 = 0; IDS();
      const char* gq = ws + WS_WQKV + h * 196608 + w * 6144; lgkm_bar();
      wait_vm<6>(); ldf<0>(q1_1_0, gq + 24576, voff); __builtin_amdgcn_s_sleep(2); ldf<1024>(q1_1_1, gq + 24576, voff); __builtin_amdgcn_s_sleep(2);
      ldf<2048>(q1_1_2, gq + 24576, voff); __builtin_amdgcn_s_sleep(2); ldf<3072>(q1_1_3, gq + 24576, voff); __builtin_amdgcn_s_sleep(2); ldf<-4096>(q1_1_4, gq + 32768, voff);
      __builtin_amdgcn_s_sleep(2); ldf<-3072>(q1_1_5, gq + 32768, voff); __builtin_amdgcn_s_sleep(2); ldf<0>(q1_3_0, gq + 73728, voff); __builtin_amdgcn_s_sleep(2);
      ldf<1024>(q1_3_1, gq + 73728, voff); __builtin_amdgcn_s_sleep(2); ldf<2048>(q1_3_2, gq + 73728, voff); __builtin_amdgcn_s_sleep(2); ldf<3072>(q1_3_3, gq + 73728, voff);
      __builtin_amdgcn_s_sleep(2); ldf<-4096>(q1_3_4, gq + 81920, voff); __builtin_amdgcn_s_sleep(2); ldf<-3072>(q1_3_5, gq + 81920, voff); __builtin_amdgcn_s_sleep(2);
      bar();
      stl<0>(lbase + voff + 65536u + 0 + w * 4096, v0_9_0); stl<1024>(lbase + voff + 65536u + 0 + w * 4096, v0_9_1); stl<2048>(lbase + voff + 65536u + 0 + w * 4096, v0_9_2);
      stl<3072>(lbase + voff + 65536u + 0 + w * 4096, v0_9_3); stl<16384>(lbase + voff + 65536u + 0 + w * 4096, v0_10_0);
      stl<17408>(lbase + voff + 65536u + 0 + w * 4096, v0_10_1); stl<18432>(lbase + voff + 65536u + 0 + w * 4096, v0_10_2);
      stl<19456>(lbase + voff + 65536u + 0 + w * 4096, v0_10_3); stl<32768>(lbase + voff + 65536u + 0 + w * 4096, v0_11_0);
      stl<33792>(lbase + voff + 65536u + 0 + w * 4096, v0_11_1); stl<34816>(lbase + voff + 65536u + 0 + w * 4096, v0_11_2);
      stl<35840>(lbase + voff + 65536u + 0 + w * 4096, v0_11_3); poll(pq, rs4, flr + K_H * 16384 + ((8 + rb8) * 4 + w) * 256, (kq * 8 + (lane & 7)) * 8); lgkm_bar();
      stl<49152>(lbase + voff + 65536u + 0 + w * 4096, v0_12_0); stl<50176>(lbase + voff + 65536u + 0 + w * 4096, v0_12_1);
      stl<51200>(lbase + voff + 65536u + 0 + w * 4096, v0_12_2); stl<52224>(lbase + voff + 65536u + 0 + w * 4096, v0_12_3);
      stl<0>(lbase + voff + 131072u + 0 + w * 4096, v0_13_0); stl<1024>(lbase + voff + 131072u + 0 + w * 4096, v0_13_1);
      stl<2048>(lbase + voff + 131072u + 0 + w * 4096, v0_13_2); stl<3072>(lbase + voff + 131072u + 0 + w * 4096, v0_13_3); wait_vm<0>();
      { asm volatile("" : "+v"(pq)); if (__builtin_amdgcn_ballot_w64(pq != tok)) await(rs, flr + K_H * 16384 + ((8 + rb8) * 4 + w) * 256, kq * 8 + (lane & 7), tok); }
      lgkm_bar();
      asm volatile("global_load_dword %0, %1, %2 sc1" : "+v"(cd) : "v"(voff), "s"(code) : "memory");
      const unsigned xt0 = WS_X + 0 * MB + ((0 + rb8) * 32 + ct * 4 + kq) * 8192 + w * 2048; ldf<0>(pr[0], ws + 4 * MB + xt0 + 0, voff);
      ldf<1024>(pr[1], ws + 4 * MB + xt0 + 0, voff); ldf<0>(pr[2], b2 + 0, (ct * 128 + kq * 32 + fq * 8) * 2); ldf<0>(q1_4_0, gq + 98304, voff); __builtin_amdgcn_s_sleep(4);
      ldf<1024>(q1_4_1, gq + 98304, voff); __builtin_amdgcn_s_sleep(4); ldf<2048>(q1_4_2, gq + 98304, voff); __builtin_amdgcn_s_sleep(4); ldf<3072>(q1_4_3, gq + 98304, voff);
      __builtin_amdgcn_s_sleep(4); ldf<-4096>(q1_4_4, gq + 106496, voff); __builtin_amdgcn_s_sleep(4); ldf<-3072>(q1_4_5, gq + 106496, voff); __builtin_amdgcn_s_sleep(4);
      wait_vm<6>(); fin2_pre(ps, pr); bar();
      ldl<0>(pp[0], pb + L_OWN + w * 4096); ldl<1024>(pp[1], pb + L_OWN + w * 4096); ldl<2048>(pp[2], pb + L_OWN + w * 4096); ldl<3072>(pp[3], pb + L_OWN + w * 4096);
      const char* gi0 = ws + WS_XCH + 0 * MB + bid * 32768 + w * 2048; ldf<0>(pi[0], gi0 + 0, voff); ldf<1024>(pi[1], gi0 + 0, voff); ldf<0>(pi[2], gi0 + 8192, voff);
      ldf<1024>(pi[3], gi0 + 8192, voff); ldf<0>(pi[4], gi0 + 16384, voff); ldf<1024>(pi[5], gi0 + 16384, voff); stl<0>(lbase + voff + 65536u + 0 + w * 4096, v0_0_0);
      stl<1024>(lbase + voff + 65536u + 0 + w * 4096, v0_0_1); stl<2048>(lbase + voff + 65536u + 0 + w * 4096, v0_0_2); stl<3072>(lbase + voff + 65536u + 0 + w * 4096, v0_0_3);
      fin2_add(pp, ps); sel = make_sel(); wait_vm<0>();
      asm volatile("s_setprio 3" ::: "memory"); fin2<0>(pp, pi, sel, gi0, rs, 8 * MB + xt0, WS_ST2 + (((0 + rb8) * 4 + w) * 16 + ct * 2 + (kq >> 1)) * 512 + (kq & 1) * 8,
           out + (((0 + rb8) * 128 + w * 32) * DM + ct * 128 + kq * 32) * 2, lane RTC);
      asm volatile("s_setprio 0" ::: "memory"); bar();
      const unsigned xt1 = WS_X + 0 * MB + ((8 + rb8) * 32 + ct * 4 + kq) * 8192 + w * 2048; ldf<0>(pr[0], ws + 4 * MB + xt1 + 0, voff);
      ldf<1024>(pr[1], ws + 4 * MB + xt1 + 0, voff); ldf<0>(pr[2], b2 + 0, (ct * 128 + kq * 32 + fq * 8) * 2); stl<16384>(lbase + voff + 65536u + 0 + w * 4096, v0_1_0);
      stl<17408>(lbase + voff + 65536u + 0 + w * 4096, v0_1_1); stl<18432>(lbase + voff + 65536u + 0 + w * 4096, v0_1_2);
      stl<19456>(lbase + voff + 65536u + 0 + w * 4096, v0_1_3); stl<32768>(lbase + voff + 65536u + 0 + w * 4096, v0_2_0);
      stl<33792>(lbase + voff + 65536u + 0 + w * 4096, v0_2_1); stl<34816>(lbase + voff + 65536u + 0 + w * 4096, v0_2_2);
      stl<35840>(lbase + voff + 65536u + 0 + w * 4096, v0_2_3); stl<49152>(lbase + voff + 65536u + 0 + w * 4096, v0_3_0);
      stl<50176>(lbase + voff + 65536u + 0 + w * 4096, v0_3_1); stl<51200>(lbase + voff + 65536u + 0 + w * 4096, v0_3_2);
      stl<52224>(lbase + voff + 65536u + 0 + w * 4096, v0_3_3); wait_vm<3>(); signal(rs4, FL_ROW + K_X2 * 16384 + ((0 + rb8) * 4 + w) * 256 + (ct * 4 + kq) * 8, tok);
      stl<0>(lbase + voff + 131072u + 0 + w * 4096, v0_4_0); stl<1024>(lbase + voff + 131072u + 0 + w * 4096, v0_4_1); stl<2048>(lbase + voff + 131072u + 0 + w * 4096, v0_4_2);
      stl<3072>(lbase + voff + 131072u + 0 + w * 4096, v0_4_3); ldf<0>(q1_5_0, gq + 122880, voff); ldf<1024>(q1_5_1, gq + 122880, voff); ldf<2048>(q1_5_2, gq + 122880, voff);
      ldf<3072>(q1_5_3, gq + 122880, voff); ldf<-4096>(q1_5_4, gq + 131072, voff); ldf<-3072>(q1_5_5, gq + 131072, voff); ldf<0>(q1_6_0, gq + 147456, voff);
      ldf<1024>(q1_6_1, gq + 147456, voff); ldf<2048>(q1_6_2, gq + 147456, voff); ldf<3072>(q1_6_3, gq + 147456, voff); ldf<-4096>(q1_6_4, gq + 155648, voff);
      ldf<-3072>(q1_6_5, gq + 155648, voff); lgkm_bar();
      ldf<0>(q1_7_0, gq + 172032, voff); ldf<1024>(q1_7_1, gq + 172032, voff); ldf<2048>(q1_7_2, gq + 172032, voff); ldf<3072>(q1_7_3, gq + 172032, voff);
      ldf<-4096>(q1_7_4, gq + 180224, voff); ldf<-3072>(q1_7_5, gq + 180224, voff); wait_vm<19>(); fin2_pre(ps, pr); bar();
      asm volatile("global_load_dword %0, %1, %2 sc1" : "+v"(cd) : "v"(voff), "s"(code) : "memory"); glds<6>(lbase + 49152 + w * 6144, gq + 49152, voff);
      stl<0>(lbase + voff + 0u + 0 + w * 6144, q1_0_0); stl<1024>(lbase + voff + 0u + 0 + w * 6144, q1_0_1); stl<2048>(lbase + voff + 0u + 0 + w * 6144, q1_0_2);
      stl<3072>(lbase + voff + 0u + 0 + w * 6144, q1_0_3); stl<4096>(lbase + voff + 0u + 0 + w * 6144, q1_0_4); stl<5120>(lbase + voff + 0u + 0 + w * 6144, q1_0_5);
      poll(pq, rs4, FL_ROW + K_X2 * 16384 + ((0 + blk) * 4 + w) * 256, (lane & 31) * 8); stl<24576>(lbase + voff + 0u + 0 + w * 6144, q1_1_0);
      stl<25600>(lbase + voff + 0u + 0 + w * 6144, q1_1_1); stl<26624>(lbase + voff + 0u + 0 + w * 6144, q1_1_2); stl<27648>(lbase + voff + 0u + 0 + w * 6144, q1_1_3);
      stl<28672>(lbase + voff + 0u + 0 + w * 6144, q1_1_4); stl<29696>(lbase + voff + 0u + 0 + w * 6144, q1_1_5); wait_vm<0>();
      { asm volatile("" : "+v"(pq)); if (__builtin_amdgcn_ballot_w64(pq != tok)) await(rs, FL_ROW + K_X2 * 16384 + ((0 + blk) * 4 + w) * 256, lane & 31, tok); }
      asm volatile("s_getpc_b64 s[96:97]\n\t0:\n\ts_mov_b32 s100, .Lpg17-0b\n\ts_cmp_eq_u32 %0, 1\n\ts_cselect_b32 s100, .Lpg18-0b, s100\n\ts_cmp_eq_u32 %0, 2\n\ts_cselect_b32 s100, .Lpg19-0b, s100\n\ts_ashr_i32 s101, s100, 31\n\ts_add_u32 s96, s96, s100\n\ts_addc_u32 s97, s97, s101\n\ts_mov_b64 exec, 0\n\ts_swappc_b64 s[98:99], s[96:97]\n\ts_mov_b64 exec, -1" VISIT);
      lgkm_bar();
      stl<8192>(lbase + voff + 65536u + 0 + w * 6144, q1_3_0); stl<9216>(lbase + voff + 65536u + 0 + w * 6144, q1_3_1); stl<10240>(lbase + voff + 65536u + 0 + w * 6144, q1_3_2);
      stl<11264>(lbase + voff + 65536u + 0 + w * 6144, q1_3_3); stl<12288>(lbase + voff + 65536u + 0 + w * 6144, q1_3_4);
      stl<13312>(lbase + voff + 65536u + 0 + w * 6144, q1_3_5); lgkm_bar();
      ldl<0>(pp[0], pb + L_OWN + w * 4096); ldl<1024>(pp[1], pb + L_OWN + w * 4096); ldl<2048>(pp[2], pb + L_OWN + w * 4096); ldl<3072>(pp[3], pb + L_OWN + w * 4096);
      stl<32768>(lbase + voff + 65536u + 0 + w * 6144, q1_4_0); stl<33792>(lbase + voff + 65536u + 0 + w * 6144, q1_4_1);
      stl<34816>(lbase + voff + 65536u + 0 + w * 6144, q1_4_2); stl<35840>(lbase + voff + 65536u + 0 + w * 6144, q1_4_3);
      stl<36864>(lbase + voff + 65536u + 0 + w * 6144, q1_4_4); stl<37888>(lbase + voff + 65536u + 0 + w * 6144, q1_4_5);
      const char* gi1 = ws + WS_XCH + 8 * MB + bid * 32768 + w * 2048; ldf<0>(pi[0], gi1 + 0, voff); ldf<1024>(pi[1], gi1 + 0, voff); ldf<0>(pi[2], gi1 + 8192, voff);
      ldf<1024>(pi[3], gi1 + 8192, voff); ldf<0>(pi[4], gi1 + 16384, voff); ldf<1024>(pi[5], gi1 + 16384, voff); stl<57344>(lbase + voff + 65536u + 0 + w * 6144, q1_5_0);
      stl<58368>(lbase + voff + 65536u + 0 + w * 6144, q1_5_1); stl<59392>(lbase + voff + 65536u + 0 + w * 6144, q1_5_2);
      stl<60416>(lbase + voff + 65536u + 0 + w * 6144, q1_5_3); stl<61440>(lbase + voff + 65536u + 0 + w * 6144, q1_5_4);
      stl<62464>(lbase + voff + 65536u + 0 + w * 6144, q1_5_5); fin2_add(pp, ps); sel = make_sel(); wait_vm<0>();
      asm volatile("s_setprio 3" ::: "memory"); fin2<1>(pp, pi, sel, gi1, rs, 8 * MB + xt1, WS_ST2 + (((8 + rb8) * 4 + w) * 16 + ct * 2 + (kq >> 1)) * 512 + (kq & 1) * 8,
           out + (((8 + rb8) * 128 + w * 32) * DM + ct * 128 + kq * 32) * 2, lane RTC);
      asm volatile("s_setprio 0" ::: "memory"); bar();
      asm volatile("global_load_dword %0, %1, %2 sc1" : "+v"(cd) : "v"(voff), "s"(code) : "memory");
      asm volatile(LD8X4 : OUT8(p4, 0) : "v"(sto * 2), "s"(ws + WS_ST2 + (blk * 4 + w) * 8192) : "memory"); stl<0>(lbase + voff + 0u + 0 + w * 6144, q1_6_0);
      stl<1024>(lbase + voff + 0u + 0 + w * 6144, q1_6_1); stl<2048>(lbase + voff + 0u + 0 + w * 6144, q1_6_2); stl<3072>(lbase + voff + 0u + 0 + w * 6144, q1_6_3);
      stl<4096>(lbase + voff + 0u + 0 + w * 6144, q1_6_4); stl<5120>(lbase + voff + 0u + 0 + w * 6144, q1_6_5); stl<24576>(lbase + voff + 0u + 0 + w * 6144, q1_7_0);
      stl<25600>(lbase + voff + 0u + 0 + w * 6144, q1_7_1); stl<26624>(lbase + voff + 0u + 0 + w * 6144, q1_7_2); stl<27648>(lbase + voff + 0u + 0 + w * 6144, q1_7_3);
      stl<28672>(lbase + voff + 0u + 0 + w * 6144, q1_7_4); stl<29696>(lbase + voff + 0u + 0 + w * 6144, q1_7_5); wait_vm<9>();
      signal(rs4, FL_ROW + K_X2 * 16384 + ((8 + rb8) * 4 + w) * 256 + (ct * 4 + kq) * 8, tok); wait_vm<1>();
      { PIN8(p4, 0); const f32x2 f = finish_stats2(p4); *(float*)(lds + L_FIN + (lane >> 5) * 1024 + 0 + w * 128 + (lane & 31) * 4) = lane < 32 ? f[0] : f[1]; }
      *(float*)(lds + L_FIN + 2048 + (w * 24 + ((lane & 31) < 24 ? (lane & 31) : 23)) * 4) = csq; lgkm_bar();
      await(rs, FL_ROW + K_X2 * 16384 + ((8 + blk) * 4 + w) * 256, lane & 31, tok); wait_vm<0>(); bar();
      asm volatile("global_load_dword %0, %1, %2 sc1" : "+v"(cd) : "v"(voff), "s"(code) : "memory");
      } { const int layer0 = 1; IDS();
      const char* gk = ws + WS_KV + layer * 8 * MB + h * 262144 + w * 4096;
      const unsigned fk = FL_KV + ((layer * 32 + h) * 4 + w) * 128;
      asm volatile(LD8X4 : OUT8(p4, 0) : "v"(sto * 2), "s"(ws + WS_ST2 + ((8 + blk) * 4 + w) * 8192) : "memory");
      asm volatile("s_getpc_b64 s[96:97]\n\t0:\n\ts_mov_b32 s100, .Lpgtop-0b\n\ts_cmp_eq_u32 %0, 1\n\ts_cselect_b32 s100, .Lpg3-0b, s100\n\ts_cmp_eq_u32 %0, 2\n\ts_cselect_b32 s100, .Lpg4-0b, s100\n\ts_cmp_eq_u32 %0, 3\n\ts_cselect_b32 s100, .Lpg5-0b, s100\n\ts_ashr_i32 s101, s100, 31\n\ts_add_u32 s96, s96, s100\n\ts_addc_u32 s97, s97, s101\n\ts_mov_b64 exec, 0\n\ts_swappc_b64 s[98:99], s[96:97]\n\ts_mov_b64 exec, -1" VISIT);
      asm volatile("s_getpc_b64 s[96:97]\n\t0:\n\ts_mov_b32 s100, .Lpg6-0b\n\ts_ashr_i32 s101, s100, 31\n\ts_add_u32 s96, s96, s100\n\ts_addc_u32 s97, s97, s101\n\ts_mov_b64 exec, 0\n\ts_swappc_b64 s[98:99], s[96:97]\n\ts_mov_b64 exec, -1" VISIT);
      bar();
      stl<0>(lbase + voff + 0u + 0 + w * 6144, q1_0_0); stl<1024>(lbase + voff + 0u + 0 + w * 6144, q1_0_1); stl<2048>(lbase + voff + 0u + 0 + w * 6144, q1_0_2);
      stl<3072>(lbase + voff + 0u + 0 + w * 6144, q1_0_3); stl<4096>(lbase + voff + 0u + 0 + w * 6144, q1_0_4); stl<5120>(lbase + voff + 0u + 0 + w * 6144, q1_0_5);
      stl<24576>(lbase + voff + 0u + 0 + w * 6144, q1_1_0); stl<25600>(lbase + voff + 0u + 0 + w * 6144, q1_1_1); stl<26624>(lbase + voff + 0u + 0 + w * 6144, q1_1_2);
      stl<27648>(lbase + voff + 0u + 0 + w * 6144, q1_1_3); stl<28672>(lbase + voff + 0u + 0 + w * 6144, q1_1_4); stl<29696>(lbase + voff + 0u + 0 + w * 6144, q1_1_5);
      wait_vm<0>();
      { PIN8(p4, 0); const f32x2 f = finish_stats2(p4); *(float*)(lds + L_FIN + (lane >> 5) * 1024 + 512 + w * 128 + (lane & 31) * 4) = lane < 32 ? f[0] : f[1]; }
      await(rs, fk, 0 + (lane & 7), tok); wait_vm<0>(); lgkm_bar();
      glds<4>(lbase + 49152 + w * 4096, gk + 0, voff); glds<4>(lbase + 65536 + w * 4096, gk + 16384, voff); glds<4>(lbase + 81920 + w * 4096, gk + 32768, voff);
      glds<4>(lbase + 98304 + w * 4096, gk + 49152, voff);
      const char* go = ws + WS_WO + tn * 131072;
      const char* g1 = ws + WS_W1 + t1 * 229376 + w * 7168; wait_vm<12>(); bar();
      glds<4>(lbase + 114688 + w * 4096, gk + 65536, voff); wait_vm<12>(); bar();
      glds<4>(lbase + 131072 + w * 4096, gk + 81920, voff); wait_vm<12>(); bar();
      glds<4>(lbase + 49152 + w * 4096, gk + 98304, voff); wait_vm<12>(); bar();
      poll(pq, rs4, fk, (8 + (lane & 7)) * 8); glds<4>(lbase + 65536 + w * 4096, gk + 114688, voff); wait_vm<13>(); bar();
      wait_vm<4>();
      { asm volatile("" : "+v"(pq)); if (__builtin_amdgcn_ballot_w64(pq != tok)) await(rs, fk, 8 + (lane & 7), tok); } glds<4>(lbase + 81920 + w * 4096, gk + 131072, voff);
      bar();
      asm volatile("global_load_dword %0, %1, %2 sc1" : "+v"(cd) : "v"(voff), "s"(code) : "memory"); glds<4>(lbase + 98304 + w * 4096, gk + 147456, voff);
      for (int i = 0; i < 2; ++i) st16(rs, WS_XCH + 0 * MB + (idx * 8 + (xcd & 1)) * 32768 + ((kq + 1) & 3) * 65536 + w * 2048 + 0, voff + i * 1024, u32x4{~0u, ~0u, ~0u, ~0u});
      ldf<0>(u1_0_0, g1 + 0, voff); ldf<1024>(u1_0_1, g1 + 0, voff); ldf<2048>(u1_0_2, g1 + 0, voff); ldf<3072>(u1_0_3, g1 + 0, voff); ldf<-4096>(u1_0_4, g1 + 8192, voff);
      bar();
      glds<4>(lbase + 114688 + w * 4096, gk + 163840, voff);
      for (int i = 0; i < 2; ++i) st16(rs, WS_XCH + 0 * MB + (idx * 8 + (xcd & 1)) * 32768 + ((kq + 2) & 3) * 65536 + w * 2048 + 8192, voff + i * 1024, u32x4{~0u, ~0u, ~0u, ~0u});
      ldf<-3072>(u1_0_5, g1 + 8192, voff); ldf<-2048>(u1_0_6, g1 + 8192, voff); ldf<-4096>(u1_1_0, g1 + 32768, voff); ldf<-3072>(u1_1_1, g1 + 32768, voff);
      ldf<-2048>(u1_1_2, g1 + 32768, voff); wait_vm<27>(); bar();
      glds<4>(lbase + 131072 + w * 4096, gk + 180224, voff);
      for (int i = 0; i < 2; ++i) st16(rs, WS_XCH + 0 * MB + (idx * 8 + (xcd & 1)) * 32768 + ((kq + 3) & 3) * 65536 + w * 2048 + 16384, voff + i * 1024, u32x4{~0u, ~0u, ~0u, ~0u});
      ldf<-1024>(u1_1_3, g1 + 32768, voff); ldf<0>(u1_1_4, g1 + 32768, voff); ldf<1024>(u1_1_5, g1 + 32768, voff); ldf<2048>(u1_1_6, g1 + 32768, voff);
      ldf<0>(u1_2_0, g1 + 57344, voff); wait_vm<34>();
      asm volatile("s_getpc_b64 s[96:97]\n\t0:\n\ts_mov_b32 s100, .Lpg7-0b\n\ts_cmp_eq_u32 %0, 1\n\ts_cselect_b32 s100, .Lpg8-0b, s100\n\ts_cmp_eq_u32 %0, 2\n\ts_cselect_b32 s100, .Lpg9-0b, s100\n\ts_ashr_i32 s101, s100, 31\n\ts_add_u32 s96, s96, s100\n\ts_addc_u32 s97, s97, s101\n\ts_mov_b64 exec, 0\n\ts_swappc_b64 s[98:99], s[96:97]\n\ts_mov_b64 exec, -1" VISIT);
      bar();
      glds<4>(lbase + 49152 + w * 4096, gk + 196608, voff);
      for (int i = 0; i < 2; ++i) st16(rs, WS_XCH + 8 * MB + (idx * 8 + (xcd & 1)) * 32768 + ((kq + 1) & 3) * 65536 + w * 2048 + 0, voff + i * 1024, u32x4{~0u, ~0u, ~0u, ~0u});
      ldf<1024>(u1_2_1, g1 + 57344, voff); ldf<2048>(u1_2_2, g1 + 57344, voff); ldf<3072>(u1_2_3, g1 + 57344, voff); ldf<-4096>(u1_2_4, g1 + 65536, voff);
      ldf<-3072>(u1_2_5, g1 + 65536, voff); wait_vm<40>(); bar();
      glds<4>(lbase + 65536 + w * 4096, gk + 212992, voff);
      for (int i = 0; i < 2; ++i) st16(rs, WS_XCH + 8 * MB + (idx * 8 + (xcd & 1)) * 32768 + ((kq + 2) & 3) * 65536 + w * 2048 + 8192, voff + i * 1024, u32x4{~0u, ~0u, ~0u, ~0u});
      ldf<-2048>(u1_2_6, g1 + 65536, voff); ldf<-4096>(u1_3_0, g1 + 90112, voff); ldf<-3072>(u1_3_1, g1 + 90112, voff); ldf<-2048>(u1_3_2, g1 + 90112, voff);
      ldf<-1024>(u1_3_3, g1 + 90112, voff); wait_vm<40>(); bar();
      asm volatile("global_load_dword %0, %1, %2 sc1" : "+v"(cd) : "v"(voff), "s"(code) : "memory"); glds<4>(lbase + 81920 + w * 4096, gk + 229376, voff);
      for (int i = 0; i < 2; ++i) st16(rs, WS_XCH + 8 * MB + (idx * 8 + (xcd & 1)) * 32768 + ((kq + 3) & 3) * 65536 + w * 2048 + 16384, voff + i * 1024, u32x4{~0u, ~0u, ~0u, ~0u});
      ldf<0>(u1_3_4, g1 + 90112, voff); ldf<1024>(u1_3_5, g1 + 90112, voff); ldf<2048>(u1_3_6, g1 + 90112, voff); ldf<0>(u1_4_0, g1 + 114688, voff);
      ldf<1024>(u1_4_1, g1 + 114688, voff); wait_vm<41>(); bar();
      glds<4>(lbase + 98304 + w * 4096, gk + 245760, voff); ldf<2048>(u1_4_2, g1 + 114688, voff); ldf<3072>(u1_4_3, g1 + 114688, voff); ldf<-4096>(u1_4_4, g1 + 122880, voff);
      ldf<-3072>(u1_4_5, g1 + 122880, voff); ldf<-2048>(u1_4_6, g1 + 122880, voff); wait_vm<39>(); bar();
      glds<4>(lbase + 114688 + w * 4096, gk + 0, voff); glds<4>(lbase + 0 + w * 4096, go + w * 4096 + 0, voff); ldf<-4096>(u1_5_0, g1 + 147456, voff);
      ldf<-3072>(u1_5_1, g1 + 147456, voff); ldf<-2048>(u1_5_2, g1 + 147456, voff); ldf<-1024>(u1_5_3, g1 + 147456, voff); ldf<0>(u1_5_4, g1 + 147456, voff); wait_vm<41>();
      bar();
      glds<4>(lbase + 131072 + w * 4096, gk + 16384, voff); glds<4>(lbase + 16384 + w * 4096, go + w * 4096 + 16384, voff); ldf<1024>(u1_5_5, g1 + 147456, voff);
      ldf<2048>(u1_5_6, g1 + 147456, voff); ldf<0>(u1_6_0, g1 + 172032, voff); ldf<1024>(u1_6_1, g1 + 172032, voff); wait_vm<41>();
      asm volatile("s_getpc_b64 s[96:97]\n\t0:\n\ts_mov_b32 s100, .Lpg10-0b\n\ts_cmp_eq_u32 %0, 1\n\ts_cselect_b32 s100, .Lpg11-0b, s100\n\ts_cmp_eq_u32 %0, 2\n\ts_cselect_b32 s100, .Lpg12-0b, s100\n\ts_cmp_eq_u32 %0, 3\n\ts_cselect_b32 s100, .Lpg13-0b, s100\n\ts_ashr_i32 s101, s100, 31\n\ts_add_u32 s96, s96, s100\n\ts_addc_u32 s97, s97, s101\n\ts_mov_b64 exec, 0\n\ts_swappc_b64 s[98:99], s[96:97]\n\ts_mov_b64 exec, -1" VISIT);
      bar();
      glds<4>(lbase + 49152 + w * 4096, gk + 32768, voff); glds<4>(lbase + 32768 + w * 4096, go + w * 4096 + 32768, voff); ldf<2048>(u1_6_2, g1 + 172032, voff);
      ldf<3072>(u1_6_3, g1 + 172032, voff); ldf<-4096>(u1_6_4, g1 + 180224, voff); ldf<-3072>(u1_6_5, g1 + 180224, voff); wait_vm<42>(); bar();
      asm volatile("global_load_dword %0, %1, %2 sc1" : "+v"(cd) : "v"(voff), "s"(code) : "memory"); glds<4>(lbase + 65536 + w * 4096, gk + 49152, voff);
      glds<4>(lbase + 147456 + w * 4096, go + w * 4096 + 49152, voff); ldf<-2048>(u1_6_6, g1 + 180224, voff); ldf<-4096>(u1_7_0, g1 + 204800, voff);
      ldf<-3072>(u1_7_1, g1 + 204800, voff); ldf<-2048>(u1_7_2, g1 + 204800, voff); wait_vm<46>(); bar();
      glds<4>(lbase + 81920 + w * 4096, go + w * 4096 + 65536, voff); ldf<-1024>(u1_7_3, g1 + 204800, voff); ldf<0>(u1_7_4, g1 + 204800, voff);
      ldf<1024>(u1_7_5, g1 + 204800, voff); ldf<2048>(u1_7_6, g1 + 204800, voff); wait_vm<41>(); bar();
      glds<4>(lbase + 98304 + w * 4096, go + w * 4096 + 81920, voff); wait_vm<33>(); bar();
      glds<4>(lbase + 114688 + w * 4096, go + w * 4096 + 98304, voff); wait_vm<24>(); poll(pq, rs4, flr + K_O * 16384 + (0 + rb16 * 2 + (w >> 1)) * 256, (lane & 31) * 8);
      wait_vm<0>();
      { asm volatile("" : "+v"(pq)); if (__builtin_amdgcn_ballot_w64(pq != tok)) await(rs, flr + K_O * 16384 + (0 + rb16 * 2 + (w >> 1)) * 256, lane & 31, tok); }
      bar();
      glds<4>(lbase + 131072 + w * 4096, go + w * 4096 + 114688, voff); bar();
      asm volatile("global_load_dword %0, %1, %2 sc1" : "+v"(cd) : "v"(voff), "s"(code) : "memory"); bar();
      wait_vm<1>(); bar();
      stl<53248>(lbase + voff + 0u + 0 + w * 7168, u1_0_0); stl<54272>(lbase + voff + 0u + 0 + w * 7168, u1_0_1); stl<55296>(lbase + voff + 0u + 0 + w * 7168, u1_0_2);
      stl<56320>(lbase + voff + 0u + 0 + w * 7168, u1_0_3); stl<57344>(lbase + voff + 0u + 0 + w * 7168, u1_0_4); stl<58368>(lbase + voff + 0u + 0 + w * 7168, u1_0_5);
      stl<59392>(lbase + voff + 0u + 0 + w * 7168, u1_0_6); poll(pq, rs4, flr + K_O * 16384 + (32 + rb16 * 2 + (w >> 1)) * 256, (lane & 31) * 8); wait_vm<0>();
      { asm volatile("" : "+v"(pq)); if (__builtin_amdgcn_ballot_w64(pq != tok)) await(rs, flr + K_O * 16384 + (32 + rb16 * 2 + (w >> 1)) * 256, lane & 31, tok); }
      lgkm_bar();
      __builtin_amdgcn_s_sleep(30); await(rs, flr + K_X1 * 16384 + ((0 + rb8) * 4 + w) * 256, lane & 31, tok); wait_vm<0>(); bar();
      } { const int layer0 = 1; IDS();
      const char* g2 = ws + WS_W2 + (ct * 112 + kq * 28) * 8192 + w * 4096;
      const int cl = w * 28 + ((lane & 31) < 28 ? (lane & 31) : 27); stl<0>(lbase + voff + 0u + 0 + w * 7168, u1_1_0); stl<1024>(lbase + voff + 0u + 0 + w * 7168, u1_1_1);
      stl<2048>(lbase + voff + 0u + 0 + w * 7168, u1_1_2); stl<3072>(lbase + voff + 0u + 0 + w * 7168, u1_1_3); stl<4096>(lbase + voff + 0u + 0 + w * 7168, u1_1_4);
      stl<5120>(lbase + voff + 0u + 0 + w * 7168, u1_1_5); stl<6144>(lbase + voff + 0u + 0 + w * 7168, u1_1_6); stl<28672>(lbase + voff + 0u + 0 + w * 6144, u1_2_0);
      stl<29696>(lbase + voff + 0u + 0 + w * 6144, u1_2_1); stl<30720>(lbase + voff + 0u + 0 + w * 6144, u1_2_2); stl<31744>(lbase + voff + 0u + 0 + w * 6144, u1_2_3);
      stl<32768>(lbase + voff + 0u + 0 + w * 6144, u1_2_4); stl<33792>(lbase + voff + 0u + 0 + w * 6144, u1_2_5); stl<8192>(lbase + voff + 131072u + 0 + w * 1024, u1_2_6);
      lgkm_bar();
      asm volatile("global_load_dword %0, %1, %2 sc1" : "+v"(cd) : "v"(voff), "s"(code) : "memory"); stl<16384>(lbase + voff + 65536u + 0 + w * 7168, u1_3_0);
      stl<17408>(lbase + voff + 65536u + 0 + w * 7168, u1_3_1); stl<18432>(lbase + voff + 65536u + 0 + w * 7168, u1_3_2);
      stl<19456>(lbase + voff + 65536u + 0 + w * 7168, u1_3_3); stl<20480>(lbase + voff + 65536u + 0 + w * 7168, u1_3_4);
      stl<21504>(lbase + voff + 65536u + 0 + w * 7168, u1_3_5); stl<22528>(lbase + voff + 65536u + 0 + w * 7168, u1_3_6);
      ldf<0>(p4[0], ws + WS_ST + 786432 + ((0 + rb8) * 4 + w) * 4096, voff); ldf<1024>(p4[1], ws + WS_ST + 786432 + ((0 + rb8) * 4 + w) * 4096, voff);
      ldf<2048>(p4[2], ws + WS_ST + 786432 + ((0 + rb8) * 4 + w) * 4096, voff); ldf<3072>(p4[3], ws + WS_ST + 786432 + ((0 + rb8) * 4 + w) * 4096, voff);
      stl<45056>(lbase + voff + 65536u + 0 + w * 7168, u1_4_0); stl<46080>(lbase + voff + 65536u + 0 + w * 7168, u1_4_1);
      stl<47104>(lbase + voff + 65536u + 0 + w * 7168, u1_4_2); stl<48128>(lbase + voff + 65536u + 0 + w * 7168, u1_4_3);
      stl<49152>(lbase + voff + 65536u + 0 + w * 7168, u1_4_4); stl<50176>(lbase + voff + 65536u + 0 + w * 7168, u1_4_5);
      stl<51200>(lbase + voff + 65536u + 0 + w * 7168, u1_4_6); wait_vm<0>();
      { PIN4(p4); const f32x2 f = finish_stats<true>(p4); *(float*)(lds + L_FIN + (fq & 1) * 1024 + 0 + w * 128 + lr * 8 + (lane >> 5) * 4) = (fq & 1) ? f[1] : f[0]; }
      *(float*)(lds + L_FIN + 2048 + (lane >> 5) * 512 + cl * 4) = csu; lgkm_bar();
      stl<53248>(lbase + voff + 0u + 0 + w * 7168, u1_5_0); stl<54272>(lbase + voff + 0u + 0 + w * 7168, u1_5_1); stl<55296>(lbase + voff + 0u + 0 + w * 7168, u1_5_2);
      stl<56320>(lbase + voff + 0u + 0 + w * 7168, u1_5_3); stl<57344>(lbase + voff + 0u + 0 + w * 7168, u1_5_4); stl<58368>(lbase + voff + 0u + 0 + w * 7168, u1_5_5);
      stl<59392>(lbase + voff + 0u + 0 + w * 7168, u1_5_6); stl<0>(lbase + voff + 0u + 0 + w * 7168, u1_6_0); stl<1024>(lbase + voff + 0u + 0 + w * 7168, u1_6_1);
      stl<2048>(lbase + voff + 0u + 0 + w * 7168, u1_6_2); stl<3072>(lbase + voff + 0u + 0 + w * 7168, u1_6_3); stl<4096>(lbase + voff + 0u + 0 + w * 7168, u1_6_4);
      stl<5120>(lbase + voff + 0u + 0 + w * 7168, u1_6_5); stl<6144>(lbase + voff + 0u + 0 + w * 7168, u1_6_6); ldf<0>(v1_0_0, g2 + 0, voff); __builtin_amdgcn_s_sleep(2);
      ldf<1024>(v1_0_1, g2 + 0, voff); __builtin_amdgcn_s_sleep(2); ldf<2048>(v1_0_2, g2 + 0, voff); __builtin_amdgcn_s_sleep(2); ldf<3072>(v1_0_3, g2 + 0, voff);
      __builtin_amdgcn_s_sleep(2); lgkm_bar();
      stl<28672>(lbase + voff + 0u + 0 + w * 6144, u1_7_0); stl<29696>(lbase + voff + 0u + 0 + w * 6144, u1_7_1); stl<30720>(lbase + voff + 0u + 0 + w * 6144, u1_7_2);
      stl<31744>(lbase + voff + 0u + 0 + w * 6144, u1_7_3); stl<32768>(lbase + voff + 0u + 0 + w * 6144, u1_7_4); stl<33792>(lbase + voff + 0u + 0 + w * 6144, u1_7_5);
      stl<8192>(lbase + voff + 131072u + 0 + w * 1024, u1_7_6); await(rs, flr + K_X1 * 16384 + ((8 + rb8) * 4 + w) * 256, lane & 31, tok); wait_vm<0>(); lgkm_bar();
      ldf<0>(v1_1_0, g2 + 16384, voff); __builtin_amdgcn_s_sleep(3); ldf<1024>(v1_1_1, g2 + 16384, voff); __builtin_amdgcn_s_sleep(3); ldf<2048>(v1_1_2, g2 + 16384, voff);
      __builtin_amdgcn_s_sleep(3); ldf<3072>(v1_1_3, g2 + 16384, voff); __builtin_amdgcn_s_sleep(3); ldf<0>(v1_2_0, g2 + 32768, voff); __builtin_amdgcn_s_sleep(3);
      ldf<1024>(v1_2_1, g2 + 32768, voff); __builtin_amdgcn_s_sleep(3); ldf<2048>(v1_2_2, g2 + 32768, voff); __builtin_amdgcn_s_sleep(3); ldf<3072>(v1_2_3, g2 + 32768, voff);
      __builtin_amdgcn_s_sleep(3); bar();
      asm volatile("global_load_dword %0, %1, %2 sc1" : "+v"(cd) : "v"(voff), "s"(code) : "memory"); ldf<0>(v1_3_0, g2 + 49152, voff); ldf<1024>(v1_3_1, g2 + 49152, voff);
      ldf<2048>(v1_3_2, g2 + 49152, voff); ldf<3072>(v1_3_3, g2 + 49152, voff); ldf<0>(v1_4_0, g2 + 65536, voff); ldf<1024>(v1_4_1, g2 + 65536, voff);
      ldf<2048>(v1_4_2, g2 + 65536, voff); ldf<3072>(v1_4_3, g2 + 65536, voff); ldf<0>(v1_9_0, g2 + 147456, voff); ldf<1024>(v1_9_1, g2 + 147456, voff);
      ldf<2048>(v1_9_2, g2 + 147456, voff); ldf<3072>(v1_9_3, g2 + 147456, voff); ldf<0>(v1_10_0, g2 + 163840, voff); ldf<1024>(v1_10_1, g2 + 163840, voff);
      ldf<2048>(v1_10_2, g2 + 163840, voff); ldf<3072>(v1_10_3, g2 + 163840, voff); ldf<0>(p4[0], ws + WS_ST + 786432 + ((8 + rb8) * 4 + w) * 4096, voff);
      ldf<1024>(p4[1], ws + WS_ST + 786432 + ((8 + rb8) * 4 + w) * 4096, voff); ldf<2048>(p4[2], ws + WS_ST + 786432 + ((8 + rb8) * 4 + w) * 4096, voff);
      ldf<3072>(p4[3], ws + WS_ST + 786432 + ((8 + rb8) * 4 + w) * 4096, voff); wait_vm<0>();
      { PIN4(p4); const f32x2 f = finish_stats<true>(p4); *(float*)(lds + L_FIN + (fq & 1) * 1024 + 512 + w * 128 + lr * 8 + (lane >> 5) * 4) = (fq & 1) ? f[1] : f[0]; }
      lgkm_bar();
      stl<53248>(lbase + voff + 0u + 0 + w * 7168, u1_0_0); stl<54272>(lbase + voff + 0u + 0 + w * 7168, u1_0_1); stl<55296>(lbase + voff + 0u + 0 + w * 7168, u1_0_2);
      stl<56320>(lbase + voff + 0u + 0 + w * 7168, u1_0_3); stl<57344>(lbase + voff + 0u + 0 + w * 7168, u1_0_4); stl<58368>(lbase + voff + 0u + 0 + w * 7168, u1_0_5);
      stl<59392>(lbase + voff + 0u + 0 + w * 7168, u1_0_6); stl<0>(lbase + voff + 0u + 0 + w * 7168, u1_1_0); stl<1024>(lbase + voff + 0u + 0 + w * 7168, u1_1_1);
      stl<2048>(lbase + voff + 0u + 0 + w * 7168, u1_1_2); stl<3072>(lbase + voff + 0u + 0 + w * 7168, u1_1_3); stl<4096>(lbase + voff + 0u + 0 + w * 7168, u1_1_4);
      stl<5120>(lbase + voff + 0u + 0 + w * 7168, u1_1_5); stl<6144>(lbase + voff + 0u + 0 + w * 7168, u1_1_6); ldf<0>(v1_11_0, g2 + 180224, voff); __builtin_amdgcn_s_sleep(2);
      ldf<1024>(v1_11_1, g2 + 180224, voff); __builtin_amdgcn_s_sleep(2); ldf<2048>(v1_11_2, g2 + 180224, voff); __builtin_amdgcn_s_sleep(2);
      ldf<3072>(v1_11_3, g2 + 180224, voff); __builtin_amdgcn_s_sleep(2); lgkm_bar();
      stl<28672>(lbase + voff + 0u + 0 + w * 6144, u1_2_0); stl<29696>(lbase + voff + 0u + 0 + w * 6144, u1_2_1); stl<30720>(lbase + voff + 0u + 0 + w * 6144, u1_2_2);
      stl<31744>(lbase + voff + 0u + 0 + w * 6144, u1_2_3); stl<32768>(lbase + voff + 0u + 0 + w * 6144, u1_2_4); stl<33792>(lbase + voff + 0u + 0 + w * 6144, u1_2_5);
      stl<8192>(lbase + voff + 131072u + 0 + w * 1024, u1_2_6); await(rs, flr + K_H * 16384 + ((0 + rb8) * 4 + w) * 256, kq * 8 + (lane & 7), tok); wait_vm<0>();
      lgkm_bar();
      ldf<0>(v1_12_0, g2 + 196608, voff); __builtin_amdgcn_s_sleep(2); ldf<1024>(v1_12_1, g2 + 196608, voff); __builtin_amdgcn_s_sleep(2); ldf<2048>(v1_12_2, g2 + 196608, voff);
      __builtin_amdgcn_s_sleep(2); ldf<3072>(v1_12_3, g2 + 196608, voff); __builtin_amdgcn_s_sleep(2); ldf<0>(v1_13_0, g2 + 212992, voff); __builtin_amdgcn_s_sleep(2);
      ldf<1024>(v1_13_1, g2 + 212992, voff); __builtin_amdgcn_s_sleep(2); ldf<2048>(v1_13_2, g2 + 212992, voff); __builtin_amdgcn_s_sleep(2);
      ldf<3072>(v1_13_3, g2 + 212992, voff); __builtin_amdgcn_s_sleep(2);
      asm volatile("s_getpc_b64 s[96:97]\n\t0:\n\ts_mov_b32 s100, .Lpg14-0b\n\ts_cmp_eq_u32 %0, 1\n\ts_cselect_b32 s100, .Lpg15-0b, s100\n\ts_cmp_eq_u32 %0, 2\n\ts_cselect_b32 s100, .Lpg16-0b, s100\n\ts_ashr_i32 s101, s100, 31\n\ts_add_u32 s96, s96, s100\n\ts_addc_u32 s97, s97, s101\n\ts_mov_b64 exec, 0\n\ts_swappc_b64 s[98:99], s[96:97]\n\ts_mov_b64 exec, -1" VISIT);
      bar();
      asm volatile("global_load_dword %0, %1, %2 sc1" : "+v"(cd) : "v"(voff), "s"(code) : "memory"); glds<4>(lbase + 0 + w * 4096, g2 + 81920, voff);
      stl<0>(lbase + voff + 65536u + 0 + w * 4096, v1_0_0); stl<1024>(lbase + voff + 65536u + 0 + w * 4096, v1_0_1); stl<2048>(lbase + voff + 65536u + 0 + w * 4096, v1_0_2);
      stl<3072>(lbase + voff + 65536u + 0 + w * 4096, v1_0_3); glds<4>(lbase + 16384 + w * 4096, g2 + 98304, voff); stl<16384>(lbase + voff + 65536u + 0 + w * 4096, v1_1_0);
      stl<17408>(lbase + voff + 65536u + 0 + w * 4096, v1_1_1); stl<18432>(lbase + voff + 65536u + 0 + w * 4096, v1_1_2);
      stl<19456>(lbase + voff + 65536u + 0 + w * 4096, v1_1_3); glds<4>(lbase + 32768 + w * 4096, g2 + 114688, voff); stl<32768>(lbase + voff + 65536u + 0 + w * 4096, v1_2_0);
      stl<33792>(lbase + voff + 65536u + 0 + w * 4096, v1_2_1); stl<34816>(lbase + voff + 65536u + 0 + w * 4096, v1_2_2);
      stl<35840>(lbase + voff + 65536u + 0 + w * 4096, v1_2_3); glds<4>(lbase + 49152 + w * 4096, g2 + 131072, voff); stl<49152>(lbase + voff + 65536u + 0 + w * 4096, v1_3_0);
      stl<50176>(lbase + voff + 65536u + 0 + w * 4096, v1_3_1); stl<51200>(lbase + voff + 65536u + 0 + w * 4096, v1_3_2);
      stl<52224>(lbase + voff + 65536u + 0 + w * 4096, v1_3_3); stl<0>(lbase + voff + 131072u + 0 + w * 4096, v1_4_0); stl<1024>(lbase + voff + 131072u + 0 + w * 4096, v1_4_1);
      stl<2048>(lbase + voff + 131072u + 0 + w * 4096, v1_4_2); stl<3072>(lbase + voff + 131072u + 0 + w * 4096, v1_4_3);
      } { const int layer0 = 1; IDS(); lgkm_bar();
      wait_vm<0>(); bar();
      stl<0>(lbase + voff + 65536u + 0 + w * 4096, v1_9_0); stl<1024>(lbase + voff + 65536u + 0 + w * 4096, v1_9_1); stl<2048>(lbase + voff + 65536u + 0 + w * 4096, v1_9_2);
      stl<3072>(lbase + voff + 65536u + 0 + w * 4096, v1_9_3); stl<16384>(lbase + voff + 65536u + 0 + w * 4096, v1_10_0);
      stl<17408>(lbase + voff + 65536u + 0 + w * 4096, v1_10_1); stl<18432>(lbase + voff + 65536u + 0 + w * 4096, v1_10_2);
      stl<19456>(lbase + voff + 65536u + 0 + w * 4096, v1_10_3); stl<32768>(lbase + voff + 65536u + 0 + w * 4096, v1_11_0);
      stl<33792>(lbase + voff + 65536u + 0 + w * 4096, v1_11_1); stl<34816>(lbase + voff + 65536u + 0 + w * 4096, v1_11_2);
      stl<35840>(lbase + voff + 65536u + 0 + w * 4096, v1_11_3); poll(pq, rs4, flr + K_H * 16384 + ((8 + rb8) * 4 + w) * 256, (kq * 8 + (lane & 7)) * 8); lgkm_bar();
      stl<49152>(lbase + voff + 65536u + 0 + w * 4096, v1_12_0); stl<50176>(lbase + voff + 65536u + 0 + w * 4096, v1_12_1);
      stl<51200>(lbase + voff + 65536u + 0 + w * 4096, v1_12_2); stl<52224>(lbase + voff + 65536u + 0 + w * 4096, v1_12_3);
      stl<0>(lbase + voff + 131072u + 0 + w * 4096, v1_13_0); stl<1024>(lbase + voff + 131072u + 0 + w * 4096, v1_13_1);
      stl<2048>(lbase + voff + 131072u + 0 + w * 4096, v1_13_2); stl<3072>(lbase + voff + 131072u + 0 + w * 4096, v1_13_3); wait_vm<0>();
      { asm volatile("" : "+v"(pq)); if (__builtin_amdgcn_ballot_w64(pq != tok)) await(rs, flr + K_H * 16384 + ((8 + rb8) * 4 + w) * 256, kq * 8 + (lane & 7), tok); }
      lgkm_bar();
      asm volatile("global_load_dword %0, %1, %2 sc1" : "+v"(cd) : "v"(voff), "s"(code) : "memory");
      const unsigned xt0 = WS_X + 8 * MB + ((0 + rb8) * 32 + ct * 4 + kq) * 8192 + w * 2048; ldf<0>(pr[0], ws + 4 * MB + xt0 + 0, voff);
      ldf<1024>(pr[1], ws + 4 * MB + xt0 + 0, voff); ldf<0>(pr[2], b2 + 0, (ct * 128 + kq * 32 + fq * 8) * 2); wait_vm<0>(); fin2_pre(ps, pr); bar();
      ldl<0>(pp[0], pb + L_OWN + w * 4096); ldl<1024>(pp[1], pb + L_OWN + w * 4096); ldl<2048>(pp[2], pb + L_OWN + w * 4096); ldl<3072>(pp[3], pb + L_OWN + w * 4096);
      const char* gi0 = ws + WS_XCH + 0 * MB + bid * 32768 + w * 2048; ldf<0>(pi[0], gi0 + 0, voff); ldf<1024>(pi[1], gi0 + 0, voff); ldf<0>(pi[2], gi0 + 8192, voff);
      ldf<1024>(pi[3], gi0 + 8192, voff); ldf<0>(pi[4], gi0 + 16384, voff); ldf<1024>(pi[5], gi0 + 16384, voff); stl<0>(lbase + voff + 65536u + 0 + w * 4096, v1_0_0);
      stl<1024>(lbase + voff + 65536u + 0 + w * 4096, v1_0_1); stl<2048>(lbase + voff + 65536u + 0 + w * 4096, v1_0_2); stl<3072>(lbase + voff + 65536u + 0 + w * 4096, v1_0_3);
      fin2_add(pp, ps); sel = make_sel(); wait_vm<0>();
      asm volatile("s_setprio 3" ::: "memory"); fin2<2>(pp, pi, sel, gi0, rs, 8 * MB + xt0, WS_ST2 + (((0 + rb8) * 4 + w) * 16 + ct * 2 + (kq >> 1)) * 512 + (kq & 1) * 8,
           out + (((0 + rb8) * 128 + w * 32) * DM + ct * 128 + kq * 32) * 2, lane RTC);
      asm volatile("s_setprio 0" ::: "memory"); bar();
      const unsigned xt1 = WS_X + 8 * MB + ((8 + rb8) * 32 + ct * 4 + kq) * 8192 + w * 2048; ldf<0>(pr[0], ws + 4 * MB + xt1 + 0, voff);
      ldf<1024>(pr[1], ws + 4 * MB + xt1 + 0, voff); ldf<0>(pr[2], b2 + 0, (ct * 128 + kq * 32 + fq * 8) * 2); stl<16384>(lbase + voff + 65536u + 0 + w * 4096, v1_1_0);
      stl<17408>(lbase + voff + 65536u + 0 + w * 4096, v1_1_1); stl<18432>(lbase + voff + 65536u + 0 + w * 4096, v1_1_2);
      stl<19456>(lbase + voff + 65536u + 0 + w * 4096, v1_1_3); stl<32768>(lbase + voff + 65536u + 0 + w * 4096, v1_2_0);
      stl<33792>(lbase + voff + 65536u + 0 + w * 4096, v1_2_1); stl<34816>(lbase + voff + 65536u + 0 + w * 4096, v1_2_2);
      stl<35840>(lbase + voff + 65536u + 0 + w * 4096, v1_2_3); stl<49152>(lbase + voff + 65536u + 0 + w * 4096, v1_3_0);
      stl<50176>(lbase + voff + 65536u + 0 + w * 4096, v1_3_1); stl<51200>(lbase + voff + 65536u + 0 + w * 4096, v1_3_2);
      stl<52224>(lbase + voff + 65536u + 0 + w * 4096, v1_3_3); stl<0>(lbase + voff + 131072u + 0 + w * 4096, v1_4_0); stl<1024>(lbase + voff + 131072u + 0 + w * 4096, v1_4_1);
      stl<2048>(lbase + voff + 131072u + 0 + w * 4096, v1_4_2); stl<3072>(lbase + voff + 131072u + 0 + w * 4096, v1_4_3); lgkm_bar();
      wait_vm<0>(); fin2_pre(ps, pr); bar();
      asm volatile("global_load_dword %0, %1, %2 sc1" : "+v"(cd) : "v"(voff), "s"(code) : "memory"); bar();
      bar();
      ldl<0>(pp[0], pb + L_OWN + w * 4096); ldl<1024>(pp[1], pb + L_OWN + w * 4096); ldl<2048>(pp[2], pb + L_OWN + w * 4096); ldl<3072>(pp[3], pb + L_OWN + w * 4096);
      __builtin_amdgcn_s_sleep(8);
      const char* gi1 = ws + WS_XCH + 8 * MB + bid * 32768 + w * 2048; ldf<0>(pi[0], gi1 + 0, voff); ldf<1024>(pi[1], gi1 + 0, voff); ldf<0>(pi[2], gi1 + 8192, voff);
      ldf<1024>(pi[3], gi1 + 8192, voff); ldf<0>(pi[4], gi1 + 16384, voff); ldf<1024>(pi[5], gi1 + 16384, voff); fin2_add(pp, ps); sel = make_sel(); wait_vm<0>();
      asm volatile("s_setprio 3" ::: "memory"); fin2<3>(pp, pi, sel, gi1, rs, 8 * MB + xt1, WS_ST2 + (((8 + rb8) * 4 + w) * 16 + ct * 2 + (kq >> 1)) * 512 + (kq & 1) * 8,
           out + (((8 + rb8) * 128 + w * 32) * DM + ct * 128 + kq * 32) * 2, lane RTC);
      asm volatile("s_setprio 0" ::: "memory");
      if (bid == 0 && w == 0) signal(rs4, WS_EPOCH, tok);
      wait_vm<0>();
      asm volatile("" : "+v"(cd));
      }
    LEND();
    asm volatile("s_endpgm\n.Lpgret:\n\ts_setpc_b64 s[98:99]\n.Lcode_end:");
    return;
  }

  __builtin_amdgcn_s_setprio(1);
  u32x32 r0, r1, r2;
  f32x16 acc[3];
  f32x16 nega, oa;
  bf16x8 qa0, qa1, qb0, qb1;
  float nma, nmb;
  const rsrc_t rt = __builtin_amdgcn_make_buffer_rsrc(ws0, 16, WS_END, 0x00800000);
  Ln n;
  unsigned sa, sn, xq, kvo, fkv, od, fo, os, xr, so, fx, xu, hd, hd7, fh, hs, sx0, sx1, sx2;
  {
    const int layer0 = 0;
    IDS();
    const unsigned fin = lbase + L_FIN + wid * 128, tile = lbase + L_BUF, fr = rb8 * 262144 + ((ct & 1) * 4 + wid) * 1024;
    n = Ln{pb, pb + 65536, pb + 131072, fin + lr * 4, fin + fq * 16, lbase + L_FIN + fq * 16, tile + k_lane(lane), tile + v_lane(lane), (unsigned)wid * 128, (unsigned)wid * 4096 + L_OWN};
    LN();
    asm volatile("" : "+s"(n.w128), "+s"(n.own));
    xq = WS_X + blk * 262144 + wid * 2048;
    kvo = WS_KV + h * 262144 + (blk * 4 + wid) * 4096;
    fkv = FL_KV + (h * 4 + wid) * 128 + blk * 8;
    od = WS_O + ((blk * 32 + h) * 8 + wid * 2) * 1024 - tile;
    fo = FL_ROW + K_O * 16384 + (blk * 4 + wid) * 256 + h * 8;
    os = WS_O + fr;
    xr = WS_X + fr + tn * 16384;
    so = WS_ST + 262144 + ((rb16 * 2 + (wid >> 1)) * 16 + tn) * 256 + (wid & 1) * 128 - 2 * fin;
    fx = FL_ROW + K_X1 * 16384 + (rb16 * 2 + (wid >> 1)) * 256 + ((wid & 1) * 16 + tn) * 8;
    xu = WS_X + 4 * MB + rb8 * 262144 + wid * 2048;
    hd = WS_H + ((rb8 * 112 + (t1 >> 1) * 7) * 8 + wid * 2) * 1024;
    hd7 = hd + 3 * 8192 + (t1 & 1) * 512 - tile;
    hd += (t1 & 1) * 32768;
    fh = FL_ROW + K_H * 16384 + (rb8 * 4 + wid) * 256 + t1 * 8;
    hs = WS_H + (rb8 * 112 + kq * 28) * 8192 + wid * 2048;
    const unsigned sxb = WS_XCH + (idx * 8 + (xcd & 1)) * 32768 + wid * 2048;
    sx0 = sxb + ((kq + 1) & 3) * 65536; sx1 = sxb + ((kq + 2) & 3) * 65536 + 8192; sx2 = sxb + ((kq + 3) & 3) * 65536 + 16384;
    asm volatile("" : "+s"(xq), "+s"(kvo), "+s"(fkv), "+s"(od), "+s"(fo), "+s"(os), "+s"(xr), "+s"(so));
    asm volatile("" : "+s"(fx), "+s"(xu), "+s"(hd), "+s"(hd7), "+s"(fh), "+s"(hs), "+s"(sx0), "+s"(sx1), "+s"(sx2));
    __builtin_amdgcn_s_sleep(4);
    idle(t0, 430u, lane);
    STAMP(23);
    await(rs, 0, lane < 32 ? FL_X0 / 8 + (blk * 4 + wid) * 32 + lane : FL_WQ / 8 + ((h >> 1) * 32 + ((lane >> 2) & 7) * 4 + wid) * 4 + min(lane & 3, 2), tok);
    wait_vm<0>();
    STAMP(24);
    bar();
#pragma unroll
    for (int i = 0; i < 1; ++i) glds<6>(lbase + i * 24576 + wid * 6144, ws + WS_WQKV + h * 196608 + i * 24576 + wid * 6144, voff);
    STAMP(0);
  }
  {
    sa = xq; sn = xq;
      sn += 2228224u;
      asm volatile(
        "; BLK qa\n\t.Lmk_qa_s:\n\tbuffer_load_dwordx4 v[152:155], off, %[rt], %[sa] offset:0 sc1\n\t"
        "buffer_load_dwordx4 v[156:159], off, %[rt], %[sa] offset:1024 sc1\n\ts_add_u32 %[sa], %[sa], 0x2000\n\t"
        "buffer_load_dwordx4 v[160:163], off, %[rt], %[sa] offset:0 sc1\n\tbuffer_load_dwordx4 v[164:167], off, %[rt], %[sa] offset:1024 sc1\n\t"
        "s_add_u32 %[sa], %[sa], 0x2000\n\tbuffer_load_dwordx4 v[168:171], off, %[rt], %[sa] offset:0 sc1\n\t"
        "buffer_load_dwordx4 v[172:175], off, %[rt], %[sa] offset:1024 sc1\n\ts_add_u32 %[sa], %[sa], 0x2000\n\t"
        "buffer_load_dwordx4 v[176:179], off, %[rt], %[sa] offset:0 sc1\n\tbuffer_load_dwordx4 v[180:183], off, %[rt], %[sa] offset:1024 sc1\n\t"
        "s_add_u32 %[sa], %[sa], 0x2000\n\ts_waitcnt vmcnt(8)\n\ts_barrier\n\tds_read_b128 v[64:67], %[pb0] offset:0\n\t"
        "ds_read_b128 v[68:71], %[pb0] offset:1024\n\tds_read_b128 v[72:75], %[pb0] offset:2048\n\tds_read_b128 v[76:79], %[pb0] offset:3072\n\t"
        "ds_read_b128 v[80:83], %[pb0] offset:4096\n\tds_read_b128 v[84:87], %[pb0] offset:5120\n\tds_read_b128 v[88:91], %[pb0] offset:6144\n\t"
        "s_waitcnt vmcnt(6) lgkmcnt(3)\n\tv_mfma_f32_16x16x32_bf16 v[96:99], v[64:67], v[152:155], 0\n\tds_read_b128 v[92:95], %[pb0] offset:7168\n\t"
        "v_mfma_f32_16x16x32_bf16 v[100:103], v[64:67], v[156:159], 0\n\tv_mfma_f32_16x16x32_bf16 v[104:107], v[68:71], v[152:155], 0\n\t"
        "ds_read_b128 v[64:67], %[pb0] offset:8192\n\tv_mfma_f32_16x16x32_bf16 v[108:111], v[68:71], v[156:159], 0\n\t"
        "v_mfma_f32_16x16x32_bf16 v[112:115], v[72:75], v[152:155], 0\n\tds_read_b128 v[68:71], %[pb0] offset:9216\n\t"
        "buffer_load_dwordx4 v[184:187], off, %[rt], %[sa] offset:0 sc1\n\tv_mfma_f32_16x16x32_bf16 v[116:119], v[72:75], v[156:159], 0\n\t"
        "v_mfma_f32_16x16x32_bf16 v[120:123], v[76:79], v[152:155], 0\n\tds_read_b128 v[72:75], %[pb0] offset:10240\n\t"
        "v_mfma_f32_16x16x32_bf16 v[124:127], v[76:79], v[156:159], 0\n\ts_waitcnt lgkmcnt(3)\n\t"
        "v_mfma_f32_16x16x32_bf16 v[128:131], v[152:155], v[80:83], 0\n\tds_read_b128 v[76:79], %[pb0] offset:11264\n\t"
        "v_mfma_f32_16x16x32_bf16 v[132:135], v[156:159], v[80:83], 0\n\tbuffer_load_dwordx4 v[188:191], off, %[rt], %[sa] offset:1024 sc1\n\t"
        "s_add_u32 %[sa], %[sa], 0x2000\n\tv_mfma_f32_16x16x32_bf16 v[136:139], v[152:155], v[84:87], 0\n\tds_read_b128 v[80:83], %[pb0] offset:12288\n\t"
        "v_mfma_f32_16x16x32_bf16 v[140:143], v[156:159], v[84:87], 0\n\ts_waitcnt vmcnt(6)\n\t"
        "v_mfma_f32_16x16x32_bf16 v[96:99], v[88:91], v[160:163], v[96:99]\n\tds_read_b128 v[84:87], %[pb0] offset:13312\n\t"
        "v_mfma_f32_16x16x32_bf16 v[100:103], v[88:91], v[164:167], v[100:103]\n\tv_mfma_f32_16x16x32_bf16 v[104:107], v[92:95], v[160:163], v[104:107]\n\t"
        "ds_read_b128 v[88:91], %[pb0] offset:14336\n\tbuffer_load_dwordx4 v[192:195], off, %[rt], %[sa] offset:0 sc1\n\t"
        "v_mfma_f32_16x16x32_bf16 v[108:111], v[92:95], v[164:167], v[108:111]\n\ts_waitcnt lgkmcnt(3)\n\t"
        "v_mfma_f32_16x16x32_bf16 v[112:115], v[64:67], v[160:163], v[112:115]\n\tds_read_b128 v[92:95], %[pb0] offset:15360\n\t"
        "v_mfma_f32_16x16x32_bf16 v[116:119], v[64:67], v[164:167], v[116:119]\n\tv_mfma_f32_16x16x32_bf16 v[120:123], v[68:71], v[160:163], v[120:123]\n\t"
        "ds_read_b128 v[64:67], %[pb0] offset:16384\n\tv_mfma_f32_16x16x32_bf16 v[124:127], v[68:71], v[164:167], v[124:127]\n\t"
        "buffer_load_dwordx4 v[196:199], off, %[rt], %[sa] offset:1024 sc1\n\ts_add_u32 %[sa], %[sa], 0x2000\n\t"
        "v_mfma_f32_16x16x32_bf16 v[128:131], v[160:163], v[72:75], v[128:131]\n\tds_read_b128 v[68:71], %[pb0] offset:17408\n\t"
        "v_mfma_f32_16x16x32_bf16 v[132:135], v[164:167], v[72:75], v[132:135]\n\tv_mfma_f32_16x16x32_bf16 v[136:139], v[160:163], v[76:79], v[136:139]\n\t"
        "ds_read_b128 v[72:75], %[pb0] offset:18432\n\tv_mfma_f32_16x16x32_bf16 v[140:143], v[164:167], v[76:79], v[140:143]\n\t"
        "s_waitcnt vmcnt(6) lgkmcnt(3)\n\tv_mfma_f32_16x16x32_bf16 v[96:99], v[80:83], v[168:171], v[96:99]\n\tds_read_b128 v[76:79], %[pb0] offset:19456\n\t"
        "buffer_load_dwordx4 v[200:203], off, %[rt], %[sa] offset:0 sc1\n\tv_mfma_f32_16x16x32_bf16 v[100:103], v[80:83], v[172:175], v[100:103]\n\t"
        "v_mfma_f32_16x16x32_bf16 v[104:107], v[84:87], v[168:171], v[104:107]\n\tds_read_b128 v[80:83], %[pb0] offset:20480\n\t"
        "v_mfma_f32_16x16x32_bf16 v[108:111], v[84:87], v[172:175], v[108:111]\n\tv_mfma_f32_16x16x32_bf16 v[112:115], v[88:91], v[168:171], v[112:115]\n\t"
        "ds_read_b128 v[84:87], %[pb0] offset:21504\n\tv_mfma_f32_16x16x32_bf16 v[116:119], v[88:91], v[172:175], v[116:119]\n\t"
        "buffer_load_dwordx4 v[204:207], off, %[rt], %[sa] offset:1024 sc1\n\ts_add_u32 %[sa], %[sa], 0x2000\n\t"
        "v_mfma_f32_16x16x32_bf16 v[120:123], v[92:95], v[168:171], v[120:123]\n\tds_read_b128 v[88:91], %[pb0] offset:22528\n\t"
        "v_mfma_f32_16x16x32_bf16 v[124:127], v[92:95], v[172:175], v[124:127]\n\ts_waitcnt lgkmcnt(3)\n\t"
        "v_mfma_f32_16x16x32_bf16 v[128:131], v[168:171], v[64:67], v[128:131]\n\tds_read_b128 v[92:95], %[pb0] offset:23552\n\t"
        "v_mfma_f32_16x16x32_bf16 v[132:135], v[172:175], v[64:67], v[132:135]\n\tv_mfma_f32_16x16x32_bf16 v[136:139], v[168:171], v[68:71], v[136:139]\n\t"
        "s_barrier\n\tds_read_b128 v[64:67], %[pb0] offset:24576\n\tbuffer_load_dwordx4 v[208:211], off, %[rt], %[sa] offset:0 sc1\n\t"
        "v_mfma_f32_16x16x32_bf16 v[140:143], v[172:175], v[68:71], v[140:143]\n\ts_waitcnt vmcnt(7)\n\t"
        "v_mfma_f32_16x16x32_bf16 v[96:99], v[72:75], v[176:179], v[96:99]\n\tds_read_b128 v[68:71], %[pb0] offset:25600\n\t"
        "v_mfma_f32_16x16x32_bf16 v[100:103], v[72:75], v[180:183], v[100:103]\n\tv_mfma_f32_16x16x32_bf16 v[104:107], v[76:79], v[176:179], v[104:107]\n\t"
        "ds_read_b128 v[72:75], %[pb0] offset:26624\n\tv_mfma_f32_16x16x32_bf16 v[108:111], v[76:79], v[180:183], v[108:111]\n\t"
        "buffer_load_dwordx4 v[212:215], off, %[rt], %[sa] offset:1024 sc1\n\ts_add_u32 %[sa], %[sa], 0x1f2000\n\ts_waitcnt lgkmcnt(3)\n\t"
        "v_mfma_f32_16x16x32_bf16 v[112:115], v[80:83], v[176:179], v[112:115]\n\tds_read_b128 v[76:79], %[pb0] offset:27648\n\t"
        "v_mfma_f32_16x16x32_bf16 v[116:119], v[80:83], v[180:183], v[116:119]\n\tv_mfma_f32_16x16x32_bf16 v[120:123], v[84:87], v[176:179], v[120:123]\n\t"
        "ds_read_b128 v[80:83], %[pb0] offset:28672\n\tv_mfma_f32_16x16x32_bf16 v[124:127], v[84:87], v[180:183], v[124:127]\n\t"
        "v_mfma_f32_16x16x32_bf16 v[128:131], v[176:179], v[88:91], v[128:131]\n\tds_read_b128 v[84:87], %[pb0] offset:29696\n\t"
        "buffer_load_dwordx4 v[216:219], off, %[rt], %[sa] offset:0 sc1\n\tv_mfma_f32_16x16x32_bf16 v[132:135], v[180:183], v[88:91], v[132:135]\n\t"
        "v_mfma_f32_16x16x32_bf16 v[136:139], v[176:179], v[92:95], v[136:139]\n\tds_read_b128 v[88:91], %[pb0] offset:30720\n\t"
        "v_mfma_f32_16x16x32_bf16 v[140:143], v[180:183], v[92:95], v[140:143]\n\ts_waitcnt vmcnt(7) lgkmcnt(3)\n\t"
        "v_mfma_f32_16x16x32_bf16 v[96:99], v[64:67], v[184:187], v[96:99]\n\tds_read_b128 v[92:95], %[pb0] offset:31744\n\t"
        "v_mfma_f32_16x16x32_bf16 v[100:103], v[64:67], v[188:191], v[100:103]\n\tbuffer_load_dwordx4 v[220:223], off, %[rt], %[sa] offset:1024 sc1\n\t"
        "s_add_u32 %[sa], %[sa], 0xffe10000\n\tv_mfma_f32_16x16x32_bf16 v[104:107], v[68:71], v[184:187], v[104:107]\n\t"
        "ds_read_b128 v[64:67], %[pb0] offset:32768\n\tv_mfma_f32_16x16x32_bf16 v[108:111], v[68:71], v[188:191], v[108:111]\n\t"
        "v_mfma_f32_16x16x32_bf16 v[112:115], v[72:75], v[184:187], v[112:115]\n\tds_read_b128 v[68:71], %[pb0] offset:33792\n\t"
        "v_mfma_f32_16x16x32_bf16 v[116:119], v[72:75], v[188:191], v[116:119]\n\tv_mfma_f32_16x16x32_bf16 v[120:123], v[76:79], v[184:187], v[120:123]\n\t"
        "ds_read_b128 v[72:75], %[pb0] offset:34816\n\tbuffer_load_dwordx4 v[224:227], off, %[rt], %[sa] offset:0 sc1\n\t"
        "v_mfma_f32_16x16x32_bf16 v[124:127], v[76:79], v[188:191], v[124:127]\n\ts_waitcnt lgkmcnt(3)\n\t"
        "v_mfma_f32_16x16x32_bf16 v[128:131], v[184:187], v[80:83], v[128:131]\n\tds_read_b128 v[76:79], %[pb0] offset:35840\n\t"
        "v_mfma_f32_16x16x32_bf16 v[132:135], v[188:191], v[80:83], v[132:135]\n\tv_mfma_f32_16x16x32_bf16 v[136:139], v[184:187], v[84:87], v[136:139]\n\t"
        "ds_read_b128 v[80:83], %[pb0] offset:36864\n\tv_mfma_f32_16x16x32_bf16 v[140:143], v[188:191], v[84:87], v[140:143]\n\t"
        "buffer_load_dwordx4 v[228:231], off, %[rt], %[sa] offset:1024 sc1\n\ts_add_u32 %[sa], %[sa], 0x1f2000\n\ts_waitcnt vmcnt(8)\n\t"
        "v_mfma_f32_16x16x32_bf16 v[96:99], v[88:91], v[192:195], v[96:99]\n\tds_read_b128 v[84:87], %[pb0] offset:37888\n\t"
        "v_mfma_f32_16x16x32_bf16 v[100:103], v[88:91], v[196:199], v[100:103]\n\tv_mfma_f32_16x16x32_bf16 v[104:107], v[92:95], v[192:195], v[104:107]\n\t"
        "ds_read_b128 v[88:91], %[pb0] offset:38912\n\tv_mfma_f32_16x16x32_bf16 v[108:111], v[92:95], v[196:199], v[108:111]\n\ts_waitcnt lgkmcnt(3)\n\t"
        "v_mfma_f32_16x16x32_bf16 v[112:115], v[64:67], v[192:195], v[112:115]\n\tds_read_b128 v[92:95], %[pb0] offset:39936\n\t"
        "buffer_load_dwordx4 v[232:235], off, %[rt], %[sa] offset:0 sc1\n\tv_mfma_f32_16x16x32_bf16 v[116:119], v[64:67], v[196:199], v[116:119]\n\t"
        "v_mfma_f32_16x16x32_bf16 v[120:123], v[68:71], v[192:195], v[120:123]\n\tds_read_b128 v[64:67], %[pb0] offset:40960\n\t"
        "v_mfma_f32_16x16x32_bf16 v[124:127], v[68:71], v[196:199], v[124:127]\n\tv_mfma_f32_16x16x32_bf16 v[128:131], v[192:195], v[72:75], v[128:131]\n\t"
        "ds_read_b128 v[68:71], %[pb0] offset:41984\n\t.p2align 12,,12\n\t.Lpg0:\n\ts_cbranch_execz .Lpgret\n\t"
        "v_mfma_f32_16x16x32_bf16 v[132:135], v[196:199], v[72:75], v[132:135]\n\tbuffer_load_dwordx4 v[236:239], off, %[rt], %[sa] offset:1024 sc1\n\t"
        "s_add_u32 %[sa], %[sa], 0xffe10000\n\tv_mfma_f32_16x16x32_bf16 v[136:139], v[192:195], v[76:79], v[136:139]\n\t"
        "ds_read_b128 v[72:75], %[pb0] offset:43008\n\tv_mfma_f32_16x16x32_bf16 v[140:143], v[196:199], v[76:79], v[140:143]\n\t"
        "s_waitcnt vmcnt(8) lgkmcnt(3)\n\tv_mfma_f32_16x16x32_bf16 v[96:99], v[80:83], v[200:203], v[96:99]\n\tds_read_b128 v[76:79], %[pb0] offset:44032\n\t"
        "v_mfma_f32_16x16x32_bf16 v[100:103], v[80:83], v[204:207], v[100:103]\n\tv_mfma_f32_16x16x32_bf16 v[104:107], v[84:87], v[200:203], v[104:107]\n\t"
        "ds_read_b128 v[80:83], %[pb0] offset:45056\n\tbuffer_load_dwordx4 v[240:243], off, %[rt], %[sa] offset:0 sc1\n\t"
        "v_mfma_f32_16x16x32_bf16 v[108:111], v[84:87], v[204:207], v[108:111]\n\tv_mfma_f32_16x16x32_bf16 v[112:115], v[88:91], v[200:203], v[112:115]\n\t"
        "ds_read_b128 v[84:87], %[pb0] offset:46080\n\tv_mfma_f32_16x16x32_bf16 v[116:119], v[88:91], v[204:207], v[116:119]\n\t"
        "v_mfma_f32_16x16x32_bf16 v[120:123], v[92:95], v[200:203], v[120:123]\n\tds_read_b128 v[88:91], %[pb0] offset:47104\n\t"
        "v_mfma_f32_16x16x32_bf16 v[124:127], v[92:95], v[204:207], v[124:127]\n\tbuffer_load_dwordx4 v[244:247], off, %[rt], %[sa] offset:1024 sc1\n\t"
        "s_add_u32 %[sa], %[sa], 0x1f2000\n\ts_waitcnt lgkmcnt(3)\n\tv_mfma_f32_16x16x32_bf16 v[128:131], v[200:203], v[64:67], v[128:131]\n\t"
        "ds_read_b128 v[92:95], %[pb0] offset:48128\n\tv_mfma_f32_16x16x32_bf16 v[132:135], v[204:207], v[64:67], v[132:135]\n\t"
        "v_mfma_f32_16x16x32_bf16 v[136:139], v[200:203], v[68:71], v[136:139]\n\tds_read_b128 v[64:67], %[pb0] offset:0\n\t"
        "v_mfma_f32_16x16x32_bf16 v[140:143], v[204:207], v[68:71], v[140:143]\n\ts_waitcnt vmcnt(8)\n\t"
        "v_mfma_f32_16x16x32_bf16 v[96:99], v[72:75], v[208:211], v[96:99]\n\tds_read_b128 v[68:71], %[pb0] offset:1024\n\t"
        "buffer_load_dwordx4 v[152:155], off, %[rt], %[sa] offset:0 sc1\n\tv_mfma_f32_16x16x32_bf16 v[100:103], v[72:75], v[212:215], v[100:103]\n\t"
        "v_mfma_f32_16x16x32_bf16 v[104:107], v[76:79], v[208:211], v[104:107]\n\tds_read_b128 v[72:75], %[pb0] offset:2048\n\t"
        "v_mfma_f32_16x16x32_bf16 v[108:111], v[76:79], v[212:215], v[108:111]\n\ts_waitcnt lgkmcnt(3)\n\t"
        "v_mfma_f32_16x16x32_bf16 v[112:115], v[80:83], v[208:211], v[112:115]\n\tds_read_b128 v[76:79], %[pb0] offset:3072\n\t"
        "v_mfma_f32_16x16x32_bf16 v[116:119], v[80:83], v[212:215], v[116:119]\n\tbuffer_load_dwordx4 v[156:159], off, %[rt], %[sa] offset:1024 sc1\n\t"
        "s_add_u32 %[sa], %[sa], 0xffe10000\n\tv_mfma_f32_16x16x32_bf16 v[120:123], v[84:87], v[208:211], v[120:123]\n\t"
        "ds_read_b128 v[80:83], %[pb0] offset:4096\n\tv_mfma_f32_16x16x32_bf16 v[124:127], v[84:87], v[212:215], v[124:127]\n\t"
        "v_mfma_f32_16x16x32_bf16 v[128:131], v[208:211], v[88:91], v[128:131]\n\tds_read_b128 v[84:87], %[pb0] offset:5120\n\t"
        "v_mfma_f32_16x16x32_bf16 v[132:135], v[212:215], v[88:91], v[132:135]\n\tv_mfma_f32_16x16x32_bf16 v[136:139], v[208:211], v[92:95], v[136:139]\n\t"
        "s_barrier\n\tds_read_b128 v[88:91], %[pb0] offset:49152\n\tbuffer_load_dwordx4 v[160:163], off, %[rt], %[sa] offset:0 sc1\n\t"
        "v_mfma_f32_16x16x32_bf16 v[140:143], v[212:215], v[92:95], v[140:143]\n\ts_waitcnt vmcnt(9) lgkmcnt(3)\n\t"
        "v_mfma_f32_16x16x32_bf16 v[16:19], v[64:67], v[216:219], 0\n\tds_read_b128 v[92:95], %[pb0] offset:50176\n\t"
        "v_mfma_f32_16x16x32_bf16 v[20:23], v[64:67], v[220:223], 0\n\tv_mfma_f32_16x16x32_bf16 v[24:27], v[68:71], v[216:219], 0\n\t"
        "ds_read_b128 v[64:67], %[pb0] offset:51200\n\tv_mfma_f32_16x16x32_bf16 v[28:31], v[68:71], v[220:223], 0\n\t"
        "buffer_load_dwordx4 v[164:167], off, %[rt], %[sa] offset:1024 sc1\n\ts_add_u32 %[sa], %[sa], 0x1f2000\n\t"
        "v_mfma_f32_16x16x32_bf16 v[32:35], v[72:75], v[216:219], 0\n\tds_read_b128 v[68:71], %[pb0] offset:52224\n\t"
        "v_mfma_f32_16x16x32_bf16 v[36:39], v[72:75], v[220:223], 0\n\tv_mfma_f32_16x16x32_bf16 v[40:43], v[76:79], v[216:219], 0\n\t"
        "ds_read_b128 v[72:75], %[pb0] offset:53248\n\tv_mfma_f32_16x16x32_bf16 v[44:47], v[76:79], v[220:223], 0\n\ts_waitcnt lgkmcnt(3)\n\t"
        "v_mfma_f32_16x16x32_bf16 v[48:51], v[216:219], v[80:83], 0\n\tds_read_b128 v[76:79], %[pb0] offset:54272\n\t"
        "buffer_load_dwordx4 v[168:171], off, %[rt], %[sa] offset:0 sc1\n\tv_mfma_f32_16x16x32_bf16 v[52:55], v[220:223], v[80:83], 0\n\t"
        "v_mfma_f32_16x16x32_bf16 v[56:59], v[216:219], v[84:87], 0\n\tds_read_b128 v[80:83], %[pb0] offset:6144\n\t"
        "v_mfma_f32_16x16x32_bf16 v[60:63], v[220:223], v[84:87], 0\n\ts_waitcnt vmcnt(9)\n\t"
        "v_mfma_f32_16x16x32_bf16 v[96:99], v[88:91], v[224:227], v[96:99]\n\tds_read_b128 v[84:87], %[pb0] offset:7168\n\t"
        "v_mfma_f32_16x16x32_bf16 v[100:103], v[88:91], v[228:231], v[100:103]\n\tbuffer_load_dwordx4 v[172:175], off, %[rt], %[sa] offset:1024 sc1\n\t"
        "s_add_u32 %[sa], %[sa], 0xffe10000\n\tv_mfma_f32_16x16x32_bf16 v[104:107], v[92:95], v[224:227], v[104:107]\n\t"
        "ds_read_b128 v[88:91], %[pb0] offset:8192\n\tv_mfma_f32_16x16x32_bf16 v[108:111], v[92:95], v[228:231], v[108:111]\n\ts_waitcnt lgkmcnt(3)\n\t"
        "v_mfma_f32_16x16x32_bf16 v[112:115], v[64:67], v[224:227], v[112:115]\n\tds_read_b128 v[92:95], %[pb0] offset:9216\n\t"
        "v_mfma_f32_16x16x32_bf16 v[116:119], v[64:67], v[228:231], v[116:119]\n\tv_mfma_f32_16x16x32_bf16 v[120:123], v[68:71], v[224:227], v[120:123]\n\t"
        "ds_read_b128 v[64:67], %[pb0] offset:10240\n\tbuffer_load_dwordx4 v[176:179], off, %[rt], %[sa] offset:0 sc1\n\t"
        "v_mfma_f32_16x16x32_bf16 v[124:127], v[68:71], v[228:231], v[124:127]\n\tv_mfma_f32_16x16x32_bf16 v[128:131], v[224:227], v[72:75], v[128:131]\n\t"
        "ds_read_b128 v[68:71], %[pb0] offset:11264\n\tv_mfma_f32_16x16x32_bf16 v[132:135], v[228:231], v[72:75], v[132:135]\n\t"
        "v_mfma_f32_16x16x32_bf16 v[136:139], v[224:227], v[76:79], v[136:139]\n\tds_read_b128 v[72:75], %[pb0] offset:55296\n\t"
        "v_mfma_f32_16x16x32_bf16 v[140:143], v[228:231], v[76:79], v[140:143]\n\tbuffer_load_dwordx4 v[180:183], off, %[rt], %[sa] offset:1024 sc1\n\t"
        "s_add_u32 %[sa], %[sa], 0x1f2000\n\ts_waitcnt vmcnt(10) lgkmcnt(3)\n\tv_mfma_f32_16x16x32_bf16 v[16:19], v[80:83], v[232:235], v[16:19]\n\t"
        "ds_read_b128 v[76:79], %[pb0] offset:56320\n\tv_mfma_f32_16x16x32_bf16 v[20:23], v[80:83], v[236:239], v[20:23]\n\t"
        "v_mfma_f32_16x16x32_bf16 v[24:27], v[84:87], v[232:235], v[24:27]\n\tds_read_b128 v[80:83], %[pb0] offset:57344\n\t"
        "v_mfma_f32_16x16x32_bf16 v[28:31], v[84:87], v[236:239], v[28:31]\n\tv_mfma_f32_16x16x32_bf16 v[32:35], v[88:91], v[232:235], v[32:35]\n\t"
        "ds_read_b128 v[84:87], %[pb0] offset:58368\n\tbuffer_load_dwordx4 v[184:187], off, %[rt], %[sa] offset:0 sc1\n\t"
        "v_mfma_f32_16x16x32_bf16 v[36:39], v[88:91], v[236:239], v[36:39]\n\tv_mfma_f32_16x16x32_bf16 v[40:43], v[92:95], v[232:235], v[40:43]\n\t"
        "ds_read_b128 v[88:91], %[pb0] offset:59392\n\tv_mfma_f32_16x16x32_bf16 v[44:47], v[92:95], v[236:239], v[44:47]\n\ts_waitcnt lgkmcnt(3)\n\t"
        "v_mfma_f32_16x16x32_bf16 v[48:51], v[232:235], v[64:67], v[48:51]\n\tds_read_b128 v[92:95], %[pb0] offset:60416\n\t"
        "v_mfma_f32_16x16x32_bf16 v[52:55], v[236:239], v[64:67], v[52:55]\n\tbuffer_load_dwordx4 v[188:191], off, %[rt], %[sa] offset:1024 sc1\n\t"
        "s_add_u32 %[sa], %[sa], 0xffe10000\n\tv_mfma_f32_16x16x32_bf16 v[56:59], v[232:235], v[68:71], v[56:59]\n\t"
        "ds_read_b128 v[64:67], %[pb0] offset:12288\n\tv_mfma_f32_16x16x32_bf16 v[60:63], v[236:239], v[68:71], v[60:63]\n\ts_waitcnt vmcnt(10)\n\t"
        "v_mfma_f32_16x16x32_bf16 v[96:99], v[72:75], v[240:243], v[96:99]\n\tds_read_b128 v[68:71], %[pb0] offset:13312\n\t"
        "v_mfma_f32_16x16x32_bf16 v[100:103], v[72:75], v[244:247], v[100:103]\n\tv_mfma_f32_16x16x32_bf16 v[104:107], v[76:79], v[240:243], v[104:107]\n\t"
        "ds_read_b128 v[72:75], %[pb0] offset:14336\n\tbuffer_load_dwordx4 v[192:195], off, %[rt], %[sa] offset:0 sc1\n\t"
        "v_mfma_f32_16x16x32_bf16 v[108:111], v[76:79], v[244:247], v[108:111]\n\ts_waitcnt lgkmcnt(3)\n\t"
        "v_mfma_f32_16x16x32_bf16 v[112:115], v[80:83], v[240:243], v[112:115]\n\tds_read_b128 v[76:79], %[pb0] offset:15360\n\t"
        "v_mfma_f32_16x16x32_bf16 v[116:119], v[80:83], v[244:247], v[116:119]\n\tv_mfma_f32_16x16x32_bf16 v[120:123], v[84:87], v[240:243], v[120:123]\n\t"
        "ds_read_b128 v[80:83], %[pb0] offset:16384\n\tv_mfma_f32_16x16x32_bf16 v[124:127], v[84:87], v[244:247], v[124:127]\n\t"
        "buffer_load_dwordx4 v[196:199], off, %[rt], %[sa] offset:1024 sc1\n\ts_add_u32 %[sa], %[sa], 0x1f2000\n\t"
        "v_mfma_f32_16x16x32_bf16 v[128:131], v[240:243], v[88:91], v[128:131]\n\tds_read_b128 v[84:87], %[pb0] offset:17408\n\t"
        "v_mfma_f32_16x16x32_bf16 v[132:135], v[244:247], v[88:91], v[132:135]\n\tv_mfma_f32_16x16x32_bf16 v[136:139], v[240:243], v[92:95], v[136:139]\n\t"
        "ds_read_b128 v[88:91], %[pb0] offset:61440\n\tv_mfma_f32_16x16x32_bf16 v[140:143], v[244:247], v[92:95], v[140:143]\n\t"
        "s_waitcnt vmcnt(10) lgkmcnt(3)\n\tv_mfma_f32_16x16x32_bf16 v[16:19], v[64:67], v[152:155], v[16:19]\n\tds_read_b128 v[92:95], %[pb0] offset:62464\n\t"
        "buffer_load_dwordx4 v[200:203], off, %[rt], %[sa] offset:0 sc1\n\tv_mfma_f32_16x16x32_bf16 v[20:23], v[64:67], v[156:159], v[20:23]\n\t"
        "v_mfma_f32_16x16x32_bf16 v[24:27], v[68:71], v[152:155], v[24:27]\n\tds_read_b128 v[64:67], %[pb0] offset:63488\n\t"
        "v_mfma_f32_16x16x32_bf16 v[28:31], v[68:71], v[156:159], v[28:31]\n\tv_mfma_f32_16x16x32_bf16 v[32:35], v[72:75], v[152:155], v[32:35]\n\t"
        "ds_read_b128 v[68:71], %[pb0] offset:64512\n\tv_mfma_f32_16x16x32_bf16 v[36:39], v[72:75], v[156:159], v[36:39]\n\t"
        "buffer_load_dwordx4 v[204:207], off, %[rt], %[sa] offset:1024 sc1\n\ts_add_u32 %[sa], %[sa], 0xffe10000\n\t"
        "v_mfma_f32_16x16x32_bf16 v[40:43], v[76:79], v[152:155], v[40:43]\n\tds_read_b128 v[72:75], %[pb1] offset:0\n\t"
        "v_mfma_f32_16x16x32_bf16 v[44:47], v[76:79], v[156:159], v[44:47]\n\ts_waitcnt lgkmcnt(3)\n\t"
        "v_mfma_f32_16x16x32_bf16 v[48:51], v[152:155], v[80:83], v[48:51]\n\tds_read_b128 v[76:79], %[pb1] offset:1024\n\t"
        "v_mfma_f32_16x16x32_bf16 v[52:55], v[156:159], v[80:83], v[52:55]\n\tv_mfma_f32_16x16x32_bf16 v[56:59], v[152:155], v[84:87], v[56:59]\n\t"
        "ds_read_b128 v[80:83], %[pb0] offset:18432\n\tv_mfma_f32_16x16x32_bf16 v[60:63], v[156:159], v[84:87], v[60:63]\n\ts_waitcnt vmcnt(10)\n\t"
        "v_mfma_f32_16x16x32_bf16 v[96:99], v[88:91], v[160:163], v[96:99]\n\tds_read_b128 v[84:87], %[pb0] offset:19456\n\t"
        "buffer_load_dwordx4 v[208:211], off, %[rt], %[sa] offset:0 sc1\n\tv_mfma_f32_16x16x32_bf16 v[100:103], v[88:91], v[164:167], v[100:103]\n\t"
        "v_mfma_f32_16x16x32_bf16 v[104:107], v[92:95], v[160:163], v[104:107]\n\tds_read_b128 v[88:91], %[pb0] offset:20480\n\t"
        "v_mfma_f32_16x16x32_bf16 v[108:111], v[92:95], v[164:167], v[108:111]\n\ts_waitcnt lgkmcnt(3)\n\t"
        "v_mfma_f32_16x16x32_bf16 v[112:115], v[64:67], v[160:163], v[112:115]\n\tds_read_b128 v[92:95], %[pb0] offset:21504\n\t"
        "v_mfma_f32_16x16x32_bf16 v[116:119], v[64:67], v[164:167], v[116:119]\n\tbuffer_load_dwordx4 v[212:215], off, %[rt], %[sa] offset:1024 sc1\n\t"
        "s_add_u32 %[sa], %[sa], 0x1f2000\n\tv_mfma_f32_16x16x32_bf16 v[120:123], v[68:71], v[160:163], v[120:123]\n\t"
        "ds_read_b128 v[64:67], %[pb0] offset:22528\n\tv_mfma_f32_16x16x32_bf16 v[124:127], v[68:71], v[164:167], v[124:127]\n\t"
        "v_mfma_f32_16x16x32_bf16 v[128:131], v[160:163], v[72:75], v[128:131]\n\tds_read_b128 v[68:71], %[pb0] offset:23552\n\t"
        "v_mfma_f32_16x16x32_bf16 v[132:135], v[164:167], v[72:75], v[132:135]\n\tv_mfma_f32_16x16x32_bf16 v[136:139], v[160:163], v[76:79], v[136:139]\n\t"
        "ds_read_b128 v[72:75], %[pb1] offset:2048\n\tv_mfma_f32_16x16x32_bf16 v[140:143], v[164:167], v[76:79], v[140:143]\n\t"
        "s_waitcnt vmcnt(10) lgkmcnt(3)\n\tv_mfma_f32_16x16x32_bf16 v[16:19], v[80:83], v[168:171], v[16:19]\n\tds_read_b128 v[76:79], %[pb1] offset:3072\n\t"
        "buffer_load_dwordx4 v[216:219], off, %[rt], %[sa] offset:0 sc1\n\tv_mfma_f32_16x16x32_bf16 v[20:23], v[80:83], v[172:175], v[20:23]\n\t"
        "v_mfma_f32_16x16x32_bf16 v[24:27], v[84:87], v[168:171], v[24:27]\n\tds_read_b128 v[80:83], %[pb1] offset:4096\n\t"
        "v_mfma_f32_16x16x32_bf16 v[28:31], v[84:87], v[172:175], v[28:31]\n\tv_mfma_f32_16x16x32_bf16 v[32:35], v[88:91], v[168:171], v[32:35]\n\t"
        "ds_read_b128 v[84:87], %[pb1] offset:5120\n\tv_mfma_f32_16x16x32_bf16 v[36:39], v[88:91], v[172:175], v[36:39]\n\t"
        "buffer_load_dwordx4 v[220:223], off, %[rt], %[sa] offset:1024 sc1\n\ts_add_u32 %[sa], %[sa], 0xffe10000\n\t"
        "v_mfma_f32_16x16x32_bf16 v[40:43], v[92:95], v[168:171], v[40:43]\n\tds_read_b128 v[88:91], %[pb1] offset:6144\n\t"
        "v_mfma_f32_16x16x32_bf16 v[44:47], v[92:95], v[172:175], v[44:47]\n\ts_waitcnt lgkmcnt(3)\n\t"
        "v_mfma_f32_16x16x32_bf16 v[48:51], v[168:171], v[64:67], v[48:51]\n\tds_read_b128 v[92:95], %[pb1] offset:7168\n\t"
        "v_mfma_f32_16x16x32_bf16 v[52:55], v[172:175], v[64:67], v[52:55]\n\tv_mfma_f32_16x16x32_bf16 v[56:59], v[168:171], v[68:71], v[56:59]\n\t"
        "ds_read_b128 v[64:67], %[pb0] offset:24576\n\tv_mfma_f32_16x16x32_bf16 v[60:63], v[172:175], v[68:71], v[60:63]\n\ts_waitcnt vmcnt(10)\n\t"
        "v_mfma_f32_16x16x32_bf16 v[96:99], v[72:75], v[176:179], v[96:99]\n\tds_read_b128 v[68:71], %[pb0] offset:25600\n\t"
        "buffer_load_dwordx4 v[224:227], off, %[rt], %[sa] offset:0 sc1\n\tv_mfma_f32_16x16x32_bf16 v[100:103], v[72:75], v[180:183], v[100:103]\n\t"
        "v_mfma_f32_16x16x32_bf16 v[104:107], v[76:79], v[176:179], v[104:107]\n\tds_read_b128 v[72:75], %[pb0] offset:26624\n\t"
        "v_mfma_f32_16x16x32_bf16 v[108:111], v[76:79], v[180:183], v[108:111]\n\ts_waitcnt lgkmcnt(3)\n\t"
        "v_mfma_f32_16x16x32_bf16 v[112:115], v[80:83], v[176:179], v[112:115]\n\tds_read_b128 v[76:79], %[pb0] offset:27648\n\t"
        "v_mfma_f32_16x16x32_bf16 v[116:119], v[80:83], v[180:183], v[116:119]\n\tbuffer_load_dwordx4 v[228:231], off, %[rt], %[sa] offset:1024 sc1\n\t"
        "s_add_u32 %[sa], %[sa], 0x1f2000\n\tv_mfma_f32_16x16x32_bf16 v[120:123], v[84:87], v[176:179], v[120:123]\n\t"
        "ds_read_b128 v[80:83], %[pb0] offset:28672\n\tv_mfma_f32_16x16x32_bf16 v[124:127], v[84:87], v[180:183], v[124:127]\n\t"
        "v_mfma_f32_16x16x32_bf16 v[128:131], v[176:179], v[88:91], v[128:131]\n\tds_read_b128 v[84:87], %[pb0] offset:29696\n\t"
        "v_mfma_f32_16x16x32_bf16 v[132:135], v[180:183], v[88:91], v[132:135]\n\tv_mfma_f32_16x16x32_bf16 v[136:139], v[176:179], v[92:95], v[136:139]\n\t"
        "s_barrier\n\tds_read_b128 v[88:91], %[pb1] offset:8192\n\tv_mfma_f32_16x16x32_bf16 v[140:143], v[180:183], v[92:95], v[140:143]\n\t"
        "s_waitcnt vmcnt(10) lgkmcnt(3)\n\tv_mfma_f32_16x16x32_bf16 v[16:19], v[64:67], v[184:187], v[16:19]\n\tds_read_b128 v[92:95], %[pb1] offset:9216\n\t"
        "buffer_load_dwordx4 v[232:235], off, %[rt], %[sa] offset:0 sc1\n\tv_mfma_f32_16x16x32_bf16 v[20:23], v[64:67], v[188:191], v[20:23]\n\t"
        "v_mfma_f32_16x16x32_bf16 v[24:27], v[68:71], v[184:187], v[24:27]\n\tds_read_b128 v[64:67], %[pb1] offset:10240\n\t"
        "v_mfma_f32_16x16x32_bf16 v[28:31], v[68:71], v[188:191], v[28:31]\n\tv_mfma_f32_16x16x32_bf16 v[32:35], v[72:75], v[184:187], v[32:35]\n\t"
        "ds_read_b128 v[68:71], %[pb1] offset:11264\n\tv_mfma_f32_16x16x32_bf16 v[36:39], v[72:75], v[188:191], v[36:39]\n\t"
        "buffer_load_dwordx4 v[236:239], off, %[rt], %[sa] offset:1024 sc1\n\ts_add_u32 %[sa], %[sa], 0xffe10000\n\t"
        "v_mfma_f32_16x16x32_bf16 v[40:43], v[76:79], v[184:187], v[40:43]\n\tds_read_b128 v[72:75], %[pb1] offset:12288\n\t"
        "v_mfma_f32_16x16x32_bf16 v[44:47], v[76:79], v[188:191], v[44:47]\n\ts_waitcnt lgkmcnt(3)\n\t"
        "v_mfma_f32_16x16x32_bf16 v[48:51], v[184:187], v[80:83], v[48:51]\n\tds_read_b128 v[76:79], %[pb1] offset:13312\n\t"
        "v_mfma_f32_16x16x32_bf16 v[52:55], v[188:191], v[80:83], v[52:55]\n\tv_mfma_f32_16x16x32_bf16 v[56:59], v[184:187], v[84:87], v[56:59]\n\t"
        "ds_read_b128 v[80:83], %[pb0] offset:30720\n\tv_mfma_f32_16x16x32_bf16 v[60:63], v[188:191], v[84:87], v[60:63]\n\ts_waitcnt vmcnt(10)\n\t"
        "v_mfma_f32_16x16x32_bf16 v[96:99], v[88:91], v[192:195], v[96:99]\n\tds_read_b128 v[84:87], %[pb0] offset:31744\n\t"
        "buffer_load_dwordx4 v[240:243], off, %[rt], %[sa] offset:0 sc1\n\tv_mfma_f32_16x16x32_bf16 v[100:103], v[88:91], v[196:199], v[100:103]\n\t"
        "v_mfma_f32_16x16x32_bf16 v[104:107], v[92:95], v[192:195], v[104:107]\n\tds_read_b128 v[88:91], %[pb0] offset:32768\n\t"
        "v_mfma_f32_16x16x32_bf16 v[108:111], v[92:95], v[196:199], v[108:111]\n\ts_waitcnt lgkmcnt(3)\n\t"
        "v_mfma_f32_16x16x32_bf16 v[112:115], v[64:67], v[192:195], v[112:115]\n\tds_read_b128 v[92:95], %[pb0] offset:33792\n\t"
        "v_mfma_f32_16x16x32_bf16 v[116:119], v[64:67], v[196:199], v[116:119]\n\tbuffer_load_dwordx4 v[244:247], off, %[rt], %[sa] offset:1024 sc1\n\t"
        "s_add_u32 %[sa], %[sa], 0x1f2000\n\tv_mfma_f32_16x16x32_bf16 v[120:123], v[68:71], v[192:195], v[120:123]\n\t"
        "ds_read_b128 v[64:67], %[pb0] offset:34816\n\tv_mfma_f32_16x16x32_bf16 v[124:127], v[68:71], v[196:199], v[124:127]\n\t"
        "v_mfma_f32_16x16x32_bf16 v[128:131], v[192:195], v[72:75], v[128:131]\n\tds_read_b128 v[68:71], %[pb0] offset:35840\n\t"
        "v_mfma_f32_16x16x32_bf16 v[132:135], v[196:199], v[72:75], v[132:135]\n\tv_mfma_f32_16x16x32_bf16 v[136:139], v[192:195], v[76:79], v[136:139]\n\t"
        "ds_read_b128 v[72:75], %[pb1] offset:14336\n\tv_mfma_f32_16x16x32_bf16 v[140:143], v[196:199], v[76:79], v[140:143]\n\t"
        "s_waitcnt vmcnt(10) lgkmcnt(3)\n\tv_mfma_f32_16x16x32_bf16 v[16:19], v[80:83], v[200:203], v[16:19]\n\tds_read_b128 v[76:79], %[pb1] offset:15360\n\t"
        "buffer_load_dwordx4 v[152:155], off, %[rt], %[sa] offset:0 sc1\n\tv_mfma_f32_16x16x32_bf16 v[20:23], v[80:83], v[204:207], v[20:23]\n\t"
        "v_mfma_f32_16x16x32_bf16 v[24:27], v[84:87], v[200:203], v[24:27]\n\tds_read_b128 v[80:83], %[pb1] offset:16384\n\t"
        "v_mfma_f32_16x16x32_bf16 v[28:31], v[84:87], v[204:207], v[28:31]\n\tv_mfma_f32_16x16x32_bf16 v[32:35], v[88:91], v[200:203], v[32:35]\n\t"
        "ds_read_b128 v[84:87], %[pb1] offset:17408\n\tv_mfma_f32_16x16x32_bf16 v[36:39], v[88:91], v[204:207], v[36:39]\n\t"
        "buffer_load_dwordx4 v[156:159], off, %[rt], %[sa] offset:1024 sc1\n\ts_add_u32 %[sa], %[sa], 0xffe10000\n\t"
        "v_mfma_f32_16x16x32_bf16 v[40:43], v[92:95], v[200:203], v[40:43]\n\tds_read_b128 v[88:91], %[pb1] offset:18432\n\t"
        "v_mfma_f32_16x16x32_bf16 v[44:47], v[92:95], v[204:207], v[44:47]\n\ts_waitcnt lgkmcnt(3)\n\t"
        "v_mfma_f32_16x16x32_bf16 v[48:51], v[200:203], v[64:67], v[48:51]\n\tds_read_b128 v[92:95], %[pb1] offset:19456\n\t"
        "v_mfma_f32_16x16x32_bf16 v[52:55], v[204:207], v[64:67], v[52:55]\n\tv_mfma_f32_16x16x32_bf16 v[56:59], v[200:203], v[68:71], v[56:59]\n\t"
        "ds_read_b128 v[64:67], %[pb0] offset:36864\n\tv_mfma_f32_16x16x32_bf16 v[60:63], v[204:207], v[68:71], v[60:63]\n\ts_waitcnt vmcnt(10)\n\t"
        "v_mfma_f32_16x16x32_bf16 v[96:99], v[72:75], v[208:211], v[96:99]\n\tds_read_b128 v[68:71], %[pb0] offset:37888\n\t"
        "buffer_load_dwordx4 v[160:163], off, %[rt], %[sa] offset:0 sc1\n\tv_mfma_f32_16x16x32_bf16 v[100:103], v[72:75], v[212:215], v[100:103]\n\t"
        "v_mfma_f32_16x16x32_bf16 v[104:107], v[76:79], v[208:211], v[104:107]\n\tds_read_b128 v[72:75], %[pb0] offset:38912\n\t"
        "v_mfma_f32_16x16x32_bf16 v[108:111], v[76:79], v[212:215], v[108:111]\n\ts_waitcnt lgkmcnt(3)\n\t"
        "v_mfma_f32_16x16x32_bf16 v[112:115], v[80:83], v[208:211], v[112:115]\n\tds_read_b128 v[76:79], %[pb0] offset:39936\n\t"
        "v_mfma_f32_16x16x32_bf16 v[116:119], v[80:83], v[212:215], v[116:119]\n\tbuffer_load_dwordx4 v[164:167], off, %[rt], %[sa] offset:1024 sc1\n\t"
        "s_add_u32 %[sa], %[sa], 0x1f2000\n\tv_mfma_f32_16x16x32_bf16 v[120:123], v[84:87], v[208:211], v[120:123]\n\t"
        "ds_read_b128 v[80:83], %[pb0] offset:40960\n\tv_mfma_f32_16x16x32_bf16 v[124:127], v[84:87], v[212:215], v[124:127]\n\t"
        "v_mfma_f32_16x16x32_bf16 v[128:131], v[208:211], v[88:91], v[128:131]\n\tds_read_b128 v[84:87], %[pb0] offset:41984\n\t"
        "v_mfma_f32_16x16x32_bf16 v[132:135], v[212:215], v[88:91], v[132:135]\n\tv_mfma_f32_16x16x32_bf16 v[136:139], v[208:211], v[92:95], v[136:139]\n\t"
        "ds_read_b128 v[88:91], %[pb1] offset:20480\n\tv_mfma_f32_16x16x32_bf16 v[140:143], v[212:215], v[92:95], v[140:143]\n\t"
        "s_waitcnt vmcnt(10) lgkmcnt(3)\n\tv_mfma_f32_16x16x32_bf16 v[16:19], v[64:67], v[216:219], v[16:19]\n\tds_read_b128 v[92:95], %[pb1] offset:21504\n\t"
        "buffer_load_dwordx4 v[168:171], off, %[rt], %[sa] offset:0 sc1\n\tv_mfma_f32_16x16x32_bf16 v[20:23], v[64:67], v[220:223], v[20:23]\n\t"
        "v_mfma_f32_16x16x32_bf16 v[24:27], v[68:71], v[216:219], v[24:27]\n\tds_read_b128 v[64:67], %[pb1] offset:22528\n\t"
        "v_mfma_f32_16x16x32_bf16 v[28:31], v[68:71], v[220:223], v[28:31]\n\tv_mfma_f32_16x16x32_bf16 v[32:35], v[72:75], v[216:219], v[32:35]\n\t"
        "ds_read_b128 v[68:71], %[pb1] offset:23552\n\tv_mfma_f32_16x16x32_bf16 v[36:39], v[72:75], v[220:223], v[36:39]\n\t"
        "buffer_load_dwordx4 v[172:175], off, %[rt], %[sa] offset:1024 sc1\n\ts_add_u32 %[sa], %[sa], 0xffe10000\n\t"
        "v_mfma_f32_16x16x32_bf16 v[40:43], v[76:79], v[216:219], v[40:43]\n\tds_read_b128 v[72:75], %[pb1] offset:24576\n\t"
        "v_mfma_f32_16x16x32_bf16 v[44:47], v[76:79], v[220:223], v[44:47]\n\ts_waitcnt lgkmcnt(3)\n\t"
        "v_mfma_f32_16x16x32_bf16 v[48:51], v[216:219], v[80:83], v[48:51]\n\tds_read_b128 v[76:79], %[pb1] offset:25600\n\t"
        "v_mfma_f32_16x16x32_bf16 v[52:55], v[220:223], v[80:83], v[52:55]\n\tv_mfma_f32_16x16x32_bf16 v[56:59], v[216:219], v[84:87], v[56:59]\n\t"
        "ds_read_b128 v[80:83], %[pb0] offset:43008\n\tv_mfma_f32_16x16x32_bf16 v[60:63], v[220:223], v[84:87], v[60:63]\n\ts_waitcnt vmcnt(10)\n\t"
        "v_mfma_f32_16x16x32_bf16 v[96:99], v[88:91], v[224:227], v[96:99]\n\tds_read_b128 v[84:87], %[pb0] offset:44032\n\t"
        "buffer_load_dwordx4 v[176:179], off, %[rt], %[sa] offset:0 sc1\n\tv_mfma_f32_16x16x32_bf16 v[100:103], v[88:91], v[228:231], v[100:103]\n\t"
        "v_mfma_f32_16x16x32_bf16 v[104:107], v[92:95], v[224:227], v[104:107]\n\tds_read_b128 v[88:91], %[pb0] offset:45056\n\t"
        "v_mfma_f32_16x16x32_bf16 v[108:111], v[92:95], v[228:231], v[108:111]\n\ts_waitcnt lgkmcnt(3)\n\t"
        "v_mfma_f32_16x16x32_bf16 v[112:115], v[64:67], v[224:227], v[112:115]\n\tds_read_b128 v[92:95], %[pb0] offset:46080\n\t"
        "v_mfma_f32_16x16x32_bf16 v[116:119], v[64:67], v[228:231], v[116:119]\n\tbuffer_load_dwordx4 v[180:183], off, %[rt], %[sa] offset:1024 sc1\n\t"
        "s_add_u32 %[sa], %[sa], 0x1f2000\n\tv_mfma_f32_16x16x32_bf16 v[120:123], v[68:71], v[224:227], v[120:123]\n\t"
        "ds_read_b128 v[64:67], %[pb0] offset:47104\n\tv_mfma_f32_16x16x32_bf16 v[124:127], v[68:71], v[228:231], v[124:127]\n\t"
        "v_mfma_f32_16x16x32_bf16 v[128:131], v[224:227], v[72:75], v[128:131]\n\tds_read_b128 v[68:71], %[pb0] offset:48128\n\t"
        "v_mfma_f32_16x16x32_bf16 v[132:135], v[228:231], v[72:75], v[132:135]\n\tv_mfma_f32_16x16x32_bf16 v[136:139], v[224:227], v[76:79], v[136:139]\n\t"
        "ds_read_b128 v[72:75], %[pb1] offset:26624\n\tv_mfma_f32_16x16x32_bf16 v[140:143], v[228:231], v[76:79], v[140:143]\n\t"
        "s_waitcnt vmcnt(10) lgkmcnt(3)\n\tv_mfma_f32_16x16x32_bf16 v[16:19], v[80:83], v[232:235], v[16:19]\n\tds_read_b128 v[76:79], %[pb1] offset:27648\n\t"
        "buffer_load_dwordx4 v[184:187], off, %[rt], %[sa] offset:0 sc1\n\tv_mfma_f32_16x16x32_bf16 v[20:23], v[80:83], v[236:239], v[20:23]\n\t"
        "v_mfma_f32_16x16x32_bf16 v[24:27], v[84:87], v[232:235], v[24:27]\n\tds_read_b128 v[80:83], %[pb1] offset:28672\n\t"
        "v_mfma_f32_16x16x32_bf16 v[28:31], v[84:87], v[236:239], v[28:31]\n\tv_mfma_f32_16x16x32_bf16 v[32:35], v[88:91], v[232:235], v[32:35]\n\t"
        "ds_read_b128 v[84:87], %[pb1] offset:29696\n\tv_mfma_f32_16x16x32_bf16 v[36:39], v[88:91], v[236:239], v[36:39]\n\t"
        "buffer_load_dwordx4 v[188:191], off, %[rt], %[sa] offset:1024 sc1\n\ts_add_u32 %[sa], %[sa], 0xffe10000\n\t"
        "v_mfma_f32_16x16x32_bf16 v[40:43], v[92:95], v[232:235], v[40:43]\n\tds_read_b128 v[88:91], %[pb1] offset:30720\n\t"
        "v_mfma_f32_16x16x32_bf16 v[44:47], v[92:95], v[236:239], v[44:47]\n\ts_waitcnt lgkmcnt(3)\n\t"
        "v_mfma_f32_16x16x32_bf16 v[48:51], v[232:235], v[64:67], v[48:51]\n\tds_read_b128 v[92:95], %[pb1] offset:31744\n\t"
        "v_mfma_f32_16x16x32_bf16 v[52:55], v[236:239], v[64:67], v[52:55]\n\tv_mfma_f32_16x16x32_bf16 v[56:59], v[232:235], v[68:71], v[56:59]\n\t"
        "ds_read_b128 v[64:67], %[pb0] offset:49152\n\tv_mfma_f32_16x16x32_bf16 v[60:63], v[236:239], v[68:71], v[60:63]\n\ts_waitcnt vmcnt(10)\n\t"
        "v_mfma_f32_16x16x32_bf16 v[96:99], v[72:75], v[240:243], v[96:99]\n\tds_read_b128 v[68:71], %[pb0] offset:50176\n\t"
        "buffer_load_dwordx4 v[192:195], off, %[rt], %[sa] offset:0 sc1\n\tv_mfma_f32_16x16x32_bf16 v[100:103], v[72:75], v[244:247], v[100:103]\n\t"
        "v_mfma_f32_16x16x32_bf16 v[104:107], v[76:79], v[240:243], v[104:107]\n\tds_read_b128 v[72:75], %[pb0] offset:51200\n\t"
        "v_mfma_f32_16x16x32_bf16 v[108:111], v[76:79], v[244:247], v[108:111]\n\ts_waitcnt lgkmcnt(3)\n\t"
        "v_mfma_f32_16x16x32_bf16 v[112:115], v[80:83], v[240:243], v[112:115]\n\tds_read_b128 v[76:79], %[pb0] offset:52224\n\t"
        "v_mfma_f32_16x16x32_bf16 v[116:119], v[80:83], v[244:247], v[116:119]\n\tbuffer_load_dwordx4 v[196:199], off, %[rt], %[sa] offset:1024 sc1\n\t"
        "s_add_u32 %[sa], %[sa], 0x1f2000\n\tv_mfma_f32_16x16x32_bf16 v[120:123], v[84:87], v[240:243], v[120:123]\n\t"
        "ds_read_b128 v[80:83], %[pb0] offset:53248\n\tv_mfma_f32_16x16x32_bf16 v[124:127], v[84:87], v[244:247], v[124:127]\n\t"
        "v_mfma_f32_16x16x32_bf16 v[128:131], v[240:243], v[88:91], v[128:131]\n\tds_read_b128 v[84:87], %[pb0] offset:54272\n\t"
        "v_mfma_f32_16x16x32_bf16 v[132:135], v[244:247], v[88:91], v[132:135]\n\tv_mfma_f32_16x16x32_bf16 v[136:139], v[240:243], v[92:95], v[136:139]\n\t"
        "s_barrier\n\tds_read_b128 v[88:91], %[pb1] offset:32768\n\tv_mfma_f32_16x16x32_bf16 v[140:143], v[244:247], v[92:95], v[140:143]\n\t"
        "s_waitcnt vmcnt(10) lgkmcnt(3)\n\tv_mfma_f32_16x16x32_bf16 v[16:19], v[64:67], v[152:155], v[16:19]\n\tds_read_b128 v[92:95], %[pb1] offset:33792\n\t"
        "buffer_load_dwordx4 v[200:203], off, %[rt], %[sa] offset:0 sc1\n\tv_mfma_f32_16x16x32_bf16 v[20:23], v[64:67], v[156:159], v[20:23]\n\t"
        "v_mfma_f32_16x16x32_bf16 v[24:27], v[68:71], v[152:155], v[24:27]\n\tds_read_b128 v[64:67], %[pb1] offset:34816\n\t"
        "v_mfma_f32_16x16x32_bf16 v[28:31], v[68:71], v[156:159], v[28:31]\n\tv_mfma_f32_16x16x32_bf16 v[32:35], v[72:75], v[152:155], v[32:35]\n\t"
        "ds_read_b128 v[68:71], %[pb1] offset:35840\n\tv_mfma_f32_16x16x32_bf16 v[36:39], v[72:75], v[156:159], v[36:39]\n\t"
        "buffer_load_dwordx4 v[204:207], off, %[rt], %[sa] offset:1024 sc1\n\ts_add_u32 %[sa], %[sa], 0xffe10000\n\t"
        "v_mfma_f32_16x16x32_bf16 v[40:43], v[76:79], v[152:155], v[40:43]\n\tds_read_b128 v[72:75], %[pb1] offset:36864\n\t"
        "v_mfma_f32_16x16x32_bf16 v[44:47], v[76:79], v[156:159], v[44:47]\n\ts_waitcnt lgkmcnt(3)\n\t"
        "v_mfma_f32_16x16x32_bf16 v[48:51], v[152:155], v[80:83], v[48:51]\n\tds_read_b128 v[76:79], %[pb1] offset:37888\n\t"
        "v_mfma_f32_16x16x32_bf16 v[52:55], v[156:159], v[80:83], v[52:55]\n\tv_mfma_f32_16x16x32_bf16 v[56:59], v[152:155], v[84:87], v[56:59]\n\t"
        "ds_read_b128 v[80:83], %[pb0] offset:55296\n\tv_mfma_f32_16x16x32_bf16 v[60:63], v[156:159], v[84:87], v[60:63]\n\ts_waitcnt vmcnt(10)\n\t"
        "v_mfma_f32_16x16x32_bf16 v[96:99], v[88:91], v[160:163], v[96:99]\n\tds_read_b128 v[84:87], %[pb0] offset:56320\n\t"
        "buffer_load_dwordx4 v[208:211], off, %[rt], %[sa] offset:0 sc1\n\tv_mfma_f32_16x16x32_bf16 v[100:103], v[88:91], v[164:167], v[100:103]\n\t"
        "v_mfma_f32_16x16x32_bf16 v[104:107], v[92:95], v[160:163], v[104:107]\n\tds_read_b128 v[88:91], %[pb0] offset:57344\n\t"
        "v_mfma_f32_16x16x32_bf16 v[108:111], v[92:95], v[164:167], v[108:111]\n\ts_waitcnt lgkmcnt(3)\n\t"
        "v_mfma_f32_16x16x32_bf16 v[112:115], v[64:67], v[160:163], v[112:115]\n\tds_read_b128 v[92:95], %[pb0] offset:58368\n\t"
        "v_mfma_f32_16x16x32_bf16 v[116:119], v[64:67], v[164:167], v[116:119]\n\tbuffer_load_dwordx4 v[212:215], off, %[rt], %[sa] offset:1024 sc1\n\t"
        "s_add_u32 %[sa], %[sa], 0x1f2000\n\tv_mfma_f32_16x16x32_bf16 v[120:123], v[68:71], v[160:163], v[120:123]\n\t"
        "ds_read_b128 v[64:67], %[pb0] offset:59392\n\tv_mfma_f32_16x16x32_bf16 v[124:127], v[68:71], v[164:167], v[124:127]\n\t"
        "v_mfma_f32_16x16x32_bf16 v[128:131], v[160:163], v[72:75], v[128:131]\n\tds_read_b128 v[68:71], %[pb0] offset:60416\n\t"
        "v_mfma_f32_16x16x32_bf16 v[132:135], v[164:167], v[72:75], v[132:135]\n\tv_mfma_f32_16x16x32_bf16 v[136:139], v[160:163], v[76:79], v[136:139]\n\t"
        "ds_read_b128 v[72:75], %[pb1] offset:38912\n\tv_mfma_f32_16x16x32_bf16 v[140:143], v[164:167], v[76:79], v[140:143]\n\t"
        "s_waitcnt vmcnt(10) lgkmcnt(3)\n\tv_mfma_f32_16x16x32_bf16 v[16:19], v[80:83], v[168:171], v[16:19]\n\tds_read_b128 v[76:79], %[pb1] offset:39936\n\t"
        "buffer_load_dwordx4 v[216:219], off, %[rt], %[sa] offset:0 sc1\n\tv_mfma_f32_16x16x32_bf16 v[20:23], v[80:83], v[172:175], v[20:23]\n\t"
        "v_mfma_f32_16x16x32_bf16 v[24:27], v[84:87], v[168:171], v[24:27]\n\tds_read_b128 v[80:83], %[pb1] offset:40960\n\t"
        "v_mfma_f32_16x16x32_bf16 v[28:31], v[84:87], v[172:175], v[28:31]\n\tv_mfma_f32_16x16x32_bf16 v[32:35], v[88:91], v[168:171], v[32:35]\n\t"
        "ds_read_b128 v[84:87], %[pb1] offset:41984\n\tv_mfma_f32_16x16x32_bf16 v[36:39], v[88:91], v[172:175], v[36:39]\n\t"
        "buffer_load_dwordx4 v[220:223], off, %[rt], %[sa] offset:1024 sc1\n\ts_add_u32 %[sa], %[sa], 0xffe10000\n\t"
        "v_mfma_f32_16x16x32_bf16 v[40:43], v[92:95], v[168:171], v[40:43]\n\tds_read_b128 v[88:91], %[pb1] offset:43008\n\t"
        "v_mfma_f32_16x16x32_bf16 v[44:47], v[92:95], v[172:175], v[44:47]\n\ts_waitcnt lgkmcnt(3)\n\t"
        "v_mfma_f32_16x16x32_bf16 v[48:51], v[168:171], v[64:67], v[48:51]\n\tds_read_b128 v[92:95], %[pb1] offset:44032\n\t"
        "v_mfma_f32_16x16x32_bf16 v[52:55], v[172:175], v[64:67], v[52:55]\n\tv_mfma_f32_16x16x32_bf16 v[56:59], v[168:171], v[68:71], v[56:59]\n\t"
        "ds_read_b128 v[64:67], %[pb0] offset:61440\n\tv_mfma_f32_16x16x32_bf16 v[60:63], v[172:175], v[68:71], v[60:63]\n\ts_waitcnt vmcnt(10)\n\t"
        "v_mfma_f32_16x16x32_bf16 v[96:99], v[72:75], v[176:179], v[96:99]\n\tds_read_b128 v[68:71], %[pb0] offset:62464\n\t"
        "buffer_load_dwordx4 v[224:227], off, %[rt], %[sa] offset:0 sc1\n\tv_mfma_f32_16x16x32_bf16 v[100:103], v[72:75], v[180:183], v[100:103]\n\t"
        "v_mfma_f32_16x16x32_bf16 v[104:107], v[76:79], v[176:179], v[104:107]\n\tds_read_b128 v[72:75], %[pb0] offset:63488\n\t"
        "v_mfma_f32_16x16x32_bf16 v[108:111], v[76:79], v[180:183], v[108:111]\n\ts_waitcnt lgkmcnt(3)\n\t"
        "v_mfma_f32_16x16x32_bf16 v[112:115], v[80:83], v[176:179], v[112:115]\n\tds_read_b128 v[76:79], %[pb0] offset:64512\n\t"
        "v_mfma_f32_16x16x32_bf16 v[116:119], v[80:83], v[180:183], v[116:119]\n\tbuffer_load_dwordx4 v[228:231], off, %[rt], %[sa] offset:1024 sc1\n\t"
        "s_add_u32 %[sa], %[sa], 0x1f2000\n\tv_mfma_f32_16x16x32_bf16 v[120:123], v[84:87], v[176:179], v[120:123]\n\t"
        "ds_read_b128 v[80:83], %[pb1] offset:0\n\tv_mfma_f32_16x16x32_bf16 v[124:127], v[84:87], v[180:183], v[124:127]\n\t"
        "v_mfma_f32_16x16x32_bf16 v[128:131], v[176:179], v[88:91], v[128:131]\n\tds_read_b128 v[84:87], %[pb1] offset:1024\n\t"
        "v_mfma_f32_16x16x32_bf16 v[132:135], v[180:183], v[88:91], v[132:135]\n\tv_mfma_f32_16x16x32_bf16 v[136:139], v[176:179], v[92:95], v[136:139]\n\t"
        "ds_read_b128 v[88:91], %[pb1] offset:45056\n\tv_mfma_f32_16x16x32_bf16 v[140:143], v[180:183], v[92:95], v[140:143]\n\t"
        "s_waitcnt vmcnt(10) lgkmcnt(3)\n\tv_mfma_f32_16x16x32_bf16 v[16:19], v[64:67], v[184:187], v[16:19]\n\tds_read_b128 v[92:95], %[pb1] offset:46080\n\t"
        "buffer_load_dwordx4 v[232:235], off, %[rt], %[sa] offset:0 sc1\n\tv_mfma_f32_16x16x32_bf16 v[20:23], v[64:67], v[188:191], v[20:23]\n\t"
        "v_mfma_f32_16x16x32_bf16 v[24:27], v[68:71], v[184:187], v[24:27]\n\tds_read_b128 v[64:67], %[pb1] offset:47104\n\t"
        "v_mfma_f32_16x16x32_bf16 v[28:31], v[68:71], v[188:191], v[28:31]\n\tv_mfma_f32_16x16x32_bf16 v[32:35], v[72:75], v[184:187], v[32:35]\n\t"
        "ds_read_b128 v[68:71], %[pb1] offset:48128\n\tv_mfma_f32_16x16x32_bf16 v[36:39], v[72:75], v[188:191], v[36:39]\n\t"
        "buffer_load_dwordx4 v[236:239], off, %[rt], %[sa] offset:1024 sc1\n\ts_add_u32 %[sa], %[sa], 0xffe10000\n\t"
        "v_mfma_f32_16x16x32_bf16 v[40:43], v[76:79], v[184:187], v[40:43]\n\tds_read_b128 v[72:75], %[pb1] offset:49152\n\t"
        "v_mfma_f32_16x16x32_bf16 v[44:47], v[76:79], v[188:191], v[44:47]\n\ts_waitcnt lgkmcnt(3)\n\t"
        "v_mfma_f32_16x16x32_bf16 v[48:51], v[184:187], v[80:83], v[48:51]\n\t.p2align 12,,12\n\t.Lpg1:\n\ts_cbranch_execz .Lpgret\n\t"
        "ds_read_b128 v[76:79], %[pb1] offset:50176\n\tv_mfma_f32_16x16x32_bf16 v[52:55], v[188:191], v[80:83], v[52:55]\n\t"
        "v_mfma_f32_16x16x32_bf16 v[56:59], v[184:187], v[84:87], v[56:59]\n\tds_read_b128 v[80:83], %[pb1] offset:2048\n\t"
        "v_mfma_f32_16x16x32_bf16 v[60:63], v[188:191], v[84:87], v[60:63]\n\ts_waitcnt vmcnt(10)\n\t"
        "v_mfma_f32_16x16x32_bf16 v[96:99], v[88:91], v[192:195], v[96:99]\n\tds_read_b128 v[84:87], %[pb1] offset:3072\n\t"
        "buffer_load_dwordx4 v[240:243], off, %[rt], %[sa] offset:0 sc1\n\tv_mfma_f32_16x16x32_bf16 v[100:103], v[88:91], v[196:199], v[100:103]\n\t"
        "v_mfma_f32_16x16x32_bf16 v[104:107], v[92:95], v[192:195], v[104:107]\n\tds_read_b128 v[88:91], %[pb1] offset:4096\n\t"
        "v_mfma_f32_16x16x32_bf16 v[108:111], v[92:95], v[196:199], v[108:111]\n\ts_waitcnt lgkmcnt(3)\n\t"
        "v_mfma_f32_16x16x32_bf16 v[112:115], v[64:67], v[192:195], v[112:115]\n\tds_read_b128 v[92:95], %[pb1] offset:5120\n\t"
        "v_mfma_f32_16x16x32_bf16 v[116:119], v[64:67], v[196:199], v[116:119]\n\tbuffer_load_dwordx4 v[244:247], off, %[rt], %[sa] offset:1024 sc1\n\t"
        "s_add_u32 %[sa], %[sa], 0x1f2000\n\tv_mfma_f32_16x16x32_bf16 v[120:123], v[68:71], v[192:195], v[120:123]\n\t"
        "ds_read_b128 v[64:67], %[pb1] offset:6144\n\tv_mfma_f32_16x16x32_bf16 v[124:127], v[68:71], v[196:199], v[124:127]\n\t"
        "v_mfma_f32_16x16x32_bf16 v[128:131], v[192:195], v[72:75], v[128:131]\n\tds_read_b128 v[68:71], %[pb1] offset:7168\n\t"
        "v_mfma_f32_16x16x32_bf16 v[132:135], v[196:199], v[72:75], v[132:135]\n\tv_mfma_f32_16x16x32_bf16 v[136:139], v[192:195], v[76:79], v[136:139]\n\t"
        "ds_read_b128 v[72:75], %[pb1] offset:51200\n\tv_mfma_f32_16x16x32_bf16 v[140:143], v[196:199], v[76:79], v[140:143]\n\t"
        "s_waitcnt vmcnt(10) lgkmcnt(3)\n\tv_mfma_f32_16x16x32_bf16 v[16:19], v[80:83], v[200:203], v[16:19]\n\tds_read_b128 v[76:79], %[pb1] offset:52224\n\t"
        "buffer_load_dwordx4 v[152:155], off, %[rt], %[sa] offset:0 sc1\n\tv_mfma_f32_16x16x32_bf16 v[20:23], v[80:83], v[204:207], v[20:23]\n\t"
        "v_mfma_f32_16x16x32_bf16 v[24:27], v[84:87], v[200:203], v[24:27]\n\tds_read_b128 v[80:83], %[pb1] offset:53248\n\t"
        "v_mfma_f32_16x16x32_bf16 v[28:31], v[84:87], v[204:207], v[28:31]\n\tv_mfma_f32_16x16x32_bf16 v[32:35], v[88:91], v[200:203], v[32:35]\n\t"
        "ds_read_b128 v[84:87], %[pb1] offset:54272\n\tv_mfma_f32_16x16x32_bf16 v[36:39], v[88:91], v[204:207], v[36:39]\n\t"
        "buffer_load_dwordx4 v[156:159], off, %[rt], %[sa] offset:1024 sc1\n\ts_add_u32 %[sa], %[sa], 0xffe10000\n\t"
        "v_mfma_f32_16x16x32_bf16 v[40:43], v[92:95], v[200:203], v[40:43]\n\tds_read_b128 v[88:91], %[pb1] offset:55296\n\t"
        "v_mfma_f32_16x16x32_bf16 v[44:47], v[92:95], v[204:207], v[44:47]\n\ts_waitcnt lgkmcnt(3)\n\t"
        "v_mfma_f32_16x16x32_bf16 v[48:51], v[200:203], v[64:67], v[48:51]\n\tds_read_b128 v[92:95], %[pb1] offset:56320\n\t"
        "v_mfma_f32_16x16x32_bf16 v[52:55], v[204:207], v[64:67], v[52:55]\n\tv_mfma_f32_16x16x32_bf16 v[56:59], v[200:203], v[68:71], v[56:59]\n\t"
        "ds_read_b128 v[64:67], %[pb1] offset:8192\n\tv_mfma_f32_16x16x32_bf16 v[60:63], v[204:207], v[68:71], v[60:63]\n\ts_waitcnt vmcnt(10)\n\t"
        "v_mfma_f32_16x16x32_bf16 v[96:99], v[72:75], v[208:211], v[96:99]\n\tds_read_b128 v[68:71], %[pb1] offset:9216\n\t"
        "buffer_load_dwordx4 v[160:163], off, %[rt], %[sa] offset:0 sc1\n\tv_mfma_f32_16x16x32_bf16 v[100:103], v[72:75], v[212:215], v[100:103]\n\t"
        "v_mfma_f32_16x16x32_bf16 v[104:107], v[76:79], v[208:211], v[104:107]\n\tds_read_b128 v[72:75], %[pb1] offset:10240\n\t"
        "v_mfma_f32_16x16x32_bf16 v[108:111], v[76:79], v[212:215], v[108:111]\n\ts_waitcnt lgkmcnt(3)\n\t"
        "v_mfma_f32_16x16x32_bf16 v[112:115], v[80:83], v[208:211], v[112:115]\n\tds_read_b128 v[76:79], %[pb1] offset:11264\n\t"
        "v_mfma_f32_16x16x32_bf16 v[116:119], v[80:83], v[212:215], v[116:119]\n\tbuffer_load_dwordx4 v[164:167], off, %[rt], %[sa] offset:1024 sc1\n\t"
        "s_add_u32 %[sa], %[sa], 0x1f2000\n\tv_mfma_f32_16x16x32_bf16 v[120:123], v[84:87], v[208:211], v[120:123]\n\t"
        "ds_read_b128 v[80:83], %[pb1] offset:12288\n\tv_mfma_f32_16x16x32_bf16 v[124:127], v[84:87], v[212:215], v[124:127]\n\t"
        "v_mfma_f32_16x16x32_bf16 v[128:131], v[208:211], v[88:91], v[128:131]\n\tds_read_b128 v[84:87], %[pb1] offset:13312\n\t"
        "v_mfma_f32_16x16x32_bf16 v[132:135], v[212:215], v[88:91], v[132:135]\n\tv_mfma_f32_16x16x32_bf16 v[136:139], v[208:211], v[92:95], v[136:139]\n\t"
        "s_barrier\n\tds_read_b128 v[88:91], %[pb1] offset:57344\n\tv_mfma_f32_16x16x32_bf16 v[140:143], v[212:215], v[92:95], v[140:143]\n\t"
        "s_waitcnt vmcnt(10) lgkmcnt(3)\n\tv_mfma_f32_16x16x32_bf16 v[16:19], v[64:67], v[216:219], v[16:19]\n\tds_read_b128 v[92:95], %[pb1] offset:58368\n\t"
        "buffer_load_dwordx4 v[168:171], off, %[rt], %[sa] offset:0 sc1\n\tv_mfma_f32_16x16x32_bf16 v[20:23], v[64:67], v[220:223], v[20:23]\n\t"
        "v_mfma_f32_16x16x32_bf16 v[24:27], v[68:71], v[216:219], v[24:27]\n\tds_read_b128 v[64:67], %[pb1] offset:59392\n\t"
        "v_mfma_f32_16x16x32_bf16 v[28:31], v[68:71], v[220:223], v[28:31]\n\tv_mfma_f32_16x16x32_bf16 v[32:35], v[72:75], v[216:219], v[32:35]\n\t"
        "ds_read_b128 v[68:71], %[pb1] offset:60416\n\tv_mfma_f32_16x16x32_bf16 v[36:39], v[72:75], v[220:223], v[36:39]\n\t"
        "buffer_load_dwordx4 v[172:175], off, %[rt], %[sa] offset:1024 sc1\n\ts_add_u32 %[sa], %[sa], 0xffe10000\n\t"
        "v_mfma_f32_16x16x32_bf16 v[40:43], v[76:79], v[216:219], v[40:43]\n\tds_read_b128 v[72:75], %[pb1] offset:61440\n\t"
        "v_mfma_f32_16x16x32_bf16 v[44:47], v[76:79], v[220:223], v[44:47]\n\ts_waitcnt lgkmcnt(3)\n\t"
        "v_mfma_f32_16x16x32_bf16 v[48:51], v[216:219], v[80:83], v[48:51]\n\tds_read_b128 v[76:79], %[pb1] offset:62464\n\t"
        "v_mfma_f32_16x16x32_bf16 v[52:55], v[220:223], v[80:83], v[52:55]\n\tv_mfma_f32_16x16x32_bf16 v[56:59], v[216:219], v[84:87], v[56:59]\n\t"
        "ds_read_b128 v[80:83], %[pb1] offset:14336\n\tv_mfma_f32_16x16x32_bf16 v[60:63], v[220:223], v[84:87], v[60:63]\n\ts_waitcnt vmcnt(10)\n\t"
        "v_mfma_f32_16x16x32_bf16 v[96:99], v[88:91], v[224:227], v[96:99]\n\tds_read_b128 v[84:87], %[pb1] offset:15360\n\t"
        "buffer_load_dwordx4 v[176:179], off, %[rt], %[sa] offset:0 sc1\n\tv_mfma_f32_16x16x32_bf16 v[100:103], v[88:91], v[228:231], v[100:103]\n\t"
        "v_mfma_f32_16x16x32_bf16 v[104:107], v[92:95], v[224:227], v[104:107]\n\tds_read_b128 v[88:91], %[pb1] offset:16384\n\t"
        "v_mfma_f32_16x16x32_bf16 v[108:111], v[92:95], v[228:231], v[108:111]\n\ts_waitcnt lgkmcnt(3)\n\t"
        "v_mfma_f32_16x16x32_bf16 v[112:115], v[64:67], v[224:227], v[112:115]\n\tds_read_b128 v[92:95], %[pb1] offset:17408\n\t"
        "v_mfma_f32_16x16x32_bf16 v[116:119], v[64:67], v[228:231], v[116:119]\n\tbuffer_load_dwordx4 v[180:183], off, %[rt], %[sa] offset:1024 sc1\n\t"
        "s_add_u32 %[sa], %[sa], 0x2000\n\tv_mfma_f32_16x16x32_bf16 v[120:123], v[68:71], v[224:227], v[120:123]\n\t"
        "ds_read_b128 v[64:67], %[pb1] offset:18432\n\tv_mfma_f32_16x16x32_bf16 v[124:127], v[68:71], v[228:231], v[124:127]\n\t"
        "v_mfma_f32_16x16x32_bf16 v[128:131], v[224:227], v[72:75], v[128:131]\n\tds_read_b128 v[68:71], %[pb1] offset:19456\n\t"
        "v_mfma_f32_16x16x32_bf16 v[132:135], v[228:231], v[72:75], v[132:135]\n\tv_mfma_f32_16x16x32_bf16 v[136:139], v[224:227], v[76:79], v[136:139]\n\t"
        "ds_read_b128 v[72:75], %[pb1] offset:63488\n\tv_mfma_f32_16x16x32_bf16 v[140:143], v[228:231], v[76:79], v[140:143]\n\t"
        "s_waitcnt vmcnt(10) lgkmcnt(3)\n\tv_mfma_f32_16x16x32_bf16 v[16:19], v[80:83], v[232:235], v[16:19]\n\tds_read_b128 v[76:79], %[pb1] offset:64512\n\t"
        "buffer_load_dwordx4 v[184:187], off, %[rt], %[sa] offset:0 sc1\n\tv_mfma_f32_16x16x32_bf16 v[20:23], v[80:83], v[236:239], v[20:23]\n\t"
        "v_mfma_f32_16x16x32_bf16 v[24:27], v[84:87], v[232:235], v[24:27]\n\tds_read_b128 v[80:83], %[pb2] offset:0\n\t"
        "v_mfma_f32_16x16x32_bf16 v[28:31], v[84:87], v[236:239], v[28:31]\n\tv_mfma_f32_16x16x32_bf16 v[32:35], v[88:91], v[232:235], v[32:35]\n\t"
        "ds_read_b128 v[84:87], %[pb2] offset:1024\n\tv_mfma_f32_16x16x32_bf16 v[36:39], v[88:91], v[236:239], v[36:39]\n\t"
        "buffer_load_dwordx4 v[188:191], off, %[rt], %[sa] offset:1024 sc1\n\ts_add_u32 %[sa], %[sa], 0x2000\n\t"
        "v_mfma_f32_16x16x32_bf16 v[40:43], v[92:95], v[232:235], v[40:43]\n\tds_read_b128 v[88:91], %[pb2] offset:2048\n\t"
        "v_mfma_f32_16x16x32_bf16 v[44:47], v[92:95], v[236:239], v[44:47]\n\ts_waitcnt lgkmcnt(3)\n\t"
        "v_mfma_f32_16x16x32_bf16 v[48:51], v[232:235], v[64:67], v[48:51]\n\tds_read_b128 v[92:95], %[pb2] offset:3072\n\t"
        "v_mfma_f32_16x16x32_bf16 v[52:55], v[236:239], v[64:67], v[52:55]\n\tv_mfma_f32_16x16x32_bf16 v[56:59], v[232:235], v[68:71], v[56:59]\n\t"
        "ds_read_b128 v[64:67], %[pb1] offset:20480\n\tv_mfma_f32_16x16x32_bf16 v[60:63], v[236:239], v[68:71], v[60:63]\n\ts_waitcnt vmcnt(10)\n\t"
        "v_mfma_f32_16x16x32_bf16 v[96:99], v[72:75], v[240:243], v[96:99]\n\tds_read_b128 v[68:71], %[pb1] offset:21504\n\t"
        "buffer_load_dwordx4 v[192:195], off, %[rt], %[sa] offset:0 sc1\n\tv_mfma_f32_16x16x32_bf16 v[100:103], v[72:75], v[244:247], v[100:103]\n\t"
        "v_mfma_f32_16x16x32_bf16 v[104:107], v[76:79], v[240:243], v[104:107]\n\tds_read_b128 v[72:75], %[pb1] offset:22528\n\t"
        "v_mfma_f32_16x16x32_bf16 v[108:111], v[76:79], v[244:247], v[108:111]\n\ts_waitcnt lgkmcnt(3)\n\t"
        "v_mfma_f32_16x16x32_bf16 v[112:115], v[80:83], v[240:243], v[112:115]\n\tds_read_b128 v[76:79], %[pb1] offset:23552\n\t"
        "v_mfma_f32_16x16x32_bf16 v[116:119], v[80:83], v[244:247], v[116:119]\n\tbuffer_load_dwordx4 v[196:199], off, %[rt], %[sa] offset:1024 sc1\n\t"
        "s_add_u32 %[sa], %[sa], 0x2000\n\tv_mfma_f32_16x16x32_bf16 v[120:123], v[84:87], v[240:243], v[120:123]\n\t"
        "ds_read_b128 v[80:83], %[pb1] offset:24576\n\tv_mfma_f32_16x16x32_bf16 v[124:127], v[84:87], v[244:247], v[124:127]\n\t"
        "v_mfma_f32_16x16x32_bf16 v[128:131], v[240:243], v[88:91], v[128:131]\n\tds_read_b128 v[84:87], %[pb1] offset:25600\n\t"
        "v_mfma_f32_16x16x32_bf16 v[132:135], v[244:247], v[88:91], v[132:135]\n\tv_mfma_f32_16x16x32_bf16 v[136:139], v[240:243], v[92:95], v[136:139]\n\t"
        "ds_read_b128 v[88:91], %[pb2] offset:4096\n\tv_mfma_f32_16x16x32_bf16 v[140:143], v[244:247], v[92:95], v[140:143]\n\t"
        "s_waitcnt vmcnt(10) lgkmcnt(3)\n\tv_mfma_f32_16x16x32_bf16 v[16:19], v[64:67], v[152:155], v[16:19]\n\tds_read_b128 v[92:95], %[pb2] offset:5120\n\t"
        "buffer_load_dwordx4 v[200:203], off, %[rt], %[sa] offset:0 sc1\n\tv_mfma_f32_16x16x32_bf16 v[20:23], v[64:67], v[156:159], v[20:23]\n\t"
        "v_mfma_f32_16x16x32_bf16 v[24:27], v[68:71], v[152:155], v[24:27]\n\tds_read_b128 v[64:67], %[pb2] offset:6144\n\t"
        "v_mfma_f32_16x16x32_bf16 v[28:31], v[68:71], v[156:159], v[28:31]\n\tv_mfma_f32_16x16x32_bf16 v[32:35], v[72:75], v[152:155], v[32:35]\n\t"
        "ds_read_b128 v[68:71], %[pb2] offset:7168\n\tv_mfma_f32_16x16x32_bf16 v[36:39], v[72:75], v[156:159], v[36:39]\n\t"
        "buffer_load_dwordx4 v[204:207], off, %[rt], %[sa] offset:1024 sc1\n\ts_add_u32 %[sa], %[sa], 0x2000\n\t"
        "v_mfma_f32_16x16x32_bf16 v[40:43], v[76:79], v[152:155], v[40:43]\n\tds_read_b128 v[72:75], %[pb2] offset:8192\n\t"
        "v_mfma_f32_16x16x32_bf16 v[44:47], v[76:79], v[156:159], v[44:47]\n\ts_waitcnt lgkmcnt(3)\n\t"
        "v_mfma_f32_16x16x32_bf16 v[48:51], v[152:155], v[80:83], v[48:51]\n\tds_read_b128 v[76:79], %[pb2] offset:9216\n\t"
        "v_mfma_f32_16x16x32_bf16 v[52:55], v[156:159], v[80:83], v[52:55]\n\tv_mfma_f32_16x16x32_bf16 v[56:59], v[152:155], v[84:87], v[56:59]\n\t"
        "ds_read_b128 v[80:83], %[pb1] offset:26624\n\tv_mfma_f32_16x16x32_bf16 v[60:63], v[156:159], v[84:87], v[60:63]\n\ts_waitcnt vmcnt(10)\n\t"
        "v_mfma_f32_16x16x32_bf16 v[96:99], v[88:91], v[160:163], v[96:99]\n\tds_read_b128 v[84:87], %[pb1] offset:27648\n\t"
        "buffer_load_dwordx4 v[208:211], off, %[rt], %[sa] offset:0 sc1\n\tv_mfma_f32_16x16x32_bf16 v[100:103], v[88:91], v[164:167], v[100:103]\n\t"
        "v_mfma_f32_16x16x32_bf16 v[104:107], v[92:95], v[160:163], v[104:107]\n\tds_read_b128 v[88:91], %[pb1] offset:28672\n\t"
        "v_mfma_f32_16x16x32_bf16 v[108:111], v[92:95], v[164:167], v[108:111]\n\ts_waitcnt lgkmcnt(3)\n\t"
        "v_mfma_f32_16x16x32_bf16 v[112:115], v[64:67], v[160:163], v[112:115]\n\tds_read_b128 v[92:95], %[pb1] offset:29696\n\t"
        "v_mfma_f32_16x16x32_bf16 v[116:119], v[64:67], v[164:167], v[116:119]\n\tbuffer_load_dwordx4 v[212:215], off, %[rt], %[sa] offset:1024 sc1\n\t"
        "s_add_u32 %[sa], %[sa], 0x2000\n\tv_mfma_f32_16x16x32_bf16 v[120:123], v[68:71], v[160:163], v[120:123]\n\t"
        "ds_read_b128 v[64:67], %[pb1] offset:30720\n\tv_mfma_f32_16x16x32_bf16 v[124:127], v[68:71], v[164:167], v[124:127]\n\t"
        "v_mfma_f32_16x16x32_bf16 v[128:131], v[160:163], v[72:75], v[128:131]\n\tds_read_b128 v[68:71], %[pb1] offset:31744\n\t"
        "v_mfma_f32_16x16x32_bf16 v[132:135], v[164:167], v[72:75], v[132:135]\n\tv_mfma_f32_16x16x32_bf16 v[136:139], v[160:163], v[76:79], v[136:139]\n\t"
        "ds_read_b128 v[72:75], %[pb2] offset:10240\n\tv_mfma_f32_16x16x32_bf16 v[140:143], v[164:167], v[76:79], v[140:143]\n\t"
        "s_waitcnt vmcnt(10) lgkmcnt(3)\n\tv_mfma_f32_16x16x32_bf16 v[16:19], v[80:83], v[168:171], v[16:19]\n\tds_read_b128 v[76:79], %[pb2] offset:11264\n\t"
        "buffer_load_dwordx4 v[216:219], off, %[rt], %[sa] offset:0 sc1\n\tv_mfma_f32_16x16x32_bf16 v[20:23], v[80:83], v[172:175], v[20:23]\n\t"
        "v_mfma_f32_16x16x32_bf16 v[24:27], v[84:87], v[168:171], v[24:27]\n\tds_read_b128 v[80:83], %[pb2] offset:12288\n\t"
        "v_mfma_f32_16x16x32_bf16 v[28:31], v[84:87], v[172:175], v[28:31]\n\tv_mfma_f32_16x16x32_bf16 v[32:35], v[88:91], v[168:171], v[32:35]\n\t"
        "ds_read_b128 v[84:87], %[pb2] offset:13312\n\tv_mfma_f32_16x16x32_bf16 v[36:39], v[88:91], v[172:175], v[36:39]\n\t"
        "buffer_load_dwordx4 v[220:223], off, %[rt], %[sa] offset:1024 sc1\n\ts_add_u32 %[sa], %[sa], 0x2000\n\t"
        "v_mfma_f32_16x16x32_bf16 v[40:43], v[92:95], v[168:171], v[40:43]\n\tds_read_b128 v[88:91], %[pb2] offset:14336\n\t"
        "v_mfma_f32_16x16x32_bf16 v[44:47], v[92:95], v[172:175], v[44:47]\n\ts_waitcnt lgkmcnt(3)\n\t"
        "v_mfma_f32_16x16x32_bf16 v[48:51], v[168:171], v[64:67], v[48:51]\n\tds_read_b128 v[92:95], %[pb2] offset:15360\n\t"
        "v_mfma_f32_16x16x32_bf16 v[52:55], v[172:175], v[64:67], v[52:55]\n\tv_mfma_f32_16x16x32_bf16 v[56:59], v[168:171], v[68:71], v[56:59]\n\t"
        "s_barrier\n\tds_read_b128 v[64:67], %[pb0] offset:0\n\tv_mfma_f32_16x16x32_bf16 v[60:63], v[172:175], v[68:71], v[60:63]\n\ts_waitcnt vmcnt(10)\n\t"
        "v_mfma_f32_16x16x32_bf16 v[96:99], v[72:75], v[176:179], v[96:99]\n\tds_read_b128 v[68:71], %[pb0] offset:1024\n\t"
        "buffer_load_dwordx4 v[224:227], off, %[rt], %[sa] offset:0 sc1\n\tv_mfma_f32_16x16x32_bf16 v[100:103], v[72:75], v[180:183], v[100:103]\n\t"
        "v_mfma_f32_16x16x32_bf16 v[104:107], v[76:79], v[176:179], v[104:107]\n\tds_read_b128 v[72:75], %[pb0] offset:2048\n\t"
        "v_mfma_f32_16x16x32_bf16 v[108:111], v[76:79], v[180:183], v[108:111]\n\ts_waitcnt lgkmcnt(3)\n\t"
        "v_mfma_f32_16x16x32_bf16 v[112:115], v[80:83], v[176:179], v[112:115]\n\tds_read_b128 v[76:79], %[pb0] offset:3072\n\t"
        "v_mfma_f32_16x16x32_bf16 v[116:119], v[80:83], v[180:183], v[116:119]\n\tbuffer_load_dwordx4 v[228:231], off, %[rt], %[sa] offset:1024 sc1\n\t"
        "s_add_u32 %[sa], %[sa], 0x2000\n\tv_mfma_f32_16x16x32_bf16 v[120:123], v[84:87], v[176:179], v[120:123]\n\t"
        "ds_read_b128 v[80:83], %[pb0] offset:4096\n\tv_mfma_f32_16x16x32_bf16 v[124:127], v[84:87], v[180:183], v[124:127]\n\t"
        "v_mfma_f32_16x16x32_bf16 v[128:131], v[176:179], v[88:91], v[128:131]\n\tds_read_b128 v[84:87], %[pb0] offset:5120\n\t"
        "v_mfma_f32_16x16x32_bf16 v[132:135], v[180:183], v[88:91], v[132:135]\n\tv_mfma_f32_16x16x32_bf16 v[136:139], v[176:179], v[92:95], v[136:139]\n\t"
        "ds_read_b128 v[88:91], %[pb0] offset:6144\n\tv_mfma_f32_16x16x32_bf16 v[140:143], v[180:183], v[92:95], v[140:143]\n\t"
        "s_waitcnt vmcnt(10) lgkmcnt(3)\n\tv_mfma_f32_16x16x32_bf16 v[96:99], v[64:67], v[184:187], v[96:99]\n\tds_read_b128 v[92:95], %[pb0] offset:7168\n\t"
        "buffer_load_dwordx4 v[232:235], off, %[rt], %[sa] offset:0 sc1\n\tv_mfma_f32_16x16x32_bf16 v[100:103], v[64:67], v[188:191], v[100:103]\n\t"
        "v_mfma_f32_16x16x32_bf16 v[104:107], v[68:71], v[184:187], v[104:107]\n\tds_read_b128 v[64:67], %[pb0] offset:8192\n\t"
        "v_mfma_f32_16x16x32_bf16 v[108:111], v[68:71], v[188:191], v[108:111]\n\tv_mfma_f32_16x16x32_bf16 v[112:115], v[72:75], v[184:187], v[112:115]\n\t"
        "ds_read_b128 v[68:71], %[pb0] offset:9216\n\tv_mfma_f32_16x16x32_bf16 v[116:119], v[72:75], v[188:191], v[116:119]\n\t"
        "buffer_load_dwordx4 v[236:239], off, %[rt], %[sa] offset:1024 sc1\n\ts_add_u32 %[sa], %[sa], 0x2000\n\t"
        "v_mfma_f32_16x16x32_bf16 v[120:123], v[76:79], v[184:187], v[120:123]\n\tds_read_b128 v[72:75], %[pb0] offset:10240\n\t"
        "v_mfma_f32_16x16x32_bf16 v[124:127], v[76:79], v[188:191], v[124:127]\n\ts_waitcnt lgkmcnt(3)\n\t"
        "v_mfma_f32_16x16x32_bf16 v[128:131], v[184:187], v[80:83], v[128:131]\n\tds_read_b128 v[76:79], %[pb0] offset:11264\n\t"
        "v_mfma_f32_16x16x32_bf16 v[132:135], v[188:191], v[80:83], v[132:135]\n\tv_mfma_f32_16x16x32_bf16 v[136:139], v[184:187], v[84:87], v[136:139]\n\t"
        "ds_read_b128 v[80:83], %[pb0] offset:12288\n\tv_mfma_f32_16x16x32_bf16 v[140:143], v[188:191], v[84:87], v[140:143]\n\ts_waitcnt vmcnt(10)\n\t"
        "v_mfma_f32_16x16x32_bf16 v[96:99], v[88:91], v[192:195], v[96:99]\n\tds_read_b128 v[84:87], %[pb0] offset:13312\n\t"
        "buffer_load_dwordx4 v[240:243], off, %[rt], %[sa] offset:0 sc1\n\tv_mfma_f32_16x16x32_bf16 v[100:103], v[88:91], v[196:199], v[100:103]\n\t"
        "v_mfma_f32_16x16x32_bf16 v[104:107], v[92:95], v[192:195], v[104:107]\n\tds_read_b128 v[88:91], %[pb0] offset:14336\n\t"
        "v_mfma_f32_16x16x32_bf16 v[108:111], v[92:95], v[196:199], v[108:111]\n\ts_waitcnt lgkmcnt(3)\n\t"
        "v_mfma_f32_16x16x32_bf16 v[112:115], v[64:67], v[192:195], v[112:115]\n\tds_read_b128 v[92:95], %[pb0] offset:15360\n\t"
        "v_mfma_f32_16x16x32_bf16 v[116:119], v[64:67], v[196:199], v[116:119]\n\tbuffer_load_dwordx4 v[244:247], off, %[rt], %[sa] offset:1024 sc1\n\t"
        "v_mfma_f32_16x16x32_bf16 v[120:123], v[68:71], v[192:195], v[120:123]\n\tds_read_b128 v[64:67], %[pb0] offset:16384\n\t"
        "v_mfma_f32_16x16x32_bf16 v[124:127], v[68:71], v[196:199], v[124:127]\n\tv_mfma_f32_16x16x32_bf16 v[128:131], v[192:195], v[72:75], v[128:131]\n\t"
        "ds_read_b128 v[68:71], %[pb0] offset:17408\n\tv_mfma_f32_16x16x32_bf16 v[132:135], v[196:199], v[72:75], v[132:135]\n\t"
        "v_mfma_f32_16x16x32_bf16 v[136:139], v[192:195], v[76:79], v[136:139]\n\tds_read_b128 v[72:75], %[pb0] offset:18432\n\t"
        "v_mfma_f32_16x16x32_bf16 v[140:143], v[196:199], v[76:79], v[140:143]\n\ts_waitcnt vmcnt(10) lgkmcnt(3)\n\t"
        "v_mfma_f32_16x16x32_bf16 v[96:99], v[80:83], v[200:203], v[96:99]\n\tds_read_b128 v[76:79], %[pb0] offset:19456\n\t"
        "v_mfma_f32_16x16x32_bf16 v[100:103], v[80:83], v[204:207], v[100:103]\n\tv_mfma_f32_16x16x32_bf16 v[104:107], v[84:87], v[200:203], v[104:107]\n\t"
        "ds_read_b128 v[80:83], %[pb0] offset:20480\n\tv_mfma_f32_16x16x32_bf16 v[108:111], v[84:87], v[204:207], v[108:111]\n\t"
        "v_mfma_f32_16x16x32_bf16 v[112:115], v[88:91], v[200:203], v[112:115]\n\tds_read_b128 v[84:87], %[pb0] offset:21504\n\t"
        "v_mfma_f32_16x16x32_bf16 v[116:119], v[88:91], v[204:207], v[116:119]\n\tv_mfma_f32_16x16x32_bf16 v[120:123], v[92:95], v[200:203], v[120:123]\n\t"
        "ds_read_b128 v[88:91], %[pb0] offset:22528\n\tv_mfma_f32_16x16x32_bf16 v[124:127], v[92:95], v[204:207], v[124:127]\n\ts_waitcnt lgkmcnt(3)\n\t"
        "v_mfma_f32_16x16x32_bf16 v[128:131], v[200:203], v[64:67], v[128:131]\n\tds_read_b128 v[92:95], %[pb0] offset:23552\n\t"
        "v_mfma_f32_16x16x32_bf16 v[132:135], v[204:207], v[64:67], v[132:135]\n\tv_mfma_f32_16x16x32_bf16 v[136:139], v[200:203], v[68:71], v[136:139]\n\t"
        "s_barrier\n\tds_read_b128 v[64:67], %[pb0] offset:24576\n\tv_mfma_f32_16x16x32_bf16 v[140:143], v[204:207], v[68:71], v[140:143]\n\t"
        "s_waitcnt vmcnt(8)\n\tv_mfma_f32_16x16x32_bf16 v[96:99], v[72:75], v[208:211], v[96:99]\n\tds_read_b128 v[68:71], %[pb0] offset:25600\n\t"
        "v_mfma_f32_16x16x32_bf16 v[100:103], v[72:75], v[212:215], v[100:103]\n\tv_mfma_f32_16x16x32_bf16 v[104:107], v[76:79], v[208:211], v[104:107]\n\t"
        "ds_read_b128 v[72:75], %[pb0] offset:26624\n\tv_mfma_f32_16x16x32_bf16 v[108:111], v[76:79], v[212:215], v[108:111]\n\ts_waitcnt lgkmcnt(3)\n\t"
        "v_mfma_f32_16x16x32_bf16 v[112:115], v[80:83], v[208:211], v[112:115]\n\tds_read_b128 v[76:79], %[pb0] offset:27648\n\t"
        "v_mfma_f32_16x16x32_bf16 v[116:119], v[80:83], v[212:215], v[116:119]\n\tv_mfma_f32_16x16x32_bf16 v[120:123], v[84:87], v[208:211], v[120:123]\n\t"
        "ds_read_b128 v[80:83], %[pb0] offset:28672\n\tv_mfma_f32_16x16x32_bf16 v[124:127], v[84:87], v[212:215], v[124:127]\n\t"
        "v_mfma_f32_16x16x32_bf16 v[128:131], v[208:211], v[88:91], v[128:131]\n\tds_read_b128 v[84:87], %[pb0] offset:29696\n\t"
        "v_mfma_f32_16x16x32_bf16 v[132:135], v[212:215], v[88:91], v[132:135]\n\tv_mfma_f32_16x16x32_bf16 v[136:139], v[208:211], v[92:95], v[136:139]\n\t"
        "ds_read_b128 v[88:91], %[pb0] offset:30720\n\tv_mfma_f32_16x16x32_bf16 v[140:143], v[212:215], v[92:95], v[140:143]\n\t"
        "s_waitcnt vmcnt(6) lgkmcnt(3)\n\tv_mfma_f32_16x16x32_bf16 v[96:99], v[64:67], v[216:219], v[96:99]\n\tds_read_b128 v[92:95], %[pb0] offset:31744\n\t"
        "v_mfma_f32_16x16x32_bf16 v[100:103], v[64:67], v[220:223], v[100:103]\n\tv_mfma_f32_16x16x32_bf16 v[104:107], v[68:71], v[216:219], v[104:107]\n\t"
        "ds_read_b128 v[64:67], %[pb0] offset:32768\n\tv_mfma_f32_16x16x32_bf16 v[108:111], v[68:71], v[220:223], v[108:111]\n\t"
        "v_mfma_f32_16x16x32_bf16 v[112:115], v[72:75], v[216:219], v[112:115]\n\tds_read_b128 v[68:71], %[pb0] offset:33792\n\t"
        "v_mfma_f32_16x16x32_bf16 v[116:119], v[72:75], v[220:223], v[116:119]\n\tv_mfma_f32_16x16x32_bf16 v[120:123], v[76:79], v[216:219], v[120:123]\n\t"
        "ds_read_b128 v[72:75], %[pb0] offset:34816\n\tv_mfma_f32_16x16x32_bf16 v[124:127], v[76:79], v[220:223], v[124:127]\n\ts_waitcnt lgkmcnt(3)\n\t"
        "v_mfma_f32_16x16x32_bf16 v[128:131], v[216:219], v[80:83], v[128:131]\n\tds_read_b128 v[76:79], %[pb0] offset:35840\n\t"
        "v_mfma_f32_16x16x32_bf16 v[132:135], v[220:223], v[80:83], v[132:135]\n\tv_mfma_f32_16x16x32_bf16 v[136:139], v[216:219], v[84:87], v[136:139]\n\t"
        "ds_read_b128 v[80:83], %[pb0] offset:36864\n\tv_mfma_f32_16x16x32_bf16 v[140:143], v[220:223], v[84:87], v[140:143]\n\ts_waitcnt vmcnt(4)\n\t"
        "v_mfma_f32_16x16x32_bf16 v[96:99], v[88:91], v[224:227], v[96:99]\n\tds_read_b128 v[84:87], %[pb0] offset:37888\n\t"
        "v_mfma_f32_16x16x32_bf16 v[100:103], v[88:91], v[228:231], v[100:103]\n\tv_mfma_f32_16x16x32_bf16 v[104:107], v[92:95], v[224:227], v[104:107]\n\t"
        "ds_read_b128 v[88:91], %[pb0] offset:38912\n\tv_mfma_f32_16x16x32_bf16 v[108:111], v[92:95], v[228:231], v[108:111]\n\ts_waitcnt lgkmcnt(3)\n\t"
        "v_mfma_f32_16x16x32_bf16 v[112:115], v[64:67], v[224:227], v[112:115]\n\tds_read_b128 v[92:95], %[pb0] offset:39936\n\t"
        "v_mfma_f32_16x16x32_bf16 v[116:119], v[64:67], v[228:231], v[116:119]\n\tv_mfma_f32_16x16x32_bf16 v[120:123], v[68:71], v[224:227], v[120:123]\n\t"
        "ds_read_b128 v[64:67], %[pb0] offset:40960\n\tv_mfma_f32_16x16x32_bf16 v[124:127], v[68:71], v[228:231], v[124:127]\n\t"
        "v_mfma_f32_16x16x32_bf16 v[128:131], v[224:227], v[72:75], v[128:131]\n\tds_read_b128 v[68:71], %[pb0] offset:41984\n\t"
        "v_mfma_f32_16x16x32_bf16 v[132:135], v[228:231], v[72:75], v[132:135]\n\tv_mfma_f32_16x16x32_bf16 v[136:139], v[224:227], v[76:79], v[136:139]\n\t"
        "ds_read_b128 v[72:75], %[pb0] offset:43008\n\tv_mfma_f32_16x16x32_bf16 v[140:143], v[228:231], v[76:79], v[140:143]\n\t"
        "s_waitcnt vmcnt(2) lgkmcnt(3)\n\tv_mfma_f32_16x16x32_bf16 v[96:99], v[80:83], v[232:235], v[96:99]\n\tds_read_b128 v[76:79], %[pb0] offset:44032\n\t"
        "buffer_load_dwordx4 v[152:155], off, %[rn], %[sn] offset:0 sc1\n\tv_mfma_f32_16x16x32_bf16 v[100:103], v[80:83], v[236:239], v[100:103]\n\t"
        "v_mfma_f32_16x16x32_bf16 v[104:107], v[84:87], v[232:235], v[104:107]\n\tds_read_b128 v[80:83], %[pb0] offset:45056\n\t"
        "v_mfma_f32_16x16x32_bf16 v[108:111], v[84:87], v[236:239], v[108:111]\n\tv_mfma_f32_16x16x32_bf16 v[112:115], v[88:91], v[232:235], v[112:115]\n\t"
        "ds_read_b128 v[84:87], %[pb0] offset:46080\n\tv_subrev_u32 v10, %[w128], v253\n\t"
        "v_mfma_f32_16x16x32_bf16 v[116:119], v[88:91], v[236:239], v[116:119]\n\tbuffer_load_dwordx4 v[156:159], off, %[rn], %[sn] offset:1024 sc1\n\t"
        "s_add_u32 %[sn], %[sn], 0x2000\n\tv_mfma_f32_16x16x32_bf16 v[120:123], v[92:95], v[232:235], v[120:123]\n\t"
        "ds_read_b128 v[88:91], %[pb0] offset:47104\n\tds_read_b32 v8, v253 offset:0\n\t"
        "v_mfma_f32_16x16x32_bf16 v[124:127], v[92:95], v[236:239], v[124:127]\n\tds_read_b32 v9, v253 offset:64\n\ts_waitcnt lgkmcnt(5)\n\t"
        "v_mfma_f32_16x16x32_bf16 v[128:131], v[232:235], v[64:67], v[128:131]\n\tds_read_b128 v[92:95], %[pb0] offset:48128\n\t"
        "ds_read_b32 v2, v253 offset:1024\n\tv_mfma_f32_16x16x32_bf16 v[132:135], v[236:239], v[64:67], v[132:135]\n\tds_read_b32 v3, v253 offset:1088\n\t"
        "ds_read_b128 v[144:147], v255 offset:2176\n\tv_mfma_f32_16x16x32_bf16 v[136:139], v[232:235], v[68:71], v[136:139]\n\t"
        "ds_read_b128 v[148:151], v255 offset:2240\n\tv_mfma_f32_16x16x32_bf16 v[140:143], v[236:239], v[68:71], v[140:143]\n\ts_waitcnt vmcnt(2)\n\t"
        "v_mfma_f32_16x16x32_bf16 v[96:99], v[72:75], v[240:243], v[96:99]\n\tbuffer_load_dwordx4 v[160:163], off, %[rn], %[sn] offset:0 sc1\n\t"
        "v_mfma_f32_16x16x32_bf16 v[100:103], v[72:75], v[244:247], v[100:103]\n\tv_mfma_f32_16x16x32_bf16 v[104:107], v[76:79], v[240:243], v[104:107]\n\t"
        "v_mfma_f32_16x16x32_bf16 v[108:111], v[76:79], v[244:247], v[108:111]\n\ts_waitcnt lgkmcnt(4)\n\t"
        "v_mfma_f32_16x16x32_bf16 v[112:115], v[80:83], v[240:243], v[112:115]\n\tv_mfma_f32_16x16x32_bf16 v[116:119], v[80:83], v[244:247], v[116:119]\n\t"
        "buffer_load_dwordx4 v[164:167], off, %[rn], %[sn] offset:1024 sc1\n\ts_add_u32 %[sn], %[sn], 0x2000\n\t"
        "v_mfma_f32_16x16x32_bf16 v[120:123], v[84:87], v[240:243], v[120:123]\n\tv_mfma_f32_16x16x32_bf16 v[124:127], v[84:87], v[244:247], v[124:127]\n\t"
        "v_mfma_f32_16x16x32_bf16 v[128:131], v[240:243], v[88:91], v[128:131]\n\tv_mfma_f32_16x16x32_bf16 v[132:135], v[244:247], v[88:91], v[132:135]\n\t"
        "v_mfma_f32_16x16x32_bf16 v[136:139], v[240:243], v[92:95], v[136:139]\n\tv_mfma_f32_16x16x32_bf16 v[140:143], v[244:247], v[92:95], v[140:143]\n\t"
        "s_barrier\n\tbuffer_load_dwordx4 v[168:171], off, %[rn], %[sn] offset:0 sc1\n\tbuffer_load_dwordx4 v[172:175], off, %[rn], %[sn] offset:1024 sc1\n\t"
        "s_add_u32 %[sn], %[sn], 0x2000\n\tbuffer_load_dwordx4 v[176:179], off, %[rn], %[sn] offset:0 sc1\n\t"
        "buffer_load_dwordx4 v[180:183], off, %[rn], %[sn] offset:1024 sc1\n\ts_add_u32 %[sn], %[sn], 0x2000\n\t"
        "buffer_load_dwordx4 v[184:187], off, %[rn], %[sn] offset:0 sc1\n\tbuffer_load_dwordx4 v[188:191], off, %[rn], %[sn] offset:1024 sc1\n\t"
        "s_add_u32 %[sn], %[sn], 0x2000\n\tds_read_b128 v[64:67], v255 offset:2048\n\tds_read_b128 v[68:71], v255 offset:2112\n\t"
        "ds_read_b32 v0, v10 offset:2304\n\tds_read_b32 v1, v10 offset:2368\n\tds_read_b128 v[80:83], v254 offset:1024\n\t"
        "ds_read_b128 v[84:87], v254 offset:1088\n\tds_read_b128 v[72:75], v254 offset:0\n\tds_read_b128 v[76:79], v254 offset:64\n\ts_waitcnt lgkmcnt(8)\n\t"
        "s_nop 0\n\tv_pk_mul_f32 v[88:89], v[2:3], v[144:145] op_sel:[0,0] op_sel_hi:[0,1]\n\t"
        "v_pk_mul_f32 v[90:91], v[2:3], v[146:147] op_sel:[0,0] op_sel_hi:[0,1]\n\tv_pk_mul_f32 v[92:93], v[2:3], v[148:149] op_sel:[0,0] op_sel_hi:[0,1]\n\t"
        "v_pk_mul_f32 v[94:95], v[2:3], v[150:151] op_sel:[0,0] op_sel_hi:[0,1]\n\tv_pk_mul_f32 v[144:145], v[2:3], v[144:145] op_sel:[1,0] op_sel_hi:[1,1]\n\t"
        "v_pk_mul_f32 v[146:147], v[2:3], v[146:147] op_sel:[1,0] op_sel_hi:[1,1]\n\t"
        "v_pk_mul_f32 v[148:149], v[2:3], v[148:149] op_sel:[1,0] op_sel_hi:[1,1]\n\t"
        "v_pk_mul_f32 v[150:151], v[2:3], v[150:151] op_sel:[1,0] op_sel_hi:[1,1]\n\tv_mul_f32 v4, 0x3e8293ee, v8\n\tv_mul_f32 v5, 0x3e8293ee, v9\n\t"
        "v_mul_f32 v6, 0x3e8293ee, v2\n\tv_mul_f32 v7, 0x3e8293ee, v3\n\t"
        "v_pk_fma_f32 v[112:113], v[112:113], v[8:9], v[88:89] op_sel:[0,0,0] op_sel_hi:[1,0,1]\n\t"
        "v_pk_fma_f32 v[114:115], v[114:115], v[8:9], v[90:91] op_sel:[0,0,0] op_sel_hi:[1,0,1]\n\t"
        "v_pk_fma_f32 v[120:121], v[120:121], v[8:9], v[92:93] op_sel:[0,0,0] op_sel_hi:[1,0,1]\n\t"
        "v_pk_fma_f32 v[122:123], v[122:123], v[8:9], v[94:95] op_sel:[0,0,0] op_sel_hi:[1,0,1]\n\t"
        "v_pk_fma_f32 v[116:117], v[116:117], v[8:9], v[144:145] op_sel:[0,1,0] op_sel_hi:[1,1,1]\n\t"
        "v_pk_fma_f32 v[118:119], v[118:119], v[8:9], v[146:147] op_sel:[0,1,0] op_sel_hi:[1,1,1]\n\t"
        "v_pk_fma_f32 v[124:125], v[124:125], v[8:9], v[148:149] op_sel:[0,1,0] op_sel_hi:[1,1,1]\n\t"
        "v_pk_fma_f32 v[126:127], v[126:127], v[8:9], v[150:151] op_sel:[0,1,0] op_sel_hi:[1,1,1]\n\tv_cvt_pk_bf16_f32 v112, v112, v113\n\t"
        "v_cvt_pk_bf16_f32 v113, v114, v115\n\tv_cvt_pk_bf16_f32 v114, v120, v121\n\tv_cvt_pk_bf16_f32 v115, v122, v123\n\t"
        "buffer_store_dwordx4 v[112:115], off, %[rt], %[kv] offset:0 sc1\n\tv_cvt_pk_bf16_f32 v116, v116, v117\n\tv_cvt_pk_bf16_f32 v117, v118, v119\n\t"
        "v_cvt_pk_bf16_f32 v118, v124, v125\n\tv_cvt_pk_bf16_f32 v119, v126, v127\n\tbuffer_store_dwordx4 v[116:119], off, %[rt], %[kv] offset:1024 sc1\n\t"
        "s_waitcnt lgkmcnt(0)\n\ts_nop 0\n\tv_pk_mul_f32 v[144:145], v[6:7], v[64:65] op_sel:[0,0] op_sel_hi:[0,1]\n\t"
        "v_pk_mul_f32 v[146:147], v[6:7], v[66:67] op_sel:[0,0] op_sel_hi:[0,1]\n\tv_pk_mul_f32 v[148:149], v[6:7], v[68:69] op_sel:[0,0] op_sel_hi:[0,1]\n\t"
        "v_pk_mul_f32 v[150:151], v[6:7], v[70:71] op_sel:[0,0] op_sel_hi:[0,1]\n\tv_pk_mul_f32 v[64:65], v[6:7], v[64:65] op_sel:[1,0] op_sel_hi:[1,1]\n\t"
        "v_pk_mul_f32 v[66:67], v[6:7], v[66:67] op_sel:[1,0] op_sel_hi:[1,1]\n\tv_pk_mul_f32 v[68:69], v[6:7], v[68:69] op_sel:[1,0] op_sel_hi:[1,1]\n\t"
        "v_pk_mul_f32 v[70:71], v[6:7], v[70:71] op_sel:[1,0] op_sel_hi:[1,1]\n\tv_pk_mul_f32 v[88:89], v[0:1], v[80:81] op_sel:[0,0] op_sel_hi:[0,1]\n\t"
        "v_pk_mul_f32 v[90:91], v[0:1], v[82:83] op_sel:[0,0] op_sel_hi:[0,1]\n\tv_pk_mul_f32 v[92:93], v[0:1], v[84:85] op_sel:[0,0] op_sel_hi:[0,1]\n\t"
        "v_pk_mul_f32 v[94:95], v[0:1], v[86:87] op_sel:[0,0] op_sel_hi:[0,1]\n\tv_pk_mul_f32 v[80:81], v[0:1], v[80:81] op_sel:[1,0] op_sel_hi:[1,1]\n\t"
        "v_pk_mul_f32 v[82:83], v[0:1], v[82:83] op_sel:[1,0] op_sel_hi:[1,1]\n\tv_pk_mul_f32 v[84:85], v[0:1], v[84:85] op_sel:[1,0] op_sel_hi:[1,1]\n\t"
        "v_pk_mul_f32 v[86:87], v[0:1], v[86:87] op_sel:[1,0] op_sel_hi:[1,1]\n\t"
        "v_pk_fma_f32 v[96:97], v[96:97], v[4:5], v[144:145] op_sel:[0,0,0] op_sel_hi:[1,0,1]\n\t"
        "v_pk_fma_f32 v[98:99], v[98:99], v[4:5], v[146:147] op_sel:[0,0,0] op_sel_hi:[1,0,1]\n\t"
        "v_pk_fma_f32 v[104:105], v[104:105], v[4:5], v[148:149] op_sel:[0,0,0] op_sel_hi:[1,0,1]\n\t"
        "v_pk_fma_f32 v[106:107], v[106:107], v[4:5], v[150:151] op_sel:[0,0,0] op_sel_hi:[1,0,1]\n\t"
        "v_pk_fma_f32 v[100:101], v[100:101], v[4:5], v[64:65] op_sel:[0,1,0] op_sel_hi:[1,1,1]\n\t"
        "v_pk_fma_f32 v[102:103], v[102:103], v[4:5], v[66:67] op_sel:[0,1,0] op_sel_hi:[1,1,1]\n\t"
        "v_pk_fma_f32 v[108:109], v[108:109], v[4:5], v[68:69] op_sel:[0,1,0] op_sel_hi:[1,1,1]\n\t"
        "v_pk_fma_f32 v[110:111], v[110:111], v[4:5], v[70:71] op_sel:[0,1,0] op_sel_hi:[1,1,1]\n\tv_pk_fma_f32 v[128:129], v[128:129], v[72:73], v[88:89]\n\t"
        "v_pk_fma_f32 v[130:131], v[130:131], v[74:75], v[90:91]\n\tv_pk_fma_f32 v[132:133], v[132:133], v[76:77], v[92:93]\n\t"
        "v_pk_fma_f32 v[134:135], v[134:135], v[78:79], v[94:95]\n\tv_pk_fma_f32 v[136:137], v[136:137], v[72:73], v[80:81]\n\t"
        "v_pk_fma_f32 v[138:139], v[138:139], v[74:75], v[82:83]\n\tv_pk_fma_f32 v[140:141], v[140:141], v[76:77], v[84:85]\n\t"
        "v_pk_fma_f32 v[142:143], v[142:143], v[78:79], v[86:87]\n\tv_cvt_pk_bf16_f32 v0, v96, v97\n\tv_cvt_pk_bf16_f32 v1, v98, v99\n\t"
        "v_cvt_pk_bf16_f32 v2, v104, v105\n\tv_cvt_pk_bf16_f32 v3, v106, v107\n\tv_cvt_pk_bf16_f32 v4, v100, v101\n\tv_cvt_pk_bf16_f32 v5, v102, v103\n\t"
        "v_cvt_pk_bf16_f32 v6, v108, v109\n\tv_cvt_pk_bf16_f32 v7, v110, v111\n\tv_cvt_pk_bf16_f32 v128, v128, v129\n\tv_cvt_pk_bf16_f32 v129, v130, v131\n\t"
        "v_mfma_f32_16x16x32_bf16 v[120:123], v[112:115], v[0:3], 0\n\tv_mfma_f32_16x16x32_bf16 v[124:127], v[116:119], v[4:7], 0\n\t"
        "v_cvt_pk_bf16_f32 v130, v132, v133\n\tv_cvt_pk_bf16_f32 v131, v134, v135\n\tbuffer_store_dwordx4 v[128:131], off, %[rt], %[kv] offset:2048 sc1\n\t"
        "v_cvt_pk_bf16_f32 v136, v136, v137\n\tv_cvt_pk_bf16_f32 v137, v138, v139\n\tv_cvt_pk_bf16_f32 v138, v140, v141\n\t"
        "v_cvt_pk_bf16_f32 v139, v142, v143\n\t.p2align 12,,12\n\t.Lpg2:\n\ts_cbranch_execz .Lpgret\n\t"
        "buffer_store_dwordx4 v[136:139], off, %[rt], %[kv] offset:3072 sc1\n\tv_max3_f32 v120, v120, v121, v122\n\tv_max3_f32 v124, v124, v125, v126\n\t"
        "v_max_f32 v120, v120, v123\n\tv_max_f32 v124, v124, v127\n\tv_permlane16_swap_b32 v0, v4\n\tv_permlane16_swap_b32 v1, v5\n\t"
        "v_permlane16_swap_b32 v120, v124\n\tv_max_f32 v120, v120, v124\n\tv_mov_b32 v124, v120\n\tv_permlane16_swap_b32 v2, v6\n\t"
        "v_permlane16_swap_b32 v3, v7\n\tv_permlane32_swap_b32 v120, v124\n\tv_min_f32_e64 v8, -v120, -v124\n\t.Lmk_qa_e:\n\t"
        : "=&{v[16:31]}"(acc[0]), "=&{v[32:47]}"(acc[1]), "=&{v[48:63]}"(acc[2]), "=&{v[152:183]}"(r0), "=&{v[184:215]}"(r1), "=&{v[216:247]}"(r2), "=&{v[0:3]}"(qa0), "=&{v[4:7]}"(qa1), "=&{v8}"(nma), [sa] "+s"(sa), [sn] "+s"(sn)
        : [pb0] "{v250}"(n.pb0), [pb1] "{v251}"(n.pb1), [pb2] "{v252}"(n.pb2), [rt] "s"(rt4), [rn] "s"(rt4), [kv] "s"(kvo), [w128] "s"(n.w128), "{v253}"(n.e1), "{v254}"(n.e2), "{v255}"(n.e3)
        : "v64", "v65", "v66", "v67", "v68", "v69", "v70", "v71", "v72", "v73", "v74", "v75", "v76", "v77", "v78", "v79", "v80", "v81", "v82", "v83", "v84", "v85", "v86", "v87", "v88", "v89", "v90", "v91", "v92", "v93", "v94", "v95", "v9", "v10", "v96", "v97", "v98", "v99", "v100", "v101", "v102", "v103", "v104", "v105", "v106", "v107", "v108", "v109", "v110", "v111", "v112", "v113", "v114", "v115", "v116", "v117", "v118", "v119", "v120", "v121", "v122", "v123", "v124", "v125", "v126", "v127", "v128", "v129", "v130", "v131", "v132", "v133", "v134", "v135", "v136", "v137", "v138", "v139", "v140", "v141", "v142", "v143", "v144", "v145", "v146", "v147", "v148", "v149", "v150", "v151", "memory", "scc");
    STAMP(2);
    const unsigned fl = fkv;
    sa = sn;
      sn += 0u;
      asm volatile(
        "; BLK qb\n\t.Lmk_qb_s:\n\tds_read_b128 v[64:67], %[pb1] offset:32768\n\tds_read_b128 v[68:71], %[pb1] offset:33792\n\t"
        "ds_read_b128 v[72:75], %[pb1] offset:34816\n\tds_read_b128 v[76:79], %[pb1] offset:35840\n\tds_read_b128 v[80:83], %[pb1] offset:36864\n\t"
        "ds_read_b128 v[84:87], %[pb1] offset:37888\n\tds_read_b128 v[88:91], %[pb1] offset:38912\n\ts_waitcnt vmcnt(12) lgkmcnt(3)\n\t"
        "v_mfma_f32_16x16x32_bf16 v[16:19], v[64:67], v[152:155], v[16:19]\n\tds_read_b128 v[92:95], %[pb1] offset:39936\n\t"
        "v_mfma_f32_16x16x32_bf16 v[20:23], v[64:67], v[156:159], v[20:23]\n\tv_mov_b32 v120, v8\n\t"
        "v_mfma_f32_16x16x32_bf16 v[24:27], v[68:71], v[152:155], v[24:27]\n\tds_read_b128 v[64:67], %[pb1] offset:40960\n\t"
        "v_mfma_f32_16x16x32_bf16 v[28:31], v[68:71], v[156:159], v[28:31]\n\tv_mov_b32 v121, v8\n\t"
        "v_mfma_f32_16x16x32_bf16 v[32:35], v[72:75], v[152:155], v[32:35]\n\tds_read_b128 v[68:71], %[pb1] offset:41984\n\t"
        "buffer_load_dwordx4 v[192:195], off, %[rt], %[sa] offset:0 sc1\n\tv_mfma_f32_16x16x32_bf16 v[36:39], v[72:75], v[156:159], v[36:39]\n\t"
        "v_mov_b32 v122, v8\n\tv_mfma_f32_16x16x32_bf16 v[40:43], v[76:79], v[152:155], v[40:43]\n\tds_read_b128 v[72:75], %[pb1] offset:43008\n\t"
        "v_mfma_f32_16x16x32_bf16 v[44:47], v[76:79], v[156:159], v[44:47]\n\tv_mov_b32 v123, v8\n\ts_waitcnt lgkmcnt(3)\n\t"
        "v_mfma_f32_16x16x32_bf16 v[48:51], v[152:155], v[80:83], v[48:51]\n\tds_read_b128 v[76:79], %[pb1] offset:44032\n\t"
        "v_mfma_f32_16x16x32_bf16 v[52:55], v[156:159], v[80:83], v[52:55]\n\tbuffer_load_dwordx4 v[196:199], off, %[rt], %[sa] offset:1024 sc1\n\t"
        "s_add_u32 %[sa], %[sa], 0x2000\n\tv_mov_b32 v124, v8\n\tv_mfma_f32_16x16x32_bf16 v[56:59], v[152:155], v[84:87], v[56:59]\n\t"
        "ds_read_b128 v[80:83], %[pb1] offset:45056\n\tv_mfma_f32_16x16x32_bf16 v[60:63], v[156:159], v[84:87], v[60:63]\n\tv_mov_b32 v125, v8\n\t"
        "s_waitcnt vmcnt(12)\n\tv_mfma_f32_16x16x32_bf16 v[16:19], v[88:91], v[160:163], v[16:19]\n\tds_read_b128 v[84:87], %[pb1] offset:46080\n\t"
        "v_mfma_f32_16x16x32_bf16 v[20:23], v[88:91], v[164:167], v[20:23]\n\tv_mov_b32 v126, v8\n\t"
        "v_mfma_f32_16x16x32_bf16 v[24:27], v[92:95], v[160:163], v[24:27]\n\tds_read_b128 v[88:91], %[pb1] offset:47104\n\t"
        "buffer_load_dwordx4 v[200:203], off, %[rt], %[sa] offset:0 sc1\n\tv_mfma_f32_16x16x32_bf16 v[28:31], v[92:95], v[164:167], v[28:31]\n\t"
        "v_mov_b32 v127, v8\n\ts_waitcnt lgkmcnt(3)\n\tv_mfma_f32_16x16x32_bf16 v[32:35], v[64:67], v[160:163], v[32:35]\n\t"
        "ds_read_b128 v[92:95], %[pb1] offset:48128\n\tv_mfma_f32_16x16x32_bf16 v[36:39], v[64:67], v[164:167], v[36:39]\n\tv_mov_b32 v128, v8\n\t"
        "v_mfma_f32_16x16x32_bf16 v[40:43], v[68:71], v[160:163], v[40:43]\n\tds_read_b128 v[64:67], %[pb1] offset:49152\n\t"
        "v_mfma_f32_16x16x32_bf16 v[44:47], v[68:71], v[164:167], v[44:47]\n\tbuffer_load_dwordx4 v[204:207], off, %[rt], %[sa] offset:1024 sc1\n\t"
        "s_add_u32 %[sa], %[sa], 0x2000\n\tv_mov_b32 v129, v8\n\tv_mfma_f32_16x16x32_bf16 v[48:51], v[160:163], v[72:75], v[48:51]\n\t"
        "ds_read_b128 v[68:71], %[pb1] offset:50176\n\tv_mfma_f32_16x16x32_bf16 v[52:55], v[164:167], v[72:75], v[52:55]\n\tv_mov_b32 v130, v8\n\t"
        "v_mfma_f32_16x16x32_bf16 v[56:59], v[160:163], v[76:79], v[56:59]\n\tds_read_b128 v[72:75], %[pb1] offset:51200\n\t"
        "v_mfma_f32_16x16x32_bf16 v[60:63], v[164:167], v[76:79], v[60:63]\n\tv_mov_b32 v131, v8\n\ts_waitcnt vmcnt(12) lgkmcnt(3)\n\t"
        "v_mfma_f32_16x16x32_bf16 v[16:19], v[80:83], v[168:171], v[16:19]\n\tds_read_b128 v[76:79], %[pb1] offset:52224\n\t"
        "buffer_load_dwordx4 v[208:211], off, %[rt], %[sa] offset:0 sc1\n\tv_mfma_f32_16x16x32_bf16 v[20:23], v[80:83], v[172:175], v[20:23]\n\t"
        "v_mov_b32 v132, v8\n\tv_mfma_f32_16x16x32_bf16 v[24:27], v[84:87], v[168:171], v[24:27]\n\tds_read_b128 v[80:83], %[pb1] offset:53248\n\t"
        "v_mfma_f32_16x16x32_bf16 v[28:31], v[84:87], v[172:175], v[28:31]\n\tv_mov_b32 v133, v8\n\t"
        "v_mfma_f32_16x16x32_bf16 v[32:35], v[88:91], v[168:171], v[32:35]\n\tds_read_b128 v[84:87], %[pb1] offset:54272\n\t"
        "v_mfma_f32_16x16x32_bf16 v[36:39], v[88:91], v[172:175], v[36:39]\n\tbuffer_load_dwordx4 v[212:215], off, %[rt], %[sa] offset:1024 sc1\n\t"
        "s_add_u32 %[sa], %[sa], 0x2000\n\tv_mov_b32 v134, v8\n\tv_mfma_f32_16x16x32_bf16 v[40:43], v[92:95], v[168:171], v[40:43]\n\t"
        "ds_read_b128 v[88:91], %[pb1] offset:55296\n\tv_mfma_f32_16x16x32_bf16 v[44:47], v[92:95], v[172:175], v[44:47]\n\tv_mov_b32 v135, v8\n\t"
        "s_waitcnt lgkmcnt(3)\n\tv_mfma_f32_16x16x32_bf16 v[48:51], v[168:171], v[64:67], v[48:51]\n\tds_read_b128 v[92:95], %[pb1] offset:56320\n\t"
        "v_mfma_f32_16x16x32_bf16 v[52:55], v[172:175], v[64:67], v[52:55]\n\tv_mov_b32 v136, 0\n\t"
        "v_mfma_f32_16x16x32_bf16 v[56:59], v[168:171], v[68:71], v[56:59]\n\tds_read_b128 v[64:67], %[pb1] offset:57344\n\t"
        "buffer_load_dwordx4 v[216:219], off, %[rt], %[sa] offset:0 sc1\n\tv_mfma_f32_16x16x32_bf16 v[60:63], v[172:175], v[68:71], v[60:63]\n\t"
        "v_mov_b32 v137, 0\n\ts_waitcnt vmcnt(13)\n\tv_mfma_f32_16x16x32_bf16 v[16:19], v[72:75], v[176:179], v[16:19]\n\t"
        "ds_read_b128 v[68:71], %[pb1] offset:58368\n\tv_mfma_f32_16x16x32_bf16 v[20:23], v[72:75], v[180:183], v[20:23]\n\tv_mov_b32 v138, 0\n\t"
        "v_mfma_f32_16x16x32_bf16 v[24:27], v[76:79], v[176:179], v[24:27]\n\tds_read_b128 v[72:75], %[pb1] offset:59392\n\t"
        "v_mfma_f32_16x16x32_bf16 v[28:31], v[76:79], v[180:183], v[28:31]\n\tbuffer_load_dwordx4 v[220:223], off, %[rt], %[sa] offset:1024 sc1\n\t"
        "s_add_u32 %[sa], %[sa], 0x2000\n\tv_mov_b32 v139, 0\n\ts_waitcnt lgkmcnt(3)\n\tv_mfma_f32_16x16x32_bf16 v[32:35], v[80:83], v[176:179], v[32:35]\n\t"
        "ds_read_b128 v[76:79], %[pb1] offset:60416\n\tv_mfma_f32_16x16x32_bf16 v[36:39], v[80:83], v[180:183], v[36:39]\n\tv_mov_b32 v140, 0\n\t"
        "v_mfma_f32_16x16x32_bf16 v[40:43], v[84:87], v[176:179], v[40:43]\n\tds_read_b128 v[80:83], %[pb1] offset:61440\n\t"
        "v_mfma_f32_16x16x32_bf16 v[44:47], v[84:87], v[180:183], v[44:47]\n\tv_mov_b32 v141, 0\n\t"
        "v_mfma_f32_16x16x32_bf16 v[48:51], v[176:179], v[88:91], v[48:51]\n\tds_read_b128 v[84:87], %[pb1] offset:62464\n\t"
        "buffer_load_dwordx4 v[224:227], off, %[rt], %[sa] offset:0 sc1\n\tv_mfma_f32_16x16x32_bf16 v[52:55], v[180:183], v[88:91], v[52:55]\n\t"
        "v_mov_b32 v142, 0\n\tv_mfma_f32_16x16x32_bf16 v[56:59], v[176:179], v[92:95], v[56:59]\n\tds_read_b128 v[88:91], %[pb1] offset:63488\n\t"
        "v_mfma_f32_16x16x32_bf16 v[60:63], v[180:183], v[92:95], v[60:63]\n\tv_mov_b32 v143, 0\n\ts_waitcnt vmcnt(13) lgkmcnt(3)\n\t"
        "v_mfma_f32_16x16x32_bf16 v[16:19], v[64:67], v[184:187], v[16:19]\n\tds_read_b128 v[92:95], %[pb1] offset:64512\n\t"
        "v_mfma_f32_16x16x32_bf16 v[20:23], v[64:67], v[188:191], v[20:23]\n\tbuffer_load_dwordx4 v[228:231], off, %[rt], %[sa] offset:1024 sc1\n\t"
        "s_add_u32 %[sa], %[sa], 0x2000\n\tv_mov_b32 v144, 0\n\tv_mfma_f32_16x16x32_bf16 v[24:27], v[68:71], v[184:187], v[24:27]\n\t"
        "ds_read_b128 v[64:67], %[pb2] offset:0\n\tv_mfma_f32_16x16x32_bf16 v[28:31], v[68:71], v[188:191], v[28:31]\n\tv_mov_b32 v145, 0\n\t"
        "v_mfma_f32_16x16x32_bf16 v[32:35], v[72:75], v[184:187], v[32:35]\n\tds_read_b128 v[68:71], %[pb2] offset:1024\n\t"
        "v_mfma_f32_16x16x32_bf16 v[36:39], v[72:75], v[188:191], v[36:39]\n\tv_mov_b32 v146, 0\n\t"
        "v_mfma_f32_16x16x32_bf16 v[40:43], v[76:79], v[184:187], v[40:43]\n\tds_read_b128 v[72:75], %[pb2] offset:2048\n\t"
        "buffer_load_dwordx4 v[232:235], off, %[rt], %[sa] offset:0 sc1\n\tv_mfma_f32_16x16x32_bf16 v[44:47], v[76:79], v[188:191], v[44:47]\n\t"
        "v_mov_b32 v147, 0\n\ts_waitcnt lgkmcnt(3)\n\tv_mfma_f32_16x16x32_bf16 v[48:51], v[184:187], v[80:83], v[48:51]\n\t"
        "ds_read_b128 v[76:79], %[pb2] offset:3072\n\tv_mfma_f32_16x16x32_bf16 v[52:55], v[188:191], v[80:83], v[52:55]\n\tv_mov_b32 v148, 0\n\t"
        "v_mfma_f32_16x16x32_bf16 v[56:59], v[184:187], v[84:87], v[56:59]\n\ts_barrier\n\tds_read_b128 v[80:83], %[pb2] offset:4096\n\t"
        "v_mfma_f32_16x16x32_bf16 v[60:63], v[188:191], v[84:87], v[60:63]\n\tbuffer_load_dwordx4 v[236:239], off, %[rt], %[sa] offset:1024 sc1\n\t"
        "s_add_u32 %[sa], %[sa], 0x2000\n\tv_mov_b32 v149, 0\n\ts_waitcnt vmcnt(10)\n\tv_mfma_f32_16x16x32_bf16 v[16:19], v[88:91], v[192:195], v[16:19]\n\t"
        "ds_read_b128 v[84:87], %[pb2] offset:5120\n\tv_mfma_f32_16x16x32_bf16 v[20:23], v[88:91], v[196:199], v[20:23]\n\tv_mov_b32 v150, 0\n\t"
        "s_waitcnt vmcnt(12)\n\tbuffer_store_dwordx2 %[one], off, %[rs], %[fl] sc1\n\tv_mfma_f32_16x16x32_bf16 v[24:27], v[92:95], v[192:195], v[24:27]\n\t"
        "ds_read_b128 v[88:91], %[pb2] offset:6144\n\tv_mfma_f32_16x16x32_bf16 v[28:31], v[92:95], v[196:199], v[28:31]\n\tv_mov_b32 v151, 0\n\t"
        "s_waitcnt lgkmcnt(3)\n\tv_mfma_f32_16x16x32_bf16 v[32:35], v[64:67], v[192:195], v[32:35]\n\tds_read_b128 v[92:95], %[pb2] offset:7168\n\t"
        "buffer_load_dwordx4 v[240:243], off, %[rt], %[sa] offset:0 sc1\n\tv_mfma_f32_16x16x32_bf16 v[36:39], v[64:67], v[196:199], v[36:39]\n\t"
        "v_mfma_f32_16x16x32_bf16 v[40:43], v[68:71], v[192:195], v[40:43]\n\tds_read_b128 v[64:67], %[pb2] offset:8192\n\t"
        "v_mfma_f32_16x16x32_bf16 v[44:47], v[68:71], v[196:199], v[44:47]\n\tv_mfma_f32_16x16x32_bf16 v[48:51], v[192:195], v[72:75], v[48:51]\n\t"
        "ds_read_b128 v[68:71], %[pb2] offset:9216\n\tv_mfma_f32_16x16x32_bf16 v[52:55], v[196:199], v[72:75], v[52:55]\n\t"
        "buffer_load_dwordx4 v[244:247], off, %[rt], %[sa] offset:1024 sc1\n\ts_add_u32 %[sa], %[sa], 0x2000\n\t"
        "v_mfma_f32_16x16x32_bf16 v[56:59], v[192:195], v[76:79], v[56:59]\n\tds_read_b128 v[72:75], %[pb2] offset:10240\n\t"
        "v_mfma_f32_16x16x32_bf16 v[60:63], v[196:199], v[76:79], v[60:63]\n\ts_waitcnt vmcnt(11) lgkmcnt(3)\n\t"
        "v_mfma_f32_16x16x32_bf16 v[16:19], v[80:83], v[200:203], v[16:19]\n\tds_read_b128 v[76:79], %[pb2] offset:11264\n\t"
        "v_mfma_f32_16x16x32_bf16 v[20:23], v[80:83], v[204:207], v[20:23]\n\tv_mfma_f32_16x16x32_bf16 v[24:27], v[84:87], v[200:203], v[24:27]\n\t"
        "ds_read_b128 v[80:83], %[pb2] offset:12288\n\tbuffer_load_dwordx4 v[152:155], off, %[rt], %[sa] offset:0 sc1\n\t"
        "v_mfma_f32_16x16x32_bf16 v[28:31], v[84:87], v[204:207], v[28:31]\n\tv_mfma_f32_16x16x32_bf16 v[32:35], v[88:91], v[200:203], v[32:35]\n\t"
        "ds_read_b128 v[84:87], %[pb2] offset:13312\n\tv_mfma_f32_16x16x32_bf16 v[36:39], v[88:91], v[204:207], v[36:39]\n\t"
        "v_mfma_f32_16x16x32_bf16 v[40:43], v[92:95], v[200:203], v[40:43]\n\tds_read_b128 v[88:91], %[pb2] offset:14336\n\t"
        "v_mfma_f32_16x16x32_bf16 v[44:47], v[92:95], v[204:207], v[44:47]\n\tbuffer_load_dwordx4 v[156:159], off, %[rt], %[sa] offset:1024 sc1\n\t"
        "s_add_u32 %[sa], %[sa], 0x2000\n\ts_waitcnt lgkmcnt(3)\n\tv_mfma_f32_16x16x32_bf16 v[48:51], v[200:203], v[64:67], v[48:51]\n\t"
        "ds_read_b128 v[92:95], %[pb2] offset:15360\n\tv_mfma_f32_16x16x32_bf16 v[52:55], v[204:207], v[64:67], v[52:55]\n\t"
        "v_mfma_f32_16x16x32_bf16 v[56:59], v[200:203], v[68:71], v[56:59]\n\tds_read_b128 v[64:67], %[pb0] offset:0\n\t"
        "v_mfma_f32_16x16x32_bf16 v[60:63], v[204:207], v[68:71], v[60:63]\n\ts_waitcnt vmcnt(11)\n\t"
        "v_mfma_f32_16x16x32_bf16 v[16:19], v[72:75], v[208:211], v[16:19]\n\tds_read_b128 v[68:71], %[pb0] offset:1024\n\t"
        "buffer_load_dwordx4 v[160:163], off, %[rt], %[sa] offset:0 sc1\n\tv_mfma_f32_16x16x32_bf16 v[20:23], v[72:75], v[212:215], v[20:23]\n\t"
        "v_mfma_f32_16x16x32_bf16 v[24:27], v[76:79], v[208:211], v[24:27]\n\tds_read_b128 v[72:75], %[pb0] offset:2048\n\t"
        "v_mfma_f32_16x16x32_bf16 v[28:31], v[76:79], v[212:215], v[28:31]\n\ts_waitcnt lgkmcnt(3)\n\t"
        "v_mfma_f32_16x16x32_bf16 v[32:35], v[80:83], v[208:211], v[32:35]\n\tds_read_b128 v[76:79], %[pb0] offset:3072\n\t"
        "v_mfma_f32_16x16x32_bf16 v[36:39], v[80:83], v[212:215], v[36:39]\n\tbuffer_load_dwordx4 v[164:167], off, %[rt], %[sa] offset:1024 sc1\n\t"
        "s_add_u32 %[sa], %[sa], 0x2000\n\tv_mfma_f32_16x16x32_bf16 v[40:43], v[84:87], v[208:211], v[40:43]\n\tds_read_b128 v[80:83], %[pb0] offset:4096\n\t"
        "v_mfma_f32_16x16x32_bf16 v[44:47], v[84:87], v[212:215], v[44:47]\n\tv_mfma_f32_16x16x32_bf16 v[48:51], v[208:211], v[88:91], v[48:51]\n\t"
        "ds_read_b128 v[84:87], %[pb0] offset:5120\n\tv_mfma_f32_16x16x32_bf16 v[52:55], v[212:215], v[88:91], v[52:55]\n\t"
        "v_mfma_f32_16x16x32_bf16 v[56:59], v[208:211], v[92:95], v[56:59]\n\tds_read_b128 v[88:91], %[pb0] offset:6144\n\t"
        "buffer_load_dwordx4 v[168:171], off, %[rt], %[sa] offset:0 sc1\n\tv_mfma_f32_16x16x32_bf16 v[60:63], v[212:215], v[92:95], v[60:63]\n\t"
        "s_waitcnt vmcnt(12) lgkmcnt(3)\n\tv_mfma_f32_16x16x32_bf16 v[16:19], v[64:67], v[216:219], v[16:19]\n\tds_read_b128 v[92:95], %[pb0] offset:7168\n\t"
        "v_mfma_f32_16x16x32_bf16 v[20:23], v[64:67], v[220:223], v[20:23]\n\tv_mfma_f32_16x16x32_bf16 v[24:27], v[68:71], v[216:219], v[24:27]\n\t"
        "ds_read_b128 v[64:67], %[pb0] offset:8192\n\tv_mfma_f32_16x16x32_bf16 v[28:31], v[68:71], v[220:223], v[28:31]\n\t"
        "buffer_load_dwordx4 v[172:175], off, %[rt], %[sa] offset:1024 sc1\n\ts_add_u32 %[sa], %[sa], 0x2000\n\t"
        "v_mfma_f32_16x16x32_bf16 v[32:35], v[72:75], v[216:219], v[32:35]\n\tds_read_b128 v[68:71], %[pb0] offset:9216\n\t"
        "v_mfma_f32_16x16x32_bf16 v[36:39], v[72:75], v[220:223], v[36:39]\n\tv_mfma_f32_16x16x32_bf16 v[40:43], v[76:79], v[216:219], v[40:43]\n\t"
        "ds_read_b128 v[72:75], %[pb0] offset:10240\n\tv_mfma_f32_16x16x32_bf16 v[44:47], v[76:79], v[220:223], v[44:47]\n\ts_waitcnt lgkmcnt(3)\n\t"
        "v_mfma_f32_16x16x32_bf16 v[48:51], v[216:219], v[80:83], v[48:51]\n\tds_read_b128 v[76:79], %[pb0] offset:11264\n\t"
        "buffer_load_dwordx4 v[176:179], off, %[rt], %[sa] offset:0 sc1\n\tv_mfma_f32_16x16x32_bf16 v[52:55], v[220:223], v[80:83], v[52:55]\n\t"
        "v_mfma_f32_16x16x32_bf16 v[56:59], v[216:219], v[84:87], v[56:59]\n\tds_read_b128 v[80:83], %[pb0] offset:12288\n\t"
        "v_mfma_f32_16x16x32_bf16 v[60:63], v[220:223], v[84:87], v[60:63]\n\ts_waitcnt vmcnt(12)\n\t"
        "v_mfma_f32_16x16x32_bf16 v[16:19], v[88:91], v[224:227], v[16:19]\n\tds_read_b128 v[84:87], %[pb0] offset:13312\n\t"
        "v_mfma_f32_16x16x32_bf16 v[20:23], v[88:91], v[228:231], v[20:23]\n\tbuffer_load_dwordx4 v[180:183], off, %[rt], %[sa] offset:1024 sc1\n\t"
        "v_mfma_f32_16x16x32_bf16 v[24:27], v[92:95], v[224:227], v[24:27]\n\tds_read_b128 v[88:91], %[pb0] offset:14336\n\t"
        "v_mfma_f32_16x16x32_bf16 v[28:31], v[92:95], v[228:231], v[28:31]\n\ts_waitcnt lgkmcnt(3)\n\t"
        "v_mfma_f32_16x16x32_bf16 v[32:35], v[64:67], v[224:227], v[32:35]\n\tds_read_b128 v[92:95], %[pb0] offset:15360\n\t"
        "v_mfma_f32_16x16x32_bf16 v[36:39], v[64:67], v[228:231], v[36:39]\n\tv_mfma_f32_16x16x32_bf16 v[40:43], v[68:71], v[224:227], v[40:43]\n\t"
        "ds_read_b128 v[64:67], %[pb0] offset:16384\n\tv_mfma_f32_16x16x32_bf16 v[44:47], v[68:71], v[228:231], v[44:47]\n\t"
        "v_mfma_f32_16x16x32_bf16 v[48:51], v[224:227], v[72:75], v[48:51]\n\tds_read_b128 v[68:71], %[pb0] offset:17408\n\t"
        "v_mfma_f32_16x16x32_bf16 v[52:55], v[228:231], v[72:75], v[52:55]\n\tv_mfma_f32_16x16x32_bf16 v[56:59], v[224:227], v[76:79], v[56:59]\n\t"
        "ds_read_b128 v[72:75], %[pb0] offset:18432\n\tv_mfma_f32_16x16x32_bf16 v[60:63], v[228:231], v[76:79], v[60:63]\n\ts_waitcnt vmcnt(11) lgkmcnt(3)\n\t"
        "v_mfma_f32_16x16x32_bf16 v[16:19], v[80:83], v[232:235], v[16:19]\n\tds_read_b128 v[76:79], %[pb0] offset:19456\n\t"
        "v_subrev_u32 v226, %[w128], v253\n\tv_mfma_f32_16x16x32_bf16 v[20:23], v[80:83], v[236:239], v[20:23]\n\tds_read_b32 v216, v253 offset:512\n\t"
        "ds_read_b32 v217, v253 offset:576\n\tv_mfma_f32_16x16x32_bf16 v[24:27], v[84:87], v[232:235], v[24:27]\n\t"
        "ds_read_b128 v[80:83], %[pb0] offset:20480\n\tds_read_b32 v218, v253 offset:1536\n\t"
        "v_mfma_f32_16x16x32_bf16 v[28:31], v[84:87], v[236:239], v[28:31]\n\tds_read_b32 v219, v253 offset:1600\n\t"
        "ds_read_b128 v[184:187], v255 offset:2176\n\tv_mfma_f32_16x16x32_bf16 v[32:35], v[88:91], v[232:235], v[32:35]\n\t"
        "ds_read_b128 v[84:87], %[pb0] offset:21504\n\tds_read_b128 v[188:191], v255 offset:2240\n\t"
        "v_mfma_f32_16x16x32_bf16 v[36:39], v[88:91], v[236:239], v[36:39]\n\tds_read_b128 v[192:195], v255 offset:2048\n\t"
        "ds_read_b128 v[196:199], v255 offset:2112\n\tv_mfma_f32_16x16x32_bf16 v[40:43], v[92:95], v[232:235], v[40:43]\n\t"
        "ds_read_b128 v[88:91], %[pb0] offset:22528\n\tds_read_b32 v220, v226 offset:2304\n\t"
        "v_mfma_f32_16x16x32_bf16 v[44:47], v[92:95], v[236:239], v[44:47]\n\tds_read_b32 v221, v226 offset:2368\n\ts_waitcnt lgkmcnt(13)\n\t"
        "v_mfma_f32_16x16x32_bf16 v[48:51], v[232:235], v[64:67], v[48:51]\n\tds_read_b128 v[92:95], %[pb0] offset:23552\n\t"
        "ds_read_b128 v[208:211], v254 offset:1536\n\tv_mfma_f32_16x16x32_bf16 v[52:55], v[236:239], v[64:67], v[52:55]\n\t"
        "ds_read_b128 v[212:215], v254 offset:1600\n\tds_read_b128 v[200:203], v254 offset:512\n\t"
        "v_mfma_f32_16x16x32_bf16 v[56:59], v[232:235], v[68:71], v[56:59]\n\tds_read_b128 v[64:67], %[pb0] offset:24576\n\t"
        "ds_read_b128 v[204:207], v254 offset:576\n\tv_mfma_f32_16x16x32_bf16 v[60:63], v[236:239], v[68:71], v[60:63]\n\ts_waitcnt vmcnt(8)\n\t"
        "v_mfma_f32_16x16x32_bf16 v[16:19], v[72:75], v[240:243], v[16:19]\n\tds_read_b128 v[68:71], %[pb0] offset:25600\n\t"
        "v_mfma_f32_16x16x32_bf16 v[20:23], v[72:75], v[244:247], v[20:23]\n\tv_mfma_f32_16x16x32_bf16 v[24:27], v[76:79], v[240:243], v[24:27]\n\t"
        "ds_read_b128 v[72:75], %[pb0] offset:26624\n\tv_mfma_f32_16x16x32_bf16 v[28:31], v[76:79], v[244:247], v[28:31]\n\ts_waitcnt lgkmcnt(7)\n\t"
        "v_mfma_f32_16x16x32_bf16 v[32:35], v[80:83], v[240:243], v[32:35]\n\tds_read_b128 v[76:79], %[pb0] offset:27648\n\t"
        "v_mfma_f32_16x16x32_bf16 v[36:39], v[80:83], v[244:247], v[36:39]\n\tv_mfma_f32_16x16x32_bf16 v[40:43], v[84:87], v[240:243], v[40:43]\n\t"
        "ds_read_b128 v[80:83], %[pb0] offset:28672\n\tv_mfma_f32_16x16x32_bf16 v[44:47], v[84:87], v[244:247], v[44:47]\n\t"
        "v_mfma_f32_16x16x32_bf16 v[48:51], v[240:243], v[88:91], v[48:51]\n\tds_read_b128 v[84:87], %[pb0] offset:29696\n\t"
        "v_mfma_f32_16x16x32_bf16 v[52:55], v[244:247], v[88:91], v[52:55]\n\tv_mfma_f32_16x16x32_bf16 v[56:59], v[240:243], v[92:95], v[56:59]\n\t"
        "ds_read_b128 v[88:91], %[pb0] offset:30720\n\tv_mfma_f32_16x16x32_bf16 v[60:63], v[244:247], v[92:95], v[60:63]\n\ts_waitcnt vmcnt(6) lgkmcnt(3)\n\t"
        "v_mfma_f32_16x16x32_bf16 v[16:19], v[64:67], v[152:155], v[16:19]\n\tds_read_b128 v[92:95], %[pb0] offset:31744\n\t"
        "v_mul_f32 v222, 0x3e8293ee, v216\n\tv_mfma_f32_16x16x32_bf16 v[20:23], v[64:67], v[156:159], v[20:23]\n\tv_mul_f32 v223, 0x3e8293ee, v217\n\t"
        "v_mul_f32 v224, 0x3e8293ee, v218\n\tv_mfma_f32_16x16x32_bf16 v[24:27], v[68:71], v[152:155], v[24:27]\n\t"
        "ds_read_b128 v[64:67], %[pb0] offset:32768\n\tv_mul_f32 v225, 0x3e8293ee, v219\n\t"
        "v_mfma_f32_16x16x32_bf16 v[28:31], v[68:71], v[156:159], v[28:31]\n\tv_mul_f32 v96, v218, v184\n\tv_mul_f32 v97, v218, v185\n\t"
        "v_mfma_f32_16x16x32_bf16 v[32:35], v[72:75], v[152:155], v[32:35]\n\tds_read_b128 v[68:71], %[pb0] offset:33792\n\tv_mul_f32 v98, v218, v186\n\t"
        "v_mfma_f32_16x16x32_bf16 v[36:39], v[72:75], v[156:159], v[36:39]\n\tv_mul_f32 v99, v218, v187\n\tv_mul_f32 v100, v218, v188\n\t"
        "v_mfma_f32_16x16x32_bf16 v[40:43], v[76:79], v[152:155], v[40:43]\n\tds_read_b128 v[72:75], %[pb0] offset:34816\n\tv_mul_f32 v101, v218, v189\n\t"
        "v_mfma_f32_16x16x32_bf16 v[44:47], v[76:79], v[156:159], v[44:47]\n\tv_mul_f32 v102, v218, v190\n\ts_waitcnt lgkmcnt(3)\n\t"
        "v_mfma_f32_16x16x32_bf16 v[48:51], v[152:155], v[80:83], v[48:51]\n\tds_read_b128 v[76:79], %[pb0] offset:35840\n\tv_mul_f32 v103, v218, v191\n\t"
        "v_mfma_f32_16x16x32_bf16 v[52:55], v[156:159], v[80:83], v[52:55]\n\tv_mul_f32 v184, v219, v184\n\tv_mul_f32 v185, v219, v185\n\t"
        "v_mfma_f32_16x16x32_bf16 v[56:59], v[152:155], v[84:87], v[56:59]\n\tds_read_b128 v[80:83], %[pb0] offset:36864\n\tv_mul_f32 v186, v219, v186\n\t"
        "v_mfma_f32_16x16x32_bf16 v[60:63], v[156:159], v[84:87], v[60:63]\n\tv_mul_f32 v187, v219, v187\n\ts_waitcnt vmcnt(4)\n\t"
        "v_mfma_f32_16x16x32_bf16 v[16:19], v[88:91], v[160:163], v[16:19]\n\tds_read_b128 v[84:87], %[pb0] offset:37888\n\tv_mul_f32 v188, v219, v188\n\t"
        "v_mfma_f32_16x16x32_bf16 v[20:23], v[88:91], v[164:167], v[20:23]\n\tv_mul_f32 v189, v219, v189\n\tv_mul_f32 v190, v219, v190\n\t"
        "v_mfma_f32_16x16x32_bf16 v[24:27], v[92:95], v[160:163], v[24:27]\n\tds_read_b128 v[88:91], %[pb0] offset:38912\n\tv_mul_f32 v191, v219, v191\n\t"
        "v_mfma_f32_16x16x32_bf16 v[28:31], v[92:95], v[164:167], v[28:31]\n\tv_mul_f32 v104, v224, v192\n\ts_waitcnt lgkmcnt(3)\n\t"
        "v_mfma_f32_16x16x32_bf16 v[32:35], v[64:67], v[160:163], v[32:35]\n\tds_read_b128 v[92:95], %[pb0] offset:39936\n\tv_mul_f32 v105, v224, v193\n\t"
        "v_mfma_f32_16x16x32_bf16 v[36:39], v[64:67], v[164:167], v[36:39]\n\tv_mul_f32 v106, v224, v194\n\tv_mul_f32 v107, v224, v195\n\t"
        "v_mfma_f32_16x16x32_bf16 v[40:43], v[68:71], v[160:163], v[40:43]\n\tds_read_b128 v[64:67], %[pb0] offset:40960\n\tv_mul_f32 v108, v224, v196\n\t"
        "v_mfma_f32_16x16x32_bf16 v[44:47], v[68:71], v[164:167], v[44:47]\n\tv_mul_f32 v109, v224, v197\n\tv_mul_f32 v110, v224, v198\n\t"
        "v_mfma_f32_16x16x32_bf16 v[48:51], v[160:163], v[72:75], v[48:51]\n\tds_read_b128 v[68:71], %[pb0] offset:41984\n\tv_mul_f32 v111, v224, v199\n\t"
        "v_mfma_f32_16x16x32_bf16 v[52:55], v[164:167], v[72:75], v[52:55]\n\tv_mul_f32 v192, v225, v192\n\tv_mul_f32 v193, v225, v193\n\t"
        "v_mfma_f32_16x16x32_bf16 v[56:59], v[160:163], v[76:79], v[56:59]\n\tds_read_b128 v[72:75], %[pb0] offset:43008\n\tv_mul_f32 v194, v225, v194\n\t"
        "v_mfma_f32_16x16x32_bf16 v[60:63], v[164:167], v[76:79], v[60:63]\n\tv_mul_f32 v195, v225, v195\n\ts_waitcnt vmcnt(2) lgkmcnt(3)\n\t"
        "v_mfma_f32_16x16x32_bf16 v[16:19], v[80:83], v[168:171], v[16:19]\n\tds_read_b128 v[76:79], %[pb0] offset:44032\n\tv_mul_f32 v196, v225, v196\n\t"
        "v_mfma_f32_16x16x32_bf16 v[20:23], v[80:83], v[172:175], v[20:23]\n\tv_mul_f32 v197, v225, v197\n\tv_mul_f32 v198, v225, v198\n\t"
        "v_mfma_f32_16x16x32_bf16 v[24:27], v[84:87], v[168:171], v[24:27]\n\tds_read_b128 v[80:83], %[pb0] offset:45056\n\tv_mul_f32 v199, v225, v199\n\t"
        "v_mfma_f32_16x16x32_bf16 v[28:31], v[84:87], v[172:175], v[28:31]\n\tv_mul_f32 v112, v220, v208\n\tv_mul_f32 v113, v220, v209\n\t"
        "v_mfma_f32_16x16x32_bf16 v[32:35], v[88:91], v[168:171], v[32:35]\n\tds_read_b128 v[84:87], %[pb0] offset:46080\n\tv_mul_f32 v114, v220, v210\n\t"
        "v_mfma_f32_16x16x32_bf16 v[36:39], v[88:91], v[172:175], v[36:39]\n\tv_mul_f32 v115, v220, v211\n\tv_mul_f32 v116, v220, v212\n\t"
        "v_mfma_f32_16x16x32_bf16 v[40:43], v[92:95], v[168:171], v[40:43]\n\tds_read_b128 v[88:91], %[pb0] offset:47104\n\tv_mul_f32 v117, v220, v213\n\t"
        "v_mfma_f32_16x16x32_bf16 v[44:47], v[92:95], v[172:175], v[44:47]\n\tv_mul_f32 v118, v220, v214\n\ts_waitcnt lgkmcnt(3)\n\t"
        "v_mfma_f32_16x16x32_bf16 v[48:51], v[168:171], v[64:67], v[48:51]\n\tds_read_b128 v[92:95], %[pb0] offset:48128\n\tv_mul_f32 v119, v220, v215\n\t"
        "v_mfma_f32_16x16x32_bf16 v[52:55], v[172:175], v[64:67], v[52:55]\n\tv_mul_f32 v208, v221, v208\n\tv_mul_f32 v209, v221, v209\n\t"
        "v_mfma_f32_16x16x32_bf16 v[56:59], v[168:171], v[68:71], v[56:59]\n\tv_mul_f32 v210, v221, v210\n\tv_mul_f32 v211, v221, v211\n\t"
        "v_mfma_f32_16x16x32_bf16 v[60:63], v[172:175], v[68:71], v[60:63]\n\tv_mul_f32 v212, v221, v212\n\ts_waitcnt vmcnt(0)\n\t"
        "v_mfma_f32_16x16x32_bf16 v[16:19], v[72:75], v[176:179], v[16:19]\n\tv_mul_f32 v213, v221, v213\n\tv_mul_f32 v214, v221, v214\n\t"
        "v_mfma_f32_16x16x32_bf16 v[20:23], v[72:75], v[180:183], v[20:23]\n\tv_mul_f32 v215, v221, v215\n\t"
        "v_mfma_f32_16x16x32_bf16 v[24:27], v[76:79], v[176:179], v[24:27]\n\tv_mfma_f32_16x16x32_bf16 v[28:31], v[76:79], v[180:183], v[28:31]\n\t"
        "s_waitcnt lgkmcnt(0)\n\tv_mfma_f32_16x16x32_bf16 v[32:35], v[80:83], v[176:179], v[32:35]\n\t"
        "v_mfma_f32_16x16x32_bf16 v[36:39], v[80:83], v[180:183], v[36:39]\n\tv_mfma_f32_16x16x32_bf16 v[40:43], v[84:87], v[176:179], v[40:43]\n\t"
        "v_mfma_f32_16x16x32_bf16 v[44:47], v[84:87], v[180:183], v[44:47]\n\tv_mfma_f32_16x16x32_bf16 v[48:51], v[176:179], v[88:91], v[48:51]\n\t"
        "v_mfma_f32_16x16x32_bf16 v[52:55], v[180:183], v[88:91], v[52:55]\n\tv_mfma_f32_16x16x32_bf16 v[56:59], v[176:179], v[92:95], v[56:59]\n\t"
        "v_mfma_f32_16x16x32_bf16 v[60:63], v[180:183], v[92:95], v[60:63]\n\ts_nop 0\n\t"
        "v_pk_fma_f32 v[32:33], v[32:33], v[216:217], v[96:97] op_sel:[0,0,0] op_sel_hi:[1,0,1]\n\t"
        "v_pk_fma_f32 v[34:35], v[34:35], v[216:217], v[98:99] op_sel:[0,0,0] op_sel_hi:[1,0,1]\n\t"
        "v_pk_fma_f32 v[40:41], v[40:41], v[216:217], v[100:101] op_sel:[0,0,0] op_sel_hi:[1,0,1]\n\t"
        "v_pk_fma_f32 v[42:43], v[42:43], v[216:217], v[102:103] op_sel:[0,0,0] op_sel_hi:[1,0,1]\n\t"
        "v_pk_fma_f32 v[36:37], v[36:37], v[216:217], v[184:185] op_sel:[0,1,0] op_sel_hi:[1,1,1]\n\t"
        "v_pk_fma_f32 v[38:39], v[38:39], v[216:217], v[186:187] op_sel:[0,1,0] op_sel_hi:[1,1,1]\n\t"
        "v_pk_fma_f32 v[44:45], v[44:45], v[216:217], v[188:189] op_sel:[0,1,0] op_sel_hi:[1,1,1]\n\t"
        "v_pk_fma_f32 v[46:47], v[46:47], v[216:217], v[190:191] op_sel:[0,1,0] op_sel_hi:[1,1,1]\n\tv_cvt_pk_bf16_f32 v32, v32, v33\n\t"
        "v_cvt_pk_bf16_f32 v33, v34, v35\n\tv_cvt_pk_bf16_f32 v34, v40, v41\n\tv_cvt_pk_bf16_f32 v35, v42, v43\n\t"
        "buffer_store_dwordx4 v[32:35], off, %[rt], %[kv] offset:0 sc1\n\tv_cvt_pk_bf16_f32 v36, v36, v37\n\tv_cvt_pk_bf16_f32 v37, v38, v39\n\t"
        "v_cvt_pk_bf16_f32 v38, v44, v45\n\tv_cvt_pk_bf16_f32 v39, v46, v47\n\tbuffer_store_dwordx4 v[36:39], off, %[rt], %[kv] offset:1024 sc1\n\t"
        "v_pk_fma_f32 v[16:17], v[16:17], v[222:223], v[104:105] op_sel:[0,0,0] op_sel_hi:[1,0,1]\n\t"
        "v_pk_fma_f32 v[18:19], v[18:19], v[222:223], v[106:107] op_sel:[0,0,0] op_sel_hi:[1,0,1]\n\t"
        "v_pk_fma_f32 v[24:25], v[24:25], v[222:223], v[108:109] op_sel:[0,0,0] op_sel_hi:[1,0,1]\n\t"
        "v_pk_fma_f32 v[26:27], v[26:27], v[222:223], v[110:111] op_sel:[0,0,0] op_sel_hi:[1,0,1]\n\t"
        "v_pk_fma_f32 v[20:21], v[20:21], v[222:223], v[192:193] op_sel:[0,1,0] op_sel_hi:[1,1,1]\n\t"
        "v_pk_fma_f32 v[22:23], v[22:23], v[222:223], v[194:195] op_sel:[0,1,0] op_sel_hi:[1,1,1]\n\t"
        "v_pk_fma_f32 v[28:29], v[28:29], v[222:223], v[196:197] op_sel:[0,1,0] op_sel_hi:[1,1,1]\n\t"
        "v_pk_fma_f32 v[30:31], v[30:31], v[222:223], v[198:199] op_sel:[0,1,0] op_sel_hi:[1,1,1]\n\t"
        "v_pk_fma_f32 v[48:49], v[48:49], v[200:201], v[112:113]\n\tv_pk_fma_f32 v[50:51], v[50:51], v[202:203], v[114:115]\n\t"
        "v_pk_fma_f32 v[52:53], v[52:53], v[204:205], v[116:117]\n\tv_pk_fma_f32 v[54:55], v[54:55], v[206:207], v[118:119]\n\t"
        "v_pk_fma_f32 v[56:57], v[56:57], v[200:201], v[208:209]\n\tv_pk_fma_f32 v[58:59], v[58:59], v[202:203], v[210:211]\n\t"
        "v_pk_fma_f32 v[60:61], v[60:61], v[204:205], v[212:213]\n\tv_pk_fma_f32 v[62:63], v[62:63], v[206:207], v[214:215]\n\t"
        "v_cvt_pk_bf16_f32 v88, v16, v17\n\tv_cvt_pk_bf16_f32 v89, v18, v19\n\tv_cvt_pk_bf16_f32 v90, v24, v25\n\tv_cvt_pk_bf16_f32 v91, v26, v27\n\t"
        "v_cvt_pk_bf16_f32 v92, v20, v21\n\tv_cvt_pk_bf16_f32 v93, v22, v23\n\tv_cvt_pk_bf16_f32 v94, v28, v29\n\tv_cvt_pk_bf16_f32 v95, v30, v31\n\t"
        "v_cvt_pk_bf16_f32 v48, v48, v49\n\tv_cvt_pk_bf16_f32 v49, v50, v51\n\tv_mfma_f32_16x16x32_bf16 v[40:43], v[32:35], v[88:91], 0\n\t"
        "v_mfma_f32_16x16x32_bf16 v[44:47], v[36:39], v[92:95], 0\n\tv_cvt_pk_bf16_f32 v50, v52, v53\n\tv_cvt_pk_bf16_f32 v51, v54, v55\n\t"
        "buffer_store_dwordx4 v[48:51], off, %[rt], %[kv] offset:2048 sc1\n\tv_cvt_pk_bf16_f32 v56, v56, v57\n\tv_cvt_pk_bf16_f32 v57, v58, v59\n\t"
        "v_cvt_pk_bf16_f32 v58, v60, v61\n\tv_cvt_pk_bf16_f32 v59, v62, v63\n\tbuffer_store_dwordx4 v[56:59], off, %[rt], %[kv] offset:3072 sc1\n\t"
        "v_max3_f32 v40, v40, v41, v42\n\tv_max3_f32 v44, v44, v45, v46\n\tv_max_f32 v40, v40, v43\n\tv_max_f32 v44, v44, v47\n\t"
        "v_permlane16_swap_b32 v88, v92\n\tv_permlane16_swap_b32 v89, v93\n\tv_permlane16_swap_b32 v40, v44\n\tv_max_f32 v40, v40, v44\n\t"
        "v_mov_b32 v44, v40\n\tv_permlane16_swap_b32 v90, v94\n\tv_permlane16_swap_b32 v91, v95\n\tv_permlane32_swap_b32 v40, v44\n\t"
        "v_min_f32_e64 v9, -v40, -v44\n\t.Lmk_qb_e:\n\t"
        : "+{v[16:31]}"(acc[0]), "+{v[32:47]}"(acc[1]), "+{v[48:63]}"(acc[2]), "+{v[152:183]}"(r0), "+{v[184:215]}"(r1), "+{v[216:247]}"(r2), "=&{v[120:135]}"(nega), "=&{v[136:151]}"(oa), "=&{v[88:91]}"(qb0), "=&{v[92:95]}"(qb1), "=&{v9}"(nmb), [sa] "+s"(sa), [sn] "+s"(sn)
        : [pb0] "{v250}"(n.pb0), [pb1] "{v251}"(n.pb1), [pb2] "{v252}"(n.pb2), [rt] "s"(rt4), [rn] "s"(rt4), "{v8}"(nma), [kv] "s"(kvo + 131072), [w128] "s"(n.w128), "{v253}"(n.e1), "{v254}"(n.e2), "{v255}"(n.e3), [rs] "s"(rs4), [one] "{v[12:13]}"(tok), [fl] "s"(fl)
        : "v64", "v65", "v66", "v67", "v68", "v69", "v70", "v71", "v72", "v73", "v74", "v75", "v76", "v77", "v78", "v79", "v80", "v81", "v82", "v83", "v84", "v85", "v86", "v87", "v96", "v97", "v98", "v99", "v100", "v101", "v102", "v103", "v104", "v105", "v106", "v107", "v108", "v109", "v110", "v111", "v112", "v113", "v114", "v115", "v116", "v117", "v118", "v119", "memory", "scc");
  }
#pragma unroll 1
  for (int layer0 = 0; layer0 < 2; ++layer0) {
    asm volatile(".Lpgtop:\n\ts_cbranch_execz .Lpgret");
    f32x16 sx, negb, ob;
    f32x4 lb;
    u32x16 kv;
    u32x4 sel;
    int six;
    {
      STAMP(4);
      f32x4 la;
      unsigned t, nx;
      const unsigned fl = fkv + 64;
      STAMP(5);
      asm volatile(
        "; BLK a0\n\t.Lmk_a0_s:\n\ts_waitcnt lgkmcnt(0)\n\ts_barrier\n\tds_read_b128 v[164:167], %[k0] offset:0\n\t"
        "ds_read_b128 v[168:171], %[k0] offset:256\n\tds_read_b128 v[96:99], %[k0] offset:4096\n\tds_read_b128 v[100:103], %[k0] offset:4352\n\t"
        "ds_read_b128 v[104:107], %[v0] offset:2048\n\tv_add_u32 v113, 0x10000, %[k0]\n\tv_add_u32 v114, 0x10000, %[v0]\n\tv_mov_b64 v[152:153], 0\n\t"
        "v_mov_b64 v[154:155], 0\n\tv_mov_b64 v[64:65], 0\n\tv_mov_b64 v[66:67], 0\n\tv_mov_b64 v[68:69], 0\n\tv_mov_b64 v[70:71], 0\n\t"
        "v_mov_b64 v[72:73], 0\n\tv_mov_b64 v[74:75], 0\n\tv_mov_b64 v[76:77], 0\n\tv_mov_b64 v[78:79], 0\n\tv_mov_b64 v[80:81], 0\n\tv_mov_b64 v[82:83], 0\n\t"
        "s_mov_b32 vcc_lo, 0x100010\n\ts_mov_b32 vcc_hi, vcc_lo\n\tv_mov_b32 v85, 0x3f803f80\n\tv_cndmask_b32 v86, 0, v85, vcc\n\ts_lshr_b64 vcc, vcc, 4\n\t"
        "v_mov_b32 v87, v86\n\ts_nop 0\n\t.p2align 12,,12\n\t.Lpg3:\n\ts_cbranch_execz .Lpgret\n\tv_cndmask_b32 v84, 0, v85, vcc\n\tv_mov_b32 v112, 0xdddd\n\t"
        "v_mov_b32 v85, v84\n\ts_waitcnt lgkmcnt(4)\n\tv_mfma_f32_32x32x16_bf16 v[16:31], v[164:167], v[0:3], v[120:135]\n\ts_waitcnt lgkmcnt(3)\n\t"
        "v_mfma_f32_32x32x16_bf16 v[16:31], v[168:171], v[4:7], v[16:31]\n\tv_mov_b32 v48, v9\n\tv_mov_b32 v49, v9\n\tv_mov_b32 v50, v9\n\t"
        "v_mov_b32 v51, v9\n\tv_mov_b32 v52, v9\n\tv_mov_b32 v53, v9\n\tv_mov_b32 v54, v9\n\tv_mov_b32 v55, v9\n\tv_mov_b32 v56, v9\n\tv_mov_b32 v57, v9\n\t"
        "v_mov_b32 v58, v9\n\tv_mov_b32 v59, v9\n\tv_mov_b32 v60, v9\n\tv_mov_b32 v61, v9\n\tv_mov_b32 v62, v9\n\tv_mov_b32 v63, v9\n\t"
        "ds_read_b128 v[108:111], %[v0] offset:2560\n\tv_exp_f32 v16, v16\n\tv_exp_f32 v17, v17\n\tv_exp_f32 v18, v18\n\tv_exp_f32 v19, v19\n\t"
        "s_waitcnt lgkmcnt(2)\n\tv_mfma_f32_32x32x16_bf16 v[32:47], v[96:99], v[0:3], v[120:135]\n\tv_exp_f32 v20, v20\n\tv_exp_f32 v21, v21\n\t"
        "v_exp_f32 v22, v22\n\tv_exp_f32 v23, v23\n\tv_mfma_f32_32x32x16_bf16 v[32:47], v[100:103], v[4:7], v[32:47]\n\t"
        "ds_read_b128 v[96:99], %[k0] offset:8192\n\tds_read_b128 v[100:103], %[k0] offset:8448\n\tv_exp_f32 v24, v24\n\tv_exp_f32 v25, v25\n\t"
        "v_exp_f32 v26, v26\n\tv_exp_f32 v27, v27\n\ts_waitcnt lgkmcnt(3)\n\tv_smfmac_f32_32x32x32_bf16 v[136:151], v[104:107], v[16:23], v112\n\t"
        "ds_read_b128 v[104:107], %[v0] offset:6144\n\tv_exp_f32 v28, v28\n\tv_exp_f32 v29, v29\n\tv_exp_f32 v30, v30\n\tv_exp_f32 v31, v31\n\t"
        "v_smfmac_f32_16x16x64_bf16 v[152:155], v[84:87], v[16:23], v112\n\ts_waitcnt lgkmcnt(3)\n\t"
        "v_smfmac_f32_32x32x32_bf16 v[136:151], v[108:111], v[24:31], v112\n\tds_read_b128 v[108:111], %[v0] offset:6656\n\tv_exp_f32 v32, v32\n\t"
        "v_exp_f32 v33, v33\n\tv_exp_f32 v34, v34\n\tv_exp_f32 v35, v35\n\tv_smfmac_f32_16x16x64_bf16 v[152:155], v[84:87], v[24:31], v112\n\t"
        "s_waitcnt lgkmcnt(2)\n\tv_mfma_f32_32x32x16_bf16 v[16:31], v[96:99], v[0:3], v[120:135]\n\tv_exp_f32 v36, v36\n\tv_exp_f32 v37, v37\n\t"
        "v_exp_f32 v38, v38\n\tv_exp_f32 v39, v39\n\tv_mfma_f32_32x32x16_bf16 v[16:31], v[100:103], v[4:7], v[16:31]\n\t"
        "ds_read_b128 v[96:99], %[k0] offset:12288\n\tds_read_b128 v[100:103], %[k0] offset:12544\n\tv_exp_f32 v40, v40\n\tv_exp_f32 v41, v41\n\t"
        "v_exp_f32 v42, v42\n\tv_exp_f32 v43, v43\n\ts_waitcnt lgkmcnt(3)\n\tv_smfmac_f32_32x32x32_bf16 v[136:151], v[104:107], v[32:39], v112\n\t"
        "ds_read_b128 v[104:107], %[v0] offset:10240\n\tv_exp_f32 v44, v44\n\tv_exp_f32 v45, v45\n\tv_exp_f32 v46, v46\n\tv_exp_f32 v47, v47\n\t"
        "v_smfmac_f32_16x16x64_bf16 v[152:155], v[84:87], v[32:39], v112\n\ts_waitcnt lgkmcnt(3)\n\t"
        "v_smfmac_f32_32x32x32_bf16 v[136:151], v[108:111], v[40:47], v112\n\tds_read_b128 v[108:111], %[v0] offset:10752\n\tv_exp_f32 v16, v16\n\t"
        "v_exp_f32 v17, v17\n\tv_exp_f32 v18, v18\n\tv_exp_f32 v19, v19\n\tv_smfmac_f32_16x16x64_bf16 v[152:155], v[84:87], v[40:47], v112\n\t"
        "s_waitcnt lgkmcnt(2)\n\tv_mfma_f32_32x32x16_bf16 v[32:47], v[96:99], v[0:3], v[120:135]\n\tv_exp_f32 v20, v20\n\tv_exp_f32 v21, v21\n\t"
        "v_exp_f32 v22, v22\n\tv_exp_f32 v23, v23\n\tv_mfma_f32_32x32x16_bf16 v[32:47], v[100:103], v[4:7], v[32:47]\n\ts_barrier\n\t"
        "ds_read_b128 v[96:99], %[k0] offset:16384\n\tds_read_b128 v[100:103], %[k0] offset:16640\n\tv_exp_f32 v24, v24\n\tv_exp_f32 v25, v25\n\t"
        "v_exp_f32 v26, v26\n\tv_exp_f32 v27, v27\n\ts_waitcnt lgkmcnt(3)\n\tv_smfmac_f32_32x32x32_bf16 v[136:151], v[104:107], v[16:23], v112\n\t"
        "ds_read_b128 v[104:107], %[v0] offset:14336\n\tv_exp_f32 v28, v28\n\tv_exp_f32 v29, v29\n\tv_exp_f32 v30, v30\n\tv_exp_f32 v31, v31\n\t"
        "v_smfmac_f32_16x16x64_bf16 v[152:155], v[84:87], v[16:23], v112\n\ts_waitcnt lgkmcnt(3)\n\t"
        "v_smfmac_f32_32x32x32_bf16 v[136:151], v[108:111], v[24:31], v112\n\tds_read_b128 v[108:111], %[v0] offset:14848\n\tv_exp_f32 v32, v32\n\t"
        "v_exp_f32 v33, v33\n\tv_exp_f32 v34, v34\n\tv_exp_f32 v35, v35\n\tv_smfmac_f32_16x16x64_bf16 v[152:155], v[84:87], v[24:31], v112\n\t"
        "s_waitcnt lgkmcnt(2)\n\tv_mfma_f32_32x32x16_bf16 v[16:31], v[96:99], v[0:3], v[120:135]\n\tv_exp_f32 v36, v36\n\tv_exp_f32 v37, v37\n\t"
        "v_exp_f32 v38, v38\n\tv_exp_f32 v39, v39\n\tv_mfma_f32_32x32x16_bf16 v[16:31], v[100:103], v[4:7], v[16:31]\n\t"
        "ds_read_b128 v[96:99], %[k0] offset:20480\n\tds_read_b128 v[100:103], %[k0] offset:20736\n\tv_exp_f32 v40, v40\n\tv_exp_f32 v41, v41\n\t"
        "v_exp_f32 v42, v42\n\tv_exp_f32 v43, v43\n\ts_waitcnt lgkmcnt(3)\n\tv_smfmac_f32_32x32x32_bf16 v[136:151], v[104:107], v[32:39], v112\n\t"
        "ds_read_b128 v[104:107], %[v0] offset:18432\n\tv_exp_f32 v44, v44\n\tv_exp_f32 v45, v45\n\tv_exp_f32 v46, v46\n\tv_exp_f32 v47, v47\n\t"
        "v_smfmac_f32_16x16x64_bf16 v[152:155], v[84:87], v[32:39], v112\n\ts_waitcnt lgkmcnt(3)\n\t"
        "v_smfmac_f32_32x32x32_bf16 v[136:151], v[108:111], v[40:47], v112\n\tds_read_b128 v[108:111], %[v0] offset:18944\n\tv_exp_f32 v16, v16\n\t"
        "v_exp_f32 v17, v17\n\tv_exp_f32 v18, v18\n\tv_exp_f32 v19, v19\n\tv_smfmac_f32_16x16x64_bf16 v[152:155], v[84:87], v[40:47], v112\n\t"
        "s_waitcnt lgkmcnt(2)\n\tv_mfma_f32_32x32x16_bf16 v[32:47], v[96:99], v[0:3], v[120:135]\n\tv_exp_f32 v20, v20\n\tv_exp_f32 v21, v21\n\t"
        "v_exp_f32 v22, v22\n\tv_exp_f32 v23, v23\n\tv_mfma_f32_32x32x16_bf16 v[32:47], v[100:103], v[4:7], v[32:47]\n\t"
        "ds_read_b128 v[96:99], %[k0] offset:24576\n\tds_read_b128 v[100:103], %[k0] offset:24832\n\tv_exp_f32 v24, v24\n\tv_exp_f32 v25, v25\n\t"
        "v_exp_f32 v26, v26\n\tv_exp_f32 v27, v27\n\ts_waitcnt lgkmcnt(3)\n\tv_smfmac_f32_32x32x32_bf16 v[136:151], v[104:107], v[16:23], v112\n\t"
        "ds_read_b128 v[104:107], %[v0] offset:22528\n\tv_exp_f32 v28, v28\n\tv_exp_f32 v29, v29\n\tv_exp_f32 v30, v30\n\tv_exp_f32 v31, v31\n\t"
        "v_smfmac_f32_16x16x64_bf16 v[152:155], v[84:87], v[16:23], v112\n\ts_waitcnt lgkmcnt(3)\n\t"
        "v_smfmac_f32_32x32x32_bf16 v[136:151], v[108:111], v[24:31], v112\n\tds_read_b128 v[108:111], %[v0] offset:23040\n\tv_exp_f32 v32, v32\n\t"
        "v_exp_f32 v33, v33\n\tv_exp_f32 v34, v34\n\tv_exp_f32 v35, v35\n\tv_smfmac_f32_16x16x64_bf16 v[152:155], v[84:87], v[24:31], v112\n\t"
        "s_waitcnt lgkmcnt(2)\n\tv_mfma_f32_32x32x16_bf16 v[16:31], v[96:99], v[0:3], v[120:135]\n\tv_exp_f32 v36, v36\n\tv_exp_f32 v37, v37\n\t"
        "v_exp_f32 v38, v38\n\tv_exp_f32 v39, v39\n\tv_mfma_f32_32x32x16_bf16 v[16:31], v[100:103], v[4:7], v[16:31]\n\t"
        "ds_read_b128 v[96:99], %[k0] offset:28672\n\tds_read_b128 v[100:103], %[k0] offset:28928\n\tv_exp_f32 v40, v40\n\tv_exp_f32 v41, v41\n\t"
        "v_exp_f32 v42, v42\n\tv_exp_f32 v43, v43\n\ts_waitcnt lgkmcnt(3)\n\tv_smfmac_f32_32x32x32_bf16 v[136:151], v[104:107], v[32:39], v112\n\t"
        "ds_read_b128 v[104:107], %[v0] offset:26624\n\tv_exp_f32 v44, v44\n\tv_exp_f32 v45, v45\n\tv_exp_f32 v46, v46\n\tv_exp_f32 v47, v47\n\t"
        "v_smfmac_f32_16x16x64_bf16 v[152:155], v[84:87], v[32:39], v112\n\ts_waitcnt lgkmcnt(3)\n\t"
        "v_smfmac_f32_32x32x32_bf16 v[136:151], v[108:111], v[40:47], v112\n\tds_read_b128 v[108:111], %[v0] offset:27136\n\tv_exp_f32 v16, v16\n\t"
        "v_exp_f32 v17, v17\n\tv_exp_f32 v18, v18\n\tv_exp_f32 v19, v19\n\tv_smfmac_f32_16x16x64_bf16 v[152:155], v[84:87], v[40:47], v112\n\t"
        "s_waitcnt lgkmcnt(2)\n\tv_mfma_f32_32x32x16_bf16 v[32:47], v[96:99], v[0:3], v[120:135]\n\tv_exp_f32 v20, v20\n\tv_exp_f32 v21, v21\n\t"
        "v_exp_f32 v22, v22\n\tv_exp_f32 v23, v23\n\tv_mfma_f32_32x32x16_bf16 v[32:47], v[100:103], v[4:7], v[32:47]\n\ts_barrier\n\t"
        "ds_read_b128 v[96:99], %[k0] offset:32768\n\tds_read_b128 v[100:103], %[k0] offset:33024\n\ts_waitcnt vmcnt(0)\n\t"
        "buffer_store_dwordx2 %[one], off, %[rs], %[fl] sc1\n\tv_exp_f32 v24, v24\n\tv_exp_f32 v25, v25\n\tv_exp_f32 v26, v26\n\tv_exp_f32 v27, v27\n\t"
        "s_waitcnt lgkmcnt(3)\n\tv_smfmac_f32_32x32x32_bf16 v[136:151], v[104:107], v[16:23], v112\n\tds_read_b128 v[104:107], %[v0] offset:30720\n\t"
        "v_exp_f32 v28, v28\n\tv_exp_f32 v29, v29\n\tv_exp_f32 v30, v30\n\tv_exp_f32 v31, v31\n\t"
        "v_smfmac_f32_16x16x64_bf16 v[152:155], v[84:87], v[16:23], v112\n\ts_waitcnt lgkmcnt(3)\n\t"
        "v_smfmac_f32_32x32x32_bf16 v[136:151], v[108:111], v[24:31], v112\n\tds_read_b128 v[108:111], %[v0] offset:31232\n\tv_exp_f32 v32, v32\n\t"
        "v_exp_f32 v33, v33\n\tv_exp_f32 v34, v34\n\tv_exp_f32 v35, v35\n\tv_smfmac_f32_16x16x64_bf16 v[152:155], v[84:87], v[24:31], v112\n\t"
        "s_waitcnt lgkmcnt(2)\n\tv_mfma_f32_32x32x16_bf16 v[16:31], v[96:99], v[0:3], v[120:135]\n\tv_exp_f32 v36, v36\n\tv_exp_f32 v37, v37\n\t"
        "v_exp_f32 v38, v38\n\tv_exp_f32 v39, v39\n\tv_mfma_f32_32x32x16_bf16 v[16:31], v[100:103], v[4:7], v[16:31]\n\t"
        "ds_read_b128 v[96:99], %[k0] offset:36864\n\tds_read_b128 v[100:103], %[k0] offset:37120\n\tv_exp_f32 v40, v40\n\tv_exp_f32 v41, v41\n\t"
        "v_exp_f32 v42, v42\n\tv_exp_f32 v43, v43\n\ts_waitcnt lgkmcnt(3)\n\tv_smfmac_f32_32x32x32_bf16 v[136:151], v[104:107], v[32:39], v112\n\t"
        "ds_read_b128 v[104:107], %[v0] offset:34816\n\tv_exp_f32 v44, v44\n\tv_exp_f32 v45, v45\n\tv_exp_f32 v46, v46\n\tv_exp_f32 v47, v47\n\t"
        "v_smfmac_f32_16x16x64_bf16 v[152:155], v[84:87], v[32:39], v112\n\ts_waitcnt lgkmcnt(3)\n\t"
        "v_smfmac_f32_32x32x32_bf16 v[136:151], v[108:111], v[40:47], v112\n\tds_read_b128 v[108:111], %[v0] offset:35328\n\tv_exp_f32 v16, v16\n\t"
        "v_exp_f32 v17, v17\n\tv_exp_f32 v18, v18\n\tv_exp_f32 v19, v19\n\tv_smfmac_f32_16x16x64_bf16 v[152:155], v[84:87], v[40:47], v112\n\t"
        "s_waitcnt lgkmcnt(2)\n\tv_mfma_f32_32x32x16_bf16 v[32:47], v[96:99], v[0:3], v[120:135]\n\tv_exp_f32 v20, v20\n\tv_exp_f32 v21, v21\n\t"
        "v_exp_f32 v22, v22\n\tv_exp_f32 v23, v23\n\tv_mfma_f32_32x32x16_bf16 v[32:47], v[100:103], v[4:7], v[32:47]\n\t"
        "ds_read_b128 v[96:99], %[k0] offset:40960\n\tds_read_b128 v[100:103], %[k0] offset:41216\n\tv_exp_f32 v24, v24\n\tv_exp_f32 v25, v25\n\t"
        "v_exp_f32 v26, v26\n\tv_exp_f32 v27, v27\n\ts_waitcnt lgkmcnt(3)\n\tv_smfmac_f32_32x32x32_bf16 v[136:151], v[104:107], v[16:23], v112\n\t"
        "ds_read_b128 v[104:107], %[v0] offset:38912\n\tv_exp_f32 v28, v28\n\tv_exp_f32 v29, v29\n\tv_exp_f32 v30, v30\n\tv_exp_f32 v31, v31\n\t"
        "v_smfmac_f32_16x16x64_bf16 v[152:155], v[84:87], v[16:23], v112\n\ts_waitcnt lgkmcnt(3)\n\t"
        "v_smfmac_f32_32x32x32_bf16 v[136:151], v[108:111], v[24:31], v112\n\tds_read_b128 v[108:111], %[v0] offset:39424\n\tv_exp_f32 v32, v32\n\t"
        "v_exp_f32 v33, v33\n\tv_exp_f32 v34, v34\n\tv_exp_f32 v35, v35\n\tv_smfmac_f32_16x16x64_bf16 v[152:155], v[84:87], v[24:31], v112\n\t"
        "s_waitcnt lgkmcnt(2)\n\tv_mfma_f32_32x32x16_bf16 v[16:31], v[96:99], v[0:3], v[120:135]\n\tv_exp_f32 v36, v36\n\tv_exp_f32 v37, v37\n\t"
        "v_exp_f32 v38, v38\n\tv_exp_f32 v39, v39\n\tv_mfma_f32_32x32x16_bf16 v[16:31], v[100:103], v[4:7], v[16:31]\n\t"
        "ds_read_b128 v[96:99], %[k0] offset:45056\n\tds_read_b128 v[100:103], %[k0] offset:45312\n\tv_exp_f32 v40, v40\n\tv_exp_f32 v41, v41\n\t"
        "v_exp_f32 v42, v42\n\tv_exp_f32 v43, v43\n\ts_waitcnt lgkmcnt(3)\n\tv_smfmac_f32_32x32x32_bf16 v[136:151], v[104:107], v[32:39], v112\n\t"
        "ds_read_b128 v[104:107], %[v0] offset:43008\n\tv_exp_f32 v44, v44\n\tv_exp_f32 v45, v45\n\tv_exp_f32 v46, v46\n\tv_exp_f32 v47, v47\n\t"
        "v_smfmac_f32_16x16x64_bf16 v[152:155], v[84:87], v[32:39], v112\n\ts_waitcnt lgkmcnt(3)\n\t"
        "v_smfmac_f32_32x32x32_bf16 v[136:151], v[108:111], v[40:47], v112\n\tds_read_b128 v[108:111], %[v0] offset:43520\n\tv_exp_f32 v16, v16\n\t"
        "v_exp_f32 v17, v17\n\tv_exp_f32 v18, v18\n\tv_exp_f32 v19, v19\n\tv_smfmac_f32_16x16x64_bf16 v[152:155], v[84:87], v[40:47], v112\n\t"
        "s_waitcnt lgkmcnt(2)\n\tv_mfma_f32_32x32x16_bf16 v[32:47], v[96:99], v[0:3], v[120:135]\n\tv_exp_f32 v20, v20\n\tv_exp_f32 v21, v21\n\t"
        "v_exp_f32 v22, v22\n\tv_exp_f32 v23, v23\n\tv_mfma_f32_32x32x16_bf16 v[32:47], v[100:103], v[4:7], v[32:47]\n\ts_barrier\n\t"
        "ds_read_b128 v[96:99], %[k0] offset:49152\n\tds_read_b128 v[100:103], %[k0] offset:49408\n\tv_exp_f32 v24, v24\n\tv_exp_f32 v25, v25\n\t"
        "v_exp_f32 v26, v26\n\tv_exp_f32 v27, v27\n\ts_waitcnt lgkmcnt(3)\n\tv_smfmac_f32_32x32x32_bf16 v[136:151], v[104:107], v[16:23], v112\n\t"
        "ds_read_b128 v[104:107], %[v0] offset:47104\n\tv_exp_f32 v28, v28\n\tv_exp_f32 v29, v29\n\tv_exp_f32 v30, v30\n\tv_exp_f32 v31, v31\n\t"
        "v_smfmac_f32_16x16x64_bf16 v[152:155], v[84:87], v[16:23], v112\n\ts_waitcnt lgkmcnt(3)\n\t"
        "v_smfmac_f32_32x32x32_bf16 v[136:151], v[108:111], v[24:31], v112\n\tds_read_b128 v[108:111], %[v0] offset:47616\n\tv_exp_f32 v32, v32\n\t"
        "v_exp_f32 v33, v33\n\tv_exp_f32 v34, v34\n\tv_exp_f32 v35, v35\n\tv_smfmac_f32_16x16x64_bf16 v[152:155], v[84:87], v[24:31], v112\n\t"
        "s_waitcnt lgkmcnt(2)\n\tv_mfma_f32_32x32x16_bf16 v[16:31], v[96:99], v[0:3], v[120:135]\n\tv_exp_f32 v36, v36\n\tv_exp_f32 v37, v37\n\t"
        "v_exp_f32 v38, v38\n\tv_exp_f32 v39, v39\n\tv_mfma_f32_32x32x16_bf16 v[16:31], v[100:103], v[4:7], v[16:31]\n\t"
        "ds_read_b128 v[96:99], %[k0] offset:53248\n\tds_read_b128 v[100:103], %[k0] offset:53504\n\tv_exp_f32 v40, v40\n\tv_exp_f32 v41, v41\n\t"
        "v_exp_f32 v42, v42\n\tv_exp_f32 v43, v43\n\ts_waitcnt lgkmcnt(3)\n\tv_smfmac_f32_32x32x32_bf16 v[136:151], v[104:107], v[32:39], v112\n\t"
        "ds_read_b128 v[104:107], %[v0] offset:51200\n\tv_exp_f32 v44, v44\n\tv_exp_f32 v45, v45\n\tv_exp_f32 v46, v46\n\tv_exp_f32 v47, v47\n\t"
        "v_smfmac_f32_16x16x64_bf16 v[152:155], v[84:87], v[32:39], v112\n\ts_waitcnt lgkmcnt(3)\n\t"
        "v_smfmac_f32_32x32x32_bf16 v[136:151], v[108:111], v[40:47], v112\n\tds_read_b128 v[108:111], %[v0] offset:51712\n\tv_exp_f32 v16, v16\n\t"
        "v_exp_f32 v17, v17\n\tv_exp_f32 v18, v18\n\tv_exp_f32 v19, v19\n\tv_smfmac_f32_16x16x64_bf16 v[152:155], v[84:87], v[40:47], v112\n\t"
        "s_waitcnt lgkmcnt(2)\n\tv_mfma_f32_32x32x16_bf16 v[32:47], v[96:99], v[0:3], v[120:135]\n\tv_exp_f32 v20, v20\n\tv_exp_f32 v21, v21\n\t"
        "v_exp_f32 v22, v22\n\tv_exp_f32 v23, v23\n\tv_mfma_f32_32x32x16_bf16 v[32:47], v[100:103], v[4:7], v[32:47]\n\t"
        "ds_read_b128 v[96:99], %[k0] offset:57344\n\tds_read_b128 v[100:103], %[k0] offset:57600\n\tv_exp_f32 v24, v24\n\tv_exp_f32 v25, v25\n\t"
        "v_exp_f32 v26, v26\n\tv_exp_f32 v27, v27\n\ts_waitcnt lgkmcnt(3)\n\tv_smfmac_f32_32x32x32_bf16 v[136:151], v[104:107], v[16:23], v112\n\t"
        "ds_read_b128 v[104:107], %[v0] offset:55296\n\tv_exp_f32 v28, v28\n\tv_exp_f32 v29, v29\n\tv_exp_f32 v30, v30\n\tv_exp_f32 v31, v31\n\t"
        "v_smfmac_f32_16x16x64_bf16 v[152:155], v[84:87], v[16:23], v112\n\ts_waitcnt lgkmcnt(3)\n\t"
        "v_smfmac_f32_32x32x32_bf16 v[136:151], v[108:111], v[24:31], v112\n\tds_read_b128 v[108:111], %[v0] offset:55808\n\tv_exp_f32 v32, v32\n\t"
        "v_exp_f32 v33, v33\n\tv_exp_f32 v34, v34\n\tv_exp_f32 v35, v35\n\tv_smfmac_f32_16x16x64_bf16 v[152:155], v[84:87], v[24:31], v112\n\t"
        "s_waitcnt lgkmcnt(2)\n\tv_mfma_f32_32x32x16_bf16 v[16:31], v[96:99], v[0:3], v[120:135]\n\tv_exp_f32 v36, v36\n\tv_exp_f32 v37, v37\n\t"
        "v_exp_f32 v38, v38\n\tv_exp_f32 v39, v39\n\tv_mfma_f32_32x32x16_bf16 v[16:31], v[100:103], v[4:7], v[16:31]\n\t"
        "ds_read_b128 v[96:99], %[k0] offset:61440\n\tds_read_b128 v[100:103], %[k0] offset:61696\n\tv_exp_f32 v40, v40\n\tv_exp_f32 v41, v41\n\t"
        "v_exp_f32 v42, v42\n\tv_exp_f32 v43, v43\n\ts_waitcnt lgkmcnt(3)\n\tv_smfmac_f32_32x32x32_bf16 v[136:151], v[104:107], v[32:39], v112\n\t"
        "ds_read_b128 v[104:107], %[v0] offset:59392\n\tv_exp_f32 v44, v44\n\tv_exp_f32 v45, v45\n\tv_exp_f32 v46, v46\n\tv_exp_f32 v47, v47\n\t"
        "v_smfmac_f32_16x16x64_bf16 v[152:155], v[84:87], v[32:39], v112\n\ts_waitcnt lgkmcnt(3)\n\t"
        "v_smfmac_f32_32x32x32_bf16 v[136:151], v[108:111], v[40:47], v112\n\tds_read_b128 v[108:111], %[v0] offset:59904\n\tv_exp_f32 v16, v16\n\t"
        "v_exp_f32 v17, v17\n\tv_exp_f32 v18, v18\n\tv_exp_f32 v19, v19\n\tv_smfmac_f32_16x16x64_bf16 v[152:155], v[84:87], v[40:47], v112\n\t"
        "s_waitcnt lgkmcnt(2)\n\tv_mfma_f32_32x32x16_bf16 v[32:47], v[96:99], v[0:3], v[120:135]\n\tv_exp_f32 v20, v20\n\tv_exp_f32 v21, v21\n\t"
        "v_exp_f32 v22, v22\n\tv_exp_f32 v23, v23\n\tv_mfma_f32_32x32x16_bf16 v[32:47], v[100:103], v[4:7], v[32:47]\n\ts_barrier\n\t"
        "ds_read_b128 v[96:99], v113 offset:0\n\tds_read_b128 v[100:103], v113 offset:256\n\tv_exp_f32 v24, v24\n\tv_exp_f32 v25, v25\n\tv_exp_f32 v26, v26\n\t"
        "v_exp_f32 v27, v27\n\ts_waitcnt lgkmcnt(3)\n\tv_smfmac_f32_32x32x32_bf16 v[136:151], v[104:107], v[16:23], v112\n\t"
        "ds_read_b128 v[104:107], %[v0] offset:63488\n\tv_exp_f32 v28, v28\n\tv_exp_f32 v29, v29\n\tv_exp_f32 v30, v30\n\tv_exp_f32 v31, v31\n\t"
        "v_smfmac_f32_16x16x64_bf16 v[152:155], v[84:87], v[16:23], v112\n\ts_waitcnt lgkmcnt(3)\n\t"
        "v_smfmac_f32_32x32x32_bf16 v[136:151], v[108:111], v[24:31], v112\n\tds_read_b128 v[108:111], %[v0] offset:64000\n\tv_exp_f32 v32, v32\n\t"
        "v_exp_f32 v33, v33\n\tv_exp_f32 v34, v34\n\tv_exp_f32 v35, v35\n\tv_smfmac_f32_16x16x64_bf16 v[152:155], v[84:87], v[24:31], v112\n\t"
        "s_waitcnt lgkmcnt(2)\n\tv_mfma_f32_32x32x16_bf16 v[16:31], v[96:99], v[0:3], v[120:135]\n\tv_exp_f32 v36, v36\n\tv_exp_f32 v37, v37\n\t"
        "v_exp_f32 v38, v38\n\tv_exp_f32 v39, v39\n\tv_mfma_f32_32x32x16_bf16 v[16:31], v[100:103], v[4:7], v[16:31]\n\t"
        "ds_read_b128 v[164:167], v113 offset:4096\n\tds_read_b128 v[168:171], v113 offset:4352\n\tv_exp_f32 v40, v40\n\tv_exp_f32 v41, v41\n\t"
        "v_exp_f32 v42, v42\n\tv_exp_f32 v43, v43\n\ts_waitcnt lgkmcnt(3)\n\tv_smfmac_f32_32x32x32_bf16 v[136:151], v[104:107], v[32:39], v112\n\t"
        "ds_read_b128 v[104:107], v114 offset:2048\n\tv_exp_f32 v44, v44\n\tv_exp_f32 v45, v45\n\tv_exp_f32 v46, v46\n\tv_exp_f32 v47, v47\n\t"
        "v_smfmac_f32_16x16x64_bf16 v[152:155], v[84:87], v[32:39], v112\n\ts_waitcnt lgkmcnt(3)\n\t"
        "v_smfmac_f32_32x32x32_bf16 v[136:151], v[108:111], v[40:47], v112\n\tds_read_b128 v[108:111], v114 offset:2560\n\t"
        "ds_read_b128 v[172:175], v114 offset:6144\n\tds_read_b128 v[176:179], v114 offset:6656\n\tv_exp_f32 v16, v16\n\tv_exp_f32 v17, v17\n\t"
        "v_exp_f32 v18, v18\n\tv_exp_f32 v19, v19\n\tv_smfmac_f32_16x16x64_bf16 v[152:155], v[84:87], v[40:47], v112\n\ts_waitcnt lgkmcnt(3)\n\t"
        "v_mfma_f32_32x32x16_bf16 v[32:47], v[96:99], v[88:91], v[48:63]\n\tv_exp_f32 v20, v20\n\tv_exp_f32 v21, v21\n\tv_exp_f32 v22, v22\n\t"
        "v_exp_f32 v23, v23\n\tv_mfma_f32_32x32x16_bf16 v[32:47], v[100:103], v[92:95], v[32:47]\n\tv_exp_f32 v24, v24\n\tv_exp_f32 v25, v25\n\t"
        "v_exp_f32 v26, v26\n\tv_exp_f32 v27, v27\n\tv_smfmac_f32_32x32x32_bf16 v[136:151], v[104:107], v[16:23], v112\n\tv_exp_f32 v28, v28\n\t"
        "v_exp_f32 v29, v29\n\tv_exp_f32 v30, v30\n\tv_smfmac_f32_16x16x64_bf16 v[152:155], v[84:87], v[16:23], v112\n\ts_waitcnt lgkmcnt(0)\n\t"
        "v_exp_f32 v31, v31\n\tv_exp_f32 v32, v32\n\tv_smfmac_f32_32x32x32_bf16 v[136:151], v[108:111], v[24:31], v112\n\tv_exp_f32 v33, v33\n\t"
        "v_exp_f32 v34, v34\n\tv_exp_f32 v35, v35\n\tv_smfmac_f32_16x16x64_bf16 v[152:155], v[84:87], v[24:31], v112\n\t"
        "v_mfma_f32_32x32x16_bf16 v[16:31], v[164:167], v[0:3], v[120:135]\n\tds_read_b128 v[96:99], v113 offset:8192\n\tv_exp_f32 v36, v36\n\t"
        "v_exp_f32 v37, v37\n\tv_exp_f32 v38, v38\n\tv_exp_f32 v39, v39\n\tv_mfma_f32_32x32x16_bf16 v[16:31], v[168:171], v[4:7], v[16:31]\n\t"
        "ds_read_b128 v[100:103], v113 offset:8448\n\tv_exp_f32 v40, v40\n\tv_exp_f32 v41, v41\n\tv_exp_f32 v42, v42\n\tv_exp_f32 v43, v43\n\t"
        "v_smfmac_f32_32x32x32_bf16 v[64:79], v[104:107], v[32:39], v112\n\tds_read_b128 v[104:107], v114 offset:10240\n\tv_exp_f32 v44, v44\n\t"
        "v_exp_f32 v45, v45\n\tv_exp_f32 v46, v46\n\tv_smfmac_f32_16x16x64_bf16 v[80:83], v[84:87], v[32:39], v112\n\tv_exp_f32 v47, v47\n\t"
        "v_exp_f32 v16, v16\n\tv_smfmac_f32_32x32x32_bf16 v[64:79], v[108:111], v[40:47], v112\n\tds_read_b128 v[108:111], v114 offset:10752\n\t"
        "v_exp_f32 v17, v17\n\tv_exp_f32 v18, v18\n\tv_exp_f32 v19, v19\n\tv_smfmac_f32_16x16x64_bf16 v[80:83], v[84:87], v[40:47], v112\n\t"
        "v_mfma_f32_32x32x16_bf16 v[32:47], v[164:167], v[88:91], v[48:63]\n\tv_exp_f32 v20, v20\n\tv_exp_f32 v21, v21\n\tv_exp_f32 v22, v22\n\t"
        "v_exp_f32 v23, v23\n\tv_mfma_f32_32x32x16_bf16 v[32:47], v[168:171], v[92:95], v[32:47]\n\tv_exp_f32 v24, v24\n\tv_exp_f32 v25, v25\n\t"
        "v_exp_f32 v26, v26\n\tv_exp_f32 v27, v27\n\tv_smfmac_f32_32x32x32_bf16 v[136:151], v[172:175], v[16:23], v112\n\tv_exp_f32 v28, v28\n\t"
        "v_exp_f32 v29, v29\n\tv_exp_f32 v30, v30\n\tv_smfmac_f32_16x16x64_bf16 v[152:155], v[84:87], v[16:23], v112\n\ts_waitcnt lgkmcnt(0)\n\t"
        "v_exp_f32 v31, v31\n\tv_exp_f32 v32, v32\n\tv_smfmac_f32_32x32x32_bf16 v[136:151], v[176:179], v[24:31], v112\n\tv_exp_f32 v33, v33\n\t"
        "v_exp_f32 v34, v34\n\tv_exp_f32 v35, v35\n\tv_smfmac_f32_16x16x64_bf16 v[152:155], v[84:87], v[24:31], v112\n\t"
        "v_mfma_f32_32x32x16_bf16 v[16:31], v[96:99], v[0:3], v[120:135]\n\tds_read_b128 v[164:167], v113 offset:12288\n\tv_exp_f32 v36, v36\n\t"
        "v_exp_f32 v37, v37\n\tv_exp_f32 v38, v38\n\tv_exp_f32 v39, v39\n\tv_mfma_f32_32x32x16_bf16 v[16:31], v[100:103], v[4:7], v[16:31]\n\t"
        "ds_read_b128 v[168:171], v113 offset:12544\n\tv_exp_f32 v40, v40\n\tv_exp_f32 v41, v41\n\tv_exp_f32 v42, v42\n\tv_exp_f32 v43, v43\n\t"
        "v_smfmac_f32_32x32x32_bf16 v[64:79], v[172:175], v[32:39], v112\n\tds_read_b128 v[172:175], v114 offset:14336\n\tv_exp_f32 v44, v44\n\t"
        "v_exp_f32 v45, v45\n\tv_exp_f32 v46, v46\n\tv_smfmac_f32_16x16x64_bf16 v[80:83], v[84:87], v[32:39], v112\n\tv_exp_f32 v47, v47\n\t"
        "v_exp_f32 v16, v16\n\tv_smfmac_f32_32x32x32_bf16 v[64:79], v[176:179], v[40:47], v112\n\tds_read_b128 v[176:179], v114 offset:14848\n\t"
        "v_exp_f32 v17, v17\n\tv_exp_f32 v18, v18\n\tv_exp_f32 v19, v19\n\tv_smfmac_f32_16x16x64_bf16 v[80:83], v[84:87], v[40:47], v112\n\t"
        "v_mfma_f32_32x32x16_bf16 v[32:47], v[96:99], v[88:91], v[48:63]\n\tv_exp_f32 v20, v20\n\tv_exp_f32 v21, v21\n\tv_exp_f32 v22, v22\n\t"
        "v_exp_f32 v23, v23\n\tv_mfma_f32_32x32x16_bf16 v[32:47], v[100:103], v[92:95], v[32:47]\n\tv_exp_f32 v24, v24\n\tv_exp_f32 v25, v25\n\t"
        "v_exp_f32 v26, v26\n\tv_exp_f32 v27, v27\n\tv_smfmac_f32_32x32x32_bf16 v[136:151], v[104:107], v[16:23], v112\n\tv_exp_f32 v28, v28\n\t"
        "v_exp_f32 v29, v29\n\tv_exp_f32 v30, v30\n\tv_smfmac_f32_16x16x64_bf16 v[152:155], v[84:87], v[16:23], v112\n\ts_waitcnt lgkmcnt(0)\n\t"
        "v_exp_f32 v31, v31\n\tv_exp_f32 v32, v32\n\tv_smfmac_f32_32x32x32_bf16 v[136:151], v[108:111], v[24:31], v112\n\tv_exp_f32 v33, v33\n\t"
        "v_exp_f32 v34, v34\n\tv_exp_f32 v35, v35\n\tv_smfmac_f32_16x16x64_bf16 v[152:155], v[84:87], v[24:31], v112\n\ts_barrier\n\t"
        "v_mfma_f32_32x32x16_bf16 v[16:31], v[164:167], v[0:3], v[120:135]\n\tds_read_b128 v[96:99], v113 offset:16384\n\tv_exp_f32 v36, v36\n\t"
        "v_exp_f32 v37, v37\n\tv_exp_f32 v38, v38\n\tv_exp_f32 v39, v39\n\tv_mfma_f32_32x32x16_bf16 v[16:31], v[168:171], v[4:7], v[16:31]\n\t"
        "ds_read_b128 v[100:103], v113 offset:16640\n\tv_exp_f32 v40, v40\n\tv_exp_f32 v41, v41\n\tv_exp_f32 v42, v42\n\tv_exp_f32 v43, v43\n\t"
        "v_smfmac_f32_32x32x32_bf16 v[64:79], v[104:107], v[32:39], v112\n\tds_read_b128 v[104:107], v114 offset:18432\n\tv_exp_f32 v44, v44\n\t"
        "v_exp_f32 v45, v45\n\tv_exp_f32 v46, v46\n\tv_smfmac_f32_16x16x64_bf16 v[80:83], v[84:87], v[32:39], v112\n\tv_exp_f32 v47, v47\n\t"
        "v_exp_f32 v16, v16\n\tv_smfmac_f32_32x32x32_bf16 v[64:79], v[108:111], v[40:47], v112\n\tds_read_b128 v[108:111], v114 offset:18944\n\t"
        "v_exp_f32 v17, v17\n\tv_exp_f32 v18, v18\n\tv_exp_f32 v19, v19\n\tv_smfmac_f32_16x16x64_bf16 v[80:83], v[84:87], v[40:47], v112\n\t"
        "v_mfma_f32_32x32x16_bf16 v[32:47], v[164:167], v[88:91], v[48:63]\n\tv_exp_f32 v20, v20\n\tv_exp_f32 v21, v21\n\tv_exp_f32 v22, v22\n\t"
        "v_exp_f32 v23, v23\n\tv_mfma_f32_32x32x16_bf16 v[32:47], v[168:171], v[92:95], v[32:47]\n\tv_exp_f32 v24, v24\n\tv_exp_f32 v25, v25\n\t"
        "v_exp_f32 v26, v26\n\tv_exp_f32 v27, v27\n\tv_smfmac_f32_32x32x32_bf16 v[136:151], v[172:175], v[16:23], v112\n\tv_exp_f32 v28, v28\n\t"
        "v_exp_f32 v29, v29\n\tv_exp_f32 v30, v30\n\tv_smfmac_f32_16x16x64_bf16 v[152:155], v[84:87], v[16:23], v112\n\ts_waitcnt lgkmcnt(0)\n\t"
        "v_exp_f32 v31, v31\n\tv_exp_f32 v32, v32\n\tv_smfmac_f32_32x32x32_bf16 v[136:151], v[176:179], v[24:31], v112\n\tv_exp_f32 v33, v33\n\t"
        "v_exp_f32 v34, v34\n\tv_exp_f32 v35, v35\n\tv_smfmac_f32_16x16x64_bf16 v[152:155], v[84:87], v[24:31], v112\n\t"
        "v_mfma_f32_32x32x16_bf16 v[16:31], v[96:99], v[0:3], v[120:135]\n\tds_read_b128 v[164:167], v113 offset:20480\n\tv_exp_f32 v36, v36\n\t"
        "v_exp_f32 v37, v37\n\tv_exp_f32 v38, v38\n\tv_exp_f32 v39, v39\n\tv_mfma_f32_32x32x16_bf16 v[16:31], v[100:103], v[4:7], v[16:31]\n\t"
        "ds_read_b128 v[168:171], v113 offset:20736\n\tv_exp_f32 v40, v40\n\tv_exp_f32 v41, v41\n\tv_exp_f32 v42, v42\n\tv_exp_f32 v43, v43\n\t"
        "v_smfmac_f32_32x32x32_bf16 v[64:79], v[172:175], v[32:39], v112\n\tds_read_b128 v[172:175], v114 offset:22528\n\tv_exp_f32 v44, v44\n\t"
        "v_exp_f32 v45, v45\n\tv_exp_f32 v46, v46\n\tv_smfmac_f32_16x16x64_bf16 v[80:83], v[84:87], v[32:39], v112\n\tv_exp_f32 v47, v47\n\t"
        "v_exp_f32 v16, v16\n\tv_smfmac_f32_32x32x32_bf16 v[64:79], v[176:179], v[40:47], v112\n\tds_read_b128 v[176:179], v114 offset:23040\n\t"
        "v_exp_f32 v17, v17\n\tv_exp_f32 v18, v18\n\tv_exp_f32 v19, v19\n\tv_smfmac_f32_16x16x64_bf16 v[80:83], v[84:87], v[40:47], v112\n\t"
        "v_mfma_f32_32x32x16_bf16 v[32:47], v[96:99], v[88:91], v[48:63]\n\tv_exp_f32 v20, v20\n\tv_exp_f32 v21, v21\n\tv_exp_f32 v22, v22\n\t"
        "v_exp_f32 v23, v23\n\tv_mfma_f32_32x32x16_bf16 v[32:47], v[100:103], v[92:95], v[32:47]\n\tv_exp_f32 v24, v24\n\tv_exp_f32 v25, v25\n\t"
        "v_exp_f32 v26, v26\n\tv_exp_f32 v27, v27\n\tv_smfmac_f32_32x32x32_bf16 v[136:151], v[104:107], v[16:23], v112\n\tv_exp_f32 v28, v28\n\t"
        "v_exp_f32 v29, v29\n\tv_exp_f32 v30, v30\n\tv_smfmac_f32_16x16x64_bf16 v[152:155], v[84:87], v[16:23], v112\n\ts_waitcnt lgkmcnt(0)\n\t"
        "v_exp_f32 v31, v31\n\tv_exp_f32 v32, v32\n\tv_smfmac_f32_32x32x32_bf16 v[136:151], v[108:111], v[24:31], v112\n\tv_exp_f32 v33, v33\n\t"
        "v_exp_f32 v34, v34\n\tv_exp_f32 v35, v35\n\tv_smfmac_f32_16x16x64_bf16 v[152:155], v[84:87], v[24:31], v112\n\t"
        "v_mfma_f32_32x32x16_bf16 v[16:31], v[164:167], v[0:3], v[120:135]\n\tds_read_b128 v[96:99], v113 offset:24576\n\tv_exp_f32 v36, v36\n\t"
        "v_exp_f32 v37, v37\n\tv_exp_f32 v38, v38\n\tv_exp_f32 v39, v39\n\tv_mfma_f32_32x32x16_bf16 v[16:31], v[168:171], v[4:7], v[16:31]\n\t"
        "ds_read_b128 v[100:103], v113 offset:24832\n\tv_exp_f32 v40, v40\n\tv_exp_f32 v41, v41\n\tv_exp_f32 v42, v42\n\tv_exp_f32 v43, v43\n\t"
        "v_smfmac_f32_32x32x32_bf16 v[64:79], v[104:107], v[32:39], v112\n\tds_read_b128 v[104:107], v114 offset:26624\n\tv_exp_f32 v44, v44\n\t"
        "v_exp_f32 v45, v45\n\tv_exp_f32 v46, v46\n\tv_smfmac_f32_16x16x64_bf16 v[80:83], v[84:87], v[32:39], v112\n\tv_exp_f32 v47, v47\n\t"
        "v_exp_f32 v16, v16\n\tv_smfmac_f32_32x32x32_bf16 v[64:79], v[108:111], v[40:47], v112\n\tds_read_b128 v[108:111], v114 offset:27136\n\t"
        "v_exp_f32 v17, v17\n\tv_exp_f32 v18, v18\n\tv_exp_f32 v19, v19\n\tv_smfmac_f32_16x16x64_bf16 v[80:83], v[84:87], v[40:47], v112\n\t"
        "v_mfma_f32_32x32x16_bf16 v[32:47], v[164:167], v[88:91], v[48:63]\n\tv_exp_f32 v20, v20\n\tv_exp_f32 v21, v21\n\tv_exp_f32 v22, v22\n\t"
        "v_exp_f32 v23, v23\n\tv_mfma_f32_32x32x16_bf16 v[32:47], v[168:171], v[92:95], v[32:47]\n\tv_exp_f32 v24, v24\n\tv_exp_f32 v25, v25\n\t"
        "v_exp_f32 v26, v26\n\tv_exp_f32 v27, v27\n\tv_smfmac_f32_32x32x32_bf16 v[136:151], v[172:175], v[16:23], v112\n\tv_exp_f32 v28, v28\n\t"
        "v_exp_f32 v29, v29\n\tv_exp_f32 v30, v30\n\tv_smfmac_f32_16x16x64_bf16 v[152:155], v[84:87], v[16:23], v112\n\ts_waitcnt lgkmcnt(0)\n\t"
        "v_exp_f32 v31, v31\n\tv_exp_f32 v32, v32\n\tv_smfmac_f32_32x32x32_bf16 v[136:151], v[176:179], v[24:31], v112\n\tv_exp_f32 v33, v33\n\t"
        "v_exp_f32 v34, v34\n\tv_exp_f32 v35, v35\n\tv_smfmac_f32_16x16x64_bf16 v[152:155], v[84:87], v[24:31], v112\n\t.p2align 12,,12\n\t.Lpg4:\n\t"
        "s_cbranch_execz .Lpgret\n\tv_mfma_f32_32x32x16_bf16 v[16:31], v[96:99], v[0:3], v[120:135]\n\tds_read_b128 v[164:167], v113 offset:28672\n\t"
        "v_exp_f32 v36, v36\n\tv_exp_f32 v37, v37\n\tv_exp_f32 v38, v38\n\tv_exp_f32 v39, v39\n\t"
        "v_mfma_f32_32x32x16_bf16 v[16:31], v[100:103], v[4:7], v[16:31]\n\tds_read_b128 v[168:171], v113 offset:28928\n\tv_exp_f32 v40, v40\n\t"
        "v_exp_f32 v41, v41\n\tv_exp_f32 v42, v42\n\tv_exp_f32 v43, v43\n\tv_smfmac_f32_32x32x32_bf16 v[64:79], v[172:175], v[32:39], v112\n\t"
        "ds_read_b128 v[172:175], v114 offset:30720\n\tv_exp_f32 v44, v44\n\tv_exp_f32 v45, v45\n\tv_exp_f32 v46, v46\n\t"
        "v_smfmac_f32_16x16x64_bf16 v[80:83], v[84:87], v[32:39], v112\n\tv_exp_f32 v47, v47\n\tv_exp_f32 v16, v16\n\t"
        "v_smfmac_f32_32x32x32_bf16 v[64:79], v[176:179], v[40:47], v112\n\tds_read_b128 v[176:179], v114 offset:31232\n\tv_exp_f32 v17, v17\n\t"
        "v_exp_f32 v18, v18\n\tv_exp_f32 v19, v19\n\tv_smfmac_f32_16x16x64_bf16 v[80:83], v[84:87], v[40:47], v112\n\t"
        "v_mfma_f32_32x32x16_bf16 v[32:47], v[96:99], v[88:91], v[48:63]\n\tv_exp_f32 v20, v20\n\tv_exp_f32 v21, v21\n\tv_exp_f32 v22, v22\n\t"
        "v_exp_f32 v23, v23\n\tv_mfma_f32_32x32x16_bf16 v[32:47], v[100:103], v[92:95], v[32:47]\n\tv_exp_f32 v24, v24\n\tv_exp_f32 v25, v25\n\t"
        "v_exp_f32 v26, v26\n\tv_exp_f32 v27, v27\n\tv_smfmac_f32_32x32x32_bf16 v[136:151], v[104:107], v[16:23], v112\n\tv_exp_f32 v28, v28\n\t"
        "v_exp_f32 v29, v29\n\tv_exp_f32 v30, v30\n\tv_smfmac_f32_16x16x64_bf16 v[152:155], v[84:87], v[16:23], v112\n\ts_waitcnt lgkmcnt(0)\n\t"
        "v_exp_f32 v31, v31\n\tv_exp_f32 v32, v32\n\tv_smfmac_f32_32x32x32_bf16 v[136:151], v[108:111], v[24:31], v112\n\tv_exp_f32 v33, v33\n\t"
        "v_exp_f32 v34, v34\n\tv_exp_f32 v35, v35\n\tv_smfmac_f32_16x16x64_bf16 v[152:155], v[84:87], v[24:31], v112\n\ts_barrier\n\t"
        "v_mfma_f32_32x32x16_bf16 v[16:31], v[164:167], v[0:3], v[120:135]\n\tds_read_b128 v[96:99], %[k0] offset:0\n\tv_exp_f32 v36, v36\n\t"
        "v_exp_f32 v37, v37\n\tv_exp_f32 v38, v38\n\tv_exp_f32 v39, v39\n\tv_mfma_f32_32x32x16_bf16 v[16:31], v[168:171], v[4:7], v[16:31]\n\t"
        "ds_read_b128 v[100:103], %[k0] offset:256\n\tv_exp_f32 v40, v40\n\tv_exp_f32 v41, v41\n\tv_exp_f32 v42, v42\n\tv_exp_f32 v43, v43\n\t"
        "v_smfmac_f32_32x32x32_bf16 v[64:79], v[104:107], v[32:39], v112\n\tds_read_b128 v[104:107], %[v0] offset:2048\n\tv_exp_f32 v44, v44\n\t"
        "v_exp_f32 v45, v45\n\tv_exp_f32 v46, v46\n\tv_smfmac_f32_16x16x64_bf16 v[80:83], v[84:87], v[32:39], v112\n\tv_exp_f32 v47, v47\n\t"
        "v_exp_f32 v16, v16\n\tv_smfmac_f32_32x32x32_bf16 v[64:79], v[108:111], v[40:47], v112\n\tds_read_b128 v[108:111], %[v0] offset:2560\n\t"
        "v_exp_f32 v17, v17\n\tv_exp_f32 v18, v18\n\tv_exp_f32 v19, v19\n\tv_smfmac_f32_16x16x64_bf16 v[80:83], v[84:87], v[40:47], v112\n\t"
        "v_mfma_f32_32x32x16_bf16 v[32:47], v[164:167], v[88:91], v[48:63]\n\tv_exp_f32 v20, v20\n\tv_exp_f32 v21, v21\n\tv_exp_f32 v22, v22\n\t"
        "v_exp_f32 v23, v23\n\tv_mfma_f32_32x32x16_bf16 v[32:47], v[168:171], v[92:95], v[32:47]\n\tv_exp_f32 v24, v24\n\tv_exp_f32 v25, v25\n\t"
        "v_exp_f32 v26, v26\n\tv_exp_f32 v27, v27\n\tv_smfmac_f32_32x32x32_bf16 v[136:151], v[172:175], v[16:23], v112\n\tv_exp_f32 v28, v28\n\t"
        "v_exp_f32 v29, v29\n\tv_exp_f32 v30, v30\n\tv_smfmac_f32_16x16x64_bf16 v[152:155], v[84:87], v[16:23], v112\n\ts_waitcnt lgkmcnt(0)\n\t"
        "v_exp_f32 v31, v31\n\tv_exp_f32 v32, v32\n\tv_smfmac_f32_32x32x32_bf16 v[136:151], v[176:179], v[24:31], v112\n\tv_exp_f32 v33, v33\n\t"
        "v_exp_f32 v34, v34\n\tv_exp_f32 v35, v35\n\tv_smfmac_f32_16x16x64_bf16 v[152:155], v[84:87], v[24:31], v112\n\t"
        "v_mfma_f32_32x32x16_bf16 v[16:31], v[96:99], v[0:3], v[120:135]\n\tds_read_b128 v[164:167], %[k0] offset:4096\n\tv_exp_f32 v36, v36\n\t"
        "v_exp_f32 v37, v37\n\tv_exp_f32 v38, v38\n\tv_exp_f32 v39, v39\n\tv_mfma_f32_32x32x16_bf16 v[16:31], v[100:103], v[4:7], v[16:31]\n\t"
        "ds_read_b128 v[168:171], %[k0] offset:4352\n\tv_exp_f32 v40, v40\n\tv_exp_f32 v41, v41\n\tv_exp_f32 v42, v42\n\tv_exp_f32 v43, v43\n\t"
        "v_smfmac_f32_32x32x32_bf16 v[64:79], v[172:175], v[32:39], v112\n\tds_read_b128 v[172:175], %[v0] offset:6144\n\tv_exp_f32 v44, v44\n\t"
        "v_exp_f32 v45, v45\n\tv_exp_f32 v46, v46\n\tv_smfmac_f32_16x16x64_bf16 v[80:83], v[84:87], v[32:39], v112\n\tv_exp_f32 v47, v47\n\t"
        "v_exp_f32 v16, v16\n\tv_smfmac_f32_32x32x32_bf16 v[64:79], v[176:179], v[40:47], v112\n\tds_read_b128 v[176:179], %[v0] offset:6656\n\t"
        "v_exp_f32 v17, v17\n\tv_exp_f32 v18, v18\n\tv_exp_f32 v19, v19\n\tv_smfmac_f32_16x16x64_bf16 v[80:83], v[84:87], v[40:47], v112\n\t"
        "v_mfma_f32_32x32x16_bf16 v[32:47], v[96:99], v[88:91], v[48:63]\n\tv_exp_f32 v20, v20\n\tv_exp_f32 v21, v21\n\tv_exp_f32 v22, v22\n\t"
        "v_exp_f32 v23, v23\n\tv_mfma_f32_32x32x16_bf16 v[32:47], v[100:103], v[92:95], v[32:47]\n\tv_exp_f32 v24, v24\n\tv_exp_f32 v25, v25\n\t"
        "v_exp_f32 v26, v26\n\tv_exp_f32 v27, v27\n\tv_smfmac_f32_32x32x32_bf16 v[136:151], v[104:107], v[16:23], v112\n\tv_exp_f32 v28, v28\n\t"
        "v_exp_f32 v29, v29\n\tv_exp_f32 v30, v30\n\tv_smfmac_f32_16x16x64_bf16 v[152:155], v[84:87], v[16:23], v112\n\ts_waitcnt lgkmcnt(0)\n\t"
        "v_exp_f32 v31, v31\n\tv_exp_f32 v32, v32\n\tv_smfmac_f32_32x32x32_bf16 v[136:151], v[108:111], v[24:31], v112\n\tv_exp_f32 v33, v33\n\t"
        "v_exp_f32 v34, v34\n\tv_exp_f32 v35, v35\n\tv_smfmac_f32_16x16x64_bf16 v[152:155], v[84:87], v[24:31], v112\n\t"
        "v_mfma_f32_32x32x16_bf16 v[16:31], v[164:167], v[0:3], v[120:135]\n\tds_read_b128 v[96:99], %[k0] offset:8192\n\tv_exp_f32 v36, v36\n\t"
        "v_exp_f32 v37, v37\n\tv_exp_f32 v38, v38\n\tv_exp_f32 v39, v39\n\tv_mfma_f32_32x32x16_bf16 v[16:31], v[168:171], v[4:7], v[16:31]\n\t"
        "ds_read_b128 v[100:103], %[k0] offset:8448\n\tv_exp_f32 v40, v40\n\tv_exp_f32 v41, v41\n\tv_exp_f32 v42, v42\n\tv_exp_f32 v43, v43\n\t"
        "v_smfmac_f32_32x32x32_bf16 v[64:79], v[104:107], v[32:39], v112\n\tds_read_b128 v[104:107], %[v0] offset:10240\n\tv_exp_f32 v44, v44\n\t"
        "v_exp_f32 v45, v45\n\tv_exp_f32 v46, v46\n\tv_smfmac_f32_16x16x64_bf16 v[80:83], v[84:87], v[32:39], v112\n\tv_exp_f32 v47, v47\n\t"
        "v_exp_f32 v16, v16\n\tv_smfmac_f32_32x32x32_bf16 v[64:79], v[108:111], v[40:47], v112\n\tds_read_b128 v[108:111], %[v0] offset:10752\n\t"
        "v_exp_f32 v17, v17\n\tv_exp_f32 v18, v18\n\tv_exp_f32 v19, v19\n\tv_smfmac_f32_16x16x64_bf16 v[80:83], v[84:87], v[40:47], v112\n\t"
        "v_mfma_f32_32x32x16_bf16 v[32:47], v[164:167], v[88:91], v[48:63]\n\tv_exp_f32 v20, v20\n\tv_exp_f32 v21, v21\n\tv_exp_f32 v22, v22\n\t"
        "v_exp_f32 v23, v23\n\tv_mfma_f32_32x32x16_bf16 v[32:47], v[168:171], v[92:95], v[32:47]\n\tv_exp_f32 v24, v24\n\tv_exp_f32 v25, v25\n\t"
        "v_exp_f32 v26, v26\n\tv_exp_f32 v27, v27\n\tv_smfmac_f32_32x32x32_bf16 v[136:151], v[172:175], v[16:23], v112\n\tv_exp_f32 v28, v28\n\t"
        "v_exp_f32 v29, v29\n\tv_exp_f32 v30, v30\n\tv_smfmac_f32_16x16x64_bf16 v[152:155], v[84:87], v[16:23], v112\n\ts_waitcnt lgkmcnt(0)\n\t"
        "v_exp_f32 v31, v31\n\tv_exp_f32 v32, v32\n\tv_smfmac_f32_32x32x32_bf16 v[136:151], v[176:179], v[24:31], v112\n\tv_exp_f32 v33, v33\n\t"
        "v_exp_f32 v34, v34\n\tv_exp_f32 v35, v35\n\tv_smfmac_f32_16x16x64_bf16 v[152:155], v[84:87], v[24:31], v112\n\t"
        "v_mfma_f32_32x32x16_bf16 v[16:31], v[96:99], v[0:3], v[120:135]\n\tds_read_b128 v[164:167], %[k0] offset:12288\n\tv_exp_f32 v36, v36\n\t"
        "v_exp_f32 v37, v37\n\tv_exp_f32 v38, v38\n\tv_exp_f32 v39, v39\n\tv_mfma_f32_32x32x16_bf16 v[16:31], v[100:103], v[4:7], v[16:31]\n\t"
        "ds_read_b128 v[168:171], %[k0] offset:12544\n\tv_exp_f32 v40, v40\n\tv_exp_f32 v41, v41\n\tv_exp_f32 v42, v42\n\tv_exp_f32 v43, v43\n\t"
        "v_smfmac_f32_32x32x32_bf16 v[64:79], v[172:175], v[32:39], v112\n\tds_read_b128 v[172:175], %[v0] offset:14336\n\tv_exp_f32 v44, v44\n\t"
        "v_exp_f32 v45, v45\n\tv_exp_f32 v46, v46\n\tv_smfmac_f32_16x16x64_bf16 v[80:83], v[84:87], v[32:39], v112\n\tv_exp_f32 v47, v47\n\t"
        "v_exp_f32 v16, v16\n\tv_smfmac_f32_32x32x32_bf16 v[64:79], v[176:179], v[40:47], v112\n\tds_read_b128 v[176:179], %[v0] offset:14848\n\t"
        "v_exp_f32 v17, v17\n\tv_exp_f32 v18, v18\n\tv_exp_f32 v19, v19\n\tv_smfmac_f32_16x16x64_bf16 v[80:83], v[84:87], v[40:47], v112\n\t"
        "v_mfma_f32_32x32x16_bf16 v[32:47], v[96:99], v[88:91], v[48:63]\n\tv_exp_f32 v20, v20\n\tv_exp_f32 v21, v21\n\tv_exp_f32 v22, v22\n\t"
        "v_exp_f32 v23, v23\n\tv_mfma_f32_32x32x16_bf16 v[32:47], v[100:103], v[92:95], v[32:47]\n\tv_exp_f32 v24, v24\n\tv_exp_f32 v25, v25\n\t"
        "v_exp_f32 v26, v26\n\tv_exp_f32 v27, v27\n\tv_smfmac_f32_32x32x32_bf16 v[136:151], v[104:107], v[16:23], v112\n\tv_exp_f32 v28, v28\n\t"
        "v_exp_f32 v29, v29\n\tv_exp_f32 v30, v30\n\tv_smfmac_f32_16x16x64_bf16 v[152:155], v[84:87], v[16:23], v112\n\ts_waitcnt lgkmcnt(0)\n\t"
        "v_exp_f32 v31, v31\n\tv_exp_f32 v32, v32\n\tv_smfmac_f32_32x32x32_bf16 v[136:151], v[108:111], v[24:31], v112\n\tv_exp_f32 v33, v33\n\t"
        "v_exp_f32 v34, v34\n\tv_exp_f32 v35, v35\n\tv_smfmac_f32_16x16x64_bf16 v[152:155], v[84:87], v[24:31], v112\n\ts_barrier\n\t"
        "v_mfma_f32_32x32x16_bf16 v[16:31], v[164:167], v[0:3], v[120:135]\n\tds_read_b128 v[96:99], %[k0] offset:16384\n\tv_exp_f32 v36, v36\n\t"
        "v_exp_f32 v37, v37\n\tv_exp_f32 v38, v38\n\tv_exp_f32 v39, v39\n\tv_mfma_f32_32x32x16_bf16 v[16:31], v[168:171], v[4:7], v[16:31]\n\t"
        "ds_read_b128 v[100:103], %[k0] offset:16640\n\tv_exp_f32 v40, v40\n\tv_exp_f32 v41, v41\n\tv_exp_f32 v42, v42\n\tv_exp_f32 v43, v43\n\t"
        "v_smfmac_f32_32x32x32_bf16 v[64:79], v[104:107], v[32:39], v112\n\tds_read_b128 v[104:107], %[v0] offset:18432\n\tv_exp_f32 v44, v44\n\t"
        "v_exp_f32 v45, v45\n\tv_exp_f32 v46, v46\n\tv_smfmac_f32_16x16x64_bf16 v[80:83], v[84:87], v[32:39], v112\n\tv_exp_f32 v47, v47\n\t"
        "v_exp_f32 v16, v16\n\tv_smfmac_f32_32x32x32_bf16 v[64:79], v[108:111], v[40:47], v112\n\tds_read_b128 v[108:111], %[v0] offset:18944\n\t"
        "v_exp_f32 v17, v17\n\tv_exp_f32 v18, v18\n\tv_exp_f32 v19, v19\n\tv_smfmac_f32_16x16x64_bf16 v[80:83], v[84:87], v[40:47], v112\n\t"
        "v_mfma_f32_32x32x16_bf16 v[32:47], v[164:167], v[88:91], v[48:63]\n\tv_exp_f32 v20, v20\n\tv_exp_f32 v21, v21\n\tv_exp_f32 v22, v22\n\t"
        "v_exp_f32 v23, v23\n\tv_mfma_f32_32x32x16_bf16 v[32:47], v[168:171], v[92:95], v[32:47]\n\tv_exp_f32 v24, v24\n\tv_exp_f32 v25, v25\n\t"
        "v_exp_f32 v26, v26\n\tv_exp_f32 v27, v27\n\tv_smfmac_f32_32x32x32_bf16 v[136:151], v[172:175], v[16:23], v112\n\tv_exp_f32 v28, v28\n\t"
        "v_exp_f32 v29, v29\n\tv_exp_f32 v30, v30\n\tv_smfmac_f32_16x16x64_bf16 v[152:155], v[84:87], v[16:23], v112\n\ts_waitcnt lgkmcnt(0)\n\t"
        "v_exp_f32 v31, v31\n\tv_exp_f32 v32, v32\n\tv_smfmac_f32_32x32x32_bf16 v[136:151], v[176:179], v[24:31], v112\n\tv_exp_f32 v33, v33\n\t"
        "v_exp_f32 v34, v34\n\tv_exp_f32 v35, v35\n\tv_smfmac_f32_16x16x64_bf16 v[152:155], v[84:87], v[24:31], v112\n\t"
        "v_mfma_f32_32x32x16_bf16 v[16:31], v[96:99], v[0:3], v[120:135]\n\tds_read_b128 v[164:167], %[k0] offset:20480\n\tv_exp_f32 v36, v36\n\t"
        "v_exp_f32 v37, v37\n\tv_exp_f32 v38, v38\n\tv_exp_f32 v39, v39\n\tv_mfma_f32_32x32x16_bf16 v[16:31], v[100:103], v[4:7], v[16:31]\n\t"
        "ds_read_b128 v[168:171], %[k0] offset:20736\n\tv_exp_f32 v40, v40\n\tv_exp_f32 v41, v41\n\tv_exp_f32 v42, v42\n\tv_exp_f32 v43, v43\n\t"
        "v_smfmac_f32_32x32x32_bf16 v[64:79], v[172:175], v[32:39], v112\n\tds_read_b128 v[172:175], %[v0] offset:22528\n\tv_exp_f32 v44, v44\n\t"
        "v_exp_f32 v45, v45\n\tv_exp_f32 v46, v46\n\tv_smfmac_f32_16x16x64_bf16 v[80:83], v[84:87], v[32:39], v112\n\tv_exp_f32 v47, v47\n\t"
        "v_exp_f32 v16, v16\n\tv_smfmac_f32_32x32x32_bf16 v[64:79], v[176:179], v[40:47], v112\n\tds_read_b128 v[176:179], %[v0] offset:23040\n\t"
        "v_exp_f32 v17, v17\n\tv_exp_f32 v18, v18\n\tv_exp_f32 v19, v19\n\tv_smfmac_f32_16x16x64_bf16 v[80:83], v[84:87], v[40:47], v112\n\t"
        "v_mfma_f32_32x32x16_bf16 v[32:47], v[96:99], v[88:91], v[48:63]\n\tv_exp_f32 v20, v20\n\tv_exp_f32 v21, v21\n\tv_exp_f32 v22, v22\n\t"
        "v_exp_f32 v23, v23\n\tv_mfma_f32_32x32x16_bf16 v[32:47], v[100:103], v[92:95], v[32:47]\n\tv_exp_f32 v24, v24\n\tv_exp_f32 v25, v25\n\t"
        "v_exp_f32 v26, v26\n\tv_exp_f32 v27, v27\n\tv_smfmac_f32_32x32x32_bf16 v[136:151], v[104:107], v[16:23], v112\n\tv_exp_f32 v28, v28\n\t"
        "v_exp_f32 v29, v29\n\tv_exp_f32 v30, v30\n\tv_smfmac_f32_16x16x64_bf16 v[152:155], v[84:87], v[16:23], v112\n\ts_waitcnt lgkmcnt(0)\n\t"
        "v_exp_f32 v31, v31\n\tv_exp_f32 v32, v32\n\tv_smfmac_f32_32x32x32_bf16 v[136:151], v[108:111], v[24:31], v112\n\tv_exp_f32 v33, v33\n\t"
        "v_exp_f32 v34, v34\n\tv_exp_f32 v35, v35\n\tv_smfmac_f32_16x16x64_bf16 v[152:155], v[84:87], v[24:31], v112\n\t"
        "v_mfma_f32_32x32x16_bf16 v[16:31], v[164:167], v[0:3], v[120:135]\n\tds_read_b128 v[96:99], %[k0] offset:24576\n\tv_exp_f32 v36, v36\n\t"
        "v_exp_f32 v37, v37\n\tv_exp_f32 v38, v38\n\tv_exp_f32 v39, v39\n\tv_mfma_f32_32x32x16_bf16 v[16:31], v[168:171], v[4:7], v[16:31]\n\t"
        "ds_read_b128 v[100:103], %[k0] offset:24832\n\tv_exp_f32 v40, v40\n\tv_exp_f32 v41, v41\n\tv_exp_f32 v42, v42\n\tv_exp_f32 v43, v43\n\t"
        "v_smfmac_f32_32x32x32_bf16 v[64:79], v[104:107], v[32:39], v112\n\tds_read_b128 v[104:107], %[v0] offset:26624\n\tv_exp_f32 v44, v44\n\t"
        "v_exp_f32 v45, v45\n\tv_exp_f32 v46, v46\n\tv_smfmac_f32_16x16x64_bf16 v[80:83], v[84:87], v[32:39], v112\n\tv_exp_f32 v47, v47\n\t"
        "v_exp_f32 v16, v16\n\tv_smfmac_f32_32x32x32_bf16 v[64:79], v[108:111], v[40:47], v112\n\tds_read_b128 v[108:111], %[v0] offset:27136\n\t"
        "v_exp_f32 v17, v17\n\tv_exp_f32 v18, v18\n\tv_exp_f32 v19, v19\n\tv_smfmac_f32_16x16x64_bf16 v[80:83], v[84:87], v[40:47], v112\n\t"
        "v_mfma_f32_32x32x16_bf16 v[32:47], v[164:167], v[88:91], v[48:63]\n\tv_exp_f32 v20, v20\n\tv_exp_f32 v21, v21\n\tv_exp_f32 v22, v22\n\t"
        "v_exp_f32 v23, v23\n\tv_mfma_f32_32x32x16_bf16 v[32:47], v[168:171], v[92:95], v[32:47]\n\tv_exp_f32 v24, v24\n\tv_exp_f32 v25, v25\n\t"
        "v_exp_f32 v26, v26\n\tv_exp_f32 v27, v27\n\tv_smfmac_f32_32x32x32_bf16 v[136:151], v[172:175], v[16:23], v112\n\tv_exp_f32 v28, v28\n\t"
        "v_exp_f32 v29, v29\n\tv_exp_f32 v30, v30\n\tv_smfmac_f32_16x16x64_bf16 v[152:155], v[84:87], v[16:23], v112\n\ts_waitcnt lgkmcnt(0)\n\t"
        "v_exp_f32 v31, v31\n\tv_exp_f32 v32, v32\n\tv_smfmac_f32_32x32x32_bf16 v[136:151], v[176:179], v[24:31], v112\n\tv_exp_f32 v33, v33\n\t"
        "v_exp_f32 v34, v34\n\tv_exp_f32 v35, v35\n\tv_smfmac_f32_16x16x64_bf16 v[152:155], v[84:87], v[24:31], v112\n\t"
        "v_mfma_f32_32x32x16_bf16 v[16:31], v[96:99], v[0:3], v[120:135]\n\tds_read_b128 v[164:167], %[k0] offset:28672\n\tv_exp_f32 v36, v36\n\t"
        "v_exp_f32 v37, v37\n\tv_exp_f32 v38, v38\n\tv_exp_f32 v39, v39\n\tv_mfma_f32_32x32x16_bf16 v[16:31], v[100:103], v[4:7], v[16:31]\n\t"
        "ds_read_b128 v[168:171], %[k0] offset:28928\n\tv_exp_f32 v40, v40\n\tv_exp_f32 v41, v41\n\tv_exp_f32 v42, v42\n\tv_exp_f32 v43, v43\n\t"
        "v_smfmac_f32_32x32x32_bf16 v[64:79], v[172:175], v[32:39], v112\n\tds_read_b128 v[172:175], %[v0] offset:30720\n\tv_exp_f32 v44, v44\n\t"
        "v_exp_f32 v45, v45\n\tv_exp_f32 v46, v46\n\tv_smfmac_f32_16x16x64_bf16 v[80:83], v[84:87], v[32:39], v112\n\tv_exp_f32 v47, v47\n\t"
        "v_exp_f32 v16, v16\n\tv_smfmac_f32_32x32x32_bf16 v[64:79], v[176:179], v[40:47], v112\n\tds_read_b128 v[176:179], %[v0] offset:31232\n\t"
        "v_exp_f32 v17, v17\n\tv_exp_f32 v18, v18\n\tv_exp_f32 v19, v19\n\tv_smfmac_f32_16x16x64_bf16 v[80:83], v[84:87], v[40:47], v112\n\t"
        "v_mfma_f32_32x32x16_bf16 v[32:47], v[96:99], v[88:91], v[48:63]\n\tv_exp_f32 v20, v20\n\tv_exp_f32 v21, v21\n\tv_exp_f32 v22, v22\n\t"
        "v_exp_f32 v23, v23\n\tv_mfma_f32_32x32x16_bf16 v[32:47], v[100:103], v[92:95], v[32:47]\n\tv_exp_f32 v24, v24\n\tv_exp_f32 v25, v25\n\t"
        "v_exp_f32 v26, v26\n\tv_exp_f32 v27, v27\n\tv_smfmac_f32_32x32x32_bf16 v[136:151], v[104:107], v[16:23], v112\n\tv_exp_f32 v28, v28\n\t"
        "v_exp_f32 v29, v29\n\tv_exp_f32 v30, v30\n\tv_smfmac_f32_16x16x64_bf16 v[152:155], v[84:87], v[16:23], v112\n\ts_waitcnt lgkmcnt(0)\n\t"
        "v_exp_f32 v31, v31\n\tv_exp_f32 v32, v32\n\tv_smfmac_f32_32x32x32_bf16 v[136:151], v[108:111], v[24:31], v112\n\tv_exp_f32 v33, v33\n\t"
        "v_exp_f32 v34, v34\n\tv_exp_f32 v35, v35\n\tv_smfmac_f32_16x16x64_bf16 v[152:155], v[84:87], v[24:31], v112\n\ts_barrier\n\t"
        "v_mfma_f32_32x32x16_bf16 v[16:31], v[164:167], v[0:3], v[120:135]\n\tds_read_b128 v[96:99], %[k0] offset:32768\n\tv_exp_f32 v36, v36\n\t"
        "v_exp_f32 v37, v37\n\tv_exp_f32 v38, v38\n\tv_exp_f32 v39, v39\n\tv_mfma_f32_32x32x16_bf16 v[16:31], v[168:171], v[4:7], v[16:31]\n\t"
        "ds_read_b128 v[100:103], %[k0] offset:33024\n\tv_exp_f32 v40, v40\n\tv_exp_f32 v41, v41\n\tv_exp_f32 v42, v42\n\tv_exp_f32 v43, v43\n\t"
        "v_smfmac_f32_32x32x32_bf16 v[64:79], v[104:107], v[32:39], v112\n\tds_read_b128 v[104:107], %[v0] offset:34816\n\tv_exp_f32 v44, v44\n\t"
        "v_exp_f32 v45, v45\n\tv_exp_f32 v46, v46\n\tv_smfmac_f32_16x16x64_bf16 v[80:83], v[84:87], v[32:39], v112\n\tv_exp_f32 v47, v47\n\t"
        "v_exp_f32 v16, v16\n\tv_smfmac_f32_32x32x32_bf16 v[64:79], v[108:111], v[40:47], v112\n\tds_read_b128 v[108:111], %[v0] offset:35328\n\t"
        "v_exp_f32 v17, v17\n\tv_exp_f32 v18, v18\n\tv_exp_f32 v19, v19\n\tv_smfmac_f32_16x16x64_bf16 v[80:83], v[84:87], v[40:47], v112\n\t"
        "v_mfma_f32_32x32x16_bf16 v[32:47], v[164:167], v[88:91], v[48:63]\n\tv_exp_f32 v20, v20\n\tv_exp_f32 v21, v21\n\tv_exp_f32 v22, v22\n\t"
        "v_exp_f32 v23, v23\n\tv_mfma_f32_32x32x16_bf16 v[32:47], v[168:171], v[92:95], v[32:47]\n\tv_exp_f32 v24, v24\n\tv_exp_f32 v25, v25\n\t"
        "v_exp_f32 v26, v26\n\tv_exp_f32 v27, v27\n\tv_smfmac_f32_32x32x32_bf16 v[136:151], v[172:175], v[16:23], v112\n\tv_exp_f32 v28, v28\n\t"
        "v_exp_f32 v29, v29\n\tv_exp_f32 v30, v30\n\tv_smfmac_f32_16x16x64_bf16 v[152:155], v[84:87], v[16:23], v112\n\ts_waitcnt lgkmcnt(0)\n\t"
        "v_exp_f32 v31, v31\n\tv_exp_f32 v32, v32\n\tv_smfmac_f32_32x32x32_bf16 v[136:151], v[176:179], v[24:31], v112\n\tv_exp_f32 v33, v33\n\t"
        "v_exp_f32 v34, v34\n\tv_exp_f32 v35, v35\n\tv_smfmac_f32_16x16x64_bf16 v[152:155], v[84:87], v[24:31], v112\n\t"
        "v_mfma_f32_32x32x16_bf16 v[16:31], v[96:99], v[0:3], v[120:135]\n\tds_read_b128 v[164:167], %[k0] offset:36864\n\tv_exp_f32 v36, v36\n\t"
        "v_exp_f32 v37, v37\n\tv_exp_f32 v38, v38\n\tv_exp_f32 v39, v39\n\tv_mfma_f32_32x32x16_bf16 v[16:31], v[100:103], v[4:7], v[16:31]\n\t"
        "ds_read_b128 v[168:171], %[k0] offset:37120\n\tv_exp_f32 v40, v40\n\tv_exp_f32 v41, v41\n\tv_exp_f32 v42, v42\n\tv_exp_f32 v43, v43\n\t"
        "v_smfmac_f32_32x32x32_bf16 v[64:79], v[172:175], v[32:39], v112\n\tds_read_b128 v[172:175], %[v0] offset:38912\n\tv_exp_f32 v44, v44\n\t"
        "v_exp_f32 v45, v45\n\tv_exp_f32 v46, v46\n\tv_smfmac_f32_16x16x64_bf16 v[80:83], v[84:87], v[32:39], v112\n\tv_exp_f32 v47, v47\n\t"
        "v_exp_f32 v16, v16\n\tv_smfmac_f32_32x32x32_bf16 v[64:79], v[176:179], v[40:47], v112\n\tds_read_b128 v[176:179], %[v0] offset:39424\n\t"
        "v_exp_f32 v17, v17\n\tv_exp_f32 v18, v18\n\tv_exp_f32 v19, v19\n\tv_smfmac_f32_16x16x64_bf16 v[80:83], v[84:87], v[40:47], v112\n\t"
        "v_mfma_f32_32x32x16_bf16 v[32:47], v[96:99], v[88:91], v[48:63]\n\tv_exp_f32 v20, v20\n\tv_exp_f32 v21, v21\n\tv_exp_f32 v22, v22\n\t"
        "v_exp_f32 v23, v23\n\tv_mfma_f32_32x32x16_bf16 v[32:47], v[100:103], v[92:95], v[32:47]\n\tv_exp_f32 v24, v24\n\tv_exp_f32 v25, v25\n\t"
        "v_exp_f32 v26, v26\n\tv_exp_f32 v27, v27\n\tv_smfmac_f32_32x32x32_bf16 v[136:151], v[104:107], v[16:23], v112\n\tv_exp_f32 v28, v28\n\t"
        "v_exp_f32 v29, v29\n\tv_exp_f32 v30, v30\n\tv_smfmac_f32_16x16x64_bf16 v[152:155], v[84:87], v[16:23], v112\n\ts_waitcnt lgkmcnt(0)\n\t"
        "v_exp_f32 v31, v31\n\tv_exp_f32 v32, v32\n\tv_smfmac_f32_32x32x32_bf16 v[136:151], v[108:111], v[24:31], v112\n\tv_exp_f32 v33, v33\n\t"
        "v_exp_f32 v34, v34\n\tv_exp_f32 v35, v35\n\tv_smfmac_f32_16x16x64_bf16 v[152:155], v[84:87], v[24:31], v112\n\t"
        "v_mfma_f32_32x32x16_bf16 v[16:31], v[164:167], v[0:3], v[120:135]\n\tds_read_b128 v[96:99], %[k0] offset:40960\n\tv_exp_f32 v36, v36\n\t"
        "v_exp_f32 v37, v37\n\tv_exp_f32 v38, v38\n\tv_exp_f32 v39, v39\n\tv_mfma_f32_32x32x16_bf16 v[16:31], v[168:171], v[4:7], v[16:31]\n\t"
        "ds_read_b128 v[100:103], %[k0] offset:41216\n\tv_exp_f32 v40, v40\n\tv_exp_f32 v41, v41\n\tv_exp_f32 v42, v42\n\tv_exp_f32 v43, v43\n\t"
        "v_smfmac_f32_32x32x32_bf16 v[64:79], v[104:107], v[32:39], v112\n\tds_read_b128 v[104:107], %[v0] offset:43008\n\tv_exp_f32 v44, v44\n\t"
        "v_exp_f32 v45, v45\n\tv_exp_f32 v46, v46\n\tv_smfmac_f32_16x16x64_bf16 v[80:83], v[84:87], v[32:39], v112\n\tv_exp_f32 v47, v47\n\t"
        "v_exp_f32 v16, v16\n\tv_smfmac_f32_32x32x32_bf16 v[64:79], v[108:111], v[40:47], v112\n\tds_read_b128 v[108:111], %[v0] offset:43520\n\t"
        "v_exp_f32 v17, v17\n\tv_exp_f32 v18, v18\n\tv_exp_f32 v19, v19\n\tv_smfmac_f32_16x16x64_bf16 v[80:83], v[84:87], v[40:47], v112\n\t"
        "v_mfma_f32_32x32x16_bf16 v[32:47], v[164:167], v[88:91], v[48:63]\n\tv_exp_f32 v20, v20\n\tv_exp_f32 v21, v21\n\tv_exp_f32 v22, v22\n\t"
        "v_exp_f32 v23, v23\n\tv_mfma_f32_32x32x16_bf16 v[32:47], v[168:171], v[92:95], v[32:47]\n\tv_exp_f32 v24, v24\n\tv_exp_f32 v25, v25\n\t"
        "v_exp_f32 v26, v26\n\tv_exp_f32 v27, v27\n\tv_smfmac_f32_32x32x32_bf16 v[136:151], v[172:175], v[16:23], v112\n\tv_exp_f32 v28, v28\n\t"
        "v_exp_f32 v29, v29\n\tv_exp_f32 v30, v30\n\tv_smfmac_f32_16x16x64_bf16 v[152:155], v[84:87], v[16:23], v112\n\ts_waitcnt lgkmcnt(0)\n\t"
        "v_exp_f32 v31, v31\n\tv_exp_f32 v32, v32\n\tv_smfmac_f32_32x32x32_bf16 v[136:151], v[176:179], v[24:31], v112\n\tv_exp_f32 v33, v33\n\t"
        "v_exp_f32 v34, v34\n\tv_exp_f32 v35, v35\n\tv_smfmac_f32_16x16x64_bf16 v[152:155], v[84:87], v[24:31], v112\n\t"
        "v_mfma_f32_32x32x16_bf16 v[16:31], v[96:99], v[0:3], v[120:135]\n\tds_read_b128 v[164:167], %[k0] offset:45056\n\tv_exp_f32 v36, v36\n\t"
        "v_exp_f32 v37, v37\n\tv_exp_f32 v38, v38\n\tv_exp_f32 v39, v39\n\tv_mfma_f32_32x32x16_bf16 v[16:31], v[100:103], v[4:7], v[16:31]\n\t"
        "ds_read_b128 v[168:171], %[k0] offset:45312\n\tv_exp_f32 v40, v40\n\tv_exp_f32 v41, v41\n\tv_exp_f32 v42, v42\n\tv_exp_f32 v43, v43\n\t"
        "v_smfmac_f32_32x32x32_bf16 v[64:79], v[172:175], v[32:39], v112\n\tds_read_b128 v[172:175], %[v0] offset:47104\n\tv_exp_f32 v44, v44\n\t"
        "v_exp_f32 v45, v45\n\tv_exp_f32 v46, v46\n\tv_smfmac_f32_16x16x64_bf16 v[80:83], v[84:87], v[32:39], v112\n\tv_exp_f32 v47, v47\n\t"
        "v_exp_f32 v16, v16\n\tv_smfmac_f32_32x32x32_bf16 v[64:79], v[176:179], v[40:47], v112\n\tds_read_b128 v[176:179], %[v0] offset:47616\n\t"
        "v_exp_f32 v17, v17\n\tv_exp_f32 v18, v18\n\tv_exp_f32 v19, v19\n\tv_smfmac_f32_16x16x64_bf16 v[80:83], v[84:87], v[40:47], v112\n\t"
        "v_mfma_f32_32x32x16_bf16 v[32:47], v[96:99], v[88:91], v[48:63]\n\tv_exp_f32 v20, v20\n\tv_exp_f32 v21, v21\n\tv_exp_f32 v22, v22\n\t"
        "v_exp_f32 v23, v23\n\tv_mfma_f32_32x32x16_bf16 v[32:47], v[100:103], v[92:95], v[32:47]\n\tv_exp_f32 v24, v24\n\tv_exp_f32 v25, v25\n\t"
        "v_exp_f32 v26, v26\n\tv_exp_f32 v27, v27\n\tv_smfmac_f32_32x32x32_bf16 v[136:151], v[104:107], v[16:23], v112\n\tv_exp_f32 v28, v28\n\t"
        "v_exp_f32 v29, v29\n\tv_exp_f32 v30, v30\n\tv_smfmac_f32_16x16x64_bf16 v[152:155], v[84:87], v[16:23], v112\n\ts_waitcnt lgkmcnt(0)\n\t"
        "v_exp_f32 v31, v31\n\tv_exp_f32 v32, v32\n\tv_smfmac_f32_32x32x32_bf16 v[136:151], v[108:111], v[24:31], v112\n\tv_exp_f32 v33, v33\n\t"
        "v_exp_f32 v34, v34\n\tv_exp_f32 v35, v35\n\tv_smfmac_f32_16x16x64_bf16 v[152:155], v[84:87], v[24:31], v112\n\ts_barrier\n\t"
        "v_mfma_f32_32x32x16_bf16 v[16:31], v[164:167], v[0:3], v[120:135]\n\tds_read_b128 v[96:99], %[k0] offset:49152\n\tv_exp_f32 v36, v36\n\t"
        "v_exp_f32 v37, v37\n\tv_exp_f32 v38, v38\n\tv_exp_f32 v39, v39\n\tv_mfma_f32_32x32x16_bf16 v[16:31], v[168:171], v[4:7], v[16:31]\n\t"
        "ds_read_b128 v[100:103], %[k0] offset:49408\n\tv_exp_f32 v40, v40\n\tv_exp_f32 v41, v41\n\tv_exp_f32 v42, v42\n\tv_exp_f32 v43, v43\n\t"
        "v_smfmac_f32_32x32x32_bf16 v[64:79], v[104:107], v[32:39], v112\n\tds_read_b128 v[104:107], %[v0] offset:51200\n\tv_exp_f32 v44, v44\n\t"
        "v_exp_f32 v45, v45\n\tv_exp_f32 v46, v46\n\tv_smfmac_f32_16x16x64_bf16 v[80:83], v[84:87], v[32:39], v112\n\tv_exp_f32 v47, v47\n\t"
        "v_exp_f32 v16, v16\n\tv_smfmac_f32_32x32x32_bf16 v[64:79], v[108:111], v[40:47], v112\n\tds_read_b128 v[108:111], %[v0] offset:51712\n\t"
        "v_exp_f32 v17, v17\n\tv_exp_f32 v18, v18\n\tv_exp_f32 v19, v19\n\tv_smfmac_f32_16x16x64_bf16 v[80:83], v[84:87], v[40:47], v112\n\t"
        "v_mfma_f32_32x32x16_bf16 v[32:47], v[164:167], v[88:91], v[48:63]\n\tv_exp_f32 v20, v20\n\tv_exp_f32 v21, v21\n\tv_exp_f32 v22, v22\n\t"
        "v_exp_f32 v23, v23\n\tv_mfma_f32_32x32x16_bf16 v[32:47], v[168:171], v[92:95], v[32:47]\n\tv_exp_f32 v24, v24\n\tv_exp_f32 v25, v25\n\t"
        "v_exp_f32 v26, v26\n\tv_exp_f32 v27, v27\n\tv_smfmac_f32_32x32x32_bf16 v[136:151], v[172:175], v[16:23], v112\n\tv_exp_f32 v28, v28\n\t"
        "v_exp_f32 v29, v29\n\tv_exp_f32 v30, v30\n\tv_smfmac_f32_16x16x64_bf16 v[152:155], v[84:87], v[16:23], v112\n\ts_waitcnt lgkmcnt(0)\n\t"
        "v_exp_f32 v31, v31\n\tv_exp_f32 v32, v32\n\tv_smfmac_f32_32x32x32_bf16 v[136:151], v[176:179], v[24:31], v112\n\tv_exp_f32 v33, v33\n\t"
        "v_exp_f32 v34, v34\n\tv_exp_f32 v35, v35\n\tv_smfmac_f32_16x16x64_bf16 v[152:155], v[84:87], v[24:31], v112\n\t"
        "v_mfma_f32_32x32x16_bf16 v[16:31], v[96:99], v[0:3], v[120:135]\n\tds_read_b128 v[164:167], %[k0] offset:53248\n\tv_exp_f32 v36, v36\n\t"
        "v_exp_f32 v37, v37\n\tv_exp_f32 v38, v38\n\tv_exp_f32 v39, v39\n\tv_mfma_f32_32x32x16_bf16 v[16:31], v[100:103], v[4:7], v[16:31]\n\t"
        "ds_read_b128 v[168:171], %[k0] offset:53504\n\tv_exp_f32 v40, v40\n\tv_exp_f32 v41, v41\n\tv_exp_f32 v42, v42\n\tv_exp_f32 v43, v43\n\t"
        "v_smfmac_f32_32x32x32_bf16 v[64:79], v[172:175], v[32:39], v112\n\tds_read_b128 v[172:175], %[v0] offset:55296\n\tv_exp_f32 v44, v44\n\t"
        "v_exp_f32 v45, v45\n\tv_exp_f32 v46, v46\n\tv_smfmac_f32_16x16x64_bf16 v[80:83], v[84:87], v[32:39], v112\n\tv_exp_f32 v47, v47\n\t"
        "v_exp_f32 v16, v16\n\tv_smfmac_f32_32x32x32_bf16 v[64:79], v[176:179], v[40:47], v112\n\tds_read_b128 v[176:179], %[v0] offset:55808\n\t"
        "v_exp_f32 v17, v17\n\tv_exp_f32 v18, v18\n\tv_exp_f32 v19, v19\n\tv_smfmac_f32_16x16x64_bf16 v[80:83], v[84:87], v[40:47], v112\n\t"
        "v_mfma_f32_32x32x16_bf16 v[32:47], v[96:99], v[88:91], v[48:63]\n\tv_exp_f32 v20, v20\n\tv_exp_f32 v21, v21\n\tv_exp_f32 v22, v22\n\t"
        "v_exp_f32 v23, v23\n\tv_mfma_f32_32x32x16_bf16 v[32:47], v[100:103], v[92:95], v[32:47]\n\tv_exp_f32 v24, v24\n\tv_exp_f32 v25, v25\n\t"
        "v_exp_f32 v26, v26\n\tv_exp_f32 v27, v27\n\tv_smfmac_f32_32x32x32_bf16 v[136:151], v[104:107], v[16:23], v112\n\tv_exp_f32 v28, v28\n\t"
        "v_exp_f32 v29, v29\n\tv_exp_f32 v30, v30\n\tv_smfmac_f32_16x16x64_bf16 v[152:155], v[84:87], v[16:23], v112\n\ts_waitcnt lgkmcnt(0)\n\t"
        "v_exp_f32 v31, v31\n\tv_exp_f32 v32, v32\n\tv_smfmac_f32_32x32x32_bf16 v[136:151], v[108:111], v[24:31], v112\n\tv_exp_f32 v33, v33\n\t"
        "v_exp_f32 v34, v34\n\tv_exp_f32 v35, v35\n\tv_smfmac_f32_16x16x64_bf16 v[152:155], v[84:87], v[24:31], v112\n\t"
        "v_mfma_f32_32x32x16_bf16 v[16:31], v[164:167], v[0:3], v[120:135]\n\tds_read_b128 v[96:99], %[k0] offset:57344\n\tv_exp_f32 v36, v36\n\t"
        "v_exp_f32 v37, v37\n\tv_exp_f32 v38, v38\n\tv_exp_f32 v39, v39\n\tv_mfma_f32_32x32x16_bf16 v[16:31], v[168:171], v[4:7], v[16:31]\n\t"
        "ds_read_b128 v[100:103], %[k0] offset:57600\n\tv_exp_f32 v40, v40\n\tv_exp_f32 v41, v41\n\tv_exp_f32 v42, v42\n\tv_exp_f32 v43, v43\n\t"
        "v_smfmac_f32_32x32x32_bf16 v[64:79], v[104:107], v[32:39], v112\n\tds_read_b128 v[104:107], %[v0] offset:59392\n\tv_exp_f32 v44, v44\n\t"
        "v_exp_f32 v45, v45\n\tv_exp_f32 v46, v46\n\tv_smfmac_f32_16x16x64_bf16 v[80:83], v[84:87], v[32:39], v112\n\tv_exp_f32 v47, v47\n\t"
        "v_exp_f32 v16, v16\n\tv_smfmac_f32_32x32x32_bf16 v[64:79], v[108:111], v[40:47], v112\n\tds_read_b128 v[108:111], %[v0] offset:59904\n\t"
        "v_exp_f32 v17, v17\n\tv_exp_f32 v18, v18\n\tv_exp_f32 v19, v19\n\tv_smfmac_f32_16x16x64_bf16 v[80:83], v[84:87], v[40:47], v112\n\t"
        "v_mfma_f32_32x32x16_bf16 v[32:47], v[164:167], v[88:91], v[48:63]\n\tv_exp_f32 v20, v20\n\tv_exp_f32 v21, v21\n\tv_exp_f32 v22, v22\n\t"
        "v_exp_f32 v23, v23\n\tv_mfma_f32_32x32x16_bf16 v[32:47], v[168:171], v[92:95], v[32:47]\n\t.p2align 12,,12\n\t.Lpg5:\n\ts_cbranch_execz .Lpgret\n\t"
        "v_exp_f32 v24, v24\n\tv_exp_f32 v25, v25\n\tv_exp_f32 v26, v26\n\tv_exp_f32 v27, v27\n\t"
        "v_smfmac_f32_32x32x32_bf16 v[136:151], v[172:175], v[16:23], v112\n\tv_exp_f32 v28, v28\n\tv_exp_f32 v29, v29\n\tv_exp_f32 v30, v30\n\t"
        "v_smfmac_f32_16x16x64_bf16 v[152:155], v[84:87], v[16:23], v112\n\ts_waitcnt lgkmcnt(0)\n\tv_exp_f32 v31, v31\n\tv_exp_f32 v32, v32\n\t"
        "v_smfmac_f32_32x32x32_bf16 v[136:151], v[176:179], v[24:31], v112\n\tv_exp_f32 v33, v33\n\tv_exp_f32 v34, v34\n\tv_exp_f32 v35, v35\n\t"
        "v_smfmac_f32_16x16x64_bf16 v[152:155], v[84:87], v[24:31], v112\n\tv_mfma_f32_32x32x16_bf16 v[16:31], v[96:99], v[0:3], v[120:135]\n\t"
        "ds_read_b128 v[164:167], %[k0] offset:61440\n\tv_exp_f32 v36, v36\n\tv_exp_f32 v37, v37\n\tv_exp_f32 v38, v38\n\tv_exp_f32 v39, v39\n\t"
        "v_mfma_f32_32x32x16_bf16 v[16:31], v[100:103], v[4:7], v[16:31]\n\tds_read_b128 v[168:171], %[k0] offset:61696\n\tv_exp_f32 v40, v40\n\t"
        "v_exp_f32 v41, v41\n\tv_exp_f32 v42, v42\n\tv_exp_f32 v43, v43\n\tv_smfmac_f32_32x32x32_bf16 v[64:79], v[172:175], v[32:39], v112\n\t"
        "ds_read_b128 v[172:175], %[v0] offset:63488\n\tv_exp_f32 v44, v44\n\tv_exp_f32 v45, v45\n\tv_exp_f32 v46, v46\n\t"
        "v_smfmac_f32_16x16x64_bf16 v[80:83], v[84:87], v[32:39], v112\n\tv_exp_f32 v47, v47\n\tv_exp_f32 v16, v16\n\t"
        "v_smfmac_f32_32x32x32_bf16 v[64:79], v[176:179], v[40:47], v112\n\tds_read_b128 v[176:179], %[v0] offset:64000\n\tv_exp_f32 v17, v17\n\t"
        "v_exp_f32 v18, v18\n\tv_exp_f32 v19, v19\n\tv_smfmac_f32_16x16x64_bf16 v[80:83], v[84:87], v[40:47], v112\n\t"
        "v_mfma_f32_32x32x16_bf16 v[32:47], v[96:99], v[88:91], v[48:63]\n\tv_exp_f32 v20, v20\n\tv_exp_f32 v21, v21\n\tv_exp_f32 v22, v22\n\t"
        "v_exp_f32 v23, v23\n\tv_mfma_f32_32x32x16_bf16 v[32:47], v[100:103], v[92:95], v[32:47]\n\tv_exp_f32 v24, v24\n\tv_exp_f32 v25, v25\n\t"
        "v_exp_f32 v26, v26\n\tv_exp_f32 v27, v27\n\tv_smfmac_f32_32x32x32_bf16 v[136:151], v[104:107], v[16:23], v112\n\tv_exp_f32 v28, v28\n\t"
        "v_exp_f32 v29, v29\n\tv_exp_f32 v30, v30\n\tv_smfmac_f32_16x16x64_bf16 v[152:155], v[84:87], v[16:23], v112\n\ts_waitcnt lgkmcnt(0)\n\t"
        "v_exp_f32 v31, v31\n\tv_exp_f32 v32, v32\n\tv_smfmac_f32_32x32x32_bf16 v[136:151], v[108:111], v[24:31], v112\n\tv_exp_f32 v33, v33\n\t"
        "v_exp_f32 v34, v34\n\tv_exp_f32 v35, v35\n\tv_smfmac_f32_16x16x64_bf16 v[152:155], v[84:87], v[24:31], v112\n\ts_barrier\n\t"
        "v_mfma_f32_32x32x16_bf16 v[16:31], v[164:167], v[0:3], v[120:135]\n\tds_read_b128 v[96:99], v113 offset:0\n\tv_exp_f32 v36, v36\n\t"
        "v_exp_f32 v37, v37\n\tv_exp_f32 v38, v38\n\tv_exp_f32 v39, v39\n\tv_mfma_f32_32x32x16_bf16 v[16:31], v[168:171], v[4:7], v[16:31]\n\t"
        "ds_read_b128 v[100:103], v113 offset:256\n\tv_exp_f32 v40, v40\n\tv_exp_f32 v41, v41\n\tv_exp_f32 v42, v42\n\tv_exp_f32 v43, v43\n\t"
        "v_smfmac_f32_32x32x32_bf16 v[64:79], v[104:107], v[32:39], v112\n\tds_read_b128 v[104:107], v114 offset:2048\n\tv_exp_f32 v44, v44\n\t"
        "v_exp_f32 v45, v45\n\tv_exp_f32 v46, v46\n\tv_smfmac_f32_16x16x64_bf16 v[80:83], v[84:87], v[32:39], v112\n\tv_exp_f32 v47, v47\n\t"
        "v_exp_f32 v16, v16\n\tv_smfmac_f32_32x32x32_bf16 v[64:79], v[108:111], v[40:47], v112\n\tds_read_b128 v[108:111], v114 offset:2560\n\t"
        "v_exp_f32 v17, v17\n\tv_exp_f32 v18, v18\n\tv_exp_f32 v19, v19\n\tv_smfmac_f32_16x16x64_bf16 v[80:83], v[84:87], v[40:47], v112\n\t"
        "v_mfma_f32_32x32x16_bf16 v[32:47], v[164:167], v[88:91], v[48:63]\n\tv_exp_f32 v20, v20\n\tv_exp_f32 v21, v21\n\tv_exp_f32 v22, v22\n\t"
        "v_exp_f32 v23, v23\n\tv_mfma_f32_32x32x16_bf16 v[32:47], v[168:171], v[92:95], v[32:47]\n\tv_exp_f32 v24, v24\n\tv_exp_f32 v25, v25\n\t"
        "v_exp_f32 v26, v26\n\tv_exp_f32 v27, v27\n\tv_smfmac_f32_32x32x32_bf16 v[136:151], v[172:175], v[16:23], v112\n\tv_exp_f32 v28, v28\n\t"
        "v_exp_f32 v29, v29\n\tv_exp_f32 v30, v30\n\tv_smfmac_f32_16x16x64_bf16 v[152:155], v[84:87], v[16:23], v112\n\ts_waitcnt lgkmcnt(0)\n\t"
        "v_exp_f32 v31, v31\n\tv_exp_f32 v32, v32\n\tv_smfmac_f32_32x32x32_bf16 v[136:151], v[176:179], v[24:31], v112\n\tv_exp_f32 v33, v33\n\t"
        "v_exp_f32 v34, v34\n\tv_exp_f32 v35, v35\n\tv_smfmac_f32_16x16x64_bf16 v[152:155], v[84:87], v[24:31], v112\n\t"
        "v_mfma_f32_32x32x16_bf16 v[16:31], v[96:99], v[0:3], v[120:135]\n\tds_read_b128 v[164:167], v113 offset:4096\n\tv_exp_f32 v36, v36\n\t"
        "v_exp_f32 v37, v37\n\tv_exp_f32 v38, v38\n\tv_exp_f32 v39, v39\n\tv_mfma_f32_32x32x16_bf16 v[16:31], v[100:103], v[4:7], v[16:31]\n\t"
        "ds_read_b128 v[168:171], v113 offset:4352\n\tv_exp_f32 v40, v40\n\tv_exp_f32 v41, v41\n\tv_exp_f32 v42, v42\n\tv_exp_f32 v43, v43\n\t"
        "v_smfmac_f32_32x32x32_bf16 v[64:79], v[172:175], v[32:39], v112\n\tds_read_b128 v[172:175], v114 offset:6144\n\tv_exp_f32 v44, v44\n\t"
        "v_exp_f32 v45, v45\n\tv_exp_f32 v46, v46\n\tv_smfmac_f32_16x16x64_bf16 v[80:83], v[84:87], v[32:39], v112\n\tv_exp_f32 v47, v47\n\t"
        "v_exp_f32 v16, v16\n\tv_smfmac_f32_32x32x32_bf16 v[64:79], v[176:179], v[40:47], v112\n\tds_read_b128 v[176:179], v114 offset:6656\n\t"
        "v_exp_f32 v17, v17\n\tv_exp_f32 v18, v18\n\tv_exp_f32 v19, v19\n\tv_smfmac_f32_16x16x64_bf16 v[80:83], v[84:87], v[40:47], v112\n\t"
        "v_mfma_f32_32x32x16_bf16 v[32:47], v[96:99], v[88:91], v[48:63]\n\tv_exp_f32 v20, v20\n\tv_exp_f32 v21, v21\n\tv_exp_f32 v22, v22\n\t"
        "v_exp_f32 v23, v23\n\tv_mfma_f32_32x32x16_bf16 v[32:47], v[100:103], v[92:95], v[32:47]\n\tv_exp_f32 v24, v24\n\tv_exp_f32 v25, v25\n\t"
        "v_exp_f32 v26, v26\n\tv_exp_f32 v27, v27\n\tv_smfmac_f32_32x32x32_bf16 v[136:151], v[104:107], v[16:23], v112\n\tv_exp_f32 v28, v28\n\t"
        "v_exp_f32 v29, v29\n\tv_exp_f32 v30, v30\n\tv_smfmac_f32_16x16x64_bf16 v[152:155], v[84:87], v[16:23], v112\n\ts_waitcnt lgkmcnt(0)\n\t"
        "v_exp_f32 v31, v31\n\tv_exp_f32 v32, v32\n\tv_smfmac_f32_32x32x32_bf16 v[136:151], v[108:111], v[24:31], v112\n\tv_exp_f32 v33, v33\n\t"
        "v_exp_f32 v34, v34\n\tv_exp_f32 v35, v35\n\tv_smfmac_f32_16x16x64_bf16 v[152:155], v[84:87], v[24:31], v112\n\t"
        "v_mfma_f32_32x32x16_bf16 v[16:31], v[164:167], v[0:3], v[120:135]\n\tds_read_b128 v[96:99], v113 offset:8192\n\tv_exp_f32 v36, v36\n\t"
        "v_exp_f32 v37, v37\n\tv_exp_f32 v38, v38\n\tv_exp_f32 v39, v39\n\tv_mfma_f32_32x32x16_bf16 v[16:31], v[168:171], v[4:7], v[16:31]\n\t"
        "ds_read_b128 v[100:103], v113 offset:8448\n\tv_exp_f32 v40, v40\n\tv_exp_f32 v41, v41\n\tv_exp_f32 v42, v42\n\tv_exp_f32 v43, v43\n\t"
        "v_smfmac_f32_32x32x32_bf16 v[64:79], v[104:107], v[32:39], v112\n\tds_read_b128 v[104:107], v114 offset:10240\n\tv_exp_f32 v44, v44\n\t"
        "v_exp_f32 v45, v45\n\tv_exp_f32 v46, v46\n\tv_smfmac_f32_16x16x64_bf16 v[80:83], v[84:87], v[32:39], v112\n\tv_exp_f32 v47, v47\n\t"
        "v_exp_f32 v16, v16\n\tv_smfmac_f32_32x32x32_bf16 v[64:79], v[108:111], v[40:47], v112\n\tds_read_b128 v[108:111], v114 offset:10752\n\t"
        "v_exp_f32 v17, v17\n\tv_exp_f32 v18, v18\n\tv_exp_f32 v19, v19\n\tv_smfmac_f32_16x16x64_bf16 v[80:83], v[84:87], v[40:47], v112\n\t"
        "v_mfma_f32_32x32x16_bf16 v[32:47], v[164:167], v[88:91], v[48:63]\n\tv_exp_f32 v20, v20\n\tv_exp_f32 v21, v21\n\tv_exp_f32 v22, v22\n\t"
        "v_exp_f32 v23, v23\n\tv_mfma_f32_32x32x16_bf16 v[32:47], v[168:171], v[92:95], v[32:47]\n\tv_exp_f32 v24, v24\n\tv_exp_f32 v25, v25\n\t"
        "v_exp_f32 v26, v26\n\tv_exp_f32 v27, v27\n\tv_smfmac_f32_32x32x32_bf16 v[136:151], v[172:175], v[16:23], v112\n\tv_exp_f32 v28, v28\n\t"
        "v_exp_f32 v29, v29\n\tv_exp_f32 v30, v30\n\tv_smfmac_f32_16x16x64_bf16 v[152:155], v[84:87], v[16:23], v112\n\ts_waitcnt lgkmcnt(0)\n\t"
        "v_exp_f32 v31, v31\n\tv_exp_f32 v32, v32\n\tv_smfmac_f32_32x32x32_bf16 v[136:151], v[176:179], v[24:31], v112\n\tv_exp_f32 v33, v33\n\t"
        "v_exp_f32 v34, v34\n\tv_exp_f32 v35, v35\n\tv_smfmac_f32_16x16x64_bf16 v[152:155], v[84:87], v[24:31], v112\n\t"
        "v_mfma_f32_32x32x16_bf16 v[16:31], v[96:99], v[0:3], v[120:135]\n\tds_read_b128 v[164:167], v113 offset:12288\n\tv_exp_f32 v36, v36\n\t"
        "v_exp_f32 v37, v37\n\tv_exp_f32 v38, v38\n\tv_exp_f32 v39, v39\n\tv_mfma_f32_32x32x16_bf16 v[16:31], v[100:103], v[4:7], v[16:31]\n\t"
        "ds_read_b128 v[168:171], v113 offset:12544\n\tv_exp_f32 v40, v40\n\tv_exp_f32 v41, v41\n\tv_exp_f32 v42, v42\n\tv_exp_f32 v43, v43\n\t"
        "v_smfmac_f32_32x32x32_bf16 v[64:79], v[172:175], v[32:39], v112\n\tds_read_b128 v[172:175], v114 offset:14336\n\tv_exp_f32 v44, v44\n\t"
        "v_exp_f32 v45, v45\n\tv_exp_f32 v46, v46\n\tv_smfmac_f32_16x16x64_bf16 v[80:83], v[84:87], v[32:39], v112\n\tv_exp_f32 v47, v47\n\t"
        "v_exp_f32 v16, v16\n\tv_smfmac_f32_32x32x32_bf16 v[64:79], v[176:179], v[40:47], v112\n\tds_read_b128 v[176:179], v114 offset:14848\n\t"
        "v_exp_f32 v17, v17\n\tv_exp_f32 v18, v18\n\tv_exp_f32 v19, v19\n\tv_smfmac_f32_16x16x64_bf16 v[80:83], v[84:87], v[40:47], v112\n\t"
        "v_mfma_f32_32x32x16_bf16 v[32:47], v[96:99], v[88:91], v[48:63]\n\tv_exp_f32 v20, v20\n\tv_exp_f32 v21, v21\n\tv_exp_f32 v22, v22\n\t"
        "v_exp_f32 v23, v23\n\tv_mfma_f32_32x32x16_bf16 v[32:47], v[100:103], v[92:95], v[32:47]\n\tv_exp_f32 v24, v24\n\tv_exp_f32 v25, v25\n\t"
        "v_exp_f32 v26, v26\n\tv_exp_f32 v27, v27\n\tv_smfmac_f32_32x32x32_bf16 v[136:151], v[104:107], v[16:23], v112\n\tv_exp_f32 v28, v28\n\t"
        "v_exp_f32 v29, v29\n\tv_exp_f32 v30, v30\n\tv_smfmac_f32_16x16x64_bf16 v[152:155], v[84:87], v[16:23], v112\n\ts_waitcnt lgkmcnt(0)\n\t"
        "v_exp_f32 v31, v31\n\tv_exp_f32 v32, v32\n\tv_smfmac_f32_32x32x32_bf16 v[136:151], v[108:111], v[24:31], v112\n\tv_exp_f32 v33, v33\n\t"
        "v_exp_f32 v34, v34\n\tv_exp_f32 v35, v35\n\tv_smfmac_f32_16x16x64_bf16 v[152:155], v[84:87], v[24:31], v112\n\ts_barrier\n\t"
        "v_mfma_f32_32x32x16_bf16 v[16:31], v[164:167], v[0:3], v[120:135]\n\tds_read_b128 v[96:99], v113 offset:16384\n\tv_exp_f32 v36, v36\n\t"
        "v_exp_f32 v37, v37\n\tv_exp_f32 v38, v38\n\tv_exp_f32 v39, v39\n\tv_mfma_f32_32x32x16_bf16 v[16:31], v[168:171], v[4:7], v[16:31]\n\t"
        "ds_read_b128 v[100:103], v113 offset:16640\n\tv_exp_f32 v40, v40\n\tv_exp_f32 v41, v41\n\tv_exp_f32 v42, v42\n\tv_exp_f32 v43, v43\n\t"
        "v_smfmac_f32_32x32x32_bf16 v[64:79], v[104:107], v[32:39], v112\n\tds_read_b128 v[104:107], v114 offset:18432\n\tv_exp_f32 v44, v44\n\t"
        "v_exp_f32 v45, v45\n\tv_exp_f32 v46, v46\n\tv_smfmac_f32_16x16x64_bf16 v[80:83], v[84:87], v[32:39], v112\n\tv_exp_f32 v47, v47\n\t"
        "v_exp_f32 v16, v16\n\tv_smfmac_f32_32x32x32_bf16 v[64:79], v[108:111], v[40:47], v112\n\tds_read_b128 v[108:111], v114 offset:18944\n\t"
        "v_exp_f32 v17, v17\n\tv_exp_f32 v18, v18\n\tv_exp_f32 v19, v19\n\tv_smfmac_f32_16x16x64_bf16 v[80:83], v[84:87], v[40:47], v112\n\t"
        "v_mfma_f32_32x32x16_bf16 v[32:47], v[164:167], v[88:91], v[48:63]\n\tv_exp_f32 v20, v20\n\tv_exp_f32 v21, v21\n\tv_exp_f32 v22, v22\n\t"
        "v_exp_f32 v23, v23\n\tv_mfma_f32_32x32x16_bf16 v[32:47], v[168:171], v[92:95], v[32:47]\n\tv_exp_f32 v24, v24\n\tv_exp_f32 v25, v25\n\t"
        "v_exp_f32 v26, v26\n\tv_exp_f32 v27, v27\n\tv_smfmac_f32_32x32x32_bf16 v[136:151], v[172:175], v[16:23], v112\n\tv_exp_f32 v28, v28\n\t"
        "v_exp_f32 v29, v29\n\tv_exp_f32 v30, v30\n\tv_smfmac_f32_16x16x64_bf16 v[152:155], v[84:87], v[16:23], v112\n\ts_waitcnt lgkmcnt(0)\n\t"
        "v_exp_f32 v31, v31\n\tv_exp_f32 v32, v32\n\tv_smfmac_f32_32x32x32_bf16 v[136:151], v[176:179], v[24:31], v112\n\tv_exp_f32 v33, v33\n\t"
        "v_exp_f32 v34, v34\n\tv_exp_f32 v35, v35\n\tv_smfmac_f32_16x16x64_bf16 v[152:155], v[84:87], v[24:31], v112\n\t"
        "v_mfma_f32_32x32x16_bf16 v[16:31], v[96:99], v[0:3], v[120:135]\n\tds_read_b128 v[164:167], v113 offset:20480\n\tv_exp_f32 v36, v36\n\t"
        "v_exp_f32 v37, v37\n\tv_exp_f32 v38, v38\n\tv_exp_f32 v39, v39\n\tv_mfma_f32_32x32x16_bf16 v[16:31], v[100:103], v[4:7], v[16:31]\n\t"
        "ds_read_b128 v[168:171], v113 offset:20736\n\tv_exp_f32 v40, v40\n\tv_exp_f32 v41, v41\n\tv_exp_f32 v42, v42\n\tv_exp_f32 v43, v43\n\t"
        "v_smfmac_f32_32x32x32_bf16 v[64:79], v[172:175], v[32:39], v112\n\tds_read_b128 v[172:175], v114 offset:22528\n\tv_exp_f32 v44, v44\n\t"
        "v_exp_f32 v45, v45\n\tv_exp_f32 v46, v46\n\tv_smfmac_f32_16x16x64_bf16 v[80:83], v[84:87], v[32:39], v112\n\tv_exp_f32 v47, v47\n\t"
        "v_exp_f32 v16, v16\n\tv_smfmac_f32_32x32x32_bf16 v[64:79], v[176:179], v[40:47], v112\n\tds_read_b128 v[176:179], v114 offset:23040\n\t"
        "v_exp_f32 v17, v17\n\tv_exp_f32 v18, v18\n\tv_exp_f32 v19, v19\n\tv_smfmac_f32_16x16x64_bf16 v[80:83], v[84:87], v[40:47], v112\n\t"
        "v_mfma_f32_32x32x16_bf16 v[32:47], v[96:99], v[88:91], v[48:63]\n\tv_exp_f32 v20, v20\n\tv_exp_f32 v21, v21\n\tv_exp_f32 v22, v22\n\t"
        "v_exp_f32 v23, v23\n\tv_mfma_f32_32x32x16_bf16 v[32:47], v[100:103], v[92:95], v[32:47]\n\tv_exp_f32 v24, v24\n\tv_exp_f32 v25, v25\n\t"
        "v_exp_f32 v26, v26\n\tv_exp_f32 v27, v27\n\tv_smfmac_f32_32x32x32_bf16 v[136:151], v[104:107], v[16:23], v112\n\tv_exp_f32 v28, v28\n\t"
        "v_exp_f32 v29, v29\n\tv_exp_f32 v30, v30\n\tv_smfmac_f32_16x16x64_bf16 v[152:155], v[84:87], v[16:23], v112\n\ts_waitcnt lgkmcnt(0)\n\t"
        "v_exp_f32 v31, v31\n\tv_exp_f32 v32, v32\n\tv_smfmac_f32_32x32x32_bf16 v[136:151], v[108:111], v[24:31], v112\n\tv_exp_f32 v33, v33\n\t"
        "v_exp_f32 v34, v34\n\tv_exp_f32 v35, v35\n\tv_smfmac_f32_16x16x64_bf16 v[152:155], v[84:87], v[24:31], v112\n\t"
        "v_mfma_f32_32x32x16_bf16 v[16:31], v[164:167], v[0:3], v[120:135]\n\tds_read_b128 v[96:99], v113 offset:24576\n\tv_exp_f32 v36, v36\n\t"
        "v_exp_f32 v37, v37\n\tv_exp_f32 v38, v38\n\tv_exp_f32 v39, v39\n\tv_mfma_f32_32x32x16_bf16 v[16:31], v[168:171], v[4:7], v[16:31]\n\t"
        "ds_read_b128 v[100:103], v113 offset:24832\n\tv_exp_f32 v40, v40\n\tv_exp_f32 v41, v41\n\tv_exp_f32 v42, v42\n\tv_exp_f32 v43, v43\n\t"
        "v_smfmac_f32_32x32x32_bf16 v[64:79], v[104:107], v[32:39], v112\n\tds_read_b128 v[104:107], v114 offset:26624\n\tv_exp_f32 v44, v44\n\t"
        "v_exp_f32 v45, v45\n\tv_exp_f32 v46, v46\n\tv_smfmac_f32_16x16x64_bf16 v[80:83], v[84:87], v[32:39], v112\n\tv_exp_f32 v47, v47\n\t"
        "v_exp_f32 v16, v16\n\tv_smfmac_f32_32x32x32_bf16 v[64:79], v[108:111], v[40:47], v112\n\tds_read_b128 v[108:111], v114 offset:27136\n\t"
        "v_exp_f32 v17, v17\n\tv_exp_f32 v18, v18\n\tv_exp_f32 v19, v19\n\tv_smfmac_f32_16x16x64_bf16 v[80:83], v[84:87], v[40:47], v112\n\t"
        "v_mfma_f32_32x32x16_bf16 v[32:47], v[164:167], v[88:91], v[48:63]\n\tv_exp_f32 v20, v20\n\tv_exp_f32 v21, v21\n\tv_exp_f32 v22, v22\n\t"
        "v_exp_f32 v23, v23\n\tv_mfma_f32_32x32x16_bf16 v[32:47], v[168:171], v[92:95], v[32:47]\n\tv_exp_f32 v24, v24\n\tv_exp_f32 v25, v25\n\t"
        "v_exp_f32 v26, v26\n\tv_exp_f32 v27, v27\n\tv_smfmac_f32_32x32x32_bf16 v[136:151], v[172:175], v[16:23], v112\n\tv_exp_f32 v28, v28\n\t"
        "v_exp_f32 v29, v29\n\tv_exp_f32 v30, v30\n\tv_smfmac_f32_16x16x64_bf16 v[152:155], v[84:87], v[16:23], v112\n\ts_waitcnt lgkmcnt(0)\n\t"
        "v_exp_f32 v31, v31\n\tv_exp_f32 v32, v32\n\tv_smfmac_f32_32x32x32_bf16 v[136:151], v[176:179], v[24:31], v112\n\tv_exp_f32 v33, v33\n\t"
        "v_exp_f32 v34, v34\n\tv_exp_f32 v35, v35\n\tv_smfmac_f32_16x16x64_bf16 v[152:155], v[84:87], v[24:31], v112\n\t"
        "v_mfma_f32_32x32x16_bf16 v[16:31], v[96:99], v[0:3], v[120:135]\n\tds_read_b128 v[164:167], v113 offset:28672\n\tv_exp_f32 v36, v36\n\t"
        "v_exp_f32 v37, v37\n\tv_exp_f32 v38, v38\n\tv_exp_f32 v39, v39\n\tv_mfma_f32_32x32x16_bf16 v[16:31], v[100:103], v[4:7], v[16:31]\n\t"
        "ds_read_b128 v[168:171], v113 offset:28928\n\tv_exp_f32 v40, v40\n\tv_exp_f32 v41, v41\n\tv_exp_f32 v42, v42\n\tv_exp_f32 v43, v43\n\t"
        "v_smfmac_f32_32x32x32_bf16 v[64:79], v[172:175], v[32:39], v112\n\tds_read_b128 v[172:175], v114 offset:30720\n\tv_exp_f32 v44, v44\n\t"
        "v_exp_f32 v45, v45\n\tv_exp_f32 v46, v46\n\tv_smfmac_f32_16x16x64_bf16 v[80:83], v[84:87], v[32:39], v112\n\tv_exp_f32 v47, v47\n\t"
        "v_exp_f32 v16, v16\n\tv_smfmac_f32_32x32x32_bf16 v[64:79], v[176:179], v[40:47], v112\n\tds_read_b128 v[176:179], v114 offset:31232\n\t"
        "v_exp_f32 v17, v17\n\tv_exp_f32 v18, v18\n\tv_exp_f32 v19, v19\n\tv_smfmac_f32_16x16x64_bf16 v[80:83], v[84:87], v[40:47], v112\n\t"
        "v_mfma_f32_32x32x16_bf16 v[32:47], v[96:99], v[88:91], v[48:63]\n\tv_exp_f32 v20, v20\n\tv_exp_f32 v21, v21\n\tv_exp_f32 v22, v22\n\t"
        "v_exp_f32 v23, v23\n\tv_mfma_f32_32x32x16_bf16 v[32:47], v[100:103], v[92:95], v[32:47]\n\tv_exp_f32 v24, v24\n\tv_exp_f32 v25, v25\n\t"
        "v_exp_f32 v26, v26\n\tv_exp_f32 v27, v27\n\tv_smfmac_f32_32x32x32_bf16 v[136:151], v[104:107], v[16:23], v112\n\tv_exp_f32 v28, v28\n\t"
        "v_exp_f32 v29, v29\n\tv_exp_f32 v30, v30\n\tv_smfmac_f32_16x16x64_bf16 v[152:155], v[84:87], v[16:23], v112\n\ts_waitcnt lgkmcnt(0)\n\t"
        "v_exp_f32 v31, v31\n\tv_exp_f32 v32, v32\n\tv_smfmac_f32_32x32x32_bf16 v[136:151], v[108:111], v[24:31], v112\n\tv_exp_f32 v33, v33\n\t"
        "v_exp_f32 v34, v34\n\tv_exp_f32 v35, v35\n\tv_smfmac_f32_16x16x64_bf16 v[152:155], v[84:87], v[24:31], v112\n\ts_barrier\n\t"
        "v_mfma_f32_32x32x16_bf16 v[16:31], v[164:167], v[0:3], v[120:135]\n\tds_read_b128 v[96:99], %[k0] offset:0\n\tv_exp_f32 v36, v36\n\t"
        "v_exp_f32 v37, v37\n\tv_exp_f32 v38, v38\n\tv_exp_f32 v39, v39\n\tv_mfma_f32_32x32x16_bf16 v[16:31], v[168:171], v[4:7], v[16:31]\n\t"
        "ds_read_b128 v[100:103], %[k0] offset:256\n\tv_exp_f32 v40, v40\n\tv_exp_f32 v41, v41\n\tv_exp_f32 v42, v42\n\tv_exp_f32 v43, v43\n\t"
        "v_smfmac_f32_32x32x32_bf16 v[64:79], v[104:107], v[32:39], v112\n\tds_read_b128 v[104:107], %[v0] offset:2048\n\tv_exp_f32 v44, v44\n\t"
        "v_exp_f32 v45, v45\n\tv_exp_f32 v46, v46\n\tv_smfmac_f32_16x16x64_bf16 v[80:83], v[84:87], v[32:39], v112\n\tv_exp_f32 v47, v47\n\t"
        "v_exp_f32 v16, v16\n\tv_smfmac_f32_32x32x32_bf16 v[64:79], v[108:111], v[40:47], v112\n\tds_read_b128 v[108:111], %[v0] offset:2560\n\t"
        "v_exp_f32 v17, v17\n\tv_exp_f32 v18, v18\n\tv_exp_f32 v19, v19\n\tv_smfmac_f32_16x16x64_bf16 v[80:83], v[84:87], v[40:47], v112\n\t"
        "v_mfma_f32_32x32x16_bf16 v[32:47], v[164:167], v[88:91], v[48:63]\n\tv_exp_f32 v20, v20\n\tv_exp_f32 v21, v21\n\tv_exp_f32 v22, v22\n\t"
        "v_exp_f32 v23, v23\n\tv_mfma_f32_32x32x16_bf16 v[32:47], v[168:171], v[92:95], v[32:47]\n\tv_exp_f32 v24, v24\n\tv_exp_f32 v25, v25\n\t"
        "v_exp_f32 v26, v26\n\tv_exp_f32 v27, v27\n\tv_smfmac_f32_32x32x32_bf16 v[136:151], v[172:175], v[16:23], v112\n\tv_exp_f32 v28, v28\n\t"
        "v_exp_f32 v29, v29\n\tv_exp_f32 v30, v30\n\tv_smfmac_f32_16x16x64_bf16 v[152:155], v[84:87], v[16:23], v112\n\ts_waitcnt lgkmcnt(0)\n\t"
        "v_exp_f32 v31, v31\n\tv_exp_f32 v32, v32\n\tv_smfmac_f32_32x32x32_bf16 v[136:151], v[176:179], v[24:31], v112\n\tv_exp_f32 v33, v33\n\t"
        "v_exp_f32 v34, v34\n\tv_exp_f32 v35, v35\n\tv_smfmac_f32_16x16x64_bf16 v[152:155], v[84:87], v[24:31], v112\n\t"
        "v_mfma_f32_32x32x16_bf16 v[16:31], v[96:99], v[0:3], v[120:135]\n\tds_read_b128 v[164:167], %[k0] offset:4096\n\tv_exp_f32 v36, v36\n\t"
        "v_exp_f32 v37, v37\n\tv_exp_f32 v38, v38\n\tv_exp_f32 v39, v39\n\tv_mfma_f32_32x32x16_bf16 v[16:31], v[100:103], v[4:7], v[16:31]\n\t"
        "ds_read_b128 v[168:171], %[k0] offset:4352\n\tv_exp_f32 v40, v40\n\tv_exp_f32 v41, v41\n\tv_exp_f32 v42, v42\n\tv_exp_f32 v43, v43\n\t"
        "v_smfmac_f32_32x32x32_bf16 v[64:79], v[172:175], v[32:39], v112\n\tds_read_b128 v[172:175], %[v0] offset:6144\n\tv_exp_f32 v44, v44\n\t"
        "v_exp_f32 v45, v45\n\tv_exp_f32 v46, v46\n\tv_smfmac_f32_16x16x64_bf16 v[80:83], v[84:87], v[32:39], v112\n\tv_exp_f32 v47, v47\n\t"
        "v_exp_f32 v16, v16\n\tv_smfmac_f32_32x32x32_bf16 v[64:79], v[176:179], v[40:47], v112\n\tds_read_b128 v[176:179], %[v0] offset:6656\n\t"
        "v_exp_f32 v17, v17\n\tv_exp_f32 v18, v18\n\tv_exp_f32 v19, v19\n\tv_smfmac_f32_16x16x64_bf16 v[80:83], v[84:87], v[40:47], v112\n\t"
        "v_mfma_f32_32x32x16_bf16 v[32:47], v[96:99], v[88:91], v[48:63]\n\tv_exp_f32 v20, v20\n\tv_exp_f32 v21, v21\n\tv_exp_f32 v22, v22\n\t"
        "v_exp_f32 v23, v23\n\tv_mfma_f32_32x32x16_bf16 v[32:47], v[100:103], v[92:95], v[32:47]\n\tv_exp_f32 v24, v24\n\tv_exp_f32 v25, v25\n\t"
        "v_exp_f32 v26, v26\n\tv_exp_f32 v27, v27\n\tv_smfmac_f32_32x32x32_bf16 v[136:151], v[104:107], v[16:23], v112\n\tv_exp_f32 v28, v28\n\t"
        "v_exp_f32 v29, v29\n\tv_exp_f32 v30, v30\n\tv_smfmac_f32_16x16x64_bf16 v[152:155], v[84:87], v[16:23], v112\n\ts_waitcnt lgkmcnt(0)\n\t"
        "v_exp_f32 v31, v31\n\tv_exp_f32 v32, v32\n\tv_smfmac_f32_32x32x32_bf16 v[136:151], v[108:111], v[24:31], v112\n\tv_exp_f32 v33, v33\n\t"
        "v_exp_f32 v34, v34\n\tv_exp_f32 v35, v35\n\tv_smfmac_f32_16x16x64_bf16 v[152:155], v[84:87], v[24:31], v112\n\t"
        "v_mfma_f32_32x32x16_bf16 v[16:31], v[164:167], v[0:3], v[120:135]\n\tds_read_b128 v[96:99], %[k0] offset:8192\n\tv_exp_f32 v36, v36\n\t"
        "v_exp_f32 v37, v37\n\tv_exp_f32 v38, v38\n\tv_exp_f32 v39, v39\n\tv_mfma_f32_32x32x16_bf16 v[16:31], v[168:171], v[4:7], v[16:31]\n\t"
        "ds_read_b128 v[100:103], %[k0] offset:8448\n\tv_exp_f32 v40, v40\n\tv_exp_f32 v41, v41\n\tv_exp_f32 v42, v42\n\tv_exp_f32 v43, v43\n\t"
        "v_smfmac_f32_32x32x32_bf16 v[64:79], v[104:107], v[32:39], v112\n\tds_read_b128 v[104:107], %[v0] offset:10240\n\tv_exp_f32 v44, v44\n\t"
        "v_exp_f32 v45, v45\n\tv_exp_f32 v46, v46\n\tv_smfmac_f32_16x16x64_bf16 v[80:83], v[84:87], v[32:39], v112\n\tv_exp_f32 v47, v47\n\t"
        "v_exp_f32 v16, v16\n\tv_smfmac_f32_32x32x32_bf16 v[64:79], v[108:111], v[40:47], v112\n\tds_read_b128 v[108:111], %[v0] offset:10752\n\t"
        "v_exp_f32 v17, v17\n\tv_exp_f32 v18, v18\n\tv_exp_f32 v19, v19\n\tv_smfmac_f32_16x16x64_bf16 v[80:83], v[84:87], v[40:47], v112\n\t"
        "v_mfma_f32_32x32x16_bf16 v[32:47], v[164:167], v[88:91], v[48:63]\n\tv_exp_f32 v20, v20\n\tv_exp_f32 v21, v21\n\tv_exp_f32 v22, v22\n\t"
        "v_exp_f32 v23, v23\n\tv_mfma_f32_32x32x16_bf16 v[32:47], v[168:171], v[92:95], v[32:47]\n\tv_exp_f32 v24, v24\n\tv_exp_f32 v25, v25\n\t"
        "v_exp_f32 v26, v26\n\tv_exp_f32 v27, v27\n\tv_smfmac_f32_32x32x32_bf16 v[136:151], v[172:175], v[16:23], v112\n\tv_exp_f32 v28, v28\n\t"
        "v_exp_f32 v29, v29\n\tv_exp_f32 v30, v30\n\tv_smfmac_f32_16x16x64_bf16 v[152:155], v[84:87], v[16:23], v112\n\ts_waitcnt lgkmcnt(0)\n\t"
        "v_exp_f32 v31, v31\n\tv_exp_f32 v32, v32\n\tv_smfmac_f32_32x32x32_bf16 v[136:151], v[176:179], v[24:31], v112\n\tv_exp_f32 v33, v33\n\t"
        "v_exp_f32 v34, v34\n\tv_exp_f32 v35, v35\n\tv_smfmac_f32_16x16x64_bf16 v[152:155], v[84:87], v[24:31], v112\n\t"
        "v_mfma_f32_32x32x16_bf16 v[16:31], v[96:99], v[0:3], v[120:135]\n\tds_read_b128 v[164:167], %[k0] offset:12288\n\tv_exp_f32 v36, v36\n\t"
        "v_exp_f32 v37, v37\n\tv_exp_f32 v38, v38\n\tv_exp_f32 v39, v39\n\tv_mfma_f32_32x32x16_bf16 v[16:31], v[100:103], v[4:7], v[16:31]\n\t"
        "ds_read_b128 v[168:171], %[k0] offset:12544\n\tv_exp_f32 v40, v40\n\tv_exp_f32 v41, v41\n\tv_exp_f32 v42, v42\n\tv_exp_f32 v43, v43\n\t"
        "v_smfmac_f32_32x32x32_bf16 v[64:79], v[172:175], v[32:39], v112\n\tds_read_b128 v[172:175], %[v0] offset:14336\n\tv_exp_f32 v44, v44\n\t"
        "v_exp_f32 v45, v45\n\tv_exp_f32 v46, v46\n\tv_smfmac_f32_16x16x64_bf16 v[80:83], v[84:87], v[32:39], v112\n\tv_exp_f32 v47, v47\n\t"
        "v_exp_f32 v16, v16\n\tv_smfmac_f32_32x32x32_bf16 v[64:79], v[176:179], v[40:47], v112\n\tds_read_b128 v[176:179], %[v0] offset:14848\n\t"
        "v_exp_f32 v17, v17\n\tv_exp_f32 v18, v18\n\tv_exp_f32 v19, v19\n\tv_smfmac_f32_16x16x64_bf16 v[80:83], v[84:87], v[40:47], v112\n\t"
        "v_mfma_f32_32x32x16_bf16 v[32:47], v[96:99], v[88:91], v[48:63]\n\tv_exp_f32 v20, v20\n\tv_exp_f32 v21, v21\n\tv_exp_f32 v22, v22\n\t"
        "v_exp_f32 v23, v23\n\tv_mfma_f32_32x32x16_bf16 v[32:47], v[100:103], v[92:95], v[32:47]\n\tv_exp_f32 v24, v24\n\tv_exp_f32 v25, v25\n\t"
        "v_exp_f32 v26, v26\n\tv_exp_f32 v27, v27\n\tv_smfmac_f32_32x32x32_bf16 v[136:151], v[104:107], v[16:23], v112\n\tv_exp_f32 v28, v28\n\t"
        "v_exp_f32 v29, v29\n\tv_exp_f32 v30, v30\n\tv_smfmac_f32_16x16x64_bf16 v[152:155], v[84:87], v[16:23], v112\n\ts_waitcnt lgkmcnt(0)\n\t"
        "v_exp_f32 v31, v31\n\tv_exp_f32 v32, v32\n\tv_smfmac_f32_32x32x32_bf16 v[136:151], v[108:111], v[24:31], v112\n\tv_exp_f32 v33, v33\n\t"
        "v_exp_f32 v34, v34\n\tv_exp_f32 v35, v35\n\tv_smfmac_f32_16x16x64_bf16 v[152:155], v[84:87], v[24:31], v112\n\ts_barrier\n\t"
        "v_mfma_f32_32x32x16_bf16 v[16:31], v[164:167], v[0:3], v[120:135]\n\tds_read_b128 v[96:99], %[k0] offset:16384\n\tv_exp_f32 v36, v36\n\t"
        "v_exp_f32 v37, v37\n\tv_exp_f32 v38, v38\n\tv_exp_f32 v39, v39\n\tv_mfma_f32_32x32x16_bf16 v[16:31], v[168:171], v[4:7], v[16:31]\n\t"
        "ds_read_b128 v[100:103], %[k0] offset:16640\n\tv_exp_f32 v40, v40\n\tv_exp_f32 v41, v41\n\tv_exp_f32 v42, v42\n\tv_exp_f32 v43, v43\n\t"
        "v_smfmac_f32_32x32x32_bf16 v[64:79], v[104:107], v[32:39], v112\n\tds_read_b128 v[104:107], %[v0] offset:18432\n\tv_exp_f32 v44, v44\n\t"
        "v_exp_f32 v45, v45\n\tv_exp_f32 v46, v46\n\tv_smfmac_f32_16x16x64_bf16 v[80:83], v[84:87], v[32:39], v112\n\tv_exp_f32 v47, v47\n\t"
        "v_exp_f32 v16, v16\n\tv_smfmac_f32_32x32x32_bf16 v[64:79], v[108:111], v[40:47], v112\n\tds_read_b128 v[108:111], %[v0] offset:18944\n\t"
        "v_exp_f32 v17, v17\n\tv_exp_f32 v18, v18\n\tv_exp_f32 v19, v19\n\tv_smfmac_f32_16x16x64_bf16 v[80:83], v[84:87], v[40:47], v112\n\t"
        "v_mfma_f32_32x32x16_bf16 v[32:47], v[164:167], v[88:91], v[48:63]\n\tv_exp_f32 v20, v20\n\tv_exp_f32 v21, v21\n\tv_exp_f32 v22, v22\n\t"
        "v_exp_f32 v23, v23\n\tv_mfma_f32_32x32x16_bf16 v[32:47], v[168:171], v[92:95], v[32:47]\n\tv_exp_f32 v24, v24\n\tv_exp_f32 v25, v25\n\t"
        "v_exp_f32 v26, v26\n\tv_exp_f32 v27, v27\n\tv_smfmac_f32_32x32x32_bf16 v[136:151], v[172:175], v[16:23], v112\n\tv_exp_f32 v28, v28\n\t"
        "v_exp_f32 v29, v29\n\tv_exp_f32 v30, v30\n\tv_smfmac_f32_16x16x64_bf16 v[152:155], v[84:87], v[16:23], v112\n\ts_waitcnt lgkmcnt(0)\n\t"
        "v_exp_f32 v31, v31\n\tv_exp_f32 v32, v32\n\tv_smfmac_f32_32x32x32_bf16 v[136:151], v[176:179], v[24:31], v112\n\tv_exp_f32 v33, v33\n\t"
        "v_exp_f32 v34, v34\n\tv_exp_f32 v35, v35\n\tv_smfmac_f32_16x16x64_bf16 v[152:155], v[84:87], v[24:31], v112\n\t"
        "v_mfma_f32_32x32x16_bf16 v[16:31], v[96:99], v[0:3], v[120:135]\n\tds_read_b128 v[164:167], %[k0] offset:20480\n\tv_exp_f32 v36, v36\n\t"
        "v_exp_f32 v37, v37\n\tv_exp_f32 v38, v38\n\tv_exp_f32 v39, v39\n\tv_mfma_f32_32x32x16_bf16 v[16:31], v[100:103], v[4:7], v[16:31]\n\t"
        "ds_read_b128 v[168:171], %[k0] offset:20736\n\tv_exp_f32 v40, v40\n\tv_exp_f32 v41, v41\n\tv_exp_f32 v42, v42\n\tv_exp_f32 v43, v43\n\t"
        "v_smfmac_f32_32x32x32_bf16 v[64:79], v[172:175], v[32:39], v112\n\tds_read_b128 v[172:175], %[v0] offset:22528\n\tv_exp_f32 v44, v44\n\t"
        "v_exp_f32 v45, v45\n\tv_exp_f32 v46, v46\n\tv_smfmac_f32_16x16x64_bf16 v[80:83], v[84:87], v[32:39], v112\n\tv_exp_f32 v47, v47\n\t"
        "v_exp_f32 v16, v16\n\tv_smfmac_f32_32x32x32_bf16 v[64:79], v[176:179], v[40:47], v112\n\tds_read_b128 v[176:179], %[v0] offset:23040\n\t"
        "v_exp_f32 v17, v17\n\tv_exp_f32 v18, v18\n\tv_exp_f32 v19, v19\n\tv_smfmac_f32_16x16x64_bf16 v[80:83], v[84:87], v[40:47], v112\n\t"
        "v_mfma_f32_32x32x16_bf16 v[32:47], v[96:99], v[88:91], v[48:63]\n\tv_exp_f32 v20, v20\n\tv_exp_f32 v21, v21\n\tv_exp_f32 v22, v22\n\t"
        "v_exp_f32 v23, v23\n\tv_mfma_f32_32x32x16_bf16 v[32:47], v[100:103], v[92:95], v[32:47]\n\tv_exp_f32 v24, v24\n\tv_exp_f32 v25, v25\n\t"
        "v_exp_f32 v26, v26\n\tv_exp_f32 v27, v27\n\tv_smfmac_f32_32x32x32_bf16 v[136:151], v[104:107], v[16:23], v112\n\tv_exp_f32 v28, v28\n\t"
        "v_exp_f32 v29, v29\n\tv_exp_f32 v30, v30\n\tv_smfmac_f32_16x16x64_bf16 v[152:155], v[84:87], v[16:23], v112\n\ts_waitcnt lgkmcnt(0)\n\t"
        "v_exp_f32 v31, v31\n\tv_exp_f32 v32, v32\n\tv_smfmac_f32_32x32x32_bf16 v[136:151], v[108:111], v[24:31], v112\n\tv_exp_f32 v33, v33\n\t"
        "v_exp_f32 v34, v34\n\tv_exp_f32 v35, v35\n\tv_smfmac_f32_16x16x64_bf16 v[152:155], v[84:87], v[24:31], v112\n\t"
        "v_mfma_f32_32x32x16_bf16 v[16:31], v[164:167], v[0:3], v[120:135]\n\tds_read_b128 v[96:99], %[k0] offset:24576\n\tv_exp_f32 v36, v36\n\t"
        "v_exp_f32 v37, v37\n\tv_exp_f32 v38, v38\n\tv_exp_f32 v39, v39\n\tv_mfma_f32_32x32x16_bf16 v[16:31], v[168:171], v[4:7], v[16:31]\n\t"
        "ds_read_b128 v[100:103], %[k0] offset:24832\n\tv_exp_f32 v40, v40\n\tv_exp_f32 v41, v41\n\tv_exp_f32 v42, v42\n\tv_exp_f32 v43, v43\n\t"
        "v_smfmac_f32_32x32x32_bf16 v[64:79], v[104:107], v[32:39], v112\n\tds_read_b128 v[104:107], %[v0] offset:26624\n\tv_exp_f32 v44, v44\n\t"
        "v_exp_f32 v45, v45\n\tv_exp_f32 v46, v46\n\t.p2align 12,,12\n\t.Lpg6:\n\ts_cbranch_execz .Lpgret\n\t"
        "v_smfmac_f32_16x16x64_bf16 v[80:83], v[84:87], v[32:39], v112\n\tv_exp_f32 v47, v47\n\tv_exp_f32 v16, v16\n\t"
        "v_smfmac_f32_32x32x32_bf16 v[64:79], v[108:111], v[40:47], v112\n\tds_read_b128 v[108:111], %[v0] offset:27136\n\tv_exp_f32 v17, v17\n\t"
        "v_exp_f32 v18, v18\n\tv_exp_f32 v19, v19\n\tv_smfmac_f32_16x16x64_bf16 v[80:83], v[84:87], v[40:47], v112\n\t"
        "v_mfma_f32_32x32x16_bf16 v[32:47], v[164:167], v[88:91], v[48:63]\n\tv_exp_f32 v20, v20\n\tv_exp_f32 v21, v21\n\tv_exp_f32 v22, v22\n\t"
        "v_exp_f32 v23, v23\n\tv_mfma_f32_32x32x16_bf16 v[32:47], v[168:171], v[92:95], v[32:47]\n\tv_exp_f32 v24, v24\n\tv_exp_f32 v25, v25\n\t"
        "v_exp_f32 v26, v26\n\tv_exp_f32 v27, v27\n\tv_smfmac_f32_32x32x32_bf16 v[136:151], v[172:175], v[16:23], v112\n\tv_exp_f32 v28, v28\n\t"
        "v_exp_f32 v29, v29\n\tv_exp_f32 v30, v30\n\tv_smfmac_f32_16x16x64_bf16 v[152:155], v[84:87], v[16:23], v112\n\ts_waitcnt lgkmcnt(0)\n\t"
        "v_exp_f32 v31, v31\n\tv_exp_f32 v32, v32\n\tv_smfmac_f32_32x32x32_bf16 v[136:151], v[176:179], v[24:31], v112\n\tv_exp_f32 v33, v33\n\t"
        "v_exp_f32 v34, v34\n\tv_exp_f32 v35, v35\n\tv_smfmac_f32_16x16x64_bf16 v[152:155], v[84:87], v[24:31], v112\n\t"
        "v_mfma_f32_32x32x16_bf16 v[16:31], v[96:99], v[0:3], v[120:135]\n\tds_read_b128 v[164:167], %[k0] offset:28672\n\tv_exp_f32 v36, v36\n\t"
        "v_exp_f32 v37, v37\n\tv_exp_f32 v38, v38\n\tv_exp_f32 v39, v39\n\tv_mfma_f32_32x32x16_bf16 v[16:31], v[100:103], v[4:7], v[16:31]\n\t"
        "ds_read_b128 v[168:171], %[k0] offset:28928\n\tv_exp_f32 v40, v40\n\tv_exp_f32 v41, v41\n\tv_exp_f32 v42, v42\n\tv_exp_f32 v43, v43\n\t"
        "v_smfmac_f32_32x32x32_bf16 v[64:79], v[172:175], v[32:39], v112\n\tds_read_b128 v[172:175], %[v0] offset:30720\n\tv_exp_f32 v44, v44\n\t"
        "v_exp_f32 v45, v45\n\tv_exp_f32 v46, v46\n\tv_smfmac_f32_16x16x64_bf16 v[80:83], v[84:87], v[32:39], v112\n\tv_exp_f32 v47, v47\n\t"
        "v_exp_f32 v16, v16\n\tv_smfmac_f32_32x32x32_bf16 v[64:79], v[176:179], v[40:47], v112\n\tds_read_b128 v[176:179], %[v0] offset:31232\n\t"
        "v_exp_f32 v17, v17\n\tv_exp_f32 v18, v18\n\tv_exp_f32 v19, v19\n\tv_smfmac_f32_16x16x64_bf16 v[80:83], v[84:87], v[40:47], v112\n\t"
        "v_mfma_f32_32x32x16_bf16 v[32:47], v[96:99], v[88:91], v[48:63]\n\tv_exp_f32 v20, v20\n\tv_exp_f32 v21, v21\n\tv_exp_f32 v22, v22\n\t"
        "v_exp_f32 v23, v23\n\tv_mfma_f32_32x32x16_bf16 v[32:47], v[100:103], v[92:95], v[32:47]\n\tv_exp_f32 v24, v24\n\tv_exp_f32 v25, v25\n\t"
        "v_exp_f32 v26, v26\n\tv_exp_f32 v27, v27\n\tv_smfmac_f32_32x32x32_bf16 v[136:151], v[104:107], v[16:23], v112\n\tv_exp_f32 v28, v28\n\t"
        "v_exp_f32 v29, v29\n\tv_exp_f32 v30, v30\n\tv_smfmac_f32_16x16x64_bf16 v[152:155], v[84:87], v[16:23], v112\n\ts_waitcnt lgkmcnt(0)\n\t"
        "v_exp_f32 v31, v31\n\tv_exp_f32 v32, v32\n\tv_smfmac_f32_32x32x32_bf16 v[136:151], v[108:111], v[24:31], v112\n\tv_exp_f32 v33, v33\n\t"
        "v_exp_f32 v34, v34\n\tv_exp_f32 v35, v35\n\tv_smfmac_f32_16x16x64_bf16 v[152:155], v[84:87], v[24:31], v112\n\ts_barrier\n\t"
        "v_mfma_f32_32x32x16_bf16 v[16:31], v[164:167], v[0:3], v[120:135]\n\tds_read_b128 v[96:99], %[k0] offset:32768\n\tv_exp_f32 v36, v36\n\t"
        "v_exp_f32 v37, v37\n\tv_exp_f32 v38, v38\n\tv_exp_f32 v39, v39\n\tv_mfma_f32_32x32x16_bf16 v[16:31], v[168:171], v[4:7], v[16:31]\n\t"
        "ds_read_b128 v[100:103], %[k0] offset:33024\n\tv_exp_f32 v40, v40\n\tv_exp_f32 v41, v41\n\tv_exp_f32 v42, v42\n\tv_exp_f32 v43, v43\n\t"
        "v_smfmac_f32_32x32x32_bf16 v[64:79], v[104:107], v[32:39], v112\n\tds_read_b128 v[104:107], %[v0] offset:34816\n\tv_exp_f32 v44, v44\n\t"
        "v_exp_f32 v45, v45\n\tv_exp_f32 v46, v46\n\tv_smfmac_f32_16x16x64_bf16 v[80:83], v[84:87], v[32:39], v112\n\tv_exp_f32 v47, v47\n\t"
        "v_exp_f32 v16, v16\n\tv_smfmac_f32_32x32x32_bf16 v[64:79], v[108:111], v[40:47], v112\n\tds_read_b128 v[108:111], %[v0] offset:35328\n\t"
        "v_exp_f32 v17, v17\n\tv_exp_f32 v18, v18\n\tv_exp_f32 v19, v19\n\tv_smfmac_f32_16x16x64_bf16 v[80:83], v[84:87], v[40:47], v112\n\t"
        "v_mfma_f32_32x32x16_bf16 v[32:47], v[164:167], v[88:91], v[48:63]\n\tv_exp_f32 v20, v20\n\tv_exp_f32 v21, v21\n\tv_exp_f32 v22, v22\n\t"
        "v_exp_f32 v23, v23\n\tv_mfma_f32_32x32x16_bf16 v[32:47], v[168:171], v[92:95], v[32:47]\n\tv_exp_f32 v24, v24\n\tv_exp_f32 v25, v25\n\t"
        "v_exp_f32 v26, v26\n\tv_exp_f32 v27, v27\n\tv_smfmac_f32_32x32x32_bf16 v[136:151], v[172:175], v[16:23], v112\n\tv_exp_f32 v28, v28\n\t"
        "v_exp_f32 v29, v29\n\tv_exp_f32 v30, v30\n\tv_smfmac_f32_16x16x64_bf16 v[152:155], v[84:87], v[16:23], v112\n\ts_waitcnt lgkmcnt(0)\n\t"
        "v_exp_f32 v31, v31\n\tv_exp_f32 v32, v32\n\tv_smfmac_f32_32x32x32_bf16 v[136:151], v[176:179], v[24:31], v112\n\tv_exp_f32 v33, v33\n\t"
        "v_exp_f32 v34, v34\n\tv_exp_f32 v35, v35\n\tv_smfmac_f32_16x16x64_bf16 v[152:155], v[84:87], v[24:31], v112\n\t"
        "v_mfma_f32_32x32x16_bf16 v[16:31], v[96:99], v[0:3], v[120:135]\n\tds_read_b128 v[164:167], %[k0] offset:36864\n\tv_exp_f32 v36, v36\n\t"
        "v_exp_f32 v37, v37\n\tv_exp_f32 v38, v38\n\tv_exp_f32 v39, v39\n\tv_mfma_f32_32x32x16_bf16 v[16:31], v[100:103], v[4:7], v[16:31]\n\t"
        "ds_read_b128 v[168:171], %[k0] offset:37120\n\tv_exp_f32 v40, v40\n\tv_exp_f32 v41, v41\n\tv_exp_f32 v42, v42\n\tv_exp_f32 v43, v43\n\t"
        "v_smfmac_f32_32x32x32_bf16 v[64:79], v[172:175], v[32:39], v112\n\tds_read_b128 v[172:175], %[v0] offset:38912\n\tv_exp_f32 v44, v44\n\t"
        "v_exp_f32 v45, v45\n\tv_exp_f32 v46, v46\n\tv_smfmac_f32_16x16x64_bf16 v[80:83], v[84:87], v[32:39], v112\n\tv_exp_f32 v47, v47\n\t"
        "v_exp_f32 v16, v16\n\tv_smfmac_f32_32x32x32_bf16 v[64:79], v[176:179], v[40:47], v112\n\tds_read_b128 v[176:179], %[v0] offset:39424\n\t"
        "v_exp_f32 v17, v17\n\tv_exp_f32 v18, v18\n\tv_exp_f32 v19, v19\n\tv_smfmac_f32_16x16x64_bf16 v[80:83], v[84:87], v[40:47], v112\n\t"
        "v_mfma_f32_32x32x16_bf16 v[32:47], v[96:99], v[88:91], v[48:63]\n\tv_exp_f32 v20, v20\n\tv_exp_f32 v21, v21\n\tv_exp_f32 v22, v22\n\t"
        "v_exp_f32 v23, v23\n\tv_mfma_f32_32x32x16_bf16 v[32:47], v[100:103], v[92:95], v[32:47]\n\tv_exp_f32 v24, v24\n\tv_exp_f32 v25, v25\n\t"
        "v_exp_f32 v26, v26\n\tv_exp_f32 v27, v27\n\tv_smfmac_f32_32x32x32_bf16 v[136:151], v[104:107], v[16:23], v112\n\tv_exp_f32 v28, v28\n\t"
        "v_exp_f32 v29, v29\n\tv_exp_f32 v30, v30\n\tv_smfmac_f32_16x16x64_bf16 v[152:155], v[84:87], v[16:23], v112\n\ts_waitcnt lgkmcnt(0)\n\t"
        "v_exp_f32 v31, v31\n\tv_exp_f32 v32, v32\n\tv_smfmac_f32_32x32x32_bf16 v[136:151], v[108:111], v[24:31], v112\n\tv_exp_f32 v33, v33\n\t"
        "v_exp_f32 v34, v34\n\tv_exp_f32 v35, v35\n\tv_smfmac_f32_16x16x64_bf16 v[152:155], v[84:87], v[24:31], v112\n\t"
        "v_mfma_f32_32x32x16_bf16 v[16:31], v[164:167], v[0:3], v[120:135]\n\tds_read_b128 v[96:99], %[k0] offset:40960\n\tv_exp_f32 v36, v36\n\t"
        "v_exp_f32 v37, v37\n\tv_exp_f32 v38, v38\n\tv_exp_f32 v39, v39\n\tv_mfma_f32_32x32x16_bf16 v[16:31], v[168:171], v[4:7], v[16:31]\n\t"
        "ds_read_b128 v[100:103], %[k0] offset:41216\n\tv_exp_f32 v40, v40\n\tv_exp_f32 v41, v41\n\tv_exp_f32 v42, v42\n\tv_exp_f32 v43, v43\n\t"
        "v_smfmac_f32_32x32x32_bf16 v[64:79], v[104:107], v[32:39], v112\n\tds_read_b128 v[104:107], %[v0] offset:43008\n\tv_exp_f32 v44, v44\n\t"
        "v_exp_f32 v45, v45\n\tv_exp_f32 v46, v46\n\tv_smfmac_f32_16x16x64_bf16 v[80:83], v[84:87], v[32:39], v112\n\tv_exp_f32 v47, v47\n\t"
        "v_exp_f32 v16, v16\n\tv_smfmac_f32_32x32x32_bf16 v[64:79], v[108:111], v[40:47], v112\n\tds_read_b128 v[108:111], %[v0] offset:43520\n\t"
        "v_exp_f32 v17, v17\n\tv_exp_f32 v18, v18\n\tv_exp_f32 v19, v19\n\tv_smfmac_f32_16x16x64_bf16 v[80:83], v[84:87], v[40:47], v112\n\t"
        "v_mfma_f32_32x32x16_bf16 v[32:47], v[164:167], v[88:91], v[48:63]\n\tv_exp_f32 v20, v20\n\tv_exp_f32 v21, v21\n\tv_exp_f32 v22, v22\n\t"
        "v_exp_f32 v23, v23\n\tv_mfma_f32_32x32x16_bf16 v[32:47], v[168:171], v[92:95], v[32:47]\n\tv_exp_f32 v24, v24\n\tv_exp_f32 v25, v25\n\t"
        "v_exp_f32 v26, v26\n\tv_exp_f32 v27, v27\n\tv_smfmac_f32_32x32x32_bf16 v[136:151], v[172:175], v[16:23], v112\n\tv_exp_f32 v28, v28\n\t"
        "v_exp_f32 v29, v29\n\tv_exp_f32 v30, v30\n\tv_smfmac_f32_16x16x64_bf16 v[152:155], v[84:87], v[16:23], v112\n\ts_waitcnt lgkmcnt(0)\n\t"
        "v_exp_f32 v31, v31\n\tv_exp_f32 v32, v32\n\tv_smfmac_f32_32x32x32_bf16 v[136:151], v[176:179], v[24:31], v112\n\tv_exp_f32 v33, v33\n\t"
        "v_exp_f32 v34, v34\n\tv_exp_f32 v35, v35\n\tv_smfmac_f32_16x16x64_bf16 v[152:155], v[84:87], v[24:31], v112\n\t"
        "v_mfma_f32_32x32x16_bf16 v[16:31], v[96:99], v[0:3], v[120:135]\n\tds_read_b128 v[164:167], %[k0] offset:45056\n\tv_exp_f32 v36, v36\n\t"
        "v_exp_f32 v37, v37\n\tv_exp_f32 v38, v38\n\tv_exp_f32 v39, v39\n\tv_mfma_f32_32x32x16_bf16 v[16:31], v[100:103], v[4:7], v[16:31]\n\t"
        "ds_read_b128 v[168:171], %[k0] offset:45312\n\tv_exp_f32 v40, v40\n\tv_exp_f32 v41, v41\n\tv_exp_f32 v42, v42\n\tv_exp_f32 v43, v43\n\t"
        "v_smfmac_f32_32x32x32_bf16 v[64:79], v[172:175], v[32:39], v112\n\tds_read_b128 v[172:175], %[v0] offset:47104\n\tv_exp_f32 v44, v44\n\t"
        "v_exp_f32 v45, v45\n\tv_exp_f32 v46, v46\n\tv_smfmac_f32_16x16x64_bf16 v[80:83], v[84:87], v[32:39], v112\n\tv_exp_f32 v47, v47\n\t"
        "v_exp_f32 v16, v16\n\tv_smfmac_f32_32x32x32_bf16 v[64:79], v[176:179], v[40:47], v112\n\tds_read_b128 v[176:179], %[v0] offset:47616\n\t"
        "v_exp_f32 v17, v17\n\tv_exp_f32 v18, v18\n\tv_exp_f32 v19, v19\n\tv_smfmac_f32_16x16x64_bf16 v[80:83], v[84:87], v[40:47], v112\n\t"
        "v_mfma_f32_32x32x16_bf16 v[32:47], v[96:99], v[88:91], v[48:63]\n\tv_exp_f32 v20, v20\n\tv_exp_f32 v21, v21\n\tv_exp_f32 v22, v22\n\t"
        "v_exp_f32 v23, v23\n\tv_mfma_f32_32x32x16_bf16 v[32:47], v[100:103], v[92:95], v[32:47]\n\tv_exp_f32 v24, v24\n\tv_exp_f32 v25, v25\n\t"
        "v_exp_f32 v26, v26\n\tv_exp_f32 v27, v27\n\tv_smfmac_f32_32x32x32_bf16 v[136:151], v[104:107], v[16:23], v112\n\tv_exp_f32 v28, v28\n\t"
        "v_exp_f32 v29, v29\n\tv_exp_f32 v30, v30\n\tv_smfmac_f32_16x16x64_bf16 v[152:155], v[84:87], v[16:23], v112\n\ts_waitcnt lgkmcnt(0)\n\t"
        "v_exp_f32 v31, v31\n\tv_exp_f32 v32, v32\n\tv_smfmac_f32_32x32x32_bf16 v[136:151], v[108:111], v[24:31], v112\n\tv_exp_f32 v33, v33\n\t"
        "v_exp_f32 v34, v34\n\tv_exp_f32 v35, v35\n\tv_smfmac_f32_16x16x64_bf16 v[152:155], v[84:87], v[24:31], v112\n\ts_barrier\n\t"
        "v_mfma_f32_32x32x16_bf16 v[16:31], v[164:167], v[0:3], v[120:135]\n\tds_read_b128 v[96:99], %[k0] offset:49152\n\tv_exp_f32 v36, v36\n\t"
        "v_exp_f32 v37, v37\n\tv_exp_f32 v38, v38\n\tv_exp_f32 v39, v39\n\tv_mfma_f32_32x32x16_bf16 v[16:31], v[168:171], v[4:7], v[16:31]\n\t"
        "ds_read_b128 v[100:103], %[k0] offset:49408\n\tv_exp_f32 v40, v40\n\tv_exp_f32 v41, v41\n\tv_exp_f32 v42, v42\n\tv_exp_f32 v43, v43\n\t"
        "v_smfmac_f32_32x32x32_bf16 v[64:79], v[104:107], v[32:39], v112\n\tds_read_b128 v[104:107], %[v0] offset:51200\n\tv_exp_f32 v44, v44\n\t"
        "v_exp_f32 v45, v45\n\tv_exp_f32 v46, v46\n\tv_smfmac_f32_16x16x64_bf16 v[80:83], v[84:87], v[32:39], v112\n\tv_exp_f32 v47, v47\n\t"
        "v_exp_f32 v16, v16\n\tv_smfmac_f32_32x32x32_bf16 v[64:79], v[108:111], v[40:47], v112\n\tds_read_b128 v[108:111], %[v0] offset:51712\n\t"
        "v_exp_f32 v17, v17\n\tv_exp_f32 v18, v18\n\tv_exp_f32 v19, v19\n\tv_smfmac_f32_16x16x64_bf16 v[80:83], v[84:87], v[40:47], v112\n\t"
        "v_mfma_f32_32x32x16_bf16 v[32:47], v[164:167], v[88:91], v[48:63]\n\tv_exp_f32 v20, v20\n\tv_exp_f32 v21, v21\n\tv_exp_f32 v22, v22\n\t"
        "v_exp_f32 v23, v23\n\tv_mfma_f32_32x32x16_bf16 v[32:47], v[168:171], v[92:95], v[32:47]\n\tv_exp_f32 v24, v24\n\tv_exp_f32 v25, v25\n\t"
        "v_exp_f32 v26, v26\n\tv_exp_f32 v27, v27\n\tv_smfmac_f32_32x32x32_bf16 v[136:151], v[172:175], v[16:23], v112\n\tv_exp_f32 v28, v28\n\t"
        "v_exp_f32 v29, v29\n\tv_exp_f32 v30, v30\n\tv_smfmac_f32_16x16x64_bf16 v[152:155], v[84:87], v[16:23], v112\n\ts_waitcnt lgkmcnt(0)\n\t"
        "v_exp_f32 v31, v31\n\tv_exp_f32 v32, v32\n\tv_smfmac_f32_32x32x32_bf16 v[136:151], v[176:179], v[24:31], v112\n\tv_exp_f32 v33, v33\n\t"
        "v_exp_f32 v34, v34\n\tv_exp_f32 v35, v35\n\tv_smfmac_f32_16x16x64_bf16 v[152:155], v[84:87], v[24:31], v112\n\t"
        "v_mfma_f32_32x32x16_bf16 v[16:31], v[96:99], v[0:3], v[120:135]\n\tds_read_b128 v[164:167], %[k0] offset:53248\n\tv_exp_f32 v36, v36\n\t"
        "v_exp_f32 v37, v37\n\tv_exp_f32 v38, v38\n\tv_exp_f32 v39, v39\n\tv_mfma_f32_32x32x16_bf16 v[16:31], v[100:103], v[4:7], v[16:31]\n\t"
        "ds_read_b128 v[168:171], %[k0] offset:53504\n\tv_exp_f32 v40, v40\n\tv_exp_f32 v41, v41\n\tv_exp_f32 v42, v42\n\tv_exp_f32 v43, v43\n\t"
        "v_smfmac_f32_32x32x32_bf16 v[64:79], v[172:175], v[32:39], v112\n\tds_read_b128 v[172:175], %[v0] offset:55296\n\tv_exp_f32 v44, v44\n\t"
        "v_exp_f32 v45, v45\n\tv_exp_f32 v46, v46\n\tv_smfmac_f32_16x16x64_bf16 v[80:83], v[84:87], v[32:39], v112\n\tv_exp_f32 v47, v47\n\t"
        "v_exp_f32 v16, v16\n\tv_smfmac_f32_32x32x32_bf16 v[64:79], v[176:179], v[40:47], v112\n\tds_read_b128 v[176:179], %[v0] offset:55808\n\t"
        "v_exp_f32 v17, v17\n\tv_exp_f32 v18, v18\n\tv_exp_f32 v19, v19\n\tv_smfmac_f32_16x16x64_bf16 v[80:83], v[84:87], v[40:47], v112\n\t"
        "v_mfma_f32_32x32x16_bf16 v[32:47], v[96:99], v[88:91], v[48:63]\n\tv_exp_f32 v20, v20\n\tv_exp_f32 v21, v21\n\tv_exp_f32 v22, v22\n\t"
        "v_exp_f32 v23, v23\n\tv_mfma_f32_32x32x16_bf16 v[32:47], v[100:103], v[92:95], v[32:47]\n\tv_exp_f32 v24, v24\n\tv_exp_f32 v25, v25\n\t"
        "v_exp_f32 v26, v26\n\tv_exp_f32 v27, v27\n\tv_smfmac_f32_32x32x32_bf16 v[136:151], v[104:107], v[16:23], v112\n\tv_exp_f32 v28, v28\n\t"
        "v_exp_f32 v29, v29\n\tv_exp_f32 v30, v30\n\tv_smfmac_f32_16x16x64_bf16 v[152:155], v[84:87], v[16:23], v112\n\ts_waitcnt lgkmcnt(0)\n\t"
        "v_exp_f32 v31, v31\n\tv_exp_f32 v32, v32\n\tv_smfmac_f32_32x32x32_bf16 v[136:151], v[108:111], v[24:31], v112\n\tv_exp_f32 v33, v33\n\t"
        "v_exp_f32 v34, v34\n\tv_exp_f32 v35, v35\n\tv_smfmac_f32_16x16x64_bf16 v[152:155], v[84:87], v[24:31], v112\n\t"
        "v_mfma_f32_32x32x16_bf16 v[16:31], v[164:167], v[0:3], v[120:135]\n\tds_read_b128 v[96:99], %[k0] offset:57344\n\tv_exp_f32 v36, v36\n\t"
        "v_exp_f32 v37, v37\n\tv_exp_f32 v38, v38\n\tv_exp_f32 v39, v39\n\tv_mfma_f32_32x32x16_bf16 v[16:31], v[168:171], v[4:7], v[16:31]\n\t"
        "ds_read_b128 v[100:103], %[k0] offset:57600\n\tv_exp_f32 v40, v40\n\tv_exp_f32 v41, v41\n\tv_exp_f32 v42, v42\n\tv_exp_f32 v43, v43\n\t"
        "v_smfmac_f32_32x32x32_bf16 v[64:79], v[104:107], v[32:39], v112\n\tds_read_b128 v[104:107], %[v0] offset:59392\n\tv_exp_f32 v44, v44\n\t"
        "v_exp_f32 v45, v45\n\tv_exp_f32 v46, v46\n\tv_smfmac_f32_16x16x64_bf16 v[80:83], v[84:87], v[32:39], v112\n\tv_exp_f32 v47, v47\n\t"
        "v_exp_f32 v16, v16\n\tv_smfmac_f32_32x32x32_bf16 v[64:79], v[108:111], v[40:47], v112\n\tds_read_b128 v[108:111], %[v0] offset:59904\n\t"
        "v_exp_f32 v17, v17\n\tv_exp_f32 v18, v18\n\tv_exp_f32 v19, v19\n\tv_smfmac_f32_16x16x64_bf16 v[80:83], v[84:87], v[40:47], v112\n\t"
        "v_mfma_f32_32x32x16_bf16 v[32:47], v[164:167], v[88:91], v[48:63]\n\tv_exp_f32 v20, v20\n\tv_exp_f32 v21, v21\n\tv_exp_f32 v22, v22\n\t"
        "v_exp_f32 v23, v23\n\tv_mfma_f32_32x32x16_bf16 v[32:47], v[168:171], v[92:95], v[32:47]\n\tv_exp_f32 v24, v24\n\tv_exp_f32 v25, v25\n\t"
        "v_exp_f32 v26, v26\n\tv_exp_f32 v27, v27\n\tv_smfmac_f32_32x32x32_bf16 v[136:151], v[172:175], v[16:23], v112\n\tv_exp_f32 v28, v28\n\t"
        "v_exp_f32 v29, v29\n\tv_exp_f32 v30, v30\n\tv_smfmac_f32_16x16x64_bf16 v[152:155], v[84:87], v[16:23], v112\n\ts_waitcnt lgkmcnt(0)\n\t"
        "v_exp_f32 v31, v31\n\tv_exp_f32 v32, v32\n\tv_smfmac_f32_32x32x32_bf16 v[136:151], v[176:179], v[24:31], v112\n\tv_exp_f32 v33, v33\n\t"
        "v_exp_f32 v34, v34\n\tv_exp_f32 v35, v35\n\tv_smfmac_f32_16x16x64_bf16 v[152:155], v[84:87], v[24:31], v112\n\t"
        "v_mfma_f32_32x32x16_bf16 v[16:31], v[96:99], v[0:3], v[120:135]\n\tds_read_b128 v[164:167], %[k0] offset:61440\n\tv_exp_f32 v36, v36\n\t"
        "v_exp_f32 v37, v37\n\tv_exp_f32 v38, v38\n\tv_exp_f32 v39, v39\n\tv_mfma_f32_32x32x16_bf16 v[16:31], v[100:103], v[4:7], v[16:31]\n\t"
        "ds_read_b128 v[168:171], %[k0] offset:61696\n\tv_exp_f32 v40, v40\n\tv_exp_f32 v41, v41\n\tv_exp_f32 v42, v42\n\tv_exp_f32 v43, v43\n\t"
        "v_smfmac_f32_32x32x32_bf16 v[64:79], v[172:175], v[32:39], v112\n\tds_read_b128 v[172:175], %[v0] offset:63488\n\tv_exp_f32 v44, v44\n\t"
        "v_exp_f32 v45, v45\n\tv_exp_f32 v46, v46\n\tv_smfmac_f32_16x16x64_bf16 v[80:83], v[84:87], v[32:39], v112\n\tv_exp_f32 v47, v47\n\t"
        "v_exp_f32 v16, v16\n\tv_smfmac_f32_32x32x32_bf16 v[64:79], v[176:179], v[40:47], v112\n\tds_read_b128 v[176:179], %[v0] offset:64000\n\t"
        "v_exp_f32 v17, v17\n\tv_exp_f32 v18, v18\n\tv_exp_f32 v19, v19\n\tv_smfmac_f32_16x16x64_bf16 v[80:83], v[84:87], v[40:47], v112\n\t"
        "v_mfma_f32_32x32x16_bf16 v[32:47], v[96:99], v[88:91], v[48:63]\n\tv_exp_f32 v20, v20\n\tv_exp_f32 v21, v21\n\tv_exp_f32 v22, v22\n\t"
        "v_exp_f32 v23, v23\n\tv_mfma_f32_32x32x16_bf16 v[32:47], v[100:103], v[92:95], v[32:47]\n\tv_exp_f32 v24, v24\n\tv_exp_f32 v25, v25\n\t"
        "v_exp_f32 v26, v26\n\tv_exp_f32 v27, v27\n\tv_smfmac_f32_32x32x32_bf16 v[136:151], v[104:107], v[16:23], v112\n\tv_exp_f32 v28, v28\n\t"
        "v_exp_f32 v29, v29\n\tv_exp_f32 v30, v30\n\tv_smfmac_f32_16x16x64_bf16 v[152:155], v[84:87], v[16:23], v112\n\ts_waitcnt lgkmcnt(0)\n\t"
        "v_exp_f32 v31, v31\n\tv_exp_f32 v32, v32\n\tv_smfmac_f32_32x32x32_bf16 v[136:151], v[108:111], v[24:31], v112\n\tv_exp_f32 v33, v33\n\t"
        "v_exp_f32 v34, v34\n\tv_exp_f32 v35, v35\n\tv_smfmac_f32_16x16x64_bf16 v[152:155], v[84:87], v[24:31], v112\n\ts_barrier\n\t"
        "v_mfma_f32_32x32x16_bf16 v[16:31], v[164:167], v[0:3], v[120:135]\n\tds_read_b128 v[96:99], v113 offset:0\n\tv_exp_f32 v36, v36\n\t"
        "v_exp_f32 v37, v37\n\tv_exp_f32 v38, v38\n\tv_exp_f32 v39, v39\n\tv_mfma_f32_32x32x16_bf16 v[16:31], v[168:171], v[4:7], v[16:31]\n\t"
        "ds_read_b128 v[100:103], v113 offset:256\n\tv_exp_f32 v40, v40\n\tv_exp_f32 v41, v41\n\tv_exp_f32 v42, v42\n\tv_exp_f32 v43, v43\n\t"
        "v_smfmac_f32_32x32x32_bf16 v[64:79], v[104:107], v[32:39], v112\n\tds_read_b128 v[104:107], v114 offset:2048\n\tv_exp_f32 v44, v44\n\t"
        "v_exp_f32 v45, v45\n\tv_exp_f32 v46, v46\n\tv_smfmac_f32_16x16x64_bf16 v[80:83], v[84:87], v[32:39], v112\n\tv_exp_f32 v47, v47\n\t"
        "v_exp_f32 v16, v16\n\tv_smfmac_f32_32x32x32_bf16 v[64:79], v[108:111], v[40:47], v112\n\tds_read_b128 v[108:111], v114 offset:2560\n\t"
        "v_exp_f32 v17, v17\n\tv_exp_f32 v18, v18\n\tv_exp_f32 v19, v19\n\tv_smfmac_f32_16x16x64_bf16 v[80:83], v[84:87], v[40:47], v112\n\t"
        "v_mfma_f32_32x32x16_bf16 v[32:47], v[164:167], v[88:91], v[48:63]\n\tv_exp_f32 v20, v20\n\tv_exp_f32 v21, v21\n\tv_exp_f32 v22, v22\n\t"
        "v_exp_f32 v23, v23\n\tv_mfma_f32_32x32x16_bf16 v[32:47], v[168:171], v[92:95], v[32:47]\n\tv_exp_f32 v24, v24\n\tv_exp_f32 v25, v25\n\t"
        "v_exp_f32 v26, v26\n\tv_exp_f32 v27, v27\n\tv_smfmac_f32_32x32x32_bf16 v[136:151], v[172:175], v[16:23], v112\n\tv_exp_f32 v28, v28\n\t"
        "v_exp_f32 v29, v29\n\tv_exp_f32 v30, v30\n\tv_smfmac_f32_16x16x64_bf16 v[152:155], v[84:87], v[16:23], v112\n\ts_waitcnt lgkmcnt(0)\n\t"
        "v_exp_f32 v31, v31\n\tv_exp_f32 v32, v32\n\tv_smfmac_f32_32x32x32_bf16 v[136:151], v[176:179], v[24:31], v112\n\tv_exp_f32 v33, v33\n\t"
        "v_exp_f32 v34, v34\n\tv_exp_f32 v35, v35\n\tv_smfmac_f32_16x16x64_bf16 v[152:155], v[84:87], v[24:31], v112\n\t"
        "v_mfma_f32_32x32x16_bf16 v[16:31], v[96:99], v[88:91], v[48:63]\n\tv_exp_f32 v36, v36\n\tv_exp_f32 v37, v37\n\tv_exp_f32 v38, v38\n\t"
        "v_exp_f32 v39, v39\n\tv_mfma_f32_32x32x16_bf16 v[16:31], v[100:103], v[92:95], v[16:31]\n\tds_read_b128 v[96:99], v113 offset:4096\n\t"
        "ds_read_b128 v[100:103], v113 offset:4352\n\tv_exp_f32 v40, v40\n\tv_exp_f32 v41, v41\n\tv_exp_f32 v42, v42\n\tv_exp_f32 v43, v43\n\t"
        "v_smfmac_f32_32x32x32_bf16 v[64:79], v[172:175], v[32:39], v112\n\tv_exp_f32 v44, v44\n\tv_exp_f32 v45, v45\n\tv_exp_f32 v46, v46\n\t"
        "v_exp_f32 v47, v47\n\tv_smfmac_f32_16x16x64_bf16 v[80:83], v[84:87], v[32:39], v112\n\ts_nop 0\n\t"
        "v_smfmac_f32_32x32x32_bf16 v[64:79], v[176:179], v[40:47], v112\n\tv_smfmac_f32_16x16x64_bf16 v[80:83], v[84:87], v[40:47], v112\n\t"
        "s_waitcnt lgkmcnt(0)\n\ts_nop 3\n\t.Lmk_a0_e:\n\t"
          : ATTN_Y("=&"), "=&{v[96:111]}"(kv), "+{v[0:3]}"(qa0), "+{v[4:7]}"(qa1), "+{v[136:151]}"(oa), "=&{v[152:155]}"(la), [t] "=&s"(t), [nx] "=&s"(nx)
          : ATTN_IN, "{v[120:135]}"(nega), "{v9}"(nmb)
          : ATTN_CLOB, "vcc", "v115", "v116", "v164", "v165", "v166", "v167", "v168", "v169", "v170", "v171", "v172", "v173", "v174", "v175", "v176", "v177", "v178", "v179");
      STAMP(6);
      LN();
      attn_epilogue(oa, la, qa0, qa1, ws0 - (lbase + L_BUF), kvo, rs, od, n);
    }
    u32x16 r3;
    u32x8 r4, res;
    {
      const unsigned fl = fo;
      unsigned sr = xr;
      sn = os + 0u;
      STAMP(7);
      asm volatile(
        "; BLK a1\n\t.Lmk_a1_s:\n\tv_add_u32 v113, 0x10000, %[k0]\n\tv_add_u32 v114, 0x10000, %[v0]\n\tv_exp_f32 v16, v16\n\tv_exp_f32 v17, v17\n\t"
        "v_exp_f32 v18, v18\n\tv_exp_f32 v19, v19\n\ts_waitcnt lgkmcnt(2)\n\tv_mfma_f32_32x32x16_bf16 v[32:47], v[96:99], v[88:91], v[48:63]\n\t"
        "v_exp_f32 v20, v20\n\tv_exp_f32 v21, v21\n\tv_exp_f32 v22, v22\n\tv_exp_f32 v23, v23\n\t"
        "v_mfma_f32_32x32x16_bf16 v[32:47], v[100:103], v[92:95], v[32:47]\n\tds_read_b128 v[96:99], v113 offset:8192\n\t"
        "ds_read_b128 v[100:103], v113 offset:8448\n\tv_exp_f32 v24, v24\n\tv_exp_f32 v25, v25\n\tv_exp_f32 v26, v26\n\tv_exp_f32 v27, v27\n\t"
        "s_waitcnt lgkmcnt(3)\n\tv_smfmac_f32_32x32x32_bf16 v[64:79], v[104:107], v[16:23], v112\n\tds_read_b128 v[104:107], v114 offset:6144\n\t"
        "v_exp_f32 v28, v28\n\tv_exp_f32 v29, v29\n\tv_exp_f32 v30, v30\n\tv_exp_f32 v31, v31\n\t"
        "v_smfmac_f32_16x16x64_bf16 v[80:83], v[84:87], v[16:23], v112\n\ts_waitcnt lgkmcnt(3)\n\t"
        "v_smfmac_f32_32x32x32_bf16 v[64:79], v[108:111], v[24:31], v112\n\tds_read_b128 v[108:111], v114 offset:6656\n\tv_exp_f32 v32, v32\n\t"
        "v_exp_f32 v33, v33\n\tv_exp_f32 v34, v34\n\tv_exp_f32 v35, v35\n\tv_smfmac_f32_16x16x64_bf16 v[80:83], v[84:87], v[24:31], v112\n\t"
        "s_waitcnt lgkmcnt(2)\n\tv_mfma_f32_32x32x16_bf16 v[16:31], v[96:99], v[88:91], v[48:63]\n\tv_exp_f32 v36, v36\n\tv_exp_f32 v37, v37\n\t"
        "v_exp_f32 v38, v38\n\tv_exp_f32 v39, v39\n\tv_mfma_f32_32x32x16_bf16 v[16:31], v[100:103], v[92:95], v[16:31]\n\t"
        "ds_read_b128 v[96:99], v113 offset:12288\n\tds_read_b128 v[100:103], v113 offset:12544\n\tv_exp_f32 v40, v40\n\tv_exp_f32 v41, v41\n\t"
        "v_exp_f32 v42, v42\n\tv_exp_f32 v43, v43\n\ts_waitcnt lgkmcnt(3)\n\tv_smfmac_f32_32x32x32_bf16 v[64:79], v[104:107], v[32:39], v112\n\t"
        "ds_read_b128 v[104:107], v114 offset:10240\n\tv_exp_f32 v44, v44\n\tv_exp_f32 v45, v45\n\tv_exp_f32 v46, v46\n\tv_exp_f32 v47, v47\n\t"
        "v_smfmac_f32_16x16x64_bf16 v[80:83], v[84:87], v[32:39], v112\n\ts_waitcnt lgkmcnt(3)\n\t"
        "v_smfmac_f32_32x32x32_bf16 v[64:79], v[108:111], v[40:47], v112\n\tds_read_b128 v[108:111], v114 offset:10752\n\tv_exp_f32 v16, v16\n\t"
        "v_exp_f32 v17, v17\n\tv_exp_f32 v18, v18\n\tv_exp_f32 v19, v19\n\tv_smfmac_f32_16x16x64_bf16 v[80:83], v[84:87], v[40:47], v112\n\t"
        "s_waitcnt lgkmcnt(2)\n\tv_mfma_f32_32x32x16_bf16 v[32:47], v[96:99], v[88:91], v[48:63]\n\tv_exp_f32 v20, v20\n\tv_exp_f32 v21, v21\n\t"
        "v_exp_f32 v22, v22\n\tv_exp_f32 v23, v23\n\tv_mfma_f32_32x32x16_bf16 v[32:47], v[100:103], v[92:95], v[32:47]\n\ts_barrier\n\t"
        "ds_read_b128 v[96:99], v113 offset:16384\n\tds_read_b128 v[100:103], v113 offset:16640\n\ts_waitcnt vmcnt(0)\n\t"
        "buffer_store_dwordx2 %[one], off, %[rs], %[fl] sc1\n\tv_exp_f32 v24, v24\n\tv_exp_f32 v25, v25\n\tv_exp_f32 v26, v26\n\tv_exp_f32 v27, v27\n\t"
        "s_waitcnt lgkmcnt(3)\n\tv_smfmac_f32_32x32x32_bf16 v[64:79], v[104:107], v[16:23], v112\n\tds_read_b128 v[104:107], v114 offset:14336\n\t"
        "v_exp_f32 v28, v28\n\tv_exp_f32 v29, v29\n\tv_exp_f32 v30, v30\n\tv_exp_f32 v31, v31\n\t"
        "v_smfmac_f32_16x16x64_bf16 v[80:83], v[84:87], v[16:23], v112\n\ts_waitcnt lgkmcnt(3)\n\t"
        "v_smfmac_f32_32x32x32_bf16 v[64:79], v[108:111], v[24:31], v112\n\tds_read_b128 v[108:111], v114 offset:14848\n\tv_exp_f32 v32, v32\n\t"
        "v_exp_f32 v33, v33\n\tv_exp_f32 v34, v34\n\tv_exp_f32 v35, v35\n\tv_smfmac_f32_16x16x64_bf16 v[80:83], v[84:87], v[24:31], v112\n\t"
        "s_waitcnt lgkmcnt(2)\n\tv_mfma_f32_32x32x16_bf16 v[16:31], v[96:99], v[88:91], v[48:63]\n\tv_exp_f32 v36, v36\n\tv_exp_f32 v37, v37\n\t"
        "v_exp_f32 v38, v38\n\tv_exp_f32 v39, v39\n\tv_mfma_f32_32x32x16_bf16 v[16:31], v[100:103], v[92:95], v[16:31]\n\t"
        "ds_read_b128 v[96:99], v113 offset:20480\n\tds_read_b128 v[100:103], v113 offset:20736\n\tv_exp_f32 v40, v40\n\tv_exp_f32 v41, v41\n\t"
        "v_exp_f32 v42, v42\n\tv_exp_f32 v43, v43\n\ts_waitcnt lgkmcnt(3)\n\tv_smfmac_f32_32x32x32_bf16 v[64:79], v[104:107], v[32:39], v112\n\t"
        "ds_read_b128 v[104:107], v114 offset:18432\n\tv_exp_f32 v44, v44\n\tv_exp_f32 v45, v45\n\tv_exp_f32 v46, v46\n\tv_exp_f32 v47, v47\n\t"
        "v_smfmac_f32_16x16x64_bf16 v[80:83], v[84:87], v[32:39], v112\n\ts_waitcnt lgkmcnt(3)\n\t"
        "v_smfmac_f32_32x32x32_bf16 v[64:79], v[108:111], v[40:47], v112\n\tds_read_b128 v[108:111], v114 offset:18944\n\tv_exp_f32 v16, v16\n\t"
        "v_exp_f32 v17, v17\n\tv_exp_f32 v18, v18\n\tv_exp_f32 v19, v19\n\tv_smfmac_f32_16x16x64_bf16 v[80:83], v[84:87], v[40:47], v112\n\t"
        "s_waitcnt lgkmcnt(2)\n\tv_mfma_f32_32x32x16_bf16 v[32:47], v[96:99], v[88:91], v[48:63]\n\tv_exp_f32 v20, v20\n\tv_exp_f32 v21, v21\n\t"
        "v_exp_f32 v22, v22\n\tv_exp_f32 v23, v23\n\tv_mfma_f32_32x32x16_bf16 v[32:47], v[100:103], v[92:95], v[32:47]\n\t"
        "ds_read_b128 v[96:99], v113 offset:24576\n\tds_read_b128 v[100:103], v113 offset:24832\n\tv_exp_f32 v24, v24\n\tv_exp_f32 v25, v25\n\t"
        "v_exp_f32 v26, v26\n\tv_exp_f32 v27, v27\n\ts_waitcnt lgkmcnt(3)\n\tv_smfmac_f32_32x32x32_bf16 v[64:79], v[104:107], v[16:23], v112\n\t"
        "ds_read_b128 v[104:107], v114 offset:22528\n\tv_exp_f32 v28, v28\n\tv_exp_f32 v29, v29\n\tv_exp_f32 v30, v30\n\tv_exp_f32 v31, v31\n\t"
        "v_smfmac_f32_16x16x64_bf16 v[80:83], v[84:87], v[16:23], v112\n\ts_waitcnt lgkmcnt(3)\n\t"
        "v_smfmac_f32_32x32x32_bf16 v[64:79], v[108:111], v[24:31], v112\n\tds_read_b128 v[108:111], v114 offset:23040\n\tv_exp_f32 v32, v32\n\t"
        "v_exp_f32 v33, v33\n\tv_exp_f32 v34, v34\n\tv_exp_f32 v35, v35\n\tv_smfmac_f32_16x16x64_bf16 v[80:83], v[84:87], v[24:31], v112\n\t"
        "s_waitcnt lgkmcnt(2)\n\tv_mfma_f32_32x32x16_bf16 v[16:31], v[96:99], v[88:91], v[48:63]\n\tv_exp_f32 v36, v36\n\tv_exp_f32 v37, v37\n\t"
        "v_exp_f32 v38, v38\n\tv_exp_f32 v39, v39\n\tv_mfma_f32_32x32x16_bf16 v[16:31], v[100:103], v[92:95], v[16:31]\n\t"
        "ds_read_b128 v[96:99], v113 offset:28672\n\tds_read_b128 v[100:103], v113 offset:28928\n\tv_exp_f32 v40, v40\n\tv_exp_f32 v41, v41\n\t"
        ".p2align 12,,12\n\t.Lpg7:\n\ts_cbranch_execz .Lpgret\n\tv_exp_f32 v42, v42\n\tv_exp_f32 v43, v43\n\ts_waitcnt lgkmcnt(3)\n\t"
        "v_smfmac_f32_32x32x32_bf16 v[64:79], v[104:107], v[32:39], v112\n\tds_read_b128 v[104:107], v114 offset:26624\n\tv_exp_f32 v44, v44\n\t"
        "v_exp_f32 v45, v45\n\tv_exp_f32 v46, v46\n\tv_exp_f32 v47, v47\n\tv_smfmac_f32_16x16x64_bf16 v[80:83], v[84:87], v[32:39], v112\n\t"
        "s_waitcnt lgkmcnt(3)\n\tv_smfmac_f32_32x32x32_bf16 v[64:79], v[108:111], v[40:47], v112\n\tds_read_b128 v[108:111], v114 offset:27136\n\t"
        "v_exp_f32 v16, v16\n\tv_exp_f32 v17, v17\n\tv_exp_f32 v18, v18\n\tv_exp_f32 v19, v19\n\t"
        "v_smfmac_f32_16x16x64_bf16 v[80:83], v[84:87], v[40:47], v112\n\ts_waitcnt lgkmcnt(2)\n\t"
        "v_mfma_f32_32x32x16_bf16 v[32:47], v[96:99], v[88:91], v[48:63]\n\tv_exp_f32 v20, v20\n\tv_exp_f32 v21, v21\n\tv_exp_f32 v22, v22\n\t"
        "v_exp_f32 v23, v23\n\tv_mfma_f32_32x32x16_bf16 v[32:47], v[100:103], v[92:95], v[32:47]\n\ts_barrier\n\tds_read_b128 v[96:99], %[k0] offset:0\n\t"
        "ds_read_b128 v[100:103], %[k0] offset:256\n\tv_exp_f32 v24, v24\n\tv_exp_f32 v25, v25\n\tv_exp_f32 v26, v26\n\tv_exp_f32 v27, v27\n\t"
        "s_waitcnt lgkmcnt(3)\n\tv_smfmac_f32_32x32x32_bf16 v[64:79], v[104:107], v[16:23], v112\n\tds_read_b128 v[104:107], v114 offset:30720\n\t"
        "v_exp_f32 v28, v28\n\tv_exp_f32 v29, v29\n\tv_exp_f32 v30, v30\n\tv_exp_f32 v31, v31\n\t"
        "v_smfmac_f32_16x16x64_bf16 v[80:83], v[84:87], v[16:23], v112\n\ts_waitcnt lgkmcnt(3)\n\t"
        "v_smfmac_f32_32x32x32_bf16 v[64:79], v[108:111], v[24:31], v112\n\tds_read_b128 v[108:111], v114 offset:31232\n\tv_exp_f32 v32, v32\n\t"
        "v_exp_f32 v33, v33\n\tv_exp_f32 v34, v34\n\tv_exp_f32 v35, v35\n\tv_smfmac_f32_16x16x64_bf16 v[80:83], v[84:87], v[24:31], v112\n\t"
        "s_waitcnt lgkmcnt(2)\n\tv_mfma_f32_32x32x16_bf16 v[16:31], v[96:99], v[88:91], v[48:63]\n\tv_exp_f32 v36, v36\n\tv_exp_f32 v37, v37\n\t"
        "v_exp_f32 v38, v38\n\tv_exp_f32 v39, v39\n\tv_mfma_f32_32x32x16_bf16 v[16:31], v[100:103], v[92:95], v[16:31]\n\t"
        "ds_read_b128 v[96:99], %[k0] offset:4096\n\tds_read_b128 v[100:103], %[k0] offset:4352\n\tv_exp_f32 v40, v40\n\tv_exp_f32 v41, v41\n\t"
        "v_exp_f32 v42, v42\n\tv_exp_f32 v43, v43\n\ts_waitcnt lgkmcnt(3)\n\tv_smfmac_f32_32x32x32_bf16 v[64:79], v[104:107], v[32:39], v112\n\t"
        "ds_read_b128 v[104:107], %[v0] offset:2048\n\tv_exp_f32 v44, v44\n\tv_exp_f32 v45, v45\n\tv_exp_f32 v46, v46\n\tv_exp_f32 v47, v47\n\t"
        "v_smfmac_f32_16x16x64_bf16 v[80:83], v[84:87], v[32:39], v112\n\ts_waitcnt lgkmcnt(3)\n\t"
        "v_smfmac_f32_32x32x32_bf16 v[64:79], v[108:111], v[40:47], v112\n\tds_read_b128 v[108:111], %[v0] offset:2560\n\tv_exp_f32 v16, v16\n\t"
        "v_exp_f32 v17, v17\n\tv_exp_f32 v18, v18\n\tv_exp_f32 v19, v19\n\tbuffer_load_dwordx4 v[120:123], off, %[rn], %[sr] sc1\n\t"
        "s_addk_i32 %[sr], 0x2000\n\tv_smfmac_f32_16x16x64_bf16 v[80:83], v[84:87], v[40:47], v112\n\ts_waitcnt lgkmcnt(2)\n\t"
        "v_mfma_f32_32x32x16_bf16 v[32:47], v[96:99], v[88:91], v[48:63]\n\tv_exp_f32 v20, v20\n\tv_exp_f32 v21, v21\n\tv_exp_f32 v22, v22\n\t"
        "v_exp_f32 v23, v23\n\tv_mfma_f32_32x32x16_bf16 v[32:47], v[100:103], v[92:95], v[32:47]\n\tds_read_b128 v[96:99], %[k0] offset:8192\n\t"
        "ds_read_b128 v[100:103], %[k0] offset:8448\n\tbuffer_load_dwordx4 v[124:127], off, %[rn], %[sr] sc1\n\tv_exp_f32 v24, v24\n\tv_exp_f32 v25, v25\n\t"
        "v_exp_f32 v26, v26\n\tv_exp_f32 v27, v27\n\ts_waitcnt lgkmcnt(3)\n\tv_smfmac_f32_32x32x32_bf16 v[64:79], v[104:107], v[16:23], v112\n\t"
        "ds_read_b128 v[104:107], %[v0] offset:6144\n\tv_exp_f32 v28, v28\n\tv_exp_f32 v29, v29\n\tv_exp_f32 v30, v30\n\tv_exp_f32 v31, v31\n\t"
        "v_smfmac_f32_16x16x64_bf16 v[80:83], v[84:87], v[16:23], v112\n\ts_waitcnt lgkmcnt(3)\n\t"
        "v_smfmac_f32_32x32x32_bf16 v[64:79], v[108:111], v[24:31], v112\n\tds_read_b128 v[108:111], %[v0] offset:6656\n\tv_exp_f32 v32, v32\n\t"
        "v_exp_f32 v33, v33\n\tv_exp_f32 v34, v34\n\tv_exp_f32 v35, v35\n\tv_smfmac_f32_16x16x64_bf16 v[80:83], v[84:87], v[24:31], v112\n\t"
        "s_waitcnt lgkmcnt(2)\n\tv_mfma_f32_32x32x16_bf16 v[16:31], v[96:99], v[88:91], v[48:63]\n\tv_exp_f32 v36, v36\n\tv_exp_f32 v37, v37\n\t"
        "v_exp_f32 v38, v38\n\tv_exp_f32 v39, v39\n\tv_mfma_f32_32x32x16_bf16 v[16:31], v[100:103], v[92:95], v[16:31]\n\t"
        "ds_read_b128 v[96:99], %[k0] offset:12288\n\tds_read_b128 v[100:103], %[k0] offset:12544\n\tv_exp_f32 v40, v40\n\tv_exp_f32 v41, v41\n\t"
        "v_exp_f32 v42, v42\n\tv_exp_f32 v43, v43\n\ts_waitcnt lgkmcnt(3)\n\tv_smfmac_f32_32x32x32_bf16 v[64:79], v[104:107], v[32:39], v112\n\t"
        "ds_read_b128 v[104:107], %[v0] offset:10240\n\tv_exp_f32 v44, v44\n\tv_exp_f32 v45, v45\n\tv_exp_f32 v46, v46\n\tv_exp_f32 v47, v47\n\t"
        "v_smfmac_f32_16x16x64_bf16 v[80:83], v[84:87], v[32:39], v112\n\ts_waitcnt lgkmcnt(3)\n\t"
        "v_smfmac_f32_32x32x32_bf16 v[64:79], v[108:111], v[40:47], v112\n\tds_read_b128 v[108:111], %[v0] offset:10752\n\tv_exp_f32 v16, v16\n\t"
        "v_exp_f32 v17, v17\n\tv_exp_f32 v18, v18\n\tv_exp_f32 v19, v19\n\tv_smfmac_f32_16x16x64_bf16 v[80:83], v[84:87], v[40:47], v112\n\t"
        "s_waitcnt lgkmcnt(2)\n\tv_mfma_f32_32x32x16_bf16 v[32:47], v[96:99], v[88:91], v[48:63]\n\tv_exp_f32 v20, v20\n\tv_exp_f32 v21, v21\n\t"
        "v_exp_f32 v22, v22\n\tv_exp_f32 v23, v23\n\tv_mfma_f32_32x32x16_bf16 v[32:47], v[100:103], v[92:95], v[32:47]\n\ts_barrier\n\t"
        "ds_read_b128 v[96:99], %[k0] offset:16384\n\tds_read_b128 v[100:103], %[k0] offset:16640\n\t"
        "buffer_load_dwordx4 v[128:131], off, %[rn], %[sn] offset:0 sc1\n\ts_add_u32 %[sn], %[sn], 0x2000\n\tv_exp_f32 v24, v24\n\tv_exp_f32 v25, v25\n\t"
        "v_exp_f32 v26, v26\n\tv_exp_f32 v27, v27\n\ts_waitcnt lgkmcnt(3)\n\tv_smfmac_f32_32x32x32_bf16 v[64:79], v[104:107], v[16:23], v112\n\t"
        "ds_read_b128 v[104:107], %[v0] offset:14336\n\tbuffer_load_dwordx4 v[132:135], off, %[rn], %[sn] offset:0 sc1\n\ts_add_u32 %[sn], %[sn], 0x2000\n\t"
        "v_exp_f32 v28, v28\n\tv_exp_f32 v29, v29\n\tv_exp_f32 v30, v30\n\tv_exp_f32 v31, v31\n\t"
        "buffer_load_dwordx4 v[136:139], off, %[rn], %[sn] offset:0 sc1\n\ts_add_u32 %[sn], %[sn], 0x2000\n\t"
        "v_smfmac_f32_16x16x64_bf16 v[80:83], v[84:87], v[16:23], v112\n\ts_waitcnt lgkmcnt(3)\n\t"
        "v_smfmac_f32_32x32x32_bf16 v[64:79], v[108:111], v[24:31], v112\n\tds_read_b128 v[108:111], %[v0] offset:14848\n\tv_exp_f32 v32, v32\n\t"
        "v_exp_f32 v33, v33\n\tv_exp_f32 v34, v34\n\tv_exp_f32 v35, v35\n\tbuffer_load_dwordx4 v[140:143], off, %[rn], %[sn] offset:0 sc1\n\t"
        "s_add_u32 %[sn], %[sn], 0x2000\n\tv_smfmac_f32_16x16x64_bf16 v[80:83], v[84:87], v[24:31], v112\n\ts_waitcnt lgkmcnt(2)\n\t"
        "v_mfma_f32_32x32x16_bf16 v[16:31], v[96:99], v[88:91], v[48:63]\n\tv_exp_f32 v36, v36\n\tv_exp_f32 v37, v37\n\tv_exp_f32 v38, v38\n\t"
        "v_exp_f32 v39, v39\n\tv_mfma_f32_32x32x16_bf16 v[16:31], v[100:103], v[92:95], v[16:31]\n\tds_read_b128 v[96:99], %[k0] offset:20480\n\t"
        "ds_read_b128 v[100:103], %[k0] offset:20736\n\tbuffer_load_dwordx4 v[144:147], off, %[rn], %[sn] offset:0 sc1\n\ts_add_u32 %[sn], %[sn], 0x2000\n\t"
        "v_exp_f32 v40, v40\n\tv_exp_f32 v41, v41\n\tv_exp_f32 v42, v42\n\tv_exp_f32 v43, v43\n\ts_waitcnt lgkmcnt(3)\n\t"
        "v_smfmac_f32_32x32x32_bf16 v[64:79], v[104:107], v[32:39], v112\n\tds_read_b128 v[104:107], %[v0] offset:18432\n\t"
        "buffer_load_dwordx4 v[148:151], off, %[rn], %[sn] offset:0 sc1\n\ts_add_u32 %[sn], %[sn], 0x2000\n\tv_exp_f32 v44, v44\n\tv_exp_f32 v45, v45\n\t"
        "v_exp_f32 v46, v46\n\tv_exp_f32 v47, v47\n\tbuffer_load_dwordx4 v[152:155], off, %[rn], %[sn] offset:0 sc1\n\ts_add_u32 %[sn], %[sn], 0x2000\n\t"
        "v_smfmac_f32_16x16x64_bf16 v[80:83], v[84:87], v[32:39], v112\n\ts_waitcnt lgkmcnt(3)\n\t"
        "v_smfmac_f32_32x32x32_bf16 v[64:79], v[108:111], v[40:47], v112\n\tds_read_b128 v[108:111], %[v0] offset:18944\n\tv_exp_f32 v16, v16\n\t"
        "v_exp_f32 v17, v17\n\tv_exp_f32 v18, v18\n\tv_exp_f32 v19, v19\n\tbuffer_load_dwordx4 v[156:159], off, %[rn], %[sn] offset:0 sc1\n\t"
        "s_add_u32 %[sn], %[sn], 0x2000\n\tv_smfmac_f32_16x16x64_bf16 v[80:83], v[84:87], v[40:47], v112\n\ts_waitcnt lgkmcnt(2)\n\t"
        "v_mfma_f32_32x32x16_bf16 v[32:47], v[96:99], v[88:91], v[48:63]\n\tv_exp_f32 v20, v20\n\tv_exp_f32 v21, v21\n\tv_exp_f32 v22, v22\n\t"
        "v_exp_f32 v23, v23\n\tv_mfma_f32_32x32x16_bf16 v[32:47], v[100:103], v[92:95], v[32:47]\n\tds_read_b128 v[96:99], %[k0] offset:24576\n\t"
        "ds_read_b128 v[100:103], %[k0] offset:24832\n\tbuffer_load_dwordx4 v[160:163], off, %[rn], %[sn] offset:0 sc1\n\ts_add_u32 %[sn], %[sn], 0x2000\n\t"
        "v_exp_f32 v24, v24\n\tv_exp_f32 v25, v25\n\tv_exp_f32 v26, v26\n\tv_exp_f32 v27, v27\n\ts_waitcnt lgkmcnt(3)\n\t"
        "v_smfmac_f32_32x32x32_bf16 v[64:79], v[104:107], v[16:23], v112\n\tds_read_b128 v[104:107], %[v0] offset:22528\n\t"
        "buffer_load_dwordx4 v[164:167], off, %[rn], %[sn] offset:0 sc1\n\ts_add_u32 %[sn], %[sn], 0x2000\n\tv_exp_f32 v28, v28\n\tv_exp_f32 v29, v29\n\t"
        "v_exp_f32 v30, v30\n\tv_exp_f32 v31, v31\n\tbuffer_load_dwordx4 v[168:171], off, %[rn], %[sn] offset:0 sc1\n\ts_add_u32 %[sn], %[sn], 0x2000\n\t"
        "v_smfmac_f32_16x16x64_bf16 v[80:83], v[84:87], v[16:23], v112\n\ts_waitcnt lgkmcnt(3)\n\t"
        "v_smfmac_f32_32x32x32_bf16 v[64:79], v[108:111], v[24:31], v112\n\tds_read_b128 v[108:111], %[v0] offset:23040\n\tv_exp_f32 v32, v32\n\t"
        "v_exp_f32 v33, v33\n\tv_exp_f32 v34, v34\n\tv_exp_f32 v35, v35\n\tbuffer_load_dwordx4 v[172:175], off, %[rn], %[sn] offset:0 sc1\n\t"
        "s_add_u32 %[sn], %[sn], 0x2000\n\tv_smfmac_f32_16x16x64_bf16 v[80:83], v[84:87], v[24:31], v112\n\ts_waitcnt lgkmcnt(2)\n\t"
        "v_mfma_f32_32x32x16_bf16 v[16:31], v[96:99], v[88:91], v[48:63]\n\tv_exp_f32 v36, v36\n\tv_exp_f32 v37, v37\n\tv_exp_f32 v38, v38\n\t"
        "v_exp_f32 v39, v39\n\tv_mfma_f32_32x32x16_bf16 v[16:31], v[100:103], v[92:95], v[16:31]\n\tds_read_b128 v[96:99], %[k0] offset:28672\n\t"
        "ds_read_b128 v[100:103], %[k0] offset:28928\n\tbuffer_load_dwordx4 v[176:179], off, %[rn], %[sn] offset:0 sc1\n\ts_add_u32 %[sn], %[sn], 0x2000\n\t"
        "v_exp_f32 v40, v40\n\tv_exp_f32 v41, v41\n\tv_exp_f32 v42, v42\n\tv_exp_f32 v43, v43\n\ts_waitcnt lgkmcnt(3)\n\t"
        "v_smfmac_f32_32x32x32_bf16 v[64:79], v[104:107], v[32:39], v112\n\tds_read_b128 v[104:107], %[v0] offset:26624\n\t"
        "buffer_load_dwordx4 v[180:183], off, %[rn], %[sn] offset:0 sc1\n\ts_add_u32 %[sn], %[sn], 0x2000\n\tv_exp_f32 v44, v44\n\tv_exp_f32 v45, v45\n\t"
        "v_exp_f32 v46, v46\n\tv_exp_f32 v47, v47\n\tbuffer_load_dwordx4 v[184:187], off, %[rn], %[sn] offset:0 sc1\n\ts_add_u32 %[sn], %[sn], 0x2000\n\t"
        "v_smfmac_f32_16x16x64_bf16 v[80:83], v[84:87], v[32:39], v112\n\ts_waitcnt lgkmcnt(3)\n\t"
        "v_smfmac_f32_32x32x32_bf16 v[64:79], v[108:111], v[40:47], v112\n\tds_read_b128 v[108:111], %[v0] offset:27136\n\tv_exp_f32 v16, v16\n\t"
        "v_exp_f32 v17, v17\n\tv_exp_f32 v18, v18\n\tv_exp_f32 v19, v19\n\tbuffer_load_dwordx4 v[188:191], off, %[rn], %[sn] offset:0 sc1\n\t"
        "s_add_u32 %[sn], %[sn], 0x2000\n\tv_smfmac_f32_16x16x64_bf16 v[80:83], v[84:87], v[40:47], v112\n\ts_waitcnt lgkmcnt(2)\n\t"
        "v_mfma_f32_32x32x16_bf16 v[32:47], v[96:99], v[88:91], v[48:63]\n\tv_exp_f32 v20, v20\n\tv_exp_f32 v21, v21\n\tv_exp_f32 v22, v22\n\t"
        "v_exp_f32 v23, v23\n\tv_mfma_f32_32x32x16_bf16 v[32:47], v[100:103], v[92:95], v[32:47]\n\ts_barrier\n\t"
        "buffer_load_dwordx4 v[192:195], off, %[rn], %[sn] offset:0 sc1\n\ts_add_u32 %[sn], %[sn], 0x2000\n\tv_exp_f32 v24, v24\n\tv_exp_f32 v25, v25\n\t"
        "v_exp_f32 v26, v26\n\tv_exp_f32 v27, v27\n\ts_waitcnt lgkmcnt(1)\n\tv_smfmac_f32_32x32x32_bf16 v[64:79], v[104:107], v[16:23], v112\n\t"
        "ds_read_b128 v[104:107], %[v0] offset:30720\n\tbuffer_load_dwordx4 v[196:199], off, %[rn], %[sn] offset:0 sc1\n\ts_add_u32 %[sn], %[sn], 0x2000\n\t"
        "v_exp_f32 v28, v28\n\tv_exp_f32 v29, v29\n\tv_exp_f32 v30, v30\n\tv_exp_f32 v31, v31\n\t"
        "buffer_load_dwordx4 v[200:203], off, %[rn], %[sn] offset:0 sc1\n\ts_add_u32 %[sn], %[sn], 0x2000\n\t"
        "v_smfmac_f32_16x16x64_bf16 v[80:83], v[84:87], v[16:23], v112\n\ts_waitcnt lgkmcnt(1)\n\t"
        "v_smfmac_f32_32x32x32_bf16 v[64:79], v[108:111], v[24:31], v112\n\tds_read_b128 v[108:111], %[v0] offset:31232\n\tv_exp_f32 v32, v32\n\t"
        "v_exp_f32 v33, v33\n\tv_exp_f32 v34, v34\n\tv_exp_f32 v35, v35\n\tbuffer_load_dwordx4 v[204:207], off, %[rn], %[sn] offset:0 sc1\n\t"
        "s_add_u32 %[sn], %[sn], 0x2000\n\tv_smfmac_f32_16x16x64_bf16 v[80:83], v[84:87], v[24:31], v112\n\tv_exp_f32 v36, v36\n\tv_exp_f32 v37, v37\n\t"
        "v_exp_f32 v38, v38\n\tv_exp_f32 v39, v39\n\tbuffer_load_dwordx4 v[208:211], off, %[rn], %[sn] offset:0 sc1\n\ts_add_u32 %[sn], %[sn], 0x2000\n\t"
        "v_exp_f32 v40, v40\n\tv_exp_f32 v41, v41\n\tv_exp_f32 v42, v42\n\tv_exp_f32 v43, v43\n\ts_waitcnt lgkmcnt(1)\n\t"
        "v_smfmac_f32_32x32x32_bf16 v[64:79], v[104:107], v[32:39], v112\n\tbuffer_load_dwordx4 v[212:215], off, %[rn], %[sn] offset:0 sc1\n\t"
        "s_add_u32 %[sn], %[sn], 0x2000\n\tv_exp_f32 v44, v44\n\tv_exp_f32 v45, v45\n\tv_exp_f32 v46, v46\n\tv_exp_f32 v47, v47\n\t"
        "buffer_load_dwordx4 v[216:219], off, %[rn], %[sn] offset:0 sc1\n\ts_add_u32 %[sn], %[sn], 0x2000\n\t"
        "v_smfmac_f32_16x16x64_bf16 v[80:83], v[84:87], v[32:39], v112\n\ts_waitcnt lgkmcnt(0)\n\t"
        "v_smfmac_f32_32x32x32_bf16 v[64:79], v[108:111], v[40:47], v112\n\tv_smfmac_f32_16x16x64_bf16 v[80:83], v[84:87], v[40:47], v112\n\t"
        "s_waitcnt lgkmcnt(0)\n\ts_nop 9\n\t.Lmk_a1_e:\n\t"
          : ATTN_Y("+"), "+{v[96:111]}"(kv), "=&{v[152:183]}"(r0), "=&{v[184:215]}"(r1), "=&{v[216:247]}"(r2), "=&{v[128:143]}"(r3), "=&{v[144:151]}"(r4), "=&{v[120:127]}"(res), [sn] "+s"(sn), [sr] "+s"(sr)
          : ATTN_IN, [rn] "s"(rt4) : ATTN_CLOB, "vcc");
      STAMP(8);
      LN();
      attn_epilogue(ob, lb, qb0, qb1, ws0 - (lbase + L_BUF), kvo, rs, od + 2 * MB, n);
      STAMP(9);
    }
    {
      f32x16 acc[1];
      const unsigned fl = fo + 8192;
      sa = sn; sn = os + 2 * MB;
      sn += 0u;
      asm volatile(
        "; BLK wo0\n\t.Lmk_wo0_s:\n\tds_read_b128 v[40:43], %[pb0] offset:0\n\tds_read_b128 v[44:47], %[pb0] offset:1024\n\t"
        "ds_read_b128 v[48:51], %[pb0] offset:2048\n\tds_read_b128 v[52:55], %[pb0] offset:3072\n\tds_read_b128 v[56:59], %[pb0] offset:4096\n\t"
        "ds_read_b128 v[60:63], %[pb0] offset:5120\n\tds_read_b128 v[64:67], %[pb0] offset:6144\n\ts_waitcnt vmcnt(24) lgkmcnt(3)\n\t"
        "v_mfma_f32_16x16x32_bf16 v[16:19], v[40:43], v[128:131], 0\n\tds_read_b128 v[68:71], %[pb0] offset:7168\n\t"
        "v_mfma_f32_16x16x32_bf16 v[20:23], v[44:47], v[128:131], 0\n\tds_read_b128 v[40:43], %[pb0] offset:8192\n\t"
        "v_mfma_f32_16x16x32_bf16 v[24:27], v[48:51], v[128:131], 0\n\tds_read_b128 v[44:47], %[pb0] offset:9216\n\t"
        "buffer_load_dwordx4 v[220:223], off, %[rt], %[sa] offset:0 sc1\n\ts_add_u32 %[sa], %[sa], 0x2000\n\t"
        "v_mfma_f32_16x16x32_bf16 v[28:31], v[52:55], v[128:131], 0\n\tds_read_b128 v[48:51], %[pb0] offset:10240\n\ts_waitcnt vmcnt(24) lgkmcnt(3)\n\t"
        "v_mfma_f32_16x16x32_bf16 v[16:19], v[56:59], v[132:135], v[16:19]\n\tds_read_b128 v[52:55], %[pb0] offset:11264\n\t"
        "v_mfma_f32_16x16x32_bf16 v[20:23], v[60:63], v[132:135], v[20:23]\n\tds_read_b128 v[56:59], %[pb0] offset:12288\n\t"
        "buffer_load_dwordx4 v[224:227], off, %[rt], %[sa] offset:0 sc1\n\ts_add_u32 %[sa], %[sa], 0x2000\n\t"
        "v_mfma_f32_16x16x32_bf16 v[24:27], v[64:67], v[132:135], v[24:27]\n\tds_read_b128 v[60:63], %[pb0] offset:13312\n\t"
        "v_mfma_f32_16x16x32_bf16 v[28:31], v[68:71], v[132:135], v[28:31]\n\tds_read_b128 v[64:67], %[pb0] offset:14336\n\ts_waitcnt vmcnt(24) lgkmcnt(3)\n\t"
        "v_mfma_f32_16x16x32_bf16 v[16:19], v[40:43], v[136:139], v[16:19]\n\tds_read_b128 v[68:71], %[pb0] offset:15360\n\t"
        "buffer_load_dwordx4 v[228:231], off, %[rt], %[sa] offset:0 sc1\n\ts_add_u32 %[sa], %[sa], 0x2000\n\t"
        "v_mfma_f32_16x16x32_bf16 v[20:23], v[44:47], v[136:139], v[20:23]\n\tds_read_b128 v[40:43], %[pb0] offset:16384\n\t"
        "v_mfma_f32_16x16x32_bf16 v[24:27], v[48:51], v[136:139], v[24:27]\n\tds_read_b128 v[44:47], %[pb0] offset:17408\n\t"
        "v_mfma_f32_16x16x32_bf16 v[28:31], v[52:55], v[136:139], v[28:31]\n\tds_read_b128 v[48:51], %[pb0] offset:18432\n\t"
        "buffer_load_dwordx4 v[232:235], off, %[rt], %[sa] offset:0 sc1\n\ts_add_u32 %[sa], %[sa], 0x2000\n\ts_waitcnt vmcnt(25) lgkmcnt(3)\n\t"
        "v_mfma_f32_16x16x32_bf16 v[16:19], v[56:59], v[140:143], v[16:19]\n\tds_read_b128 v[52:55], %[pb0] offset:19456\n\t"
        "v_mfma_f32_16x16x32_bf16 v[20:23], v[60:63], v[140:143], v[20:23]\n\tds_read_b128 v[56:59], %[pb0] offset:20480\n\t"
        "v_mfma_f32_16x16x32_bf16 v[24:27], v[64:67], v[140:143], v[24:27]\n\tds_read_b128 v[60:63], %[pb0] offset:21504\n\t"
        "buffer_load_dwordx4 v[236:239], off, %[rt], %[sa] offset:0 sc1\n\ts_add_u32 %[sa], %[sa], 0x2000\n\t"
        "v_mfma_f32_16x16x32_bf16 v[28:31], v[68:71], v[140:143], v[28:31]\n\tds_read_b128 v[64:67], %[pb0] offset:22528\n\ts_waitcnt vmcnt(25) lgkmcnt(3)\n\t"
        "v_mfma_f32_16x16x32_bf16 v[16:19], v[40:43], v[144:147], v[16:19]\n\tds_read_b128 v[68:71], %[pb0] offset:23552\n\t"
        "v_mfma_f32_16x16x32_bf16 v[20:23], v[44:47], v[144:147], v[20:23]\n\tds_read_b128 v[40:43], %[pb0] offset:24576\n\t"
        "buffer_load_dwordx4 v[240:243], off, %[rt], %[sa] offset:0 sc1\n\ts_add_u32 %[sa], %[sa], 0x2000\n\t"
        "v_mfma_f32_16x16x32_bf16 v[24:27], v[48:51], v[144:147], v[24:27]\n\tds_read_b128 v[44:47], %[pb0] offset:25600\n\t"
        "v_mfma_f32_16x16x32_bf16 v[28:31], v[52:55], v[144:147], v[28:31]\n\tds_read_b128 v[48:51], %[pb0] offset:26624\n\ts_waitcnt vmcnt(25) lgkmcnt(3)\n\t"
        "v_mfma_f32_16x16x32_bf16 v[16:19], v[56:59], v[148:151], v[16:19]\n\tds_read_b128 v[52:55], %[pb0] offset:27648\n\t"
        "buffer_load_dwordx4 v[244:247], off, %[rt], %[sa] offset:0 sc1\n\ts_add_u32 %[sa], %[sa], 0x2000\n\t"
        "v_mfma_f32_16x16x32_bf16 v[20:23], v[60:63], v[148:151], v[20:23]\n\tds_read_b128 v[56:59], %[pb0] offset:28672\n\t"
        "v_mfma_f32_16x16x32_bf16 v[24:27], v[64:67], v[148:151], v[24:27]\n\tds_read_b128 v[60:63], %[pb0] offset:29696\n\t"
        "v_mfma_f32_16x16x32_bf16 v[28:31], v[68:71], v[148:151], v[28:31]\n\tds_read_b128 v[64:67], %[pb0] offset:30720\n\t"
        "buffer_load_dwordx4 v[128:131], off, %[rt], %[sa] offset:0 sc1\n\ts_add_u32 %[sa], %[sa], 0x2000\n\ts_waitcnt vmcnt(26) lgkmcnt(3)\n\t"
        "v_mfma_f32_16x16x32_bf16 v[16:19], v[40:43], v[152:155], v[16:19]\n\tds_read_b128 v[68:71], %[pb0] offset:31744\n\t"
        "v_mfma_f32_16x16x32_bf16 v[20:23], v[44:47], v[152:155], v[20:23]\n\tds_read_b128 v[40:43], %[pb0] offset:32768\n\t"
        "v_mfma_f32_16x16x32_bf16 v[24:27], v[48:51], v[152:155], v[24:27]\n\tds_read_b128 v[44:47], %[pb0] offset:33792\n\t"
        "buffer_load_dwordx4 v[132:135], off, %[rt], %[sa] offset:0 sc1\n\tv_mfma_f32_16x16x32_bf16 v[28:31], v[52:55], v[152:155], v[28:31]\n\t"
        "ds_read_b128 v[48:51], %[pb0] offset:34816\n\ts_waitcnt vmcnt(26) lgkmcnt(3)\n\tv_mfma_f32_16x16x32_bf16 v[16:19], v[56:59], v[156:159], v[16:19]\n\t"
        "ds_read_b128 v[52:55], %[pb0] offset:35840\n\tv_mfma_f32_16x16x32_bf16 v[20:23], v[60:63], v[156:159], v[20:23]\n\t"
        "ds_read_b128 v[56:59], %[pb0] offset:36864\n\tv_mfma_f32_16x16x32_bf16 v[24:27], v[64:67], v[156:159], v[24:27]\n\t"
        "ds_read_b128 v[60:63], %[pb0] offset:37888\n\tv_mfma_f32_16x16x32_bf16 v[28:31], v[68:71], v[156:159], v[28:31]\n\t"
        "ds_read_b128 v[64:67], %[pb0] offset:38912\n\ts_waitcnt vmcnt(25) lgkmcnt(3)\n\tv_mfma_f32_16x16x32_bf16 v[16:19], v[40:43], v[160:163], v[16:19]\n\t"
        "ds_read_b128 v[68:71], %[pb0] offset:39936\n\ts_waitcnt vmcnt(9)\n\tbuffer_store_dwordx2 %[one], off, %[rs], %[fl] sc1\n\t"
        "v_mfma_f32_16x16x32_bf16 v[20:23], v[44:47], v[160:163], v[20:23]\n\tds_read_b128 v[40:43], %[pb0] offset:40960\n\t"
        "v_mfma_f32_16x16x32_bf16 v[24:27], v[48:51], v[160:163], v[24:27]\n\tds_read_b128 v[44:47], %[pb0] offset:41984\n\t"
        "v_mfma_f32_16x16x32_bf16 v[28:31], v[52:55], v[160:163], v[28:31]\n\tds_read_b128 v[48:51], %[pb0] offset:43008\n\ts_waitcnt vmcnt(25) lgkmcnt(3)\n\t"
        "v_mfma_f32_16x16x32_bf16 v[16:19], v[56:59], v[164:167], v[16:19]\n\tds_read_b128 v[52:55], %[pb0] offset:44032\n\t"
        "v_mfma_f32_16x16x32_bf16 v[20:23], v[60:63], v[164:167], v[20:23]\n\tds_read_b128 v[56:59], %[pb0] offset:45056\n\t"
        "v_mfma_f32_16x16x32_bf16 v[24:27], v[64:67], v[164:167], v[24:27]\n\tds_read_b128 v[60:63], %[pb0] offset:46080\n\t"
        "v_mfma_f32_16x16x32_bf16 v[28:31], v[68:71], v[164:167], v[28:31]\n\tds_read_b128 v[64:67], %[pb0] offset:47104\n\ts_waitcnt vmcnt(24) lgkmcnt(3)\n\t"
        "v_mfma_f32_16x16x32_bf16 v[16:19], v[40:43], v[168:171], v[16:19]\n\tds_read_b128 v[68:71], %[pb0] offset:48128\n\t"
        "v_mfma_f32_16x16x32_bf16 v[20:23], v[44:47], v[168:171], v[20:23]\n\tds_read_b128 v[40:43], %[pb2] offset:16384\n\t"
        "v_mfma_f32_16x16x32_bf16 v[24:27], v[48:51], v[168:171], v[24:27]\n\tds_read_b128 v[44:47], %[pb2] offset:17408\n\t"
        "v_mfma_f32_16x16x32_bf16 v[28:31], v[52:55], v[168:171], v[28:31]\n\tds_read_b128 v[48:51], %[pb2] offset:18432\n\ts_waitcnt vmcnt(23) lgkmcnt(3)\n\t"
        "v_mfma_f32_16x16x32_bf16 v[16:19], v[56:59], v[172:175], v[16:19]\n\tds_read_b128 v[52:55], %[pb2] offset:19456\n\t"
        "v_mfma_f32_16x16x32_bf16 v[20:23], v[60:63], v[172:175], v[20:23]\n\tds_read_b128 v[56:59], %[pb2] offset:20480\n\t"
        "v_mfma_f32_16x16x32_bf16 v[24:27], v[64:67], v[172:175], v[24:27]\n\tds_read_b128 v[60:63], %[pb2] offset:21504\n\t"
        "v_mfma_f32_16x16x32_bf16 v[28:31], v[68:71], v[172:175], v[28:31]\n\tds_read_b128 v[64:67], %[pb2] offset:22528\n\ts_waitcnt vmcnt(22) lgkmcnt(3)\n\t"
        "v_mfma_f32_16x16x32_bf16 v[16:19], v[40:43], v[176:179], v[16:19]\n\tds_read_b128 v[68:71], %[pb2] offset:23552\n\t"
        "v_mfma_f32_16x16x32_bf16 v[20:23], v[44:47], v[176:179], v[20:23]\n\tds_read_b128 v[40:43], %[pb2] offset:24576\n\t"
        "v_mfma_f32_16x16x32_bf16 v[24:27], v[48:51], v[176:179], v[24:27]\n\tds_read_b128 v[44:47], %[pb2] offset:25600\n\t"
        "v_mfma_f32_16x16x32_bf16 v[28:31], v[52:55], v[176:179], v[28:31]\n\tds_read_b128 v[48:51], %[pb2] offset:26624\n\ts_waitcnt vmcnt(21) lgkmcnt(3)\n\t"
        "v_mfma_f32_16x16x32_bf16 v[16:19], v[56:59], v[180:183], v[16:19]\n\tds_read_b128 v[52:55], %[pb2] offset:27648\n\t"
        "v_mfma_f32_16x16x32_bf16 v[20:23], v[60:63], v[180:183], v[20:23]\n\tds_read_b128 v[56:59], %[pb2] offset:28672\n\t"
        "v_mfma_f32_16x16x32_bf16 v[24:27], v[64:67], v[180:183], v[24:27]\n\tds_read_b128 v[60:63], %[pb2] offset:29696\n\t"
        "v_mfma_f32_16x16x32_bf16 v[28:31], v[68:71], v[180:183], v[28:31]\n\tds_read_b128 v[64:67], %[pb2] offset:30720\n\ts_waitcnt vmcnt(20) lgkmcnt(3)\n\t"
        "v_mfma_f32_16x16x32_bf16 v[16:19], v[40:43], v[184:187], v[16:19]\n\tds_read_b128 v[68:71], %[pb2] offset:31744\n\t"
        "v_mfma_f32_16x16x32_bf16 v[20:23], v[44:47], v[184:187], v[20:23]\n\ts_barrier\n\tds_read_b128 v[40:43], %[pb1] offset:16384\n\t"
        "v_mfma_f32_16x16x32_bf16 v[24:27], v[48:51], v[184:187], v[24:27]\n\tds_read_b128 v[44:47], %[pb1] offset:17408\n\t"
        "v_mfma_f32_16x16x32_bf16 v[28:31], v[52:55], v[184:187], v[28:31]\n\tds_read_b128 v[48:51], %[pb1] offset:18432\n\ts_waitcnt vmcnt(19) lgkmcnt(3)\n\t"
        "v_mfma_f32_16x16x32_bf16 v[16:19], v[56:59], v[188:191], v[16:19]\n\tds_read_b128 v[52:55], %[pb1] offset:19456\n\t"
        "v_mfma_f32_16x16x32_bf16 v[20:23], v[60:63], v[188:191], v[20:23]\n\tds_read_b128 v[56:59], %[pb1] offset:20480\n\t"
        "v_mfma_f32_16x16x32_bf16 v[24:27], v[64:67], v[188:191], v[24:27]\n\tds_read_b128 v[60:63], %[pb1] offset:21504\n\t"
        "v_mfma_f32_16x16x32_bf16 v[28:31], v[68:71], v[188:191], v[28:31]\n\tds_read_b128 v[64:67], %[pb1] offset:22528\n\ts_waitcnt vmcnt(18) lgkmcnt(3)\n\t"
        "v_mfma_f32_16x16x32_bf16 v[16:19], v[40:43], v[192:195], v[16:19]\n\tds_read_b128 v[68:71], %[pb1] offset:23552\n\t"
        "v_mfma_f32_16x16x32_bf16 v[20:23], v[44:47], v[192:195], v[20:23]\n\tds_read_b128 v[40:43], %[pb1] offset:24576\n\t"
        "v_mfma_f32_16x16x32_bf16 v[24:27], v[48:51], v[192:195], v[24:27]\n\tds_read_b128 v[44:47], %[pb1] offset:25600\n\t"
        "v_mfma_f32_16x16x32_bf16 v[28:31], v[52:55], v[192:195], v[28:31]\n\tds_read_b128 v[48:51], %[pb1] offset:26624\n\ts_waitcnt vmcnt(17) lgkmcnt(3)\n\t"
        "v_mfma_f32_16x16x32_bf16 v[16:19], v[56:59], v[196:199], v[16:19]\n\tds_read_b128 v[52:55], %[pb1] offset:27648\n\t"
        "v_mfma_f32_16x16x32_bf16 v[20:23], v[60:63], v[196:199], v[20:23]\n\tds_read_b128 v[56:59], %[pb1] offset:28672\n\t"
        "v_mfma_f32_16x16x32_bf16 v[24:27], v[64:67], v[196:199], v[24:27]\n\tds_read_b128 v[60:63], %[pb1] offset:29696\n\t"
        "v_mfma_f32_16x16x32_bf16 v[28:31], v[68:71], v[196:199], v[28:31]\n\tds_read_b128 v[64:67], %[pb1] offset:30720\n\ts_waitcnt vmcnt(16) lgkmcnt(3)\n\t"
        "v_mfma_f32_16x16x32_bf16 v[16:19], v[40:43], v[200:203], v[16:19]\n\tds_read_b128 v[68:71], %[pb1] offset:31744\n\t"
        "v_mfma_f32_16x16x32_bf16 v[20:23], v[44:47], v[200:203], v[20:23]\n\tds_read_b128 v[40:43], %[pb1] offset:32768\n\t"
        "v_mfma_f32_16x16x32_bf16 v[24:27], v[48:51], v[200:203], v[24:27]\n\tds_read_b128 v[44:47], %[pb1] offset:33792\n\t"
        "v_mfma_f32_16x16x32_bf16 v[28:31], v[52:55], v[200:203], v[28:31]\n\tds_read_b128 v[48:51], %[pb1] offset:34816\n\ts_waitcnt vmcnt(15) lgkmcnt(3)\n\t"
        "v_mfma_f32_16x16x32_bf16 v[16:19], v[56:59], v[204:207], v[16:19]\n\tds_read_b128 v[52:55], %[pb1] offset:35840\n\t"
        "v_mfma_f32_16x16x32_bf16 v[20:23], v[60:63], v[204:207], v[20:23]\n\tds_read_b128 v[56:59], %[pb1] offset:36864\n\t"
        "v_mfma_f32_16x16x32_bf16 v[24:27], v[64:67], v[204:207], v[24:27]\n\tds_read_b128 v[60:63], %[pb1] offset:37888\n\t"
        "v_mfma_f32_16x16x32_bf16 v[28:31], v[68:71], v[204:207], v[28:31]\n\tds_read_b128 v[64:67], %[pb1] offset:38912\n\ts_waitcnt vmcnt(14) lgkmcnt(3)\n\t"
        "v_mfma_f32_16x16x32_bf16 v[16:19], v[40:43], v[208:211], v[16:19]\n\tds_read_b128 v[68:71], %[pb1] offset:39936\n\t"
        "v_mfma_f32_16x16x32_bf16 v[20:23], v[44:47], v[208:211], v[20:23]\n\tds_read_b128 v[40:43], %[pb1] offset:40960\n\t"
        "v_mfma_f32_16x16x32_bf16 v[24:27], v[48:51], v[208:211], v[24:27]\n\tds_read_b128 v[44:47], %[pb1] offset:41984\n\t"
        "v_mfma_f32_16x16x32_bf16 v[28:31], v[52:55], v[208:211], v[28:31]\n\tds_read_b128 v[48:51], %[pb1] offset:43008\n\ts_waitcnt vmcnt(13) lgkmcnt(3)\n\t"
        "v_mfma_f32_16x16x32_bf16 v[16:19], v[56:59], v[212:215], v[16:19]\n\tds_read_b128 v[52:55], %[pb1] offset:44032\n\t"
        "v_mfma_f32_16x16x32_bf16 v[20:23], v[60:63], v[212:215], v[20:23]\n\tds_read_b128 v[56:59], %[pb1] offset:45056\n\t"
        "v_mfma_f32_16x16x32_bf16 v[24:27], v[64:67], v[212:215], v[24:27]\n\tds_read_b128 v[60:63], %[pb1] offset:46080\n\t"
        "v_mfma_f32_16x16x32_bf16 v[28:31], v[68:71], v[212:215], v[28:31]\n\tds_read_b128 v[64:67], %[pb1] offset:47104\n\ts_waitcnt vmcnt(12) lgkmcnt(3)\n\t"
        "v_mfma_f32_16x16x32_bf16 v[16:19], v[40:43], v[216:219], v[16:19]\n\tds_read_b128 v[68:71], %[pb1] offset:48128\n\t"
        "v_mfma_f32_16x16x32_bf16 v[20:23], v[44:47], v[216:219], v[20:23]\n\ts_barrier\n\tds_read_b128 v[40:43], %[pb1] offset:49152\n\t"
        "v_mfma_f32_16x16x32_bf16 v[24:27], v[48:51], v[216:219], v[24:27]\n\tds_read_b128 v[44:47], %[pb1] offset:50176\n\t"
        "v_mfma_f32_16x16x32_bf16 v[28:31], v[52:55], v[216:219], v[28:31]\n\tds_read_b128 v[48:51], %[pb1] offset:51200\n\ts_waitcnt vmcnt(9) lgkmcnt(3)\n\t"
        "v_mfma_f32_16x16x32_bf16 v[16:19], v[56:59], v[220:223], v[16:19]\n\tds_read_b128 v[52:55], %[pb1] offset:52224\n\t"
        "v_mfma_f32_16x16x32_bf16 v[20:23], v[60:63], v[220:223], v[20:23]\n\t.p2align 12,,12\n\t.Lpg8:\n\ts_cbranch_execz .Lpgret\n\t"
        "ds_read_b128 v[56:59], %[pb1] offset:53248\n\tv_mfma_f32_16x16x32_bf16 v[24:27], v[64:67], v[220:223], v[24:27]\n\t"
        "ds_read_b128 v[60:63], %[pb1] offset:54272\n\tv_mfma_f32_16x16x32_bf16 v[28:31], v[68:71], v[220:223], v[28:31]\n\t"
        "ds_read_b128 v[64:67], %[pb1] offset:55296\n\ts_waitcnt vmcnt(8) lgkmcnt(3)\n\tv_mfma_f32_16x16x32_bf16 v[16:19], v[40:43], v[224:227], v[16:19]\n\t"
        "ds_read_b128 v[68:71], %[pb1] offset:56320\n\tv_mfma_f32_16x16x32_bf16 v[20:23], v[44:47], v[224:227], v[20:23]\n\t"
        "ds_read_b128 v[40:43], %[pb1] offset:57344\n\tv_mfma_f32_16x16x32_bf16 v[24:27], v[48:51], v[224:227], v[24:27]\n\t"
        "ds_read_b128 v[44:47], %[pb1] offset:58368\n\tv_mfma_f32_16x16x32_bf16 v[28:31], v[52:55], v[224:227], v[28:31]\n\t"
        "ds_read_b128 v[48:51], %[pb1] offset:59392\n\ts_waitcnt vmcnt(7) lgkmcnt(3)\n\tv_mfma_f32_16x16x32_bf16 v[16:19], v[56:59], v[228:231], v[16:19]\n\t"
        "ds_read_b128 v[52:55], %[pb1] offset:60416\n\tv_mfma_f32_16x16x32_bf16 v[20:23], v[60:63], v[228:231], v[20:23]\n\t"
        "ds_read_b128 v[56:59], %[pb1] offset:61440\n\tv_mfma_f32_16x16x32_bf16 v[24:27], v[64:67], v[228:231], v[24:27]\n\t"
        "ds_read_b128 v[60:63], %[pb1] offset:62464\n\tv_mfma_f32_16x16x32_bf16 v[28:31], v[68:71], v[228:231], v[28:31]\n\t"
        "ds_read_b128 v[64:67], %[pb1] offset:63488\n\ts_waitcnt vmcnt(6) lgkmcnt(3)\n\tv_mfma_f32_16x16x32_bf16 v[16:19], v[40:43], v[232:235], v[16:19]\n\t"
        "ds_read_b128 v[68:71], %[pb1] offset:64512\n\tv_mfma_f32_16x16x32_bf16 v[20:23], v[44:47], v[232:235], v[20:23]\n\t"
        "ds_read_b128 v[40:43], %[pb2] offset:0\n\tv_mfma_f32_16x16x32_bf16 v[24:27], v[48:51], v[232:235], v[24:27]\n\t"
        "ds_read_b128 v[44:47], %[pb2] offset:1024\n\tv_mfma_f32_16x16x32_bf16 v[28:31], v[52:55], v[232:235], v[28:31]\n\t"
        "ds_read_b128 v[48:51], %[pb2] offset:2048\n\ts_waitcnt vmcnt(5) lgkmcnt(3)\n\tv_mfma_f32_16x16x32_bf16 v[16:19], v[56:59], v[236:239], v[16:19]\n\t"
        "ds_read_b128 v[52:55], %[pb2] offset:3072\n\tv_mfma_f32_16x16x32_bf16 v[20:23], v[60:63], v[236:239], v[20:23]\n\t"
        "ds_read_b128 v[56:59], %[pb2] offset:4096\n\tv_mfma_f32_16x16x32_bf16 v[24:27], v[64:67], v[236:239], v[24:27]\n\t"
        "ds_read_b128 v[60:63], %[pb2] offset:5120\n\tv_mfma_f32_16x16x32_bf16 v[28:31], v[68:71], v[236:239], v[28:31]\n\t"
        "ds_read_b128 v[64:67], %[pb2] offset:6144\n\ts_waitcnt vmcnt(4) lgkmcnt(3)\n\tv_mfma_f32_16x16x32_bf16 v[16:19], v[40:43], v[240:243], v[16:19]\n\t"
        "ds_read_b128 v[68:71], %[pb2] offset:7168\n\tv_mfma_f32_16x16x32_bf16 v[20:23], v[44:47], v[240:243], v[20:23]\n\t"
        "ds_read_b128 v[40:43], %[pb2] offset:8192\n\tv_mfma_f32_16x16x32_bf16 v[24:27], v[48:51], v[240:243], v[24:27]\n\t"
        "ds_read_b128 v[44:47], %[pb2] offset:9216\n\tv_mfma_f32_16x16x32_bf16 v[28:31], v[52:55], v[240:243], v[28:31]\n\t"
        "ds_read_b128 v[48:51], %[pb2] offset:10240\n\ts_waitcnt vmcnt(3) lgkmcnt(3)\n\tv_mfma_f32_16x16x32_bf16 v[16:19], v[56:59], v[244:247], v[16:19]\n\t"
        "ds_read_b128 v[52:55], %[pb2] offset:11264\n\tv_mfma_f32_16x16x32_bf16 v[20:23], v[60:63], v[244:247], v[20:23]\n\t"
        "ds_read_b128 v[56:59], %[pb2] offset:12288\n\tv_mfma_f32_16x16x32_bf16 v[24:27], v[64:67], v[244:247], v[24:27]\n\t"
        "ds_read_b128 v[60:63], %[pb2] offset:13312\n\tv_mfma_f32_16x16x32_bf16 v[28:31], v[68:71], v[244:247], v[28:31]\n\t"
        "ds_read_b128 v[64:67], %[pb2] offset:14336\n\ts_waitcnt vmcnt(2) lgkmcnt(3)\n\tv_mfma_f32_16x16x32_bf16 v[16:19], v[40:43], v[128:131], v[16:19]\n\t"
        "ds_read_b128 v[68:71], %[pb2] offset:15360\n\tv_mfma_f32_16x16x32_bf16 v[20:23], v[44:47], v[128:131], v[20:23]\n\t"
        "v_mfma_f32_16x16x32_bf16 v[24:27], v[48:51], v[128:131], v[24:27]\n\tv_mfma_f32_16x16x32_bf16 v[28:31], v[52:55], v[128:131], v[28:31]\n\t"
        "s_waitcnt vmcnt(1) lgkmcnt(0)\n\tv_mfma_f32_16x16x32_bf16 v[16:19], v[56:59], v[132:135], v[16:19]\n\t"
        "v_mfma_f32_16x16x32_bf16 v[20:23], v[60:63], v[132:135], v[20:23]\n\tv_mfma_f32_16x16x32_bf16 v[24:27], v[64:67], v[132:135], v[24:27]\n\t"
        "v_mfma_f32_16x16x32_bf16 v[28:31], v[68:71], v[132:135], v[28:31]\n\ts_nop 7\n\t.Lmk_wo0_e:\n\t"
        : "=&{v[16:31]}"(acc[0]), "+{v[152:183]}"(r0), "+{v[184:215]}"(r1), "+{v[216:247]}"(r2), "+{v[128:143]}"(r3), "+{v[144:151]}"(r4), "+{v[120:127]}"(res), [sa] "+s"(sa), [sn] "+s"(sn)
        : [pb0] "{v250}"(n.pb0), [pb1] "{v251}"(n.pb1), [pb2] "{v252}"(n.pb2), [rt] "s"(rt4), [rn] "s"(rt4), [rs] "s"(rs4), [one] "{v[12:13]}"(tok), [fl] "s"(fl)
        : "v40", "v41", "v42", "v43", "v44", "v45", "v46", "v47", "v48", "v49", "v50", "v51", "v52", "v53", "v54", "v55", "v56", "v57", "v58", "v59", "v60", "v61", "v62", "v63", "v64", "v65", "v66", "v67", "v68", "v69", "v70", "v71", "memory", "scc");
      STAMP(10);
      LN();
      wo_epilogue(acc[0], res, rs, rt, xr + 4 * MB, so, n);
      STAMP(11);
    }
    {
      f32x16 acc[1];
      const unsigned fl = fx;
      unsigned sr = xr + 2 * MB;
      sa = sn; sn = xu;
      sn += 0u;
      asm volatile(
        "; BLK wo1\n\t.Lmk_wo1_s:\n\ts_barrier\n\tbuffer_load_dwordx4 v[128:131], off, %[rt], %[sa] offset:0 sc1\n\ts_add_u32 %[sa], %[sa], 0x2000\n\t"
        "buffer_load_dwordx4 v[132:135], off, %[rt], %[sa] offset:0 sc1\n\ts_add_u32 %[sa], %[sa], 0x2000\n\t"
        "buffer_load_dwordx4 v[136:139], off, %[rt], %[sa] offset:0 sc1\n\ts_add_u32 %[sa], %[sa], 0x2000\n\t"
        "buffer_load_dwordx4 v[140:143], off, %[rt], %[sa] offset:0 sc1\n\ts_add_u32 %[sa], %[sa], 0x2000\n\t"
        "buffer_load_dwordx4 v[144:147], off, %[rt], %[sa] offset:0 sc1\n\ts_add_u32 %[sa], %[sa], 0x2000\n\t"
        "buffer_load_dwordx4 v[148:151], off, %[rt], %[sa] offset:0 sc1\n\ts_add_u32 %[sa], %[sa], 0x2000\n\t"
        "buffer_load_dwordx4 v[152:155], off, %[rt], %[sa] offset:0 sc1\n\ts_add_u32 %[sa], %[sa], 0x2000\n\t"
        "buffer_load_dwordx4 v[156:159], off, %[rt], %[sa] offset:0 sc1\n\ts_add_u32 %[sa], %[sa], 0x2000\n\t"
        "buffer_load_dwordx4 v[120:123], off, %[rn], %[sr] sc1\n\ts_addk_i32 %[sr], 0x2000\n\tbuffer_load_dwordx4 v[124:127], off, %[rn], %[sr] sc1\n\t"
        "ds_read_b128 v[40:43], %[pb0] offset:0\n\tds_read_b128 v[44:47], %[pb0] offset:1024\n\tds_read_b128 v[48:51], %[pb0] offset:2048\n\t"
        "ds_read_b128 v[52:55], %[pb0] offset:3072\n\tds_read_b128 v[56:59], %[pb0] offset:4096\n\tds_read_b128 v[60:63], %[pb0] offset:5120\n\t"
        "ds_read_b128 v[64:67], %[pb0] offset:6144\n\ts_waitcnt vmcnt(9) lgkmcnt(3)\n\tv_mfma_f32_16x16x32_bf16 v[16:19], v[40:43], v[128:131], 0\n\t"
        "ds_read_b128 v[68:71], %[pb0] offset:7168\n\tv_mfma_f32_16x16x32_bf16 v[20:23], v[44:47], v[128:131], 0\n\t"
        "ds_read_b128 v[40:43], %[pb0] offset:8192\n\ts_waitcnt vmcnt(10)\n\tbuffer_store_dwordx2 %[one], off, %[rs], %[fl] sc1\n\t"
        "v_mfma_f32_16x16x32_bf16 v[24:27], v[48:51], v[128:131], 0\n\tds_read_b128 v[44:47], %[pb0] offset:9216\n\t"
        "buffer_load_dwordx4 v[160:163], off, %[rt], %[sa] offset:0 sc1\n\ts_add_u32 %[sa], %[sa], 0x2000\n\t"
        "v_mfma_f32_16x16x32_bf16 v[28:31], v[52:55], v[128:131], 0\n\tds_read_b128 v[48:51], %[pb0] offset:10240\n\ts_waitcnt vmcnt(10) lgkmcnt(3)\n\t"
        "v_mfma_f32_16x16x32_bf16 v[16:19], v[56:59], v[132:135], v[16:19]\n\tds_read_b128 v[52:55], %[pb0] offset:11264\n\t"
        "v_mfma_f32_16x16x32_bf16 v[20:23], v[60:63], v[132:135], v[20:23]\n\tds_read_b128 v[56:59], %[pb0] offset:12288\n\t"
        "buffer_load_dwordx4 v[164:167], off, %[rt], %[sa] offset:0 sc1\n\ts_add_u32 %[sa], %[sa], 0x2000\n\t"
        "v_mfma_f32_16x16x32_bf16 v[24:27], v[64:67], v[132:135], v[24:27]\n\tds_read_b128 v[60:63], %[pb0] offset:13312\n\t"
        "v_mfma_f32_16x16x32_bf16 v[28:31], v[68:71], v[132:135], v[28:31]\n\tds_read_b128 v[64:67], %[pb0] offset:14336\n\ts_waitcnt vmcnt(10) lgkmcnt(3)\n\t"
        "v_mfma_f32_16x16x32_bf16 v[16:19], v[40:43], v[136:139], v[16:19]\n\tds_read_b128 v[68:71], %[pb0] offset:15360\n\t"
        "buffer_load_dwordx4 v[168:171], off, %[rt], %[sa] offset:0 sc1\n\ts_add_u32 %[sa], %[sa], 0x2000\n\t"
        "v_mfma_f32_16x16x32_bf16 v[20:23], v[44:47], v[136:139], v[20:23]\n\tds_read_b128 v[40:43], %[pb0] offset:16384\n\t"
        "v_mfma_f32_16x16x32_bf16 v[24:27], v[48:51], v[136:139], v[24:27]\n\tds_read_b128 v[44:47], %[pb0] offset:17408\n\t"
        "v_mfma_f32_16x16x32_bf16 v[28:31], v[52:55], v[136:139], v[28:31]\n\tds_read_b128 v[48:51], %[pb0] offset:18432\n\t"
        "buffer_load_dwordx4 v[172:175], off, %[rt], %[sa] offset:0 sc1\n\ts_add_u32 %[sa], %[sa], 0x2000\n\ts_waitcnt vmcnt(11) lgkmcnt(3)\n\t"
        "v_mfma_f32_16x16x32_bf16 v[16:19], v[56:59], v[140:143], v[16:19]\n\tds_read_b128 v[52:55], %[pb0] offset:19456\n\t"
        "v_mfma_f32_16x16x32_bf16 v[20:23], v[60:63], v[140:143], v[20:23]\n\tds_read_b128 v[56:59], %[pb0] offset:20480\n\t"
        "v_mfma_f32_16x16x32_bf16 v[24:27], v[64:67], v[140:143], v[24:27]\n\tds_read_b128 v[60:63], %[pb0] offset:21504\n\t"
        "buffer_load_dwordx4 v[176:179], off, %[rt], %[sa] offset:0 sc1\n\ts_add_u32 %[sa], %[sa], 0x2000\n\t"
        "v_mfma_f32_16x16x32_bf16 v[28:31], v[68:71], v[140:143], v[28:31]\n\tds_read_b128 v[64:67], %[pb0] offset:22528\n\ts_waitcnt vmcnt(11) lgkmcnt(3)\n\t"
        "v_mfma_f32_16x16x32_bf16 v[16:19], v[40:43], v[144:147], v[16:19]\n\tds_read_b128 v[68:71], %[pb0] offset:23552\n\t"
        "v_mfma_f32_16x16x32_bf16 v[20:23], v[44:47], v[144:147], v[20:23]\n\tds_read_b128 v[40:43], %[pb0] offset:24576\n\t"
        "buffer_load_dwordx4 v[180:183], off, %[rt], %[sa] offset:0 sc1\n\ts_add_u32 %[sa], %[sa], 0x2000\n\t"
        "v_mfma_f32_16x16x32_bf16 v[24:27], v[48:51], v[144:147], v[24:27]\n\tds_read_b128 v[44:47], %[pb0] offset:25600\n\t"
        "v_mfma_f32_16x16x32_bf16 v[28:31], v[52:55], v[144:147], v[28:31]\n\tds_read_b128 v[48:51], %[pb0] offset:26624\n\ts_waitcnt vmcnt(11) lgkmcnt(3)\n\t"
        "v_mfma_f32_16x16x32_bf16 v[16:19], v[56:59], v[148:151], v[16:19]\n\tds_read_b128 v[52:55], %[pb0] offset:27648\n\t"
        "buffer_load_dwordx4 v[184:187], off, %[rt], %[sa] offset:0 sc1\n\ts_add_u32 %[sa], %[sa], 0x2000\n\t"
        "v_mfma_f32_16x16x32_bf16 v[20:23], v[60:63], v[148:151], v[20:23]\n\tds_read_b128 v[56:59], %[pb0] offset:28672\n\t"
        "v_mfma_f32_16x16x32_bf16 v[24:27], v[64:67], v[148:151], v[24:27]\n\tds_read_b128 v[60:63], %[pb0] offset:29696\n\t"
        "v_mfma_f32_16x16x32_bf16 v[28:31], v[68:71], v[148:151], v[28:31]\n\tds_read_b128 v[64:67], %[pb0] offset:30720\n\t"
        "buffer_load_dwordx4 v[188:191], off, %[rt], %[sa] offset:0 sc1\n\ts_add_u32 %[sa], %[sa], 0x2000\n\ts_waitcnt vmcnt(12) lgkmcnt(3)\n\t"
        "v_mfma_f32_16x16x32_bf16 v[16:19], v[40:43], v[152:155], v[16:19]\n\tds_read_b128 v[68:71], %[pb0] offset:31744\n\t"
        "v_mfma_f32_16x16x32_bf16 v[20:23], v[44:47], v[152:155], v[20:23]\n\tds_read_b128 v[40:43], %[pb0] offset:32768\n\t"
        "v_mfma_f32_16x16x32_bf16 v[24:27], v[48:51], v[152:155], v[24:27]\n\tds_read_b128 v[44:47], %[pb0] offset:33792\n\t"
        "buffer_load_dwordx4 v[192:195], off, %[rt], %[sa] offset:0 sc1\n\ts_add_u32 %[sa], %[sa], 0x2000\n\t"
        "v_mfma_f32_16x16x32_bf16 v[28:31], v[52:55], v[152:155], v[28:31]\n\tds_read_b128 v[48:51], %[pb0] offset:34816\n\ts_waitcnt vmcnt(12) lgkmcnt(3)\n\t"
        "v_mfma_f32_16x16x32_bf16 v[16:19], v[56:59], v[156:159], v[16:19]\n\tds_read_b128 v[52:55], %[pb0] offset:35840\n\t"
        "v_mfma_f32_16x16x32_bf16 v[20:23], v[60:63], v[156:159], v[20:23]\n\tds_read_b128 v[56:59], %[pb0] offset:36864\n\t"
        "buffer_load_dwordx4 v[196:199], off, %[rt], %[sa] offset:0 sc1\n\ts_add_u32 %[sa], %[sa], 0x2000\n\t"
        "v_mfma_f32_16x16x32_bf16 v[24:27], v[64:67], v[156:159], v[24:27]\n\tds_read_b128 v[60:63], %[pb0] offset:37888\n\t"
        "v_mfma_f32_16x16x32_bf16 v[28:31], v[68:71], v[156:159], v[28:31]\n\tds_read_b128 v[64:67], %[pb0] offset:38912\n\ts_waitcnt vmcnt(9) lgkmcnt(3)\n\t"
        "v_mfma_f32_16x16x32_bf16 v[16:19], v[40:43], v[160:163], v[16:19]\n\tds_read_b128 v[68:71], %[pb0] offset:39936\n\t"
        "buffer_load_dwordx4 v[200:203], off, %[rt], %[sa] offset:0 sc1\n\ts_add_u32 %[sa], %[sa], 0x2000\n\t"
        "v_mfma_f32_16x16x32_bf16 v[20:23], v[44:47], v[160:163], v[20:23]\n\tds_read_b128 v[40:43], %[pb0] offset:40960\n\t"
        "v_mfma_f32_16x16x32_bf16 v[24:27], v[48:51], v[160:163], v[24:27]\n\tds_read_b128 v[44:47], %[pb0] offset:41984\n\t"
        "v_mfma_f32_16x16x32_bf16 v[28:31], v[52:55], v[160:163], v[28:31]\n\tds_read_b128 v[48:51], %[pb0] offset:43008\n\t"
        "buffer_load_dwordx4 v[204:207], off, %[rt], %[sa] offset:0 sc1\n\ts_add_u32 %[sa], %[sa], 0x2000\n\ts_waitcnt vmcnt(10) lgkmcnt(3)\n\t"
        "v_mfma_f32_16x16x32_bf16 v[16:19], v[56:59], v[164:167], v[16:19]\n\tds_read_b128 v[52:55], %[pb0] offset:44032\n\t"
        "v_mfma_f32_16x16x32_bf16 v[20:23], v[60:63], v[164:167], v[20:23]\n\tds_read_b128 v[56:59], %[pb0] offset:45056\n\t"
        "v_mfma_f32_16x16x32_bf16 v[24:27], v[64:67], v[164:167], v[24:27]\n\tds_read_b128 v[60:63], %[pb0] offset:46080\n\t"
        "buffer_load_dwordx4 v[208:211], off, %[rt], %[sa] offset:0 sc1\n\ts_add_u32 %[sa], %[sa], 0x2000\n\t"
        "v_mfma_f32_16x16x32_bf16 v[28:31], v[68:71], v[164:167], v[28:31]\n\tds_read_b128 v[64:67], %[pb0] offset:47104\n\ts_waitcnt vmcnt(10) lgkmcnt(3)\n\t"
        "v_mfma_f32_16x16x32_bf16 v[16:19], v[40:43], v[168:171], v[16:19]\n\tds_read_b128 v[68:71], %[pb0] offset:48128\n\t"
        "v_mfma_f32_16x16x32_bf16 v[20:23], v[44:47], v[168:171], v[20:23]\n\tds_read_b128 v[40:43], %[pb2] offset:16384\n\t"
        "buffer_load_dwordx4 v[212:215], off, %[rt], %[sa] offset:0 sc1\n\ts_add_u32 %[sa], %[sa], 0x2000\n\t"
        "v_mfma_f32_16x16x32_bf16 v[24:27], v[48:51], v[168:171], v[24:27]\n\tds_read_b128 v[44:47], %[pb2] offset:17408\n\t"
        "v_mfma_f32_16x16x32_bf16 v[28:31], v[52:55], v[168:171], v[28:31]\n\tds_read_b128 v[48:51], %[pb2] offset:18432\n\ts_waitcnt vmcnt(10) lgkmcnt(3)\n\t"
        "v_mfma_f32_16x16x32_bf16 v[16:19], v[56:59], v[172:175], v[16:19]\n\tds_read_b128 v[52:55], %[pb2] offset:19456\n\t"
        "buffer_load_dwordx4 v[216:219], off, %[rt], %[sa] offset:0 sc1\n\ts_add_u32 %[sa], %[sa], 0x2000\n\t"
        "v_mfma_f32_16x16x32_bf16 v[20:23], v[60:63], v[172:175], v[20:23]\n\tds_read_b128 v[56:59], %[pb2] offset:20480\n\t"
        "v_mfma_f32_16x16x32_bf16 v[24:27], v[64:67], v[172:175], v[24:27]\n\tds_read_b128 v[60:63], %[pb2] offset:21504\n\t"
        "v_mfma_f32_16x16x32_bf16 v[28:31], v[68:71], v[172:175], v[28:31]\n\tds_read_b128 v[64:67], %[pb2] offset:22528\n\t"
        "buffer_load_dwordx4 v[220:223], off, %[rt], %[sa] offset:0 sc1\n\ts_add_u32 %[sa], %[sa], 0x2000\n\ts_waitcnt vmcnt(11) lgkmcnt(3)\n\t"
        "v_mfma_f32_16x16x32_bf16 v[16:19], v[40:43], v[176:179], v[16:19]\n\tds_read_b128 v[68:71], %[pb2] offset:23552\n\t"
        "v_mfma_f32_16x16x32_bf16 v[20:23], v[44:47], v[176:179], v[20:23]\n\tds_read_b128 v[40:43], %[pb2] offset:24576\n\t"
        "v_mfma_f32_16x16x32_bf16 v[24:27], v[48:51], v[176:179], v[24:27]\n\tds_read_b128 v[44:47], %[pb2] offset:25600\n\t"
        "buffer_load_dwordx4 v[224:227], off, %[rt], %[sa] offset:0 sc1\n\ts_add_u32 %[sa], %[sa], 0x2000\n\t"
        "v_mfma_f32_16x16x32_bf16 v[28:31], v[52:55], v[176:179], v[28:31]\n\tds_read_b128 v[48:51], %[pb2] offset:26624\n\ts_waitcnt vmcnt(11) lgkmcnt(3)\n\t"
        "v_mfma_f32_16x16x32_bf16 v[16:19], v[56:59], v[180:183], v[16:19]\n\tds_read_b128 v[52:55], %[pb2] offset:27648\n\t"
        "v_mfma_f32_16x16x32_bf16 v[20:23], v[60:63], v[180:183], v[20:23]\n\tds_read_b128 v[56:59], %[pb2] offset:28672\n\t"
        "buffer_load_dwordx4 v[228:231], off, %[rt], %[sa] offset:0 sc1\n\ts_add_u32 %[sa], %[sa], 0x2000\n\t"
        "v_mfma_f32_16x16x32_bf16 v[24:27], v[64:67], v[180:183], v[24:27]\n\tds_read_b128 v[60:63], %[pb2] offset:29696\n\t"
        "v_mfma_f32_16x16x32_bf16 v[28:31], v[68:71], v[180:183], v[28:31]\n\tds_read_b128 v[64:67], %[pb2] offset:30720\n\ts_waitcnt vmcnt(11) lgkmcnt(3)\n\t"
        "v_mfma_f32_16x16x32_bf16 v[16:19], v[40:43], v[184:187], v[16:19]\n\tds_read_b128 v[68:71], %[pb2] offset:31744\n\t"
        "buffer_load_dwordx4 v[232:235], off, %[rt], %[sa] offset:0 sc1\n\ts_add_u32 %[sa], %[sa], 0x2000\n\t"
        "v_mfma_f32_16x16x32_bf16 v[20:23], v[44:47], v[184:187], v[20:23]\n\tds_read_b128 v[40:43], %[pb1] offset:16384\n\t"
        "v_mfma_f32_16x16x32_bf16 v[24:27], v[48:51], v[184:187], v[24:27]\n\tds_read_b128 v[44:47], %[pb1] offset:17408\n\t"
        "v_mfma_f32_16x16x32_bf16 v[28:31], v[52:55], v[184:187], v[28:31]\n\tds_read_b128 v[48:51], %[pb1] offset:18432\n\t"
        "buffer_load_dwordx4 v[236:239], off, %[rt], %[sa] offset:0 sc1\n\ts_add_u32 %[sa], %[sa], 0x2000\n\ts_waitcnt vmcnt(12) lgkmcnt(3)\n\t"
        "v_mfma_f32_16x16x32_bf16 v[16:19], v[56:59], v[188:191], v[16:19]\n\tds_read_b128 v[52:55], %[pb1] offset:19456\n\t"
        "v_mfma_f32_16x16x32_bf16 v[20:23], v[60:63], v[188:191], v[20:23]\n\tds_read_b128 v[56:59], %[pb1] offset:20480\n\t"
        "v_mfma_f32_16x16x32_bf16 v[24:27], v[64:67], v[188:191], v[24:27]\n\tds_read_b128 v[60:63], %[pb1] offset:21504\n\t"
        "buffer_load_dwordx4 v[240:243], off, %[rt], %[sa] offset:0 sc1\n\ts_add_u32 %[sa], %[sa], 0x2000\n\t"
        "v_mfma_f32_16x16x32_bf16 v[28:31], v[68:71], v[188:191], v[28:31]\n\tds_read_b128 v[64:67], %[pb1] offset:22528\n\ts_waitcnt vmcnt(12) lgkmcnt(3)\n\t"
        "v_mfma_f32_16x16x32_bf16 v[16:19], v[40:43], v[192:195], v[16:19]\n\tds_read_b128 v[68:71], %[pb1] offset:23552\n\t"
        "v_mfma_f32_16x16x32_bf16 v[20:23], v[44:47], v[192:195], v[20:23]\n\tds_read_b128 v[40:43], %[pb1] offset:24576\n\t"
        "buffer_load_dwordx4 v[244:247], off, %[rt], %[sa] offset:0 sc1\n\ts_add_u32 %[sa], %[sa], 0x2000\n\t"
        "v_mfma_f32_16x16x32_bf16 v[24:27], v[48:51], v[192:195], v[24:27]\n\tds_read_b128 v[44:47], %[pb1] offset:25600\n\t"
        "v_mfma_f32_16x16x32_bf16 v[28:31], v[52:55], v[192:195], v[28:31]\n\tds_read_b128 v[48:51], %[pb1] offset:26624\n\ts_waitcnt vmcnt(12) lgkmcnt(3)\n\t"
        "v_mfma_f32_16x16x32_bf16 v[16:19], v[56:59], v[196:199], v[16:19]\n\tds_read_b128 v[52:55], %[pb1] offset:27648\n\t"
        "buffer_load_dwordx4 v[128:131], off, %[rt], %[sa] offset:0 sc1\n\ts_add_u32 %[sa], %[sa], 0x2000\n\t"
        "v_mfma_f32_16x16x32_bf16 v[20:23], v[60:63], v[196:199], v[20:23]\n\tds_read_b128 v[56:59], %[pb1] offset:28672\n\t"
        "v_mfma_f32_16x16x32_bf16 v[24:27], v[64:67], v[196:199], v[24:27]\n\tds_read_b128 v[60:63], %[pb1] offset:29696\n\t"
        "v_mfma_f32_16x16x32_bf16 v[28:31], v[68:71], v[196:199], v[28:31]\n\tds_read_b128 v[64:67], %[pb1] offset:30720\n\t"
        "buffer_load_dwordx4 v[132:135], off, %[rt], %[sa] offset:0 sc1\n\ts_waitcnt vmcnt(13) lgkmcnt(3)\n\t"
        "v_mfma_f32_16x16x32_bf16 v[16:19], v[40:43], v[200:203], v[16:19]\n\tds_read_b128 v[68:71], %[pb1] offset:31744\n\t"
        "v_mfma_f32_16x16x32_bf16 v[20:23], v[44:47], v[200:203], v[20:23]\n\tds_read_b128 v[40:43], %[pb1] offset:32768\n\t"
        "v_mfma_f32_16x16x32_bf16 v[24:27], v[48:51], v[200:203], v[24:27]\n\tds_read_b128 v[44:47], %[pb1] offset:33792\n\t"
        "v_mfma_f32_16x16x32_bf16 v[28:31], v[52:55], v[200:203], v[28:31]\n\tds_read_b128 v[48:51], %[pb1] offset:34816\n\ts_waitcnt vmcnt(12) lgkmcnt(3)\n\t"
        "v_mfma_f32_16x16x32_bf16 v[16:19], v[56:59], v[204:207], v[16:19]\n\tds_read_b128 v[52:55], %[pb1] offset:35840\n\t"
        "v_mfma_f32_16x16x32_bf16 v[20:23], v[60:63], v[204:207], v[20:23]\n\tds_read_b128 v[56:59], %[pb1] offset:36864\n\t"
        "v_mfma_f32_16x16x32_bf16 v[24:27], v[64:67], v[204:207], v[24:27]\n\tds_read_b128 v[60:63], %[pb1] offset:37888\n\t"
        "v_mfma_f32_16x16x32_bf16 v[28:31], v[68:71], v[204:207], v[28:31]\n\tds_read_b128 v[64:67], %[pb1] offset:38912\n\ts_waitcnt vmcnt(11) lgkmcnt(3)\n\t"
        "v_mfma_f32_16x16x32_bf16 v[16:19], v[40:43], v[208:211], v[16:19]\n\tds_read_b128 v[68:71], %[pb1] offset:39936\n\t"
        "v_mfma_f32_16x16x32_bf16 v[20:23], v[44:47], v[208:211], v[20:23]\n\tds_read_b128 v[40:43], %[pb1] offset:40960\n\t"
        "v_mfma_f32_16x16x32_bf16 v[24:27], v[48:51], v[208:211], v[24:27]\n\tds_read_b128 v[44:47], %[pb1] offset:41984\n\t"
        "v_mfma_f32_16x16x32_bf16 v[28:31], v[52:55], v[208:211], v[28:31]\n\tds_read_b128 v[48:51], %[pb1] offset:43008\n\ts_waitcnt vmcnt(10) lgkmcnt(3)\n\t"
        "v_mfma_f32_16x16x32_bf16 v[16:19], v[56:59], v[212:215], v[16:19]\n\tds_read_b128 v[52:55], %[pb1] offset:44032\n\t"
        "v_mfma_f32_16x16x32_bf16 v[20:23], v[60:63], v[212:215], v[20:23]\n\tds_read_b128 v[56:59], %[pb1] offset:45056\n\t"
        "v_mfma_f32_16x16x32_bf16 v[24:27], v[64:67], v[212:215], v[24:27]\n\tds_read_b128 v[60:63], %[pb1] offset:46080\n\t"
        "v_mfma_f32_16x16x32_bf16 v[28:31], v[68:71], v[212:215], v[28:31]\n\tds_read_b128 v[64:67], %[pb1] offset:47104\n\ts_waitcnt vmcnt(9) lgkmcnt(3)\n\t"
        "v_mfma_f32_16x16x32_bf16 v[16:19], v[40:43], v[216:219], v[16:19]\n\tds_read_b128 v[68:71], %[pb1] offset:48128\n\t"
        "v_mfma_f32_16x16x32_bf16 v[20:23], v[44:47], v[216:219], v[20:23]\n\tds_read_b128 v[40:43], %[pb1] offset:49152\n\t"
        "v_mfma_f32_16x16x32_bf16 v[24:27], v[48:51], v[216:219], v[24:27]\n\tds_read_b128 v[44:47], %[pb1] offset:50176\n\t"
        "v_mfma_f32_16x16x32_bf16 v[28:31], v[52:55], v[216:219], v[28:31]\n\tds_read_b128 v[48:51], %[pb1] offset:51200\n\ts_waitcnt vmcnt(8) lgkmcnt(3)\n\t"
        "v_mfma_f32_16x16x32_bf16 v[16:19], v[56:59], v[220:223], v[16:19]\n\tds_read_b128 v[52:55], %[pb1] offset:52224\n\t"
        "v_mfma_f32_16x16x32_bf16 v[20:23], v[60:63], v[220:223], v[20:23]\n\tds_read_b128 v[56:59], %[pb1] offset:53248\n\t"
        "v_mfma_f32_16x16x32_bf16 v[24:27], v[64:67], v[220:223], v[24:27]\n\tds_read_b128 v[60:63], %[pb1] offset:54272\n\t"
        "v_mfma_f32_16x16x32_bf16 v[28:31], v[68:71], v[220:223], v[28:31]\n\tds_read_b128 v[64:67], %[pb1] offset:55296\n\ts_waitcnt vmcnt(7) lgkmcnt(3)\n\t"
        "v_mfma_f32_16x16x32_bf16 v[16:19], v[40:43], v[224:227], v[16:19]\n\tds_read_b128 v[68:71], %[pb1] offset:56320\n\t"
        "v_mfma_f32_16x16x32_bf16 v[20:23], v[44:47], v[224:227], v[20:23]\n\tds_read_b128 v[40:43], %[pb1] offset:57344\n\t"
        "v_mfma_f32_16x16x32_bf16 v[24:27], v[48:51], v[224:227], v[24:27]\n\tds_read_b128 v[44:47], %[pb1] offset:58368\n\t"
        "v_mfma_f32_16x16x32_bf16 v[28:31], v[52:55], v[224:227], v[28:31]\n\tds_read_b128 v[48:51], %[pb1] offset:59392\n\ts_waitcnt vmcnt(6) lgkmcnt(3)\n\t"
        "v_mfma_f32_16x16x32_bf16 v[16:19], v[56:59], v[228:231], v[16:19]\n\tds_read_b128 v[52:55], %[pb1] offset:60416\n\t"
        "v_mfma_f32_16x16x32_bf16 v[20:23], v[60:63], v[228:231], v[20:23]\n\tds_read_b128 v[56:59], %[pb1] offset:61440\n\t"
        "v_mfma_f32_16x16x32_bf16 v[24:27], v[64:67], v[228:231], v[24:27]\n\tds_read_b128 v[60:63], %[pb1] offset:62464\n\t"
        "v_mfma_f32_16x16x32_bf16 v[28:31], v[68:71], v[228:231], v[28:31]\n\tds_read_b128 v[64:67], %[pb1] offset:63488\n\ts_waitcnt vmcnt(5) lgkmcnt(3)\n\t"
        "v_mfma_f32_16x16x32_bf16 v[16:19], v[40:43], v[232:235], v[16:19]\n\tds_read_b128 v[68:71], %[pb1] offset:64512\n\t"
        "v_mfma_f32_16x16x32_bf16 v[20:23], v[44:47], v[232:235], v[20:23]\n\tds_read_b128 v[40:43], %[pb2] offset:0\n\t"
        "v_mfma_f32_16x16x32_bf16 v[24:27], v[48:51], v[232:235], v[24:27]\n\tds_read_b128 v[44:47], %[pb2] offset:1024\n\t"
        "v_mfma_f32_16x16x32_bf16 v[28:31], v[52:55], v[232:235], v[28:31]\n\tds_read_b128 v[48:51], %[pb2] offset:2048\n\ts_waitcnt vmcnt(4) lgkmcnt(3)\n\t"
        "v_mfma_f32_16x16x32_bf16 v[16:19], v[56:59], v[236:239], v[16:19]\n\tds_read_b128 v[52:55], %[pb2] offset:3072\n\t"
        "v_mfma_f32_16x16x32_bf16 v[20:23], v[60:63], v[236:239], v[20:23]\n\tds_read_b128 v[56:59], %[pb2] offset:4096\n\t"
        "v_mfma_f32_16x16x32_bf16 v[24:27], v[64:67], v[236:239], v[24:27]\n\tds_read_b128 v[60:63], %[pb2] offset:5120\n\t"
        "v_mfma_f32_16x16x32_bf16 v[28:31], v[68:71], v[236:239], v[28:31]\n\tds_read_b128 v[64:67], %[pb2] offset:6144\n\ts_waitcnt vmcnt(3) lgkmcnt(3)\n\t"
        "v_mfma_f32_16x16x32_bf16 v[16:19], v[40:43], v[240:243], v[16:19]\n\tds_read_b128 v[68:71], %[pb2] offset:7168\n\t"
        "v_mfma_f32_16x16x32_bf16 v[20:23], v[44:47], v[240:243], v[20:23]\n\tds_read_b128 v[40:43], %[pb2] offset:8192\n\t"
        "v_mfma_f32_16x16x32_bf16 v[24:27], v[48:51], v[240:243], v[24:27]\n\tds_read_b128 v[44:47], %[pb2] offset:9216\n\t"
        "v_mfma_f32_16x16x32_bf16 v[28:31], v[52:55], v[240:243], v[28:31]\n\tds_read_b128 v[48:51], %[pb2] offset:10240\n\ts_waitcnt vmcnt(2) lgkmcnt(3)\n\t"
        "v_mfma_f32_16x16x32_bf16 v[16:19], v[56:59], v[244:247], v[16:19]\n\tds_read_b128 v[52:55], %[pb2] offset:11264\n\t"
        "v_mfma_f32_16x16x32_bf16 v[20:23], v[60:63], v[244:247], v[20:23]\n\tds_read_b128 v[56:59], %[pb2] offset:12288\n\t"
        "v_mfma_f32_16x16x32_bf16 v[24:27], v[64:67], v[244:247], v[24:27]\n\tds_read_b128 v[60:63], %[pb2] offset:13312\n\t"
        "v_mfma_f32_16x16x32_bf16 v[28:31], v[68:71], v[244:247], v[28:31]\n\tds_read_b128 v[64:67], %[pb2] offset:14336\n\ts_waitcnt vmcnt(1) lgkmcnt(3)\n\t"
        "v_mfma_f32_16x16x32_bf16 v[16:19], v[40:43], v[128:131], v[16:19]\n\tds_read_b128 v[68:71], %[pb2] offset:15360\n\t"
        "v_mfma_f32_16x16x32_bf16 v[20:23], v[44:47], v[128:131], v[20:23]\n\ts_waitcnt lgkmcnt(0)\n\ts_barrier\n\t"
        "v_mfma_f32_16x16x32_bf16 v[24:27], v[48:51], v[128:131], v[24:27]\n\tbuffer_load_dwordx4 v[152:155], off, %[rn], %[sn] offset:0 sc1\n\t"
        "v_mfma_f32_16x16x32_bf16 v[28:31], v[52:55], v[128:131], v[28:31]\n\ts_waitcnt vmcnt(1)\n\t"
        "v_mfma_f32_16x16x32_bf16 v[16:19], v[56:59], v[132:135], v[16:19]\n\tv_mfma_f32_16x16x32_bf16 v[20:23], v[60:63], v[132:135], v[20:23]\n\t"
        "buffer_load_dwordx4 v[156:159], off, %[rn], %[sn] offset:1024 sc1\n\ts_add_u32 %[sn], %[sn], 0x2000\n\t"
        "v_mfma_f32_16x16x32_bf16 v[24:27], v[64:67], v[132:135], v[24:27]\n\tv_mfma_f32_16x16x32_bf16 v[28:31], v[68:71], v[132:135], v[28:31]\n\t"
        "buffer_load_dwordx4 v[160:163], off, %[rn], %[sn] offset:0 sc1\n\tbuffer_load_dwordx4 v[164:167], off, %[rn], %[sn] offset:1024 sc1\n\t"
        "s_add_u32 %[sn], %[sn], 0x2000\n\tbuffer_load_dwordx4 v[168:171], off, %[rn], %[sn] offset:0 sc1\n\t"
        "buffer_load_dwordx4 v[172:175], off, %[rn], %[sn] offset:1024 sc1\n\ts_add_u32 %[sn], %[sn], 0x2000\n\t"
        "buffer_load_dwordx4 v[176:179], off, %[rn], %[sn] offset:0 sc1\n\tbuffer_load_dwordx4 v[180:183], off, %[rn], %[sn] offset:1024 sc1\n\t"
        "s_add_u32 %[sn], %[sn], 0x2000\n\ts_branch 8f\n\t.p2align 12\n\t.Lpg9:\n\ts_setpc_b64 s[98:99]\n\t8:\n\t.Lmk_wo1_e:\n\t"
        : "=&{v[16:31]}"(acc[0]), "+{v[152:183]}"(r0), "+{v[184:215]}"(r1), "+{v[216:247]}"(r2), "+{v[128:143]}"(r3), "+{v[144:151]}"(r4), "=&{v[120:127]}"(res), [sr] "+s"(sr), [sa] "+s"(sa), [sn] "+s"(sn)
        : [pb0] "{v250}"(n.pb0), [pb1] "{v251}"(n.pb1), [pb2] "{v252}"(n.pb2), [rt] "s"(rt4), [rn] "s"(rt4), [rs] "s"(rs4), [one] "{v[12:13]}"(tok), [fl] "s"(fl)
        : "v40", "v41", "v42", "v43", "v44", "v45", "v46", "v47", "v48", "v49", "v50", "v51", "v52", "v53", "v54", "v55", "v56", "v57", "v58", "v59", "v60", "v61", "v62", "v63", "v64", "v65", "v66", "v67", "v68", "v69", "v70", "v71", "memory", "scc");
      STAMP(12);
      LN();
      wo_epilogue(acc[0], res, rs, rt, xr + 6 * MB, so + 131072, n);
      STAMP(13);
    }
    u32x32 ho;
    unsigned hx;
    {
      const unsigned fl = fx + 8192;
      sa = sn; sn = xu + 2 * MB;
      sn += 163840u;
      asm volatile(
        "; BLK u0\n\t.Lmk_u0_s:\n\tds_read_b128 v[72:75], %[pb0] offset:53248\n\tds_read_b128 v[76:79], %[pb0] offset:54272\n\t"
        "ds_read_b128 v[80:83], %[pb0] offset:55296\n\tds_read_b128 v[84:87], %[pb0] offset:56320\n\tds_read_b128 v[88:91], %[pb0] offset:57344\n\t"
        "ds_read_b128 v[92:95], %[pb0] offset:58368\n\tds_read_b128 v[96:99], %[pb0] offset:59392\n\ts_waitcnt vmcnt(9) lgkmcnt(3)\n\t"
        "v_mfma_f32_16x16x32_bf16 v[16:19], v[72:75], v[152:155], 0\n\tds_read_b128 v[100:103], %[pb0] offset:60416\n\t"
        "v_mfma_f32_16x16x32_bf16 v[20:23], v[72:75], v[156:159], 0\n\tv_mfma_f32_16x16x32_bf16 v[24:27], v[76:79], v[152:155], 0\n\t"
        "ds_read_b128 v[72:75], %[pb0] offset:61440\n\tv_mfma_f32_16x16x32_bf16 v[28:31], v[76:79], v[156:159], 0\n\t"
        "v_mfma_f32_16x16x32_bf16 v[32:35], v[80:83], v[152:155], 0\n\tds_read_b128 v[76:79], %[pb0] offset:62464\n\t"
        "v_mfma_f32_16x16x32_bf16 v[36:39], v[80:83], v[156:159], 0\n\tbuffer_load_dwordx4 v[184:187], off, %[rt], %[sa] offset:0 sc1\n\t"
        "v_mfma_f32_16x16x32_bf16 v[40:43], v[84:87], v[152:155], 0\n\tds_read_b128 v[80:83], %[pb0] offset:63488\n\t"
        "v_mfma_f32_16x16x32_bf16 v[44:47], v[84:87], v[156:159], 0\n\ts_waitcnt lgkmcnt(3)\n\tv_mfma_f32_16x16x32_bf16 v[48:51], v[88:91], v[152:155], 0\n\t"
        "ds_read_b128 v[84:87], %[pb0] offset:64512\n\tv_mfma_f32_16x16x32_bf16 v[52:55], v[88:91], v[156:159], 0\n\t"
        "v_mfma_f32_16x16x32_bf16 v[56:59], v[92:95], v[152:155], 0\n\tds_read_b128 v[88:91], %[pb1] offset:0\n\t"
        "v_mfma_f32_16x16x32_bf16 v[60:63], v[92:95], v[156:159], 0\n\tbuffer_load_dwordx4 v[188:191], off, %[rt], %[sa] offset:1024 sc1\n\t"
        "s_add_u32 %[sa], %[sa], 0x2000\n\tv_mfma_f32_16x16x32_bf16 v[64:67], v[96:99], v[152:155], 0\n\tds_read_b128 v[92:95], %[pb1] offset:1024\n\t"
        "v_mfma_f32_16x16x32_bf16 v[68:71], v[96:99], v[156:159], 0\n\ts_waitcnt vmcnt(9)\n\t"
        "v_mfma_f32_16x16x32_bf16 v[16:19], v[100:103], v[160:163], v[16:19]\n\tds_read_b128 v[96:99], %[pb1] offset:2048\n\t"
        "v_mfma_f32_16x16x32_bf16 v[20:23], v[100:103], v[164:167], v[20:23]\n\ts_waitcnt lgkmcnt(3)\n\t"
        "v_mfma_f32_16x16x32_bf16 v[24:27], v[72:75], v[160:163], v[24:27]\n\tds_read_b128 v[100:103], %[pb1] offset:3072\n\t"
        "v_mfma_f32_16x16x32_bf16 v[28:31], v[72:75], v[164:167], v[28:31]\n\tbuffer_load_dwordx4 v[192:195], off, %[rt], %[sa] offset:0 sc1\n\t"
        "v_mfma_f32_16x16x32_bf16 v[32:35], v[76:79], v[160:163], v[32:35]\n\tds_read_b128 v[72:75], %[pb1] offset:4096\n\t"
        "v_mfma_f32_16x16x32_bf16 v[36:39], v[76:79], v[164:167], v[36:39]\n\tv_mfma_f32_16x16x32_bf16 v[40:43], v[80:83], v[160:163], v[40:43]\n\t"
        "ds_read_b128 v[76:79], %[pb1] offset:5120\n\tv_mfma_f32_16x16x32_bf16 v[44:47], v[80:83], v[164:167], v[44:47]\n\t"
        "v_mfma_f32_16x16x32_bf16 v[48:51], v[84:87], v[160:163], v[48:51]\n\tds_read_b128 v[80:83], %[pb1] offset:6144\n\t"
        "v_mfma_f32_16x16x32_bf16 v[52:55], v[84:87], v[164:167], v[52:55]\n\tbuffer_load_dwordx4 v[196:199], off, %[rt], %[sa] offset:1024 sc1\n\t"
        "s_add_u32 %[sa], %[sa], 0x2000\n\ts_waitcnt lgkmcnt(3)\n\tv_mfma_f32_16x16x32_bf16 v[56:59], v[88:91], v[160:163], v[56:59]\n\t"
        "ds_read_b128 v[84:87], %[pb1] offset:7168\n\tv_mfma_f32_16x16x32_bf16 v[60:63], v[88:91], v[164:167], v[60:63]\n\t"
        "v_mfma_f32_16x16x32_bf16 v[64:67], v[92:95], v[160:163], v[64:67]\n\tds_read_b128 v[88:91], %[pb1] offset:8192\n\t"
        "v_mfma_f32_16x16x32_bf16 v[68:71], v[92:95], v[164:167], v[68:71]\n\ts_waitcnt vmcnt(9)\n\t"
        "v_mfma_f32_16x16x32_bf16 v[16:19], v[96:99], v[168:171], v[16:19]\n\tds_read_b128 v[92:95], %[pb1] offset:9216\n\t"
        "v_mfma_f32_16x16x32_bf16 v[20:23], v[96:99], v[172:175], v[20:23]\n\tbuffer_load_dwordx4 v[200:203], off, %[rt], %[sa] offset:0 sc1\n\t"
        "v_mfma_f32_16x16x32_bf16 v[24:27], v[100:103], v[168:171], v[24:27]\n\tds_read_b128 v[96:99], %[pb1] offset:10240\n\t"
        "v_mfma_f32_16x16x32_bf16 v[28:31], v[100:103], v[172:175], v[28:31]\n\ts_waitcnt lgkmcnt(3)\n\t"
        "v_mfma_f32_16x16x32_bf16 v[32:35], v[72:75], v[168:171], v[32:35]\n\tds_read_b128 v[100:103], %[pb1] offset:11264\n\t"
        "v_mfma_f32_16x16x32_bf16 v[36:39], v[72:75], v[172:175], v[36:39]\n\tv_mfma_f32_16x16x32_bf16 v[40:43], v[76:79], v[168:171], v[40:43]\n\t"
        "ds_read_b128 v[72:75], %[pb1] offset:12288\n\tv_mfma_f32_16x16x32_bf16 v[44:47], v[76:79], v[172:175], v[44:47]\n\t"
        "buffer_load_dwordx4 v[204:207], off, %[rt], %[sa] offset:1024 sc1\n\ts_add_u32 %[sa], %[sa], 0x2000\n\t"
        "v_mfma_f32_16x16x32_bf16 v[48:51], v[80:83], v[168:171], v[48:51]\n\tds_read_b128 v[76:79], %[pb1] offset:13312\n\t"
        "v_mfma_f32_16x16x32_bf16 v[52:55], v[80:83], v[172:175], v[52:55]\n\tv_mfma_f32_16x16x32_bf16 v[56:59], v[84:87], v[168:171], v[56:59]\n\t"
        "ds_read_b128 v[80:83], %[pb1] offset:14336\n\tv_mfma_f32_16x16x32_bf16 v[60:63], v[84:87], v[172:175], v[60:63]\n\ts_waitcnt lgkmcnt(3)\n\t"
        "v_mfma_f32_16x16x32_bf16 v[64:67], v[88:91], v[168:171], v[64:67]\n\tds_read_b128 v[84:87], %[pb1] offset:15360\n\t"
        "v_mfma_f32_16x16x32_bf16 v[68:71], v[88:91], v[172:175], v[68:71]\n\tbuffer_load_dwordx4 v[208:211], off, %[rt], %[sa] offset:0 sc1\n\t"
        "s_waitcnt vmcnt(10)\n\tv_mfma_f32_16x16x32_bf16 v[16:19], v[92:95], v[176:179], v[16:19]\n\ts_barrier\n\tds_read_b128 v[88:91], %[pb0] offset:0\n\t"
        "v_mfma_f32_16x16x32_bf16 v[20:23], v[92:95], v[180:183], v[20:23]\n\tv_mfma_f32_16x16x32_bf16 v[24:27], v[96:99], v[176:179], v[24:27]\n\t"
        "ds_read_b128 v[92:95], %[pb0] offset:1024\n\tv_mfma_f32_16x16x32_bf16 v[28:31], v[96:99], v[180:183], v[28:31]\n\t"
        "v_mfma_f32_16x16x32_bf16 v[32:35], v[100:103], v[176:179], v[32:35]\n\tds_read_b128 v[96:99], %[pb0] offset:2048\n\t"
        "v_mfma_f32_16x16x32_bf16 v[36:39], v[100:103], v[180:183], v[36:39]\n\tbuffer_load_dwordx4 v[212:215], off, %[rt], %[sa] offset:1024 sc1\n\t"
        "s_add_u32 %[sa], %[sa], 0x2000\n\ts_waitcnt lgkmcnt(3)\n\tv_mfma_f32_16x16x32_bf16 v[40:43], v[72:75], v[176:179], v[40:43]\n\t"
        "ds_read_b128 v[100:103], %[pb0] offset:3072\n\tv_mfma_f32_16x16x32_bf16 v[44:47], v[72:75], v[180:183], v[44:47]\n\t"
        "v_mfma_f32_16x16x32_bf16 v[48:51], v[76:79], v[176:179], v[48:51]\n\tds_read_b128 v[72:75], %[pb0] offset:4096\n\t"
        "v_mfma_f32_16x16x32_bf16 v[52:55], v[76:79], v[180:183], v[52:55]\n\tv_mfma_f32_16x16x32_bf16 v[56:59], v[80:83], v[176:179], v[56:59]\n\t"
        "ds_read_b128 v[76:79], %[pb0] offset:5120\n\tv_mfma_f32_16x16x32_bf16 v[60:63], v[80:83], v[180:183], v[60:63]\n\t"
        "buffer_load_dwordx4 v[216:219], off, %[rt], %[sa] offset:0 sc1\n\tv_mfma_f32_16x16x32_bf16 v[64:67], v[84:87], v[176:179], v[64:67]\n\t"
        "ds_read_b128 v[80:83], %[pb0] offset:6144\n\tv_mfma_f32_16x16x32_bf16 v[68:71], v[84:87], v[180:183], v[68:71]\n\ts_waitcnt vmcnt(7) lgkmcnt(3)\n\t"
        "v_mfma_f32_16x16x32_bf16 v[16:19], v[88:91], v[184:187], v[16:19]\n\tds_read_b128 v[84:87], %[pb0] offset:7168\n\t"
        "v_mfma_f32_16x16x32_bf16 v[20:23], v[88:91], v[188:191], v[20:23]\n\tv_mfma_f32_16x16x32_bf16 v[24:27], v[92:95], v[184:187], v[24:27]\n\t"
        "ds_read_b128 v[88:91], %[pb0] offset:8192\n\tv_mfma_f32_16x16x32_bf16 v[28:31], v[92:95], v[188:191], v[28:31]\n\t"
        "buffer_load_dwordx4 v[220:223], off, %[rt], %[sa] offset:1024 sc1\n\ts_add_u32 %[sa], %[sa], 0x2000\n\t"
        "v_mfma_f32_16x16x32_bf16 v[32:35], v[96:99], v[184:187], v[32:35]\n\tds_read_b128 v[92:95], %[pb0] offset:9216\n\t"
        "v_mfma_f32_16x16x32_bf16 v[36:39], v[96:99], v[188:191], v[36:39]\n\tv_mfma_f32_16x16x32_bf16 v[40:43], v[100:103], v[184:187], v[40:43]\n\t"
        "ds_read_b128 v[96:99], %[pb0] offset:10240\n\tv_mfma_f32_16x16x32_bf16 v[44:47], v[100:103], v[188:191], v[44:47]\n\ts_waitcnt lgkmcnt(3)\n\t"
        "v_mfma_f32_16x16x32_bf16 v[48:51], v[72:75], v[184:187], v[48:51]\n\tds_read_b128 v[100:103], %[pb0] offset:11264\n\t"
        "v_mfma_f32_16x16x32_bf16 v[52:55], v[72:75], v[188:191], v[52:55]\n\tbuffer_load_dwordx4 v[224:227], off, %[rt], %[sa] offset:0 sc1\n\t"
        "v_mfma_f32_16x16x32_bf16 v[56:59], v[76:79], v[184:187], v[56:59]\n\tds_read_b128 v[72:75], %[pb0] offset:12288\n\t"
        "v_mfma_f32_16x16x32_bf16 v[60:63], v[76:79], v[188:191], v[60:63]\n\tv_mfma_f32_16x16x32_bf16 v[64:67], v[80:83], v[184:187], v[64:67]\n\t"
        "ds_read_b128 v[76:79], %[pb0] offset:13312\n\tv_mfma_f32_16x16x32_bf16 v[68:71], v[80:83], v[188:191], v[68:71]\n\ts_waitcnt vmcnt(7)\n\t"
        "v_mfma_f32_16x16x32_bf16 v[16:19], v[84:87], v[192:195], v[16:19]\n\tds_read_b128 v[80:83], %[pb0] offset:14336\n\t"
        "v_mfma_f32_16x16x32_bf16 v[20:23], v[84:87], v[196:199], v[20:23]\n\tbuffer_load_dwordx4 v[228:231], off, %[rt], %[sa] offset:1024 sc1\n\t"
        "s_add_u32 %[sa], %[sa], 0x2000\n\ts_waitcnt lgkmcnt(3)\n\tv_mfma_f32_16x16x32_bf16 v[24:27], v[88:91], v[192:195], v[24:27]\n\t"
        "ds_read_b128 v[84:87], %[pb0] offset:15360\n\tv_mfma_f32_16x16x32_bf16 v[28:31], v[88:91], v[196:199], v[28:31]\n\t"
        "v_mfma_f32_16x16x32_bf16 v[32:35], v[92:95], v[192:195], v[32:35]\n\tds_read_b128 v[88:91], %[pb0] offset:16384\n\t"
        "v_mfma_f32_16x16x32_bf16 v[36:39], v[92:95], v[196:199], v[36:39]\n\tv_mfma_f32_16x16x32_bf16 v[40:43], v[96:99], v[192:195], v[40:43]\n\t"
        "ds_read_b128 v[92:95], %[pb0] offset:17408\n\tv_mfma_f32_16x16x32_bf16 v[44:47], v[96:99], v[196:199], v[44:47]\n\t"
        "buffer_load_dwordx4 v[232:235], off, %[rt], %[sa] offset:0 sc1\n\tv_mfma_f32_16x16x32_bf16 v[48:51], v[100:103], v[192:195], v[48:51]\n\t"
        "ds_read_b128 v[96:99], %[pb0] offset:18432\n\tv_mfma_f32_16x16x32_bf16 v[52:55], v[100:103], v[196:199], v[52:55]\n\ts_waitcnt lgkmcnt(3)\n\t"
        "v_mfma_f32_16x16x32_bf16 v[56:59], v[72:75], v[192:195], v[56:59]\n\tds_read_b128 v[100:103], %[pb0] offset:19456\n\t"
        "v_mfma_f32_16x16x32_bf16 v[60:63], v[72:75], v[196:199], v[60:63]\n\ts_waitcnt vmcnt(13)\n\tbuffer_store_dwordx2 %[one], off, %[rs], %[fl] sc1\n\t"
        "v_mfma_f32_16x16x32_bf16 v[64:67], v[76:79], v[192:195], v[64:67]\n\tds_read_b128 v[72:75], %[pb0] offset:20480\n\t"
        "v_mfma_f32_16x16x32_bf16 v[68:71], v[76:79], v[196:199], v[68:71]\n\tbuffer_load_dwordx4 v[236:239], off, %[rt], %[sa] offset:1024 sc1\n\t"
        "s_add_u32 %[sa], %[sa], 0x2000\n\ts_waitcnt vmcnt(9)\n\tv_mfma_f32_16x16x32_bf16 v[16:19], v[80:83], v[200:203], v[16:19]\n\t"
        "ds_read_b128 v[76:79], %[pb0] offset:21504\n\tv_mfma_f32_16x16x32_bf16 v[20:23], v[80:83], v[204:207], v[20:23]\n\t"
        "v_mfma_f32_16x16x32_bf16 v[24:27], v[84:87], v[200:203], v[24:27]\n\tds_read_b128 v[80:83], %[pb0] offset:22528\n\t"
        "v_mfma_f32_16x16x32_bf16 v[28:31], v[84:87], v[204:207], v[28:31]\n\ts_waitcnt lgkmcnt(3)\n\t"
        "v_mfma_f32_16x16x32_bf16 v[32:35], v[88:91], v[200:203], v[32:35]\n\tds_read_b128 v[84:87], %[pb0] offset:23552\n\t"
        "v_mfma_f32_16x16x32_bf16 v[36:39], v[88:91], v[204:207], v[36:39]\n\tbuffer_load_dwordx4 v[240:243], off, %[rt], %[sa] offset:0 sc1\n\t"
        "v_mfma_f32_16x16x32_bf16 v[40:43], v[92:95], v[200:203], v[40:43]\n\tds_read_b128 v[88:91], %[pb0] offset:24576\n\t"
        "v_mfma_f32_16x16x32_bf16 v[44:47], v[92:95], v[204:207], v[44:47]\n\tv_mfma_f32_16x16x32_bf16 v[48:51], v[96:99], v[200:203], v[48:51]\n\t"
        "ds_read_b128 v[92:95], %[pb0] offset:25600\n\tv_mfma_f32_16x16x32_bf16 v[52:55], v[96:99], v[204:207], v[52:55]\n\t"
        "v_mfma_f32_16x16x32_bf16 v[56:59], v[100:103], v[200:203], v[56:59]\n\tds_read_b128 v[96:99], %[pb0] offset:26624\n\t"
        "v_mfma_f32_16x16x32_bf16 v[60:63], v[100:103], v[204:207], v[60:63]\n\tbuffer_load_dwordx4 v[244:247], off, %[rt], %[sa] offset:1024 sc1\n\t"
        "s_add_u32 %[sa], %[sa], 0x2000\n\ts_waitcnt lgkmcnt(3)\n\tv_mfma_f32_16x16x32_bf16 v[64:67], v[72:75], v[200:203], v[64:67]\n\t"
        "ds_read_b128 v[100:103], %[pb0] offset:27648\n\tv_mfma_f32_16x16x32_bf16 v[68:71], v[72:75], v[204:207], v[68:71]\n\ts_waitcnt vmcnt(9)\n\t"
        "v_mfma_f32_16x16x32_bf16 v[16:19], v[76:79], v[208:211], v[16:19]\n\tds_read_b128 v[72:75], %[pb0] offset:28672\n\t"
        "v_mfma_f32_16x16x32_bf16 v[20:23], v[76:79], v[212:215], v[20:23]\n\tv_mfma_f32_16x16x32_bf16 v[24:27], v[80:83], v[208:211], v[24:27]\n\t"
        "ds_read_b128 v[76:79], %[pb0] offset:29696\n\tv_mfma_f32_16x16x32_bf16 v[28:31], v[80:83], v[212:215], v[28:31]\n\t"
        "buffer_load_dwordx4 v[152:155], off, %[rt], %[sa] offset:0 sc1\n\tv_mfma_f32_16x16x32_bf16 v[32:35], v[84:87], v[208:211], v[32:35]\n\t"
        "ds_read_b128 v[80:83], %[pb0] offset:30720\n\tv_mfma_f32_16x16x32_bf16 v[36:39], v[84:87], v[212:215], v[36:39]\n\ts_waitcnt lgkmcnt(3)\n\t"
        "v_mfma_f32_16x16x32_bf16 v[40:43], v[88:91], v[208:211], v[40:43]\n\tds_read_b128 v[84:87], %[pb0] offset:31744\n\t"
        "v_mfma_f32_16x16x32_bf16 v[44:47], v[88:91], v[212:215], v[44:47]\n\tv_mfma_f32_16x16x32_bf16 v[48:51], v[92:95], v[208:211], v[48:51]\n\t"
        "ds_read_b128 v[88:91], %[pb0] offset:32768\n\tv_mfma_f32_16x16x32_bf16 v[52:55], v[92:95], v[212:215], v[52:55]\n\t"
        "buffer_load_dwordx4 v[156:159], off, %[rt], %[sa] offset:1024 sc1\n\ts_add_u32 %[sa], %[sa], 0x2000\n\t"
        "v_mfma_f32_16x16x32_bf16 v[56:59], v[96:99], v[208:211], v[56:59]\n\tds_read_b128 v[92:95], %[pb0] offset:33792\n\t"
        "v_mfma_f32_16x16x32_bf16 v[60:63], v[96:99], v[212:215], v[60:63]\n\tv_mfma_f32_16x16x32_bf16 v[64:67], v[100:103], v[208:211], v[64:67]\n\t"
        "ds_read_b128 v[96:99], %[pb2] offset:8192\n\tv_mfma_f32_16x16x32_bf16 v[68:71], v[100:103], v[212:215], v[68:71]\n\ts_waitcnt vmcnt(9) lgkmcnt(3)\n\t"
        "v_mfma_f32_16x16x32_bf16 v[16:19], v[72:75], v[216:219], v[16:19]\n\tds_read_b128 v[100:103], %[pb0] offset:34816\n\t"
        "v_mfma_f32_16x16x32_bf16 v[20:23], v[72:75], v[220:223], v[20:23]\n\tbuffer_load_dwordx4 v[160:163], off, %[rt], %[sa] offset:0 sc1\n\t"
        "v_mfma_f32_16x16x32_bf16 v[24:27], v[76:79], v[216:219], v[24:27]\n\tds_read_b128 v[72:75], %[pb0] offset:35840\n\t"
        "v_mfma_f32_16x16x32_bf16 v[28:31], v[76:79], v[220:223], v[28:31]\n\tv_mfma_f32_16x16x32_bf16 v[32:35], v[80:83], v[216:219], v[32:35]\n\t"
        "ds_read_b128 v[76:79], %[pb0] offset:36864\n\tv_mfma_f32_16x16x32_bf16 v[36:39], v[80:83], v[220:223], v[36:39]\n\t"
        "v_mfma_f32_16x16x32_bf16 v[40:43], v[84:87], v[216:219], v[40:43]\n\tds_read_b128 v[80:83], %[pb0] offset:37888\n\t"
        "v_mfma_f32_16x16x32_bf16 v[44:47], v[84:87], v[220:223], v[44:47]\n\tbuffer_load_dwordx4 v[164:167], off, %[rt], %[sa] offset:1024 sc1\n\t"
        "s_add_u32 %[sa], %[sa], 0x2000\n\ts_waitcnt lgkmcnt(3)\n\tv_mfma_f32_16x16x32_bf16 v[48:51], v[88:91], v[216:219], v[48:51]\n\t"
        "ds_read_b128 v[84:87], %[pb0] offset:38912\n\tv_mfma_f32_16x16x32_bf16 v[52:55], v[88:91], v[220:223], v[52:55]\n\t"
        "v_mfma_f32_16x16x32_bf16 v[56:59], v[92:95], v[216:219], v[56:59]\n\tds_read_b128 v[88:91], %[pb0] offset:39936\n\t"
        "v_mfma_f32_16x16x32_bf16 v[60:63], v[92:95], v[220:223], v[60:63]\n\tv_mfma_f32_16x16x32_bf16 v[64:67], v[96:99], v[216:219], v[64:67]\n\t"
        "ds_read_b128 v[92:95], %[pb2] offset:9216\n\tv_mfma_f32_16x16x32_bf16 v[68:71], v[96:99], v[220:223], v[68:71]\n\t"
        "buffer_load_dwordx4 v[168:171], off, %[rt], %[sa] offset:0 sc1\n\ts_waitcnt vmcnt(10)\n\t"
        "v_mfma_f32_16x16x32_bf16 v[16:19], v[100:103], v[224:227], v[16:19]\n\tds_read_b128 v[96:99], %[pb0] offset:40960\n\t"
        "v_mfma_f32_16x16x32_bf16 v[20:23], v[100:103], v[228:231], v[20:23]\n\ts_waitcnt lgkmcnt(3)\n\t"
        "v_mfma_f32_16x16x32_bf16 v[24:27], v[72:75], v[224:227], v[24:27]\n\tds_read_b128 v[100:103], %[pb0] offset:41984\n\t"
        "v_mfma_f32_16x16x32_bf16 v[28:31], v[72:75], v[228:231], v[28:31]\n\tv_mfma_f32_16x16x32_bf16 v[32:35], v[76:79], v[224:227], v[32:35]\n\t"
        "ds_read_b128 v[72:75], %[pb0] offset:43008\n\tv_mfma_f32_16x16x32_bf16 v[36:39], v[76:79], v[228:231], v[36:39]\n\t"
        "buffer_load_dwordx4 v[172:175], off, %[rt], %[sa] offset:1024 sc1\n\ts_add_u32 %[sa], %[sa], 0x2000\n\t"
        "v_mfma_f32_16x16x32_bf16 v[40:43], v[80:83], v[224:227], v[40:43]\n\tds_read_b128 v[76:79], %[pb0] offset:44032\n\t"
        "v_mfma_f32_16x16x32_bf16 v[44:47], v[80:83], v[228:231], v[44:47]\n\tv_mfma_f32_16x16x32_bf16 v[48:51], v[84:87], v[224:227], v[48:51]\n\t"
        "ds_read_b128 v[80:83], %[pb0] offset:45056\n\tv_mfma_f32_16x16x32_bf16 v[52:55], v[84:87], v[228:231], v[52:55]\n\ts_waitcnt lgkmcnt(3)\n\t"
        "v_mfma_f32_16x16x32_bf16 v[56:59], v[88:91], v[224:227], v[56:59]\n\tds_read_b128 v[84:87], %[pb0] offset:46080\n\t"
        "v_mfma_f32_16x16x32_bf16 v[60:63], v[88:91], v[228:231], v[60:63]\n\tbuffer_load_dwordx4 v[176:179], off, %[rt], %[sa] offset:0 sc1\n\t"
        "v_mfma_f32_16x16x32_bf16 v[64:67], v[92:95], v[224:227], v[64:67]\n\tds_read_b128 v[88:91], %[pb2] offset:10240\n\t"
        "v_mfma_f32_16x16x32_bf16 v[68:71], v[92:95], v[228:231], v[68:71]\n\ts_waitcnt vmcnt(9)\n\t"
        "v_mfma_f32_16x16x32_bf16 v[16:19], v[96:99], v[232:235], v[16:19]\n\tds_read_b128 v[92:95], %[pb0] offset:47104\n\t"
        "v_mfma_f32_16x16x32_bf16 v[20:23], v[96:99], v[236:239], v[20:23]\n\tv_mfma_f32_16x16x32_bf16 v[24:27], v[100:103], v[232:235], v[24:27]\n\t"
        "ds_read_b128 v[96:99], %[pb0] offset:48128\n\tv_mfma_f32_16x16x32_bf16 v[28:31], v[100:103], v[236:239], v[28:31]\n\t"
        "buffer_load_dwordx4 v[180:183], off, %[rt], %[sa] offset:1024 sc1\n\ts_add_u32 %[sa], %[sa], 0x2000\n\ts_waitcnt lgkmcnt(3)\n\t"
        "v_mfma_f32_16x16x32_bf16 v[32:35], v[72:75], v[232:235], v[32:35]\n\tds_read_b128 v[100:103], %[pb0] offset:49152\n\t"
        "v_mfma_f32_16x16x32_bf16 v[36:39], v[72:75], v[236:239], v[36:39]\n\tv_mfma_f32_16x16x32_bf16 v[40:43], v[76:79], v[232:235], v[40:43]\n\t"
        "ds_read_b128 v[72:75], %[pb0] offset:50176\n\tv_mfma_f32_16x16x32_bf16 v[44:47], v[76:79], v[236:239], v[44:47]\n\t"
        "v_mfma_f32_16x16x32_bf16 v[48:51], v[80:83], v[232:235], v[48:51]\n\tds_read_b128 v[76:79], %[pb0] offset:51200\n\t"
        "v_mfma_f32_16x16x32_bf16 v[52:55], v[80:83], v[236:239], v[52:55]\n\tbuffer_load_dwordx4 v[184:187], off, %[rt], %[sa] offset:0 sc1\n\t"
        "v_mfma_f32_16x16x32_bf16 v[56:59], v[84:87], v[232:235], v[56:59]\n\tds_read_b128 v[80:83], %[pb0] offset:52224\n\t"
        "v_mfma_f32_16x16x32_bf16 v[60:63], v[84:87], v[236:239], v[60:63]\n\ts_waitcnt lgkmcnt(3)\n\t"
        "v_mfma_f32_16x16x32_bf16 v[64:67], v[88:91], v[232:235], v[64:67]\n\tds_read_b128 v[84:87], %[pb2] offset:11264\n\t"
        "v_mfma_f32_16x16x32_bf16 v[68:71], v[88:91], v[236:239], v[68:71]\n\ts_waitcnt vmcnt(9)\n\t"
        "v_mfma_f32_16x16x32_bf16 v[16:19], v[92:95], v[240:243], v[16:19]\n\ts_barrier\n\tds_read_b128 v[88:91], %[pb1] offset:16384\n\t"
        "v_mfma_f32_16x16x32_bf16 v[20:23], v[92:95], v[244:247], v[20:23]\n\tbuffer_load_dwordx4 v[188:191], off, %[rt], %[sa] offset:1024 sc1\n\t"
        "s_add_u32 %[sa], %[sa], 0x2000\n\tv_mfma_f32_16x16x32_bf16 v[24:27], v[96:99], v[240:243], v[24:27]\n\tds_read_b128 v[92:95], %[pb1] offset:17408\n\t"
        "v_mfma_f32_16x16x32_bf16 v[28:31], v[96:99], v[244:247], v[28:31]\n\tv_mfma_f32_16x16x32_bf16 v[32:35], v[100:103], v[240:243], v[32:35]\n\t"
        "ds_read_b128 v[96:99], %[pb1] offset:18432\n\tv_mfma_f32_16x16x32_bf16 v[36:39], v[100:103], v[244:247], v[36:39]\n\ts_waitcnt lgkmcnt(3)\n\t"
        "v_mfma_f32_16x16x32_bf16 v[40:43], v[72:75], v[240:243], v[40:43]\n\tds_read_b128 v[100:103], %[pb1] offset:19456\n\t"
        "v_mfma_f32_16x16x32_bf16 v[44:47], v[72:75], v[244:247], v[44:47]\n\tbuffer_load_dwordx4 v[192:195], off, %[rt], %[sa] offset:0 sc1\n\t"
        "v_mfma_f32_16x16x32_bf16 v[48:51], v[76:79], v[240:243], v[48:51]\n\tds_read_b128 v[72:75], %[pb1] offset:20480\n\t"
        "v_mfma_f32_16x16x32_bf16 v[52:55], v[76:79], v[244:247], v[52:55]\n\tv_mfma_f32_16x16x32_bf16 v[56:59], v[80:83], v[240:243], v[56:59]\n\t"
        "ds_read_b128 v[76:79], %[pb1] offset:21504\n\tv_mfma_f32_16x16x32_bf16 v[60:63], v[80:83], v[244:247], v[60:63]\n\t"
        "v_mfma_f32_16x16x32_bf16 v[64:67], v[84:87], v[240:243], v[64:67]\n\tds_read_b128 v[80:83], %[pb1] offset:22528\n\t"
        "v_mfma_f32_16x16x32_bf16 v[68:71], v[84:87], v[244:247], v[68:71]\n\tbuffer_load_dwordx4 v[196:199], off, %[rt], %[sa] offset:1024 sc1\n\t"
        "s_add_u32 %[sa], %[sa], 0x2000\n\ts_waitcnt vmcnt(10) lgkmcnt(3)\n\tv_mfma_f32_16x16x32_bf16 v[16:19], v[88:91], v[152:155], v[16:19]\n\t"
        "ds_read_b128 v[84:87], %[pb1] offset:23552\n\tv_mfma_f32_16x16x32_bf16 v[20:23], v[88:91], v[156:159], v[20:23]\n\t"
        "v_mfma_f32_16x16x32_bf16 v[24:27], v[92:95], v[152:155], v[24:27]\n\tds_read_b128 v[88:91], %[pb1] offset:24576\n\t"
        "v_mfma_f32_16x16x32_bf16 v[28:31], v[92:95], v[156:159], v[28:31]\n\tv_mfma_f32_16x16x32_bf16 v[32:35], v[96:99], v[152:155], v[32:35]\n\t"
        "ds_read_b128 v[92:95], %[pb1] offset:25600\n\tv_mfma_f32_16x16x32_bf16 v[36:39], v[96:99], v[156:159], v[36:39]\n\t"
        "buffer_load_dwordx4 v[200:203], off, %[rt], %[sa] offset:0 sc1\n\tv_mfma_f32_16x16x32_bf16 v[40:43], v[100:103], v[152:155], v[40:43]\n\t"
        "ds_read_b128 v[96:99], %[pb1] offset:26624\n\tv_mfma_f32_16x16x32_bf16 v[44:47], v[100:103], v[156:159], v[44:47]\n\ts_waitcnt lgkmcnt(3)\n\t"
        "v_mfma_f32_16x16x32_bf16 v[48:51], v[72:75], v[152:155], v[48:51]\n\tds_read_b128 v[100:103], %[pb1] offset:27648\n\t"
        "v_mfma_f32_16x16x32_bf16 v[52:55], v[72:75], v[156:159], v[52:55]\n\tv_mfma_f32_16x16x32_bf16 v[56:59], v[76:79], v[152:155], v[56:59]\n\t"
        "ds_read_b128 v[72:75], %[pb1] offset:28672\n\tv_mfma_f32_16x16x32_bf16 v[60:63], v[76:79], v[156:159], v[60:63]\n\t"
        "buffer_load_dwordx4 v[204:207], off, %[rt], %[sa] offset:1024 sc1\n\ts_add_u32 %[sa], %[sa], 0x2000\n\t"
        "v_mfma_f32_16x16x32_bf16 v[64:67], v[80:83], v[152:155], v[64:67]\n\tds_read_b128 v[76:79], %[pb1] offset:29696\n\t"
        "v_mfma_f32_16x16x32_bf16 v[68:71], v[80:83], v[156:159], v[68:71]\n\ts_waitcnt vmcnt(10)\n\t"
        "v_mfma_f32_16x16x32_bf16 v[16:19], v[84:87], v[160:163], v[16:19]\n\tds_read_b128 v[80:83], %[pb1] offset:30720\n\t"
        "v_mfma_f32_16x16x32_bf16 v[20:23], v[84:87], v[164:167], v[20:23]\n\ts_waitcnt lgkmcnt(3)\n\t"
        "v_mfma_f32_16x16x32_bf16 v[24:27], v[88:91], v[160:163], v[24:27]\n\tds_read_b128 v[84:87], %[pb1] offset:31744\n\t"
        "v_mfma_f32_16x16x32_bf16 v[28:31], v[88:91], v[164:167], v[28:31]\n\tbuffer_load_dwordx4 v[208:211], off, %[rt], %[sa] offset:0 sc1\n\t"
        "v_mfma_f32_16x16x32_bf16 v[32:35], v[92:95], v[160:163], v[32:35]\n\tds_read_b128 v[88:91], %[pb1] offset:32768\n\t"
        "v_mfma_f32_16x16x32_bf16 v[36:39], v[92:95], v[164:167], v[36:39]\n\tv_mfma_f32_16x16x32_bf16 v[40:43], v[96:99], v[160:163], v[40:43]\n\t"
        "ds_read_b128 v[92:95], %[pb1] offset:33792\n\tv_mfma_f32_16x16x32_bf16 v[44:47], v[96:99], v[164:167], v[44:47]\n\t"
        "v_mfma_f32_16x16x32_bf16 v[48:51], v[100:103], v[160:163], v[48:51]\n\tds_read_b128 v[96:99], %[pb1] offset:34816\n\t"
        "v_mfma_f32_16x16x32_bf16 v[52:55], v[100:103], v[164:167], v[52:55]\n\tbuffer_load_dwordx4 v[212:215], off, %[rt], %[sa] offset:1024 sc1\n\t"
        "s_add_u32 %[sa], %[sa], 0x2000\n\ts_waitcnt lgkmcnt(3)\n\tv_mfma_f32_16x16x32_bf16 v[56:59], v[72:75], v[160:163], v[56:59]\n\t"
        "ds_read_b128 v[100:103], %[pb1] offset:35840\n\tv_mfma_f32_16x16x32_bf16 v[60:63], v[72:75], v[164:167], v[60:63]\n\t"
        "v_mfma_f32_16x16x32_bf16 v[64:67], v[76:79], v[160:163], v[64:67]\n\tds_read_b128 v[72:75], %[pb1] offset:36864\n\t"
        "v_mfma_f32_16x16x32_bf16 v[68:71], v[76:79], v[164:167], v[68:71]\n\ts_waitcnt vmcnt(10)\n\t"
        "v_mfma_f32_16x16x32_bf16 v[16:19], v[80:83], v[168:171], v[16:19]\n\tds_read_b128 v[76:79], %[pb1] offset:37888\n\t"
        "v_mfma_f32_16x16x32_bf16 v[20:23], v[80:83], v[172:175], v[20:23]\n\tbuffer_load_dwordx4 v[216:219], off, %[rt], %[sa] offset:0 sc1\n\t"
        "v_mfma_f32_16x16x32_bf16 v[24:27], v[84:87], v[168:171], v[24:27]\n\tds_read_b128 v[80:83], %[pb1] offset:38912\n\t"
        "v_mfma_f32_16x16x32_bf16 v[28:31], v[84:87], v[172:175], v[28:31]\n\ts_waitcnt lgkmcnt(3)\n\t"
        "v_mfma_f32_16x16x32_bf16 v[32:35], v[88:91], v[168:171], v[32:35]\n\tds_read_b128 v[84:87], %[pb1] offset:39936\n\t"
        "v_mfma_f32_16x16x32_bf16 v[36:39], v[88:91], v[172:175], v[36:39]\n\tv_mfma_f32_16x16x32_bf16 v[40:43], v[92:95], v[168:171], v[40:43]\n\t"
        "ds_read_b128 v[88:91], %[pb1] offset:40960\n\tv_mfma_f32_16x16x32_bf16 v[44:47], v[92:95], v[172:175], v[44:47]\n\t"
        "buffer_load_dwordx4 v[220:223], off, %[rt], %[sa] offset:1024 sc1\n\ts_add_u32 %[sa], %[sa], 0x2000\n\t"
        "v_mfma_f32_16x16x32_bf16 v[48:51], v[96:99], v[168:171], v[48:51]\n\tds_read_b128 v[92:95], %[pb1] offset:41984\n\t"
        "v_mfma_f32_16x16x32_bf16 v[52:55], v[96:99], v[172:175], v[52:55]\n\tv_mfma_f32_16x16x32_bf16 v[56:59], v[100:103], v[168:171], v[56:59]\n\t"
        "ds_read_b128 v[96:99], %[pb1] offset:43008\n\tv_mfma_f32_16x16x32_bf16 v[60:63], v[100:103], v[172:175], v[60:63]\n\ts_waitcnt lgkmcnt(3)\n\t"
        "v_mfma_f32_16x16x32_bf16 v[64:67], v[72:75], v[168:171], v[64:67]\n\tds_read_b128 v[100:103], %[pb1] offset:44032\n\t"
        "v_mfma_f32_16x16x32_bf16 v[68:71], v[72:75], v[172:175], v[68:71]\n\tbuffer_load_dwordx4 v[224:227], off, %[rt], %[sa] offset:0 sc1\n\t"
        "s_waitcnt vmcnt(11)\n\tv_mfma_f32_16x16x32_bf16 v[16:19], v[76:79], v[176:179], v[16:19]\n\tds_read_b128 v[72:75], %[pb1] offset:45056\n\t"
        "v_mfma_f32_16x16x32_bf16 v[20:23], v[76:79], v[180:183], v[20:23]\n\tv_mfma_f32_16x16x32_bf16 v[24:27], v[80:83], v[176:179], v[24:27]\n\t"
        "ds_read_b128 v[76:79], %[pb1] offset:46080\n\tv_mfma_f32_16x16x32_bf16 v[28:31], v[80:83], v[180:183], v[28:31]\n\t"
        "v_mfma_f32_16x16x32_bf16 v[32:35], v[84:87], v[176:179], v[32:35]\n\tds_read_b128 v[80:83], %[pb1] offset:47104\n\t"
        "v_mfma_f32_16x16x32_bf16 v[36:39], v[84:87], v[180:183], v[36:39]\n\tbuffer_load_dwordx4 v[228:231], off, %[rt], %[sa] offset:1024 sc1\n\t"
        "s_add_u32 %[sa], %[sa], 0x2000\n\ts_waitcnt lgkmcnt(3)\n\tv_mfma_f32_16x16x32_bf16 v[40:43], v[88:91], v[176:179], v[40:43]\n\t"
        "ds_read_b128 v[84:87], %[pb1] offset:48128\n\tv_mfma_f32_16x16x32_bf16 v[44:47], v[88:91], v[180:183], v[44:47]\n\t"
        "v_mfma_f32_16x16x32_bf16 v[48:51], v[92:95], v[176:179], v[48:51]\n\tds_read_b128 v[88:91], %[pb1] offset:49152\n\t"
        "v_mfma_f32_16x16x32_bf16 v[52:55], v[92:95], v[180:183], v[52:55]\n\tv_mfma_f32_16x16x32_bf16 v[56:59], v[96:99], v[176:179], v[56:59]\n\t"
        "ds_read_b128 v[92:95], %[pb1] offset:50176\n\tv_mfma_f32_16x16x32_bf16 v[60:63], v[96:99], v[180:183], v[60:63]\n\t"
        "buffer_load_dwordx4 v[232:235], off, %[rt], %[sa] offset:0 sc1\n\tv_mfma_f32_16x16x32_bf16 v[64:67], v[100:103], v[176:179], v[64:67]\n\t"
        "ds_read_b128 v[96:99], %[pb1] offset:51200\n\tv_mfma_f32_16x16x32_bf16 v[68:71], v[100:103], v[180:183], v[68:71]\n\t"
        "s_waitcnt vmcnt(11) lgkmcnt(3)\n\tv_mfma_f32_16x16x32_bf16 v[16:19], v[72:75], v[184:187], v[16:19]\n\t"
        "ds_read_b128 v[100:103], %[pb1] offset:52224\n\tv_mfma_f32_16x16x32_bf16 v[20:23], v[72:75], v[188:191], v[20:23]\n\t"
        "v_mfma_f32_16x16x32_bf16 v[24:27], v[76:79], v[184:187], v[24:27]\n\tds_read_b128 v[72:75], %[pb1] offset:53248\n\t"
        "v_mfma_f32_16x16x32_bf16 v[28:31], v[76:79], v[188:191], v[28:31]\n\tbuffer_load_dwordx4 v[236:239], off, %[rt], %[sa] offset:1024 sc1\n\t"
        "s_add_u32 %[sa], %[sa], 0x2000\n\tv_mfma_f32_16x16x32_bf16 v[32:35], v[80:83], v[184:187], v[32:35]\n\tds_read_b128 v[76:79], %[pb1] offset:54272\n\t"
        "v_mfma_f32_16x16x32_bf16 v[36:39], v[80:83], v[188:191], v[36:39]\n\tv_mfma_f32_16x16x32_bf16 v[40:43], v[84:87], v[184:187], v[40:43]\n\t"
        "ds_read_b128 v[80:83], %[pb1] offset:55296\n\tv_mfma_f32_16x16x32_bf16 v[44:47], v[84:87], v[188:191], v[44:47]\n\ts_waitcnt lgkmcnt(3)\n\t"
        "v_mfma_f32_16x16x32_bf16 v[48:51], v[88:91], v[184:187], v[48:51]\n\tds_read_b128 v[84:87], %[pb1] offset:56320\n\t"
        "v_mfma_f32_16x16x32_bf16 v[52:55], v[88:91], v[188:191], v[52:55]\n\tbuffer_load_dwordx4 v[240:243], off, %[rt], %[sa] offset:0 sc1\n\t"
        "v_mfma_f32_16x16x32_bf16 v[56:59], v[92:95], v[184:187], v[56:59]\n\tds_read_b128 v[88:91], %[pb1] offset:57344\n\t"
        "v_mfma_f32_16x16x32_bf16 v[60:63], v[92:95], v[188:191], v[60:63]\n\tv_mfma_f32_16x16x32_bf16 v[64:67], v[96:99], v[184:187], v[64:67]\n\t"
        "ds_read_b128 v[92:95], %[pb1] offset:58368\n\tv_mfma_f32_16x16x32_bf16 v[68:71], v[96:99], v[188:191], v[68:71]\n\ts_waitcnt vmcnt(11)\n\t"
        "v_mfma_f32_16x16x32_bf16 v[16:19], v[100:103], v[192:195], v[16:19]\n\tds_read_b128 v[96:99], %[pb1] offset:59392\n\t"
        "v_mfma_f32_16x16x32_bf16 v[20:23], v[100:103], v[196:199], v[20:23]\n\tbuffer_load_dwordx4 v[244:247], off, %[rt], %[sa] offset:1024 sc1\n\t"
        "s_add_u32 %[sa], %[sa], 0x2000\n\ts_waitcnt lgkmcnt(3)\n\tv_mfma_f32_16x16x32_bf16 v[24:27], v[72:75], v[192:195], v[24:27]\n\t"
        "ds_read_b128 v[100:103], %[pb1] offset:60416\n\tv_mfma_f32_16x16x32_bf16 v[28:31], v[72:75], v[196:199], v[28:31]\n\t"
        "v_mfma_f32_16x16x32_bf16 v[32:35], v[76:79], v[192:195], v[32:35]\n\tds_read_b128 v[72:75], %[pb1] offset:61440\n\t"
        "v_mfma_f32_16x16x32_bf16 v[36:39], v[76:79], v[196:199], v[36:39]\n\tv_mfma_f32_16x16x32_bf16 v[40:43], v[80:83], v[192:195], v[40:43]\n\t"
        "ds_read_b128 v[76:79], %[pb1] offset:62464\n\tv_mfma_f32_16x16x32_bf16 v[44:47], v[80:83], v[196:199], v[44:47]\n\t"
        "buffer_load_dwordx4 v[152:155], off, %[rt], %[sa] offset:0 sc1\n\tv_mfma_f32_16x16x32_bf16 v[48:51], v[84:87], v[192:195], v[48:51]\n\t"
        "ds_read_b128 v[80:83], %[pb1] offset:63488\n\tv_mfma_f32_16x16x32_bf16 v[52:55], v[84:87], v[196:199], v[52:55]\n\ts_waitcnt lgkmcnt(3)\n\t"
        ".p2align 12,,12\n\t.Lpg10:\n\ts_cbranch_execz .Lpgret\n\tv_mfma_f32_16x16x32_bf16 v[56:59], v[88:91], v[192:195], v[56:59]\n\t"
        "ds_read_b128 v[84:87], %[pb1] offset:64512\n\tv_mfma_f32_16x16x32_bf16 v[60:63], v[88:91], v[196:199], v[60:63]\n\t"
        "v_mfma_f32_16x16x32_bf16 v[64:67], v[92:95], v[192:195], v[64:67]\n\tds_read_b128 v[88:91], %[pb2] offset:0\n\t"
        "v_mfma_f32_16x16x32_bf16 v[68:71], v[92:95], v[196:199], v[68:71]\n\tbuffer_load_dwordx4 v[156:159], off, %[rt], %[sa] offset:1024 sc1\n\t"
        "s_add_u32 %[sa], %[sa], 0x2000\n\ts_waitcnt vmcnt(12)\n\tv_mfma_f32_16x16x32_bf16 v[16:19], v[96:99], v[200:203], v[16:19]\n\t"
        "ds_read_b128 v[92:95], %[pb2] offset:1024\n\tv_mfma_f32_16x16x32_bf16 v[20:23], v[96:99], v[204:207], v[20:23]\n\t"
        "v_mfma_f32_16x16x32_bf16 v[24:27], v[100:103], v[200:203], v[24:27]\n\tds_read_b128 v[96:99], %[pb2] offset:2048\n\t"
        "v_mfma_f32_16x16x32_bf16 v[28:31], v[100:103], v[204:207], v[28:31]\n\ts_waitcnt lgkmcnt(3)\n\t"
        "v_mfma_f32_16x16x32_bf16 v[32:35], v[72:75], v[200:203], v[32:35]\n\tds_read_b128 v[100:103], %[pb2] offset:3072\n\t"
        "v_mfma_f32_16x16x32_bf16 v[36:39], v[72:75], v[204:207], v[36:39]\n\tbuffer_load_dwordx4 v[160:163], off, %[rt], %[sa] offset:0 sc1\n\t"
        "v_mfma_f32_16x16x32_bf16 v[40:43], v[76:79], v[200:203], v[40:43]\n\tds_read_b128 v[72:75], %[pb2] offset:4096\n\t"
        "v_mfma_f32_16x16x32_bf16 v[44:47], v[76:79], v[204:207], v[44:47]\n\tv_mfma_f32_16x16x32_bf16 v[48:51], v[80:83], v[200:203], v[48:51]\n\t"
        "ds_read_b128 v[76:79], %[pb2] offset:5120\n\tv_mfma_f32_16x16x32_bf16 v[52:55], v[80:83], v[204:207], v[52:55]\n\t"
        "v_mfma_f32_16x16x32_bf16 v[56:59], v[84:87], v[200:203], v[56:59]\n\tds_read_b128 v[80:83], %[pb2] offset:6144\n\t"
        "v_mfma_f32_16x16x32_bf16 v[60:63], v[84:87], v[204:207], v[60:63]\n\tbuffer_load_dwordx4 v[164:167], off, %[rt], %[sa] offset:1024 sc1\n\t"
        "s_add_u32 %[sa], %[sa], 0x2000\n\ts_waitcnt lgkmcnt(3)\n\tv_mfma_f32_16x16x32_bf16 v[64:67], v[88:91], v[200:203], v[64:67]\n\t"
        "ds_read_b128 v[84:87], %[pb2] offset:7168\n\tv_mfma_f32_16x16x32_bf16 v[68:71], v[88:91], v[204:207], v[68:71]\n\ts_waitcnt vmcnt(12)\n\t"
        "v_mfma_f32_16x16x32_bf16 v[16:19], v[92:95], v[208:211], v[16:19]\n\ts_barrier\n\tds_read_b128 v[88:91], %[pb0] offset:53248\n\t"
        "v_mfma_f32_16x16x32_bf16 v[20:23], v[92:95], v[212:215], v[20:23]\n\tv_mfma_f32_16x16x32_bf16 v[24:27], v[96:99], v[208:211], v[24:27]\n\t"
        "ds_read_b128 v[92:95], %[pb0] offset:54272\n\tv_mfma_f32_16x16x32_bf16 v[28:31], v[96:99], v[212:215], v[28:31]\n\t"
        "buffer_load_dwordx4 v[168:171], off, %[rt], %[sa] offset:0 sc1\n\tv_mfma_f32_16x16x32_bf16 v[32:35], v[100:103], v[208:211], v[32:35]\n\t"
        "ds_read_b128 v[96:99], %[pb0] offset:55296\n\tv_mfma_f32_16x16x32_bf16 v[36:39], v[100:103], v[212:215], v[36:39]\n\ts_waitcnt lgkmcnt(3)\n\t"
        "v_mfma_f32_16x16x32_bf16 v[40:43], v[72:75], v[208:211], v[40:43]\n\tds_read_b128 v[100:103], %[pb0] offset:56320\n\t"
        "v_mfma_f32_16x16x32_bf16 v[44:47], v[72:75], v[212:215], v[44:47]\n\tv_mfma_f32_16x16x32_bf16 v[48:51], v[76:79], v[208:211], v[48:51]\n\t"
        "ds_read_b128 v[72:75], %[pb0] offset:57344\n\tv_mfma_f32_16x16x32_bf16 v[52:55], v[76:79], v[212:215], v[52:55]\n\t"
        "buffer_load_dwordx4 v[172:175], off, %[rt], %[sa] offset:1024 sc1\n\ts_add_u32 %[sa], %[sa], 0x2000\n\t"
        "v_mfma_f32_16x16x32_bf16 v[56:59], v[80:83], v[208:211], v[56:59]\n\tds_read_b128 v[76:79], %[pb0] offset:58368\n\t"
        "v_mfma_f32_16x16x32_bf16 v[60:63], v[80:83], v[212:215], v[60:63]\n\tv_mfma_f32_16x16x32_bf16 v[64:67], v[84:87], v[208:211], v[64:67]\n\t"
        "ds_read_b128 v[80:83], %[pb0] offset:59392\n\tv_mfma_f32_16x16x32_bf16 v[68:71], v[84:87], v[212:215], v[68:71]\n\ts_waitcnt vmcnt(12) lgkmcnt(3)\n\t"
        "v_mfma_f32_16x16x32_bf16 v[16:19], v[88:91], v[216:219], v[16:19]\n\tds_read_b128 v[84:87], %[pb0] offset:60416\n\t"
        "v_mfma_f32_16x16x32_bf16 v[20:23], v[88:91], v[220:223], v[20:23]\n\tbuffer_load_dwordx4 v[176:179], off, %[rt], %[sa] offset:0 sc1\n\t"
        "v_mfma_f32_16x16x32_bf16 v[24:27], v[92:95], v[216:219], v[24:27]\n\tds_read_b128 v[88:91], %[pb0] offset:61440\n\t"
        "v_mfma_f32_16x16x32_bf16 v[28:31], v[92:95], v[220:223], v[28:31]\n\tv_mfma_f32_16x16x32_bf16 v[32:35], v[96:99], v[216:219], v[32:35]\n\t"
        "ds_read_b128 v[92:95], %[pb0] offset:62464\n\tv_mfma_f32_16x16x32_bf16 v[36:39], v[96:99], v[220:223], v[36:39]\n\t"
        "v_mfma_f32_16x16x32_bf16 v[40:43], v[100:103], v[216:219], v[40:43]\n\tds_read_b128 v[96:99], %[pb0] offset:63488\n\t"
        "v_mfma_f32_16x16x32_bf16 v[44:47], v[100:103], v[220:223], v[44:47]\n\tbuffer_load_dwordx4 v[180:183], off, %[rt], %[sa] offset:1024 sc1\n\t"
        "s_add_u32 %[sa], %[sa], 0x2000\n\ts_waitcnt lgkmcnt(3)\n\tv_mfma_f32_16x16x32_bf16 v[48:51], v[72:75], v[216:219], v[48:51]\n\t"
        "ds_read_b128 v[100:103], %[pb0] offset:64512\n\tv_mfma_f32_16x16x32_bf16 v[52:55], v[72:75], v[220:223], v[52:55]\n\t"
        "v_mfma_f32_16x16x32_bf16 v[56:59], v[76:79], v[216:219], v[56:59]\n\tds_read_b128 v[72:75], %[pb1] offset:0\n\t"
        "v_mfma_f32_16x16x32_bf16 v[60:63], v[76:79], v[220:223], v[60:63]\n\tv_mfma_f32_16x16x32_bf16 v[64:67], v[80:83], v[216:219], v[64:67]\n\t"
        "ds_read_b128 v[76:79], %[pb1] offset:1024\n\tv_mfma_f32_16x16x32_bf16 v[68:71], v[80:83], v[220:223], v[68:71]\n\t"
        "buffer_load_dwordx4 v[184:187], off, %[rt], %[sa] offset:0 sc1\n\ts_waitcnt vmcnt(13)\n\t"
        "v_mfma_f32_16x16x32_bf16 v[16:19], v[84:87], v[224:227], v[16:19]\n\tds_read_b128 v[80:83], %[pb1] offset:2048\n\t"
        "v_mfma_f32_16x16x32_bf16 v[20:23], v[84:87], v[228:231], v[20:23]\n\ts_waitcnt lgkmcnt(3)\n\t"
        "v_mfma_f32_16x16x32_bf16 v[24:27], v[88:91], v[224:227], v[24:27]\n\tds_read_b128 v[84:87], %[pb1] offset:3072\n\t"
        "v_mfma_f32_16x16x32_bf16 v[28:31], v[88:91], v[228:231], v[28:31]\n\tv_mfma_f32_16x16x32_bf16 v[32:35], v[92:95], v[224:227], v[32:35]\n\t"
        "ds_read_b128 v[88:91], %[pb1] offset:4096\n\tv_mfma_f32_16x16x32_bf16 v[36:39], v[92:95], v[228:231], v[36:39]\n\t"
        "buffer_load_dwordx4 v[188:191], off, %[rt], %[sa] offset:1024 sc1\n\ts_add_u32 %[sa], %[sa], 0x2000\n\t"
        "v_mfma_f32_16x16x32_bf16 v[40:43], v[96:99], v[224:227], v[40:43]\n\tds_read_b128 v[92:95], %[pb1] offset:5120\n\t"
        "v_mfma_f32_16x16x32_bf16 v[44:47], v[96:99], v[228:231], v[44:47]\n\tv_mfma_f32_16x16x32_bf16 v[48:51], v[100:103], v[224:227], v[48:51]\n\t"
        "ds_read_b128 v[96:99], %[pb1] offset:6144\n\tv_mfma_f32_16x16x32_bf16 v[52:55], v[100:103], v[228:231], v[52:55]\n\ts_waitcnt lgkmcnt(3)\n\t"
        "v_mfma_f32_16x16x32_bf16 v[56:59], v[72:75], v[224:227], v[56:59]\n\tds_read_b128 v[100:103], %[pb1] offset:7168\n\t"
        "v_mfma_f32_16x16x32_bf16 v[60:63], v[72:75], v[228:231], v[60:63]\n\tbuffer_load_dwordx4 v[192:195], off, %[rt], %[sa] offset:0 sc1\n\t"
        "v_mfma_f32_16x16x32_bf16 v[64:67], v[76:79], v[224:227], v[64:67]\n\tds_read_b128 v[72:75], %[pb1] offset:8192\n\t"
        "v_mfma_f32_16x16x32_bf16 v[68:71], v[76:79], v[228:231], v[68:71]\n\ts_waitcnt vmcnt(13)\n\t"
        "v_mfma_f32_16x16x32_bf16 v[16:19], v[80:83], v[232:235], v[16:19]\n\tds_read_b128 v[76:79], %[pb1] offset:9216\n\t"
        "v_mfma_f32_16x16x32_bf16 v[20:23], v[80:83], v[236:239], v[20:23]\n\tv_mfma_f32_16x16x32_bf16 v[24:27], v[84:87], v[232:235], v[24:27]\n\t"
        "ds_read_b128 v[80:83], %[pb1] offset:10240\n\tv_mfma_f32_16x16x32_bf16 v[28:31], v[84:87], v[236:239], v[28:31]\n\t"
        "buffer_load_dwordx4 v[196:199], off, %[rt], %[sa] offset:1024 sc1\n\ts_add_u32 %[sa], %[sa], 0x2000\n\ts_waitcnt lgkmcnt(3)\n\t"
        "v_mfma_f32_16x16x32_bf16 v[32:35], v[88:91], v[232:235], v[32:35]\n\tds_read_b128 v[84:87], %[pb1] offset:11264\n\t"
        "v_mfma_f32_16x16x32_bf16 v[36:39], v[88:91], v[236:239], v[36:39]\n\tv_mfma_f32_16x16x32_bf16 v[40:43], v[92:95], v[232:235], v[40:43]\n\t"
        "ds_read_b128 v[88:91], %[pb1] offset:12288\n\tv_mfma_f32_16x16x32_bf16 v[44:47], v[92:95], v[236:239], v[44:47]\n\t"
        "v_mfma_f32_16x16x32_bf16 v[48:51], v[96:99], v[232:235], v[48:51]\n\tds_read_b128 v[92:95], %[pb1] offset:13312\n\t"
        "v_mfma_f32_16x16x32_bf16 v[52:55], v[96:99], v[236:239], v[52:55]\n\tbuffer_load_dwordx4 v[200:203], off, %[rt], %[sa] offset:0 sc1\n\t"
        "v_mfma_f32_16x16x32_bf16 v[56:59], v[100:103], v[232:235], v[56:59]\n\tds_read_b128 v[96:99], %[pb1] offset:14336\n\t"
        "v_mfma_f32_16x16x32_bf16 v[60:63], v[100:103], v[236:239], v[60:63]\n\ts_waitcnt lgkmcnt(3)\n\t"
        "v_mfma_f32_16x16x32_bf16 v[64:67], v[72:75], v[232:235], v[64:67]\n\tds_read_b128 v[100:103], %[pb1] offset:15360\n\t"
        "v_mfma_f32_16x16x32_bf16 v[68:71], v[72:75], v[236:239], v[68:71]\n\ts_waitcnt vmcnt(13)\n\t"
        "v_mfma_f32_16x16x32_bf16 v[16:19], v[76:79], v[240:243], v[16:19]\n\ts_barrier\n\tds_read_b128 v[72:75], %[pb0] offset:0\n\t"
        "v_mfma_f32_16x16x32_bf16 v[20:23], v[76:79], v[244:247], v[20:23]\n\tbuffer_load_dwordx4 v[204:207], off, %[rt], %[sa] offset:1024 sc1\n\t"
        "s_add_u32 %[sa], %[sa], 0x2000\n\tv_mfma_f32_16x16x32_bf16 v[24:27], v[80:83], v[240:243], v[24:27]\n\tds_read_b128 v[76:79], %[pb0] offset:1024\n\t"
        "v_mfma_f32_16x16x32_bf16 v[28:31], v[80:83], v[244:247], v[28:31]\n\tv_mfma_f32_16x16x32_bf16 v[32:35], v[84:87], v[240:243], v[32:35]\n\t"
        "ds_read_b128 v[80:83], %[pb0] offset:2048\n\tv_mfma_f32_16x16x32_bf16 v[36:39], v[84:87], v[244:247], v[36:39]\n\ts_waitcnt lgkmcnt(3)\n\t"
        "v_mfma_f32_16x16x32_bf16 v[40:43], v[88:91], v[240:243], v[40:43]\n\tds_read_b128 v[84:87], %[pb0] offset:3072\n\t"
        "v_mfma_f32_16x16x32_bf16 v[44:47], v[88:91], v[244:247], v[44:47]\n\tbuffer_load_dwordx4 v[208:211], off, %[rt], %[sa] offset:0 sc1\n\t"
        "v_mfma_f32_16x16x32_bf16 v[48:51], v[92:95], v[240:243], v[48:51]\n\tds_read_b128 v[88:91], %[pb0] offset:4096\n\t"
        "v_mfma_f32_16x16x32_bf16 v[52:55], v[92:95], v[244:247], v[52:55]\n\tv_mfma_f32_16x16x32_bf16 v[56:59], v[96:99], v[240:243], v[56:59]\n\t"
        "ds_read_b128 v[92:95], %[pb0] offset:5120\n\tv_mfma_f32_16x16x32_bf16 v[60:63], v[96:99], v[244:247], v[60:63]\n\t"
        "v_mfma_f32_16x16x32_bf16 v[64:67], v[100:103], v[240:243], v[64:67]\n\tds_read_b128 v[96:99], %[pb0] offset:6144\n\t"
        "v_mfma_f32_16x16x32_bf16 v[68:71], v[100:103], v[244:247], v[68:71]\n\tbuffer_load_dwordx4 v[212:215], off, %[rt], %[sa] offset:1024 sc1\n\t"
        "s_waitcnt vmcnt(14) lgkmcnt(3)\n\tv_mfma_f32_16x16x32_bf16 v[16:19], v[72:75], v[152:155], v[16:19]\n\tds_read_b128 v[100:103], %[pb0] offset:7168\n\t"
        "v_mfma_f32_16x16x32_bf16 v[20:23], v[72:75], v[156:159], v[20:23]\n\tv_mfma_f32_16x16x32_bf16 v[24:27], v[76:79], v[152:155], v[24:27]\n\t"
        "ds_read_b128 v[72:75], %[pb0] offset:8192\n\tv_mfma_f32_16x16x32_bf16 v[28:31], v[76:79], v[156:159], v[28:31]\n\t"
        "v_mfma_f32_16x16x32_bf16 v[32:35], v[80:83], v[152:155], v[32:35]\n\tds_read_b128 v[76:79], %[pb0] offset:9216\n\t"
        "v_mfma_f32_16x16x32_bf16 v[36:39], v[80:83], v[156:159], v[36:39]\n\tbuffer_load_dwordx4 v[216:219], off, %[rn], %[sn] offset:0 sc1\n\t"
        "v_mfma_f32_16x16x32_bf16 v[40:43], v[84:87], v[152:155], v[40:43]\n\tds_read_b128 v[80:83], %[pb0] offset:10240\n\t"
        "v_mfma_f32_16x16x32_bf16 v[44:47], v[84:87], v[156:159], v[44:47]\n\ts_waitcnt lgkmcnt(3)\n\t"
        "v_mfma_f32_16x16x32_bf16 v[48:51], v[88:91], v[152:155], v[48:51]\n\tds_read_b128 v[84:87], %[pb0] offset:11264\n\t"
        "v_mfma_f32_16x16x32_bf16 v[52:55], v[88:91], v[156:159], v[52:55]\n\tv_mfma_f32_16x16x32_bf16 v[56:59], v[92:95], v[152:155], v[56:59]\n\t"
        "ds_read_b128 v[88:91], %[pb0] offset:12288\n\tv_mfma_f32_16x16x32_bf16 v[60:63], v[92:95], v[156:159], v[60:63]\n\t"
        "buffer_load_dwordx4 v[220:223], off, %[rn], %[sn] offset:1024 sc1\n\ts_add_u32 %[sn], %[sn], 0x2000\n\t"
        "v_mfma_f32_16x16x32_bf16 v[64:67], v[96:99], v[152:155], v[64:67]\n\tds_read_b128 v[92:95], %[pb0] offset:13312\n\t"
        "v_mfma_f32_16x16x32_bf16 v[68:71], v[96:99], v[156:159], v[68:71]\n\ts_waitcnt vmcnt(14)\n\t"
        "v_mfma_f32_16x16x32_bf16 v[16:19], v[100:103], v[160:163], v[16:19]\n\tds_read_b128 v[96:99], %[pb0] offset:14336\n\t"
        "v_mfma_f32_16x16x32_bf16 v[20:23], v[100:103], v[164:167], v[20:23]\n\ts_waitcnt lgkmcnt(3)\n\t"
        "v_mfma_f32_16x16x32_bf16 v[24:27], v[72:75], v[160:163], v[24:27]\n\tds_read_b128 v[100:103], %[pb0] offset:15360\n\t"
        "v_mfma_f32_16x16x32_bf16 v[28:31], v[72:75], v[164:167], v[28:31]\n\tbuffer_load_dwordx4 v[224:227], off, %[rn], %[sn] offset:0 sc1\n\t"
        "v_mfma_f32_16x16x32_bf16 v[32:35], v[76:79], v[160:163], v[32:35]\n\tds_read_b128 v[72:75], %[pb0] offset:16384\n\t"
        "v_mfma_f32_16x16x32_bf16 v[36:39], v[76:79], v[164:167], v[36:39]\n\tv_mfma_f32_16x16x32_bf16 v[40:43], v[80:83], v[160:163], v[40:43]\n\t"
        "ds_read_b128 v[76:79], %[pb0] offset:17408\n\tv_mfma_f32_16x16x32_bf16 v[44:47], v[80:83], v[164:167], v[44:47]\n\t"
        "v_mfma_f32_16x16x32_bf16 v[48:51], v[84:87], v[160:163], v[48:51]\n\tds_read_b128 v[80:83], %[pb0] offset:18432\n\t"
        "v_mfma_f32_16x16x32_bf16 v[52:55], v[84:87], v[164:167], v[52:55]\n\tbuffer_load_dwordx4 v[228:231], off, %[rn], %[sn] offset:1024 sc1\n\t"
        "s_add_u32 %[sn], %[sn], 0x2000\n\ts_waitcnt lgkmcnt(3)\n\tv_mfma_f32_16x16x32_bf16 v[56:59], v[88:91], v[160:163], v[56:59]\n\t"
        "ds_read_b128 v[84:87], %[pb0] offset:19456\n\tv_mfma_f32_16x16x32_bf16 v[60:63], v[88:91], v[164:167], v[60:63]\n\t"
        "v_mfma_f32_16x16x32_bf16 v[64:67], v[92:95], v[160:163], v[64:67]\n\tds_read_b128 v[88:91], %[pb0] offset:20480\n\t"
        "v_mfma_f32_16x16x32_bf16 v[68:71], v[92:95], v[164:167], v[68:71]\n\ts_waitcnt vmcnt(14)\n\t"
        "v_mfma_f32_16x16x32_bf16 v[16:19], v[96:99], v[168:171], v[16:19]\n\tds_read_b128 v[92:95], %[pb0] offset:21504\n\t"
        "v_mfma_f32_16x16x32_bf16 v[20:23], v[96:99], v[172:175], v[20:23]\n\tbuffer_load_dwordx4 v[232:235], off, %[rn], %[sn] offset:0 sc1\n\t"
        "v_mfma_f32_16x16x32_bf16 v[24:27], v[100:103], v[168:171], v[24:27]\n\tds_read_b128 v[96:99], %[pb0] offset:22528\n\t"
        "v_mfma_f32_16x16x32_bf16 v[28:31], v[100:103], v[172:175], v[28:31]\n\ts_waitcnt lgkmcnt(3)\n\t"
        "v_mfma_f32_16x16x32_bf16 v[32:35], v[72:75], v[168:171], v[32:35]\n\tds_read_b128 v[100:103], %[pb0] offset:23552\n\t"
        "v_mfma_f32_16x16x32_bf16 v[36:39], v[72:75], v[172:175], v[36:39]\n\tv_mfma_f32_16x16x32_bf16 v[40:43], v[76:79], v[168:171], v[40:43]\n\t"
        "ds_read_b128 v[72:75], %[pb0] offset:24576\n\tv_mfma_f32_16x16x32_bf16 v[44:47], v[76:79], v[172:175], v[44:47]\n\t"
        "buffer_load_dwordx4 v[236:239], off, %[rn], %[sn] offset:1024 sc1\n\ts_add_u32 %[sn], %[sn], 0x2000\n\t"
        "v_mfma_f32_16x16x32_bf16 v[48:51], v[80:83], v[168:171], v[48:51]\n\tds_read_b128 v[76:79], %[pb0] offset:25600\n\t"
        "v_mfma_f32_16x16x32_bf16 v[52:55], v[80:83], v[172:175], v[52:55]\n\tv_mfma_f32_16x16x32_bf16 v[56:59], v[84:87], v[168:171], v[56:59]\n\t"
        "ds_read_b128 v[80:83], %[pb0] offset:26624\n\tv_mfma_f32_16x16x32_bf16 v[60:63], v[84:87], v[172:175], v[60:63]\n\ts_waitcnt lgkmcnt(3)\n\t"
        "v_mfma_f32_16x16x32_bf16 v[64:67], v[88:91], v[168:171], v[64:67]\n\tds_read_b128 v[84:87], %[pb0] offset:27648\n\t"
        "v_mfma_f32_16x16x32_bf16 v[68:71], v[88:91], v[172:175], v[68:71]\n\tbuffer_load_dwordx4 v[240:243], off, %[rn], %[sn] offset:0 sc1\n\t"
        "s_waitcnt vmcnt(15)\n\tv_mfma_f32_16x16x32_bf16 v[16:19], v[92:95], v[176:179], v[16:19]\n\tds_read_b128 v[88:91], %[pb0] offset:28672\n\t"
        "v_mfma_f32_16x16x32_bf16 v[20:23], v[92:95], v[180:183], v[20:23]\n\tv_mfma_f32_16x16x32_bf16 v[24:27], v[96:99], v[176:179], v[24:27]\n\t"
        "ds_read_b128 v[92:95], %[pb0] offset:29696\n\tv_mfma_f32_16x16x32_bf16 v[28:31], v[96:99], v[180:183], v[28:31]\n\t"
        "v_mfma_f32_16x16x32_bf16 v[32:35], v[100:103], v[176:179], v[32:35]\n\tds_read_b128 v[96:99], %[pb0] offset:30720\n\t"
        "v_mfma_f32_16x16x32_bf16 v[36:39], v[100:103], v[180:183], v[36:39]\n\tbuffer_load_dwordx4 v[244:247], off, %[rn], %[sn] offset:1024 sc1\n\t"
        "s_add_u32 %[sn], %[sn], 0x2000\n\ts_waitcnt lgkmcnt(3)\n\tv_mfma_f32_16x16x32_bf16 v[40:43], v[72:75], v[176:179], v[40:43]\n\t"
        "ds_read_b128 v[100:103], %[pb0] offset:31744\n\tv_mfma_f32_16x16x32_bf16 v[44:47], v[72:75], v[180:183], v[44:47]\n\t"
        "v_mfma_f32_16x16x32_bf16 v[48:51], v[76:79], v[176:179], v[48:51]\n\tds_read_b128 v[72:75], %[pb0] offset:32768\n\t"
        "v_mfma_f32_16x16x32_bf16 v[52:55], v[76:79], v[180:183], v[52:55]\n\tv_mfma_f32_16x16x32_bf16 v[56:59], v[80:83], v[176:179], v[56:59]\n\t"
        "ds_read_b128 v[76:79], %[pb0] offset:33792\n\tv_mfma_f32_16x16x32_bf16 v[60:63], v[80:83], v[180:183], v[60:63]\n\t"
        "buffer_load_dwordx4 v[152:155], off, %[rn], %[sn] offset:0 sc1\n\tv_mfma_f32_16x16x32_bf16 v[64:67], v[84:87], v[176:179], v[64:67]\n\t"
        "ds_read_b128 v[80:83], %[pb2] offset:8192\n\tv_mfma_f32_16x16x32_bf16 v[68:71], v[84:87], v[180:183], v[68:71]\n\ts_waitcnt vmcnt(15) lgkmcnt(3)\n\t"
        "v_mfma_f32_16x16x32_bf16 v[16:19], v[88:91], v[184:187], v[16:19]\n\tds_read_b128 v[84:87], %[pb0] offset:34816\n\t"
        "v_mfma_f32_16x16x32_bf16 v[20:23], v[88:91], v[188:191], v[20:23]\n\tv_mfma_f32_16x16x32_bf16 v[24:27], v[92:95], v[184:187], v[24:27]\n\t"
        "ds_read_b128 v[88:91], %[pb0] offset:35840\n\tv_mfma_f32_16x16x32_bf16 v[28:31], v[92:95], v[188:191], v[28:31]\n\t"
        "buffer_load_dwordx4 v[156:159], off, %[rn], %[sn] offset:1024 sc1\n\ts_add_u32 %[sn], %[sn], 0x2000\n\t"
        "v_mfma_f32_16x16x32_bf16 v[32:35], v[96:99], v[184:187], v[32:35]\n\tds_read_b128 v[92:95], %[pb0] offset:36864\n\t"
        "v_mfma_f32_16x16x32_bf16 v[36:39], v[96:99], v[188:191], v[36:39]\n\tv_mfma_f32_16x16x32_bf16 v[40:43], v[100:103], v[184:187], v[40:43]\n\t"
        "ds_read_b128 v[96:99], %[pb0] offset:37888\n\tv_mfma_f32_16x16x32_bf16 v[44:47], v[100:103], v[188:191], v[44:47]\n\ts_waitcnt lgkmcnt(3)\n\t"
        "v_mfma_f32_16x16x32_bf16 v[48:51], v[72:75], v[184:187], v[48:51]\n\tds_read_b128 v[100:103], %[pb0] offset:38912\n\t"
        "v_mfma_f32_16x16x32_bf16 v[52:55], v[72:75], v[188:191], v[52:55]\n\tbuffer_load_dwordx4 v[160:163], off, %[rn], %[sn] offset:0 sc1\n\t"
        "v_mfma_f32_16x16x32_bf16 v[56:59], v[76:79], v[184:187], v[56:59]\n\tds_read_b128 v[72:75], %[pb0] offset:39936\n\t"
        "v_mfma_f32_16x16x32_bf16 v[60:63], v[76:79], v[188:191], v[60:63]\n\tv_mfma_f32_16x16x32_bf16 v[64:67], v[80:83], v[184:187], v[64:67]\n\t"
        "ds_read_b128 v[76:79], %[pb2] offset:9216\n\tv_mfma_f32_16x16x32_bf16 v[68:71], v[80:83], v[188:191], v[68:71]\n\ts_waitcnt vmcnt(15)\n\t"
        "v_mfma_f32_16x16x32_bf16 v[16:19], v[84:87], v[192:195], v[16:19]\n\tds_read_b128 v[80:83], %[pb0] offset:40960\n\t"
        "v_mfma_f32_16x16x32_bf16 v[20:23], v[84:87], v[196:199], v[20:23]\n\tbuffer_load_dwordx4 v[164:167], off, %[rn], %[sn] offset:1024 sc1\n\t"
        "s_add_u32 %[sn], %[sn], 0x2000\n\ts_waitcnt lgkmcnt(3)\n\tv_mfma_f32_16x16x32_bf16 v[24:27], v[88:91], v[192:195], v[24:27]\n\t"
        "ds_read_b128 v[84:87], %[pb0] offset:41984\n\tv_mfma_f32_16x16x32_bf16 v[28:31], v[88:91], v[196:199], v[28:31]\n\t"
        "v_mfma_f32_16x16x32_bf16 v[32:35], v[92:95], v[192:195], v[32:35]\n\tds_read_b128 v[88:91], %[pb0] offset:43008\n\t"
        "v_mfma_f32_16x16x32_bf16 v[36:39], v[92:95], v[196:199], v[36:39]\n\tv_mfma_f32_16x16x32_bf16 v[40:43], v[96:99], v[192:195], v[40:43]\n\t"
        "ds_read_b128 v[92:95], %[pb0] offset:44032\n\tv_mfma_f32_16x16x32_bf16 v[44:47], v[96:99], v[196:199], v[44:47]\n\t"
        "buffer_load_dwordx4 v[168:171], off, %[rn], %[sn] offset:0 sc1\n\tv_mfma_f32_16x16x32_bf16 v[48:51], v[100:103], v[192:195], v[48:51]\n\t"
        "ds_read_b128 v[96:99], %[pb0] offset:45056\n\tv_mfma_f32_16x16x32_bf16 v[52:55], v[100:103], v[196:199], v[52:55]\n\ts_waitcnt lgkmcnt(3)\n\t"
        "v_mfma_f32_16x16x32_bf16 v[56:59], v[72:75], v[192:195], v[56:59]\n\tds_read_b128 v[100:103], %[pb0] offset:46080\n\t"
        "v_mfma_f32_16x16x32_bf16 v[60:63], v[72:75], v[196:199], v[60:63]\n\tv_mfma_f32_16x16x32_bf16 v[64:67], v[76:79], v[192:195], v[64:67]\n\t"
        "ds_read_b128 v[72:75], %[pb2] offset:10240\n\tv_mfma_f32_16x16x32_bf16 v[68:71], v[76:79], v[196:199], v[68:71]\n\t"
        "buffer_load_dwordx4 v[172:175], off, %[rn], %[sn] offset:1024 sc1\n\ts_add_u32 %[sn], %[sn], 0x2000\n\ts_waitcnt vmcnt(16)\n\t"
        "v_mfma_f32_16x16x32_bf16 v[16:19], v[80:83], v[200:203], v[16:19]\n\tds_read_b128 v[76:79], %[pb0] offset:47104\n\t"
        "v_mfma_f32_16x16x32_bf16 v[20:23], v[80:83], v[204:207], v[20:23]\n\tv_mfma_f32_16x16x32_bf16 v[24:27], v[84:87], v[200:203], v[24:27]\n\t"
        "ds_read_b128 v[80:83], %[pb0] offset:48128\n\tv_mfma_f32_16x16x32_bf16 v[28:31], v[84:87], v[204:207], v[28:31]\n\ts_waitcnt lgkmcnt(3)\n\t"
        "v_mfma_f32_16x16x32_bf16 v[32:35], v[88:91], v[200:203], v[32:35]\n\tds_read_b128 v[84:87], %[pb0] offset:49152\n\t"
        "v_mfma_f32_16x16x32_bf16 v[36:39], v[88:91], v[204:207], v[36:39]\n\tbuffer_load_dwordx4 v[176:179], off, %[rn], %[sn] offset:0 sc1\n\t"
        "v_mfma_f32_16x16x32_bf16 v[40:43], v[92:95], v[200:203], v[40:43]\n\tds_read_b128 v[88:91], %[pb0] offset:50176\n\t"
        "v_mfma_f32_16x16x32_bf16 v[44:47], v[92:95], v[204:207], v[44:47]\n\tv_mfma_f32_16x16x32_bf16 v[48:51], v[96:99], v[200:203], v[48:51]\n\t"
        "ds_read_b128 v[92:95], %[pb0] offset:51200\n\tv_mfma_f32_16x16x32_bf16 v[52:55], v[96:99], v[204:207], v[52:55]\n\t"
        "v_mfma_f32_16x16x32_bf16 v[56:59], v[100:103], v[200:203], v[56:59]\n\tds_read_b128 v[96:99], %[pb0] offset:52224\n\t"
        "v_mfma_f32_16x16x32_bf16 v[60:63], v[100:103], v[204:207], v[60:63]\n\tbuffer_load_dwordx4 v[180:183], off, %[rn], %[sn] offset:1024 sc1\n\t"
        "s_add_u32 %[sn], %[sn], 0x2000\n\ts_waitcnt lgkmcnt(3)\n\tv_mfma_f32_16x16x32_bf16 v[64:67], v[72:75], v[200:203], v[64:67]\n\t"
        "ds_read_b128 v[100:103], %[pb2] offset:11264\n\tv_mfma_f32_16x16x32_bf16 v[68:71], v[72:75], v[204:207], v[68:71]\n\ts_waitcnt vmcnt(16)\n\t"
        "v_mfma_f32_16x16x32_bf16 v[16:19], v[76:79], v[208:211], v[16:19]\n\tv_mfma_f32_16x16x32_bf16 v[20:23], v[76:79], v[212:215], v[20:23]\n\t"
        "v_mfma_f32_16x16x32_bf16 v[24:27], v[80:83], v[208:211], v[24:27]\n\tv_mfma_f32_16x16x32_bf16 v[28:31], v[80:83], v[212:215], v[28:31]\n\t"
        "buffer_load_dwordx4 v[184:187], off, %[rn], %[sn] offset:0 sc1\n\tv_mov_b32 v8, 0x409d30fc\n\t"
        "v_mfma_f32_16x16x32_bf16 v[32:35], v[84:87], v[208:211], v[32:35]\n\tv_mfma_f32_16x16x32_bf16 v[36:39], v[84:87], v[212:215], v[36:39]\n\t"
        "v_mov_b32 v9, 0x3eeff558\n\ts_waitcnt lgkmcnt(0)\n\tv_mfma_f32_16x16x32_bf16 v[40:43], v[88:91], v[208:211], v[40:43]\n\t"
        "v_mfma_f32_16x16x32_bf16 v[44:47], v[88:91], v[212:215], v[44:47]\n\tds_read_b32 v104, v253 offset:0\n\tds_read_b32 v105, v253 offset:64\n\t"
        "v_mfma_f32_16x16x32_bf16 v[48:51], v[92:95], v[208:211], v[48:51]\n\tv_mfma_f32_16x16x32_bf16 v[52:55], v[92:95], v[212:215], v[52:55]\n\t"
        "buffer_load_dwordx4 v[188:191], off, %[rn], %[sn] offset:1024 sc1\n\ts_add_u32 %[sn], %[sn], 0x2000\n\tds_read_b32 v106, v253 offset:1024\n\t"
        "ds_read_b32 v107, v253 offset:1088\n\tv_mfma_f32_16x16x32_bf16 v[56:59], v[96:99], v[208:211], v[56:59]\n\t"
        "v_mfma_f32_16x16x32_bf16 v[60:63], v[96:99], v[212:215], v[60:63]\n\tds_read_b128 v[108:111], v255 offset:2048\n\t"
        "ds_read_b128 v[116:119], v255 offset:2560\n\tv_mfma_f32_16x16x32_bf16 v[64:67], v[100:103], v[208:211], v[64:67]\n\t"
        "v_mfma_f32_16x16x32_bf16 v[68:71], v[100:103], v[212:215], v[68:71]\n\tds_read_b128 v[112:115], v255 offset:2112\n\t"
        "ds_read_b128 v[120:123], v255 offset:2624\n\ts_barrier\n\ts_waitcnt lgkmcnt(0)\n\tds_read_b128 v[72:75], v255 offset:2176\n\t"
        "ds_read_b128 v[80:83], v255 offset:2688\n\tds_read_b128 v[76:79], v255 offset:2240\n\tds_read_b128 v[84:87], v255 offset:2752\n\t"
        "v_pk_fma_f32 v[88:89], v[106:107], v[108:109], v[116:117] op_sel:[0,0,0] op_sel_hi:[0,1,1]\n\t"
        "v_pk_fma_f32 v[90:91], v[106:107], v[110:111], v[118:119] op_sel:[0,0,0] op_sel_hi:[0,1,1]\n\t"
        "v_pk_fma_f32 v[92:93], v[106:107], v[112:113], v[120:121] op_sel:[0,0,0] op_sel_hi:[0,1,1]\n\t"
        "v_pk_fma_f32 v[94:95], v[106:107], v[114:115], v[122:123] op_sel:[0,0,0] op_sel_hi:[0,1,1]\n\t"
        "v_pk_fma_f32 v[16:17], v[16:17], v[104:105], v[88:89] op_sel:[0,0,0] op_sel_hi:[1,0,1]\n\t"
        "v_pk_fma_f32 v[18:19], v[18:19], v[104:105], v[90:91] op_sel:[0,0,0] op_sel_hi:[1,0,1]\n\t"
        "v_pk_fma_f32 v[24:25], v[24:25], v[104:105], v[92:93] op_sel:[0,0,0] op_sel_hi:[1,0,1]\n\t"
        "v_pk_fma_f32 v[26:27], v[26:27], v[104:105], v[94:95] op_sel:[0,0,0] op_sel_hi:[1,0,1]\n\t"
        "v_pk_fma_f32 v[88:89], v[16:17], v[16:17], v[8:9] op_sel_hi:[1,1,0]\n\tv_pk_fma_f32 v[90:91], v[18:19], v[18:19], v[8:9] op_sel_hi:[1,1,0]\n\t"
        "v_pk_fma_f32 v[92:93], v[24:25], v[24:25], v[8:9] op_sel_hi:[1,1,0]\n\tv_pk_fma_f32 v[94:95], v[26:27], v[26:27], v[8:9] op_sel_hi:[1,1,0]\n\t"
        "v_pk_mul_f32 v[88:89], v[16:17], v[88:89] neg_lo:[1,0] neg_hi:[1,0]\n\tv_pk_mul_f32 v[90:91], v[18:19], v[90:91] neg_lo:[1,0] neg_hi:[1,0]\n\t"
        "v_pk_mul_f32 v[92:93], v[24:25], v[92:93] neg_lo:[1,0] neg_hi:[1,0]\n\tv_pk_mul_f32 v[94:95], v[26:27], v[94:95] neg_lo:[1,0] neg_hi:[1,0]\n\t"
        "v_exp_f32 v88, v88\n\tv_exp_f32 v89, v89\n\tv_exp_f32 v90, v90\n\tv_exp_f32 v91, v91\n\tv_exp_f32 v92, v92\n\tv_exp_f32 v93, v93\n\t"
        "v_exp_f32 v94, v94\n\tv_exp_f32 v95, v95\n\tv_pk_fma_f32 v[88:89], v[88:89], v[8:9], v[8:9] op_sel:[0,1,1] op_sel_hi:[1,1,1]\n\t"
        "v_pk_fma_f32 v[90:91], v[90:91], v[8:9], v[8:9] op_sel:[0,1,1] op_sel_hi:[1,1,1]\n\t"
        "v_pk_fma_f32 v[92:93], v[92:93], v[8:9], v[8:9] op_sel:[0,1,1] op_sel_hi:[1,1,1]\n\t"
        "v_pk_fma_f32 v[94:95], v[94:95], v[8:9], v[8:9] op_sel:[0,1,1] op_sel_hi:[1,1,1]\n\tv_rcp_f32 v88, v88\n\tv_rcp_f32 v89, v89\n\tv_rcp_f32 v90, v90\n\t"
        "v_rcp_f32 v91, v91\n\tv_rcp_f32 v92, v92\n\tv_rcp_f32 v93, v93\n\tv_rcp_f32 v94, v94\n\tv_rcp_f32 v95, v95\n\t"
        "v_pk_mul_f32 v[16:17], v[16:17], v[88:89]\n\tv_pk_mul_f32 v[18:19], v[18:19], v[90:91]\n\tv_pk_mul_f32 v[24:25], v[24:25], v[92:93]\n\t"
        "v_pk_mul_f32 v[26:27], v[26:27], v[94:95]\n\tv_cvt_pk_bf16_f32 v124, v16, v17\n\tv_cvt_pk_bf16_f32 v125, v18, v19\n\t"
        "v_cvt_pk_bf16_f32 v126, v24, v25\n\tv_cvt_pk_bf16_f32 v127, v26, v27\n\tbuffer_store_dwordx4 v[124:127], off, %[rt], %[hd] offset:0 sc1\n\t"
        "v_pk_fma_f32 v[88:89], v[106:107], v[108:109], v[116:117] op_sel:[1,0,0] op_sel_hi:[1,1,1]\n\t"
        "v_pk_fma_f32 v[90:91], v[106:107], v[110:111], v[118:119] op_sel:[1,0,0] op_sel_hi:[1,1,1]\n\t"
        "v_pk_fma_f32 v[92:93], v[106:107], v[112:113], v[120:121] op_sel:[1,0,0] op_sel_hi:[1,1,1]\n\t"
        "v_pk_fma_f32 v[94:95], v[106:107], v[114:115], v[122:123] op_sel:[1,0,0] op_sel_hi:[1,1,1]\n\t"
        "v_pk_fma_f32 v[20:21], v[20:21], v[104:105], v[88:89] op_sel:[0,1,0] op_sel_hi:[1,1,1]\n\t"
        "v_pk_fma_f32 v[22:23], v[22:23], v[104:105], v[90:91] op_sel:[0,1,0] op_sel_hi:[1,1,1]\n\t"
        "v_pk_fma_f32 v[28:29], v[28:29], v[104:105], v[92:93] op_sel:[0,1,0] op_sel_hi:[1,1,1]\n\t"
        "v_pk_fma_f32 v[30:31], v[30:31], v[104:105], v[94:95] op_sel:[0,1,0] op_sel_hi:[1,1,1]\n\t"
        "v_pk_fma_f32 v[88:89], v[20:21], v[20:21], v[8:9] op_sel_hi:[1,1,0]\n\tv_pk_fma_f32 v[90:91], v[22:23], v[22:23], v[8:9] op_sel_hi:[1,1,0]\n\t"
        "v_pk_fma_f32 v[92:93], v[28:29], v[28:29], v[8:9] op_sel_hi:[1,1,0]\n\tv_pk_fma_f32 v[94:95], v[30:31], v[30:31], v[8:9] op_sel_hi:[1,1,0]\n\t"
        "v_pk_mul_f32 v[88:89], v[20:21], v[88:89] neg_lo:[1,0] neg_hi:[1,0]\n\tv_pk_mul_f32 v[90:91], v[22:23], v[90:91] neg_lo:[1,0] neg_hi:[1,0]\n\t"
        "v_pk_mul_f32 v[92:93], v[28:29], v[92:93] neg_lo:[1,0] neg_hi:[1,0]\n\tv_pk_mul_f32 v[94:95], v[30:31], v[94:95] neg_lo:[1,0] neg_hi:[1,0]\n\t"
        "v_exp_f32 v88, v88\n\tv_exp_f32 v89, v89\n\tv_exp_f32 v90, v90\n\tv_exp_f32 v91, v91\n\tv_exp_f32 v92, v92\n\tv_exp_f32 v93, v93\n\t"
        "v_exp_f32 v94, v94\n\tv_exp_f32 v95, v95\n\tv_pk_fma_f32 v[88:89], v[88:89], v[8:9], v[8:9] op_sel:[0,1,1] op_sel_hi:[1,1,1]\n\t"
        "v_pk_fma_f32 v[90:91], v[90:91], v[8:9], v[8:9] op_sel:[0,1,1] op_sel_hi:[1,1,1]\n\t"
        "v_pk_fma_f32 v[92:93], v[92:93], v[8:9], v[8:9] op_sel:[0,1,1] op_sel_hi:[1,1,1]\n\t"
        "v_pk_fma_f32 v[94:95], v[94:95], v[8:9], v[8:9] op_sel:[0,1,1] op_sel_hi:[1,1,1]\n\tv_rcp_f32 v88, v88\n\tv_rcp_f32 v89, v89\n\tv_rcp_f32 v90, v90\n\t"
        "v_rcp_f32 v91, v91\n\tv_rcp_f32 v92, v92\n\tv_rcp_f32 v93, v93\n\tv_rcp_f32 v94, v94\n\tv_rcp_f32 v95, v95\n\t"
        "v_pk_mul_f32 v[20:21], v[20:21], v[88:89]\n\tv_pk_mul_f32 v[22:23], v[22:23], v[90:91]\n\tv_pk_mul_f32 v[28:29], v[28:29], v[92:93]\n\t"
        "v_pk_mul_f32 v[30:31], v[30:31], v[94:95]\n\tv_cvt_pk_bf16_f32 v128, v20, v21\n\tv_cvt_pk_bf16_f32 v129, v22, v23\n\t"
        "v_cvt_pk_bf16_f32 v130, v28, v29\n\tv_cvt_pk_bf16_f32 v131, v30, v31\n\ts_waitcnt lgkmcnt(0)\n\ts_nop 0\n\t"
        "ds_read_b128 v[108:111], v255 offset:2304\n\tds_read_b128 v[116:119], v255 offset:2816\n\tds_read_b128 v[112:115], v255 offset:2368\n\t"
        "ds_read_b128 v[120:123], v255 offset:2880\n\tv_pk_fma_f32 v[88:89], v[106:107], v[72:73], v[80:81] op_sel:[0,0,0] op_sel_hi:[0,1,1]\n\t"
        "v_pk_fma_f32 v[90:91], v[106:107], v[74:75], v[82:83] op_sel:[0,0,0] op_sel_hi:[0,1,1]\n\t"
        "v_pk_fma_f32 v[92:93], v[106:107], v[76:77], v[84:85] op_sel:[0,0,0] op_sel_hi:[0,1,1]\n\t"
        "v_pk_fma_f32 v[94:95], v[106:107], v[78:79], v[86:87] op_sel:[0,0,0] op_sel_hi:[0,1,1]\n\t"
        "v_pk_fma_f32 v[32:33], v[32:33], v[104:105], v[88:89] op_sel:[0,0,0] op_sel_hi:[1,0,1]\n\t"
        "v_pk_fma_f32 v[34:35], v[34:35], v[104:105], v[90:91] op_sel:[0,0,0] op_sel_hi:[1,0,1]\n\t"
        "v_pk_fma_f32 v[40:41], v[40:41], v[104:105], v[92:93] op_sel:[0,0,0] op_sel_hi:[1,0,1]\n\t"
        "v_pk_fma_f32 v[42:43], v[42:43], v[104:105], v[94:95] op_sel:[0,0,0] op_sel_hi:[1,0,1]\n\t"
        "v_pk_fma_f32 v[88:89], v[32:33], v[32:33], v[8:9] op_sel_hi:[1,1,0]\n\tv_pk_fma_f32 v[90:91], v[34:35], v[34:35], v[8:9] op_sel_hi:[1,1,0]\n\t"
        "v_pk_fma_f32 v[92:93], v[40:41], v[40:41], v[8:9] op_sel_hi:[1,1,0]\n\tv_pk_fma_f32 v[94:95], v[42:43], v[42:43], v[8:9] op_sel_hi:[1,1,0]\n\t"
        "v_pk_mul_f32 v[88:89], v[32:33], v[88:89] neg_lo:[1,0] neg_hi:[1,0]\n\tv_pk_mul_f32 v[90:91], v[34:35], v[90:91] neg_lo:[1,0] neg_hi:[1,0]\n\t"
        "v_pk_mul_f32 v[92:93], v[40:41], v[92:93] neg_lo:[1,0] neg_hi:[1,0]\n\tv_pk_mul_f32 v[94:95], v[42:43], v[94:95] neg_lo:[1,0] neg_hi:[1,0]\n\t"
        "v_exp_f32 v88, v88\n\tv_exp_f32 v89, v89\n\tv_exp_f32 v90, v90\n\tv_exp_f32 v91, v91\n\tv_exp_f32 v92, v92\n\tv_exp_f32 v93, v93\n\t"
        "v_exp_f32 v94, v94\n\tv_exp_f32 v95, v95\n\tv_pk_fma_f32 v[88:89], v[88:89], v[8:9], v[8:9] op_sel:[0,1,1] op_sel_hi:[1,1,1]\n\t"
        "v_pk_fma_f32 v[90:91], v[90:91], v[8:9], v[8:9] op_sel:[0,1,1] op_sel_hi:[1,1,1]\n\t"
        "v_pk_fma_f32 v[92:93], v[92:93], v[8:9], v[8:9] op_sel:[0,1,1] op_sel_hi:[1,1,1]\n\t"
        "v_pk_fma_f32 v[94:95], v[94:95], v[8:9], v[8:9] op_sel:[0,1,1] op_sel_hi:[1,1,1]\n\tv_rcp_f32 v88, v88\n\tv_rcp_f32 v89, v89\n\tv_rcp_f32 v90, v90\n\t"
        "v_rcp_f32 v91, v91\n\tv_rcp_f32 v92, v92\n\tv_rcp_f32 v93, v93\n\tv_rcp_f32 v94, v94\n\tv_rcp_f32 v95, v95\n\t"
        "v_pk_mul_f32 v[32:33], v[32:33], v[88:89]\n\tbuffer_store_dwordx4 v[128:131], off, %[rt], %[hd] offset:1024 sc1\n\t"
        "v_pk_mul_f32 v[34:35], v[34:35], v[90:91]\n\tv_pk_mul_f32 v[40:41], v[40:41], v[92:93]\n\tv_pk_mul_f32 v[42:43], v[42:43], v[94:95]\n\t"
        "v_cvt_pk_bf16_f32 v132, v32, v33\n\tv_cvt_pk_bf16_f32 v133, v34, v35\n\tv_cvt_pk_bf16_f32 v134, v40, v41\n\tv_cvt_pk_bf16_f32 v135, v42, v43\n\t"
        "v_pk_fma_f32 v[88:89], v[106:107], v[72:73], v[80:81] op_sel:[1,0,0] op_sel_hi:[1,1,1]\n\t"
        "v_pk_fma_f32 v[90:91], v[106:107], v[74:75], v[82:83] op_sel:[1,0,0] op_sel_hi:[1,1,1]\n\t"
        "v_pk_fma_f32 v[92:93], v[106:107], v[76:77], v[84:85] op_sel:[1,0,0] op_sel_hi:[1,1,1]\n\t"
        "v_pk_fma_f32 v[94:95], v[106:107], v[78:79], v[86:87] op_sel:[1,0,0] op_sel_hi:[1,1,1]\n\t"
        "v_pk_fma_f32 v[36:37], v[36:37], v[104:105], v[88:89] op_sel:[0,1,0] op_sel_hi:[1,1,1]\n\t"
        "v_pk_fma_f32 v[38:39], v[38:39], v[104:105], v[90:91] op_sel:[0,1,0] op_sel_hi:[1,1,1]\n\t"
        "v_pk_fma_f32 v[44:45], v[44:45], v[104:105], v[92:93] op_sel:[0,1,0] op_sel_hi:[1,1,1]\n\t"
        "v_pk_fma_f32 v[46:47], v[46:47], v[104:105], v[94:95] op_sel:[0,1,0] op_sel_hi:[1,1,1]\n\t"
        "v_pk_fma_f32 v[88:89], v[36:37], v[36:37], v[8:9] op_sel_hi:[1,1,0]\n\tv_pk_fma_f32 v[90:91], v[38:39], v[38:39], v[8:9] op_sel_hi:[1,1,0]\n\t"
        "v_pk_fma_f32 v[92:93], v[44:45], v[44:45], v[8:9] op_sel_hi:[1,1,0]\n\tv_pk_fma_f32 v[94:95], v[46:47], v[46:47], v[8:9] op_sel_hi:[1,1,0]\n\t"
        "v_pk_mul_f32 v[88:89], v[36:37], v[88:89] neg_lo:[1,0] neg_hi:[1,0]\n\tv_pk_mul_f32 v[90:91], v[38:39], v[90:91] neg_lo:[1,0] neg_hi:[1,0]\n\t"
        "v_pk_mul_f32 v[92:93], v[44:45], v[92:93] neg_lo:[1,0] neg_hi:[1,0]\n\tv_pk_mul_f32 v[94:95], v[46:47], v[94:95] neg_lo:[1,0] neg_hi:[1,0]\n\t"
        "v_exp_f32 v88, v88\n\tv_exp_f32 v89, v89\n\tv_exp_f32 v90, v90\n\tv_exp_f32 v91, v91\n\tv_exp_f32 v92, v92\n\tv_exp_f32 v93, v93\n\t"
        "v_exp_f32 v94, v94\n\tv_exp_f32 v95, v95\n\tv_pk_fma_f32 v[88:89], v[88:89], v[8:9], v[8:9] op_sel:[0,1,1] op_sel_hi:[1,1,1]\n\t.p2align 12,,12\n\t"
        ".Lpg11:\n\ts_cbranch_execz .Lpgret\n\tv_pk_fma_f32 v[90:91], v[90:91], v[8:9], v[8:9] op_sel:[0,1,1] op_sel_hi:[1,1,1]\n\t"
        "v_pk_fma_f32 v[92:93], v[92:93], v[8:9], v[8:9] op_sel:[0,1,1] op_sel_hi:[1,1,1]\n\t"
        "v_pk_fma_f32 v[94:95], v[94:95], v[8:9], v[8:9] op_sel:[0,1,1] op_sel_hi:[1,1,1]\n\tv_rcp_f32 v88, v88\n\tv_rcp_f32 v89, v89\n\tv_rcp_f32 v90, v90\n\t"
        "v_rcp_f32 v91, v91\n\tv_rcp_f32 v92, v92\n\tv_rcp_f32 v93, v93\n\tv_rcp_f32 v94, v94\n\tv_rcp_f32 v95, v95\n\t"
        "v_pk_mul_f32 v[36:37], v[36:37], v[88:89]\n\tv_pk_mul_f32 v[38:39], v[38:39], v[90:91]\n\tv_pk_mul_f32 v[44:45], v[44:45], v[92:93]\n\t"
        "v_pk_mul_f32 v[46:47], v[46:47], v[94:95]\n\tv_cvt_pk_bf16_f32 v136, v36, v37\n\tv_cvt_pk_bf16_f32 v137, v38, v39\n\t"
        "v_cvt_pk_bf16_f32 v138, v44, v45\n\tv_cvt_pk_bf16_f32 v139, v46, v47\n\ts_waitcnt lgkmcnt(0)\n\tds_read_b128 v[72:75], v255 offset:2432\n\t"
        "ds_read_b128 v[80:83], v255 offset:2944\n\tv_pk_fma_f32 v[88:89], v[106:107], v[108:109], v[116:117] op_sel:[0,0,0] op_sel_hi:[0,1,1]\n\t"
        "v_pk_fma_f32 v[90:91], v[106:107], v[110:111], v[118:119] op_sel:[0,0,0] op_sel_hi:[0,1,1]\n\t"
        "v_pk_fma_f32 v[92:93], v[106:107], v[112:113], v[120:121] op_sel:[0,0,0] op_sel_hi:[0,1,1]\n\t"
        "v_pk_fma_f32 v[94:95], v[106:107], v[114:115], v[122:123] op_sel:[0,0,0] op_sel_hi:[0,1,1]\n\t"
        "v_pk_fma_f32 v[48:49], v[48:49], v[104:105], v[88:89] op_sel:[0,0,0] op_sel_hi:[1,0,1]\n\t"
        "v_pk_fma_f32 v[50:51], v[50:51], v[104:105], v[90:91] op_sel:[0,0,0] op_sel_hi:[1,0,1]\n\t"
        "v_pk_fma_f32 v[56:57], v[56:57], v[104:105], v[92:93] op_sel:[0,0,0] op_sel_hi:[1,0,1]\n\t"
        "v_pk_fma_f32 v[58:59], v[58:59], v[104:105], v[94:95] op_sel:[0,0,0] op_sel_hi:[1,0,1]\n\t"
        "v_pk_fma_f32 v[88:89], v[48:49], v[48:49], v[8:9] op_sel_hi:[1,1,0]\n\tv_pk_fma_f32 v[90:91], v[50:51], v[50:51], v[8:9] op_sel_hi:[1,1,0]\n\t"
        "v_pk_fma_f32 v[92:93], v[56:57], v[56:57], v[8:9] op_sel_hi:[1,1,0]\n\tv_pk_fma_f32 v[94:95], v[58:59], v[58:59], v[8:9] op_sel_hi:[1,1,0]\n\t"
        "v_pk_mul_f32 v[88:89], v[48:49], v[88:89] neg_lo:[1,0] neg_hi:[1,0]\n\tv_pk_mul_f32 v[90:91], v[50:51], v[90:91] neg_lo:[1,0] neg_hi:[1,0]\n\t"
        "v_pk_mul_f32 v[92:93], v[56:57], v[92:93] neg_lo:[1,0] neg_hi:[1,0]\n\tv_pk_mul_f32 v[94:95], v[58:59], v[94:95] neg_lo:[1,0] neg_hi:[1,0]\n\t"
        "v_exp_f32 v88, v88\n\tv_exp_f32 v89, v89\n\tv_exp_f32 v90, v90\n\tv_exp_f32 v91, v91\n\tv_exp_f32 v92, v92\n\tv_exp_f32 v93, v93\n\t"
        "v_exp_f32 v94, v94\n\tv_exp_f32 v95, v95\n\tv_pk_fma_f32 v[88:89], v[88:89], v[8:9], v[8:9] op_sel:[0,1,1] op_sel_hi:[1,1,1]\n\t"
        "v_pk_fma_f32 v[90:91], v[90:91], v[8:9], v[8:9] op_sel:[0,1,1] op_sel_hi:[1,1,1]\n\t"
        "v_pk_fma_f32 v[92:93], v[92:93], v[8:9], v[8:9] op_sel:[0,1,1] op_sel_hi:[1,1,1]\n\t"
        "v_pk_fma_f32 v[94:95], v[94:95], v[8:9], v[8:9] op_sel:[0,1,1] op_sel_hi:[1,1,1]\n\tv_rcp_f32 v88, v88\n\tv_rcp_f32 v89, v89\n\tv_rcp_f32 v90, v90\n\t"
        "v_rcp_f32 v91, v91\n\tv_rcp_f32 v92, v92\n\tv_rcp_f32 v93, v93\n\tv_rcp_f32 v94, v94\n\tv_rcp_f32 v95, v95\n\t"
        "v_pk_mul_f32 v[48:49], v[48:49], v[88:89]\n\ts_add_u32 %[hx], %[hd], 0x2000\n\tbuffer_store_dwordx4 v[132:135], off, %[rt], %[hx] offset:0 sc1\n\t"
        "v_pk_mul_f32 v[50:51], v[50:51], v[90:91]\n\tv_pk_mul_f32 v[56:57], v[56:57], v[92:93]\n\tv_pk_mul_f32 v[58:59], v[58:59], v[94:95]\n\t"
        "v_cvt_pk_bf16_f32 v140, v48, v49\n\tv_cvt_pk_bf16_f32 v141, v50, v51\n\tv_cvt_pk_bf16_f32 v142, v56, v57\n\tv_cvt_pk_bf16_f32 v143, v58, v59\n\t"
        "v_pk_fma_f32 v[88:89], v[106:107], v[108:109], v[116:117] op_sel:[1,0,0] op_sel_hi:[1,1,1]\n\t"
        "v_pk_fma_f32 v[90:91], v[106:107], v[110:111], v[118:119] op_sel:[1,0,0] op_sel_hi:[1,1,1]\n\t"
        "v_pk_fma_f32 v[92:93], v[106:107], v[112:113], v[120:121] op_sel:[1,0,0] op_sel_hi:[1,1,1]\n\t"
        "v_pk_fma_f32 v[94:95], v[106:107], v[114:115], v[122:123] op_sel:[1,0,0] op_sel_hi:[1,1,1]\n\t"
        "v_pk_fma_f32 v[52:53], v[52:53], v[104:105], v[88:89] op_sel:[0,1,0] op_sel_hi:[1,1,1]\n\t"
        "v_pk_fma_f32 v[54:55], v[54:55], v[104:105], v[90:91] op_sel:[0,1,0] op_sel_hi:[1,1,1]\n\t"
        "v_pk_fma_f32 v[60:61], v[60:61], v[104:105], v[92:93] op_sel:[0,1,0] op_sel_hi:[1,1,1]\n\t"
        "v_pk_fma_f32 v[62:63], v[62:63], v[104:105], v[94:95] op_sel:[0,1,0] op_sel_hi:[1,1,1]\n\t"
        "v_pk_fma_f32 v[88:89], v[52:53], v[52:53], v[8:9] op_sel_hi:[1,1,0]\n\tv_pk_fma_f32 v[90:91], v[54:55], v[54:55], v[8:9] op_sel_hi:[1,1,0]\n\t"
        "v_pk_fma_f32 v[92:93], v[60:61], v[60:61], v[8:9] op_sel_hi:[1,1,0]\n\tv_pk_fma_f32 v[94:95], v[62:63], v[62:63], v[8:9] op_sel_hi:[1,1,0]\n\t"
        "v_pk_mul_f32 v[88:89], v[52:53], v[88:89] neg_lo:[1,0] neg_hi:[1,0]\n\tv_pk_mul_f32 v[90:91], v[54:55], v[90:91] neg_lo:[1,0] neg_hi:[1,0]\n\t"
        "v_pk_mul_f32 v[92:93], v[60:61], v[92:93] neg_lo:[1,0] neg_hi:[1,0]\n\tv_pk_mul_f32 v[94:95], v[62:63], v[94:95] neg_lo:[1,0] neg_hi:[1,0]\n\t"
        "v_exp_f32 v88, v88\n\tv_exp_f32 v89, v89\n\tv_exp_f32 v90, v90\n\tv_exp_f32 v91, v91\n\tv_exp_f32 v92, v92\n\tv_exp_f32 v93, v93\n\t"
        "v_exp_f32 v94, v94\n\tv_exp_f32 v95, v95\n\tv_pk_fma_f32 v[88:89], v[88:89], v[8:9], v[8:9] op_sel:[0,1,1] op_sel_hi:[1,1,1]\n\t"
        "v_pk_fma_f32 v[90:91], v[90:91], v[8:9], v[8:9] op_sel:[0,1,1] op_sel_hi:[1,1,1]\n\t"
        "v_pk_fma_f32 v[92:93], v[92:93], v[8:9], v[8:9] op_sel:[0,1,1] op_sel_hi:[1,1,1]\n\t"
        "v_pk_fma_f32 v[94:95], v[94:95], v[8:9], v[8:9] op_sel:[0,1,1] op_sel_hi:[1,1,1]\n\tv_rcp_f32 v88, v88\n\tv_rcp_f32 v89, v89\n\tv_rcp_f32 v90, v90\n\t"
        "v_rcp_f32 v91, v91\n\tv_rcp_f32 v92, v92\n\tv_rcp_f32 v93, v93\n\tv_rcp_f32 v94, v94\n\tv_rcp_f32 v95, v95\n\t"
        "v_pk_mul_f32 v[52:53], v[52:53], v[88:89]\n\tv_pk_mul_f32 v[54:55], v[54:55], v[90:91]\n\tv_pk_mul_f32 v[60:61], v[60:61], v[92:93]\n\t"
        "v_pk_mul_f32 v[62:63], v[62:63], v[94:95]\n\tv_cvt_pk_bf16_f32 v144, v52, v53\n\tv_cvt_pk_bf16_f32 v145, v54, v55\n\t"
        "v_cvt_pk_bf16_f32 v146, v60, v61\n\tv_cvt_pk_bf16_f32 v147, v62, v63\n\ts_waitcnt lgkmcnt(0)\n\ts_nop 0\n\t"
        "v_pk_fma_f32 v[88:89], v[106:107], v[72:73], v[80:81] op_sel:[0,0,0] op_sel_hi:[0,1,1]\n\t"
        "v_pk_fma_f32 v[90:91], v[106:107], v[74:75], v[82:83] op_sel:[0,0,0] op_sel_hi:[0,1,1]\n\t"
        "v_pk_fma_f32 v[92:93], v[106:107], v[72:73], v[80:81] op_sel:[1,0,0] op_sel_hi:[1,1,1]\n\t"
        "v_pk_fma_f32 v[94:95], v[106:107], v[74:75], v[82:83] op_sel:[1,0,0] op_sel_hi:[1,1,1]\n\t"
        "v_pk_fma_f32 v[64:65], v[64:65], v[104:105], v[88:89] op_sel:[0,0,0] op_sel_hi:[1,0,1]\n\t"
        "v_pk_fma_f32 v[66:67], v[66:67], v[104:105], v[90:91] op_sel:[0,0,0] op_sel_hi:[1,0,1]\n\t"
        "v_pk_fma_f32 v[68:69], v[68:69], v[104:105], v[92:93] op_sel:[0,1,0] op_sel_hi:[1,1,1]\n\t"
        "v_pk_fma_f32 v[70:71], v[70:71], v[104:105], v[94:95] op_sel:[0,1,0] op_sel_hi:[1,1,1]\n\t"
        "v_pk_fma_f32 v[88:89], v[64:65], v[64:65], v[8:9] op_sel_hi:[1,1,0]\n\tv_pk_fma_f32 v[90:91], v[66:67], v[66:67], v[8:9] op_sel_hi:[1,1,0]\n\t"
        "v_pk_fma_f32 v[92:93], v[68:69], v[68:69], v[8:9] op_sel_hi:[1,1,0]\n\tv_pk_fma_f32 v[94:95], v[70:71], v[70:71], v[8:9] op_sel_hi:[1,1,0]\n\t"
        "v_pk_mul_f32 v[88:89], v[64:65], v[88:89] neg_lo:[1,0] neg_hi:[1,0]\n\tv_pk_mul_f32 v[90:91], v[66:67], v[90:91] neg_lo:[1,0] neg_hi:[1,0]\n\t"
        "v_pk_mul_f32 v[92:93], v[68:69], v[92:93] neg_lo:[1,0] neg_hi:[1,0]\n\tv_pk_mul_f32 v[94:95], v[70:71], v[94:95] neg_lo:[1,0] neg_hi:[1,0]\n\t"
        "v_exp_f32 v88, v88\n\tv_exp_f32 v89, v89\n\tv_exp_f32 v90, v90\n\tv_exp_f32 v91, v91\n\tv_exp_f32 v92, v92\n\tv_exp_f32 v93, v93\n\t"
        "v_exp_f32 v94, v94\n\tv_exp_f32 v95, v95\n\tv_pk_fma_f32 v[88:89], v[88:89], v[8:9], v[8:9] op_sel:[0,1,1] op_sel_hi:[1,1,1]\n\t"
        "v_pk_fma_f32 v[90:91], v[90:91], v[8:9], v[8:9] op_sel:[0,1,1] op_sel_hi:[1,1,1]\n\t"
        "v_pk_fma_f32 v[92:93], v[92:93], v[8:9], v[8:9] op_sel:[0,1,1] op_sel_hi:[1,1,1]\n\t"
        "v_pk_fma_f32 v[94:95], v[94:95], v[8:9], v[8:9] op_sel:[0,1,1] op_sel_hi:[1,1,1]\n\tv_rcp_f32 v88, v88\n\tv_rcp_f32 v89, v89\n\tv_rcp_f32 v90, v90\n\t"
        "v_rcp_f32 v91, v91\n\tv_rcp_f32 v92, v92\n\tv_rcp_f32 v93, v93\n\tv_rcp_f32 v94, v94\n\tv_rcp_f32 v95, v95\n\t"
        "v_pk_mul_f32 v[64:65], v[64:65], v[88:89]\n\tbuffer_store_dwordx4 v[136:139], off, %[rt], %[hx] offset:1024 sc1\n\t"
        "v_pk_mul_f32 v[66:67], v[66:67], v[90:91]\n\tv_pk_mul_f32 v[68:69], v[68:69], v[92:93]\n\tv_pk_mul_f32 v[70:71], v[70:71], v[94:95]\n\t"
        "v_cvt_pk_bf16_f32 v148, v64, v65\n\tv_cvt_pk_bf16_f32 v150, v68, v69\n\tv_cvt_pk_bf16_f32 v149, v66, v67\n\tv_cvt_pk_bf16_f32 v151, v70, v71\n\t"
        "v_permlane16_swap_b32 v148, v150\n\ts_nop 0\n\tv_permlane16_swap_b32 v149, v151\n\t.Lmk_u0_e:\n\t"
        : "+{v[152:183]}"(r0), "+{v[184:215]}"(r1), "+{v[216:247]}"(r2), "=&{v[120:151]}"(ho), [hx] "=&s"(hx), [sa] "+s"(sa), [sn] "+s"(sn)
        : [pb0] "{v250}"(n.pb0), [pb1] "{v251}"(n.pb1), [pb2] "{v252}"(n.pb2), [rt] "s"(rt4), [rn] "s"(rt4), [hd] "s"(hd), [hd7] "s"(hd7), "{v253}"(n.e1), "{v255}"(n.e3), "{v15}"(n.av), [rs] "s"(rs4), [one] "{v[12:13]}"(tok), [fl] "s"(fl)
        : "v72", "v73", "v74", "v75", "v76", "v77", "v78", "v79", "v80", "v81", "v82", "v83", "v84", "v85", "v86", "v87", "v88", "v89", "v90", "v91", "v92", "v93", "v94", "v95", "v96", "v97", "v98", "v99", "v100", "v101", "v102", "v103", "v8", "v9", "v16", "v17", "v18", "v19", "v20", "v21", "v22", "v23", "v24", "v25", "v26", "v27", "v28", "v29", "v30", "v31", "v32", "v33", "v34", "v35", "v36", "v37", "v38", "v39", "v40", "v41", "v42", "v43", "v44", "v45", "v46", "v47", "v48", "v49", "v50", "v51", "v52", "v53", "v54", "v55", "v56", "v57", "v58", "v59", "v60", "v61", "v62", "v63", "v64", "v65", "v66", "v67", "v68", "v69", "v70", "v71", "v104", "v105", "v106", "v107", "v108", "v109", "v110", "v111", "v112", "v113", "v114", "v115", "v116", "v117", "v118", "v119", "memory", "scc");
      STAMP(15);
    }
    {
      const unsigned fl = fh;
      sa = sn; sn = hs;
      sn += 0u;
      asm volatile(
        "; BLK u1\n\t.Lmk_u1_s:\n\tds_read_b128 v[72:75], %[pb0] offset:53248\n\tds_read_b128 v[76:79], %[pb0] offset:54272\n\t"
        "ds_read_b128 v[80:83], %[pb0] offset:55296\n\tds_read_b128 v[84:87], %[pb0] offset:56320\n\tds_read_b128 v[88:91], %[pb0] offset:57344\n\t"
        "ds_read_b128 v[92:95], %[pb0] offset:58368\n\tds_read_b128 v[96:99], %[pb0] offset:59392\n\ts_waitcnt vmcnt(20) lgkmcnt(3)\n\t"
        "v_mfma_f32_16x16x32_bf16 v[16:19], v[72:75], v[216:219], 0\n\tds_read_b128 v[100:103], %[pb0] offset:60416\n\t"
        "v_mfma_f32_16x16x32_bf16 v[20:23], v[72:75], v[220:223], 0\n\tv_mfma_f32_16x16x32_bf16 v[24:27], v[76:79], v[216:219], 0\n\t"
        "ds_read_b128 v[72:75], %[pb0] offset:61440\n\tv_mfma_f32_16x16x32_bf16 v[28:31], v[76:79], v[220:223], 0\n\t"
        "v_mfma_f32_16x16x32_bf16 v[32:35], v[80:83], v[216:219], 0\n\tds_read_b128 v[76:79], %[pb0] offset:62464\n\t"
        "v_mfma_f32_16x16x32_bf16 v[36:39], v[80:83], v[220:223], 0\n\tbuffer_load_dwordx4 v[192:195], off, %[rt], %[sa] offset:0 sc1\n\t"
        "v_mfma_f32_16x16x32_bf16 v[40:43], v[84:87], v[216:219], 0\n\tds_read_b128 v[80:83], %[pb0] offset:63488\n\t"
        "v_mfma_f32_16x16x32_bf16 v[44:47], v[84:87], v[220:223], 0\n\ts_waitcnt lgkmcnt(3)\n\tv_mfma_f32_16x16x32_bf16 v[48:51], v[88:91], v[216:219], 0\n\t"
        "ds_read_b128 v[84:87], %[pb0] offset:64512\n\tv_mfma_f32_16x16x32_bf16 v[52:55], v[88:91], v[220:223], 0\n\t"
        "v_mfma_f32_16x16x32_bf16 v[56:59], v[92:95], v[216:219], 0\n\tds_read_b128 v[88:91], %[pb1] offset:0\n\t"
        "v_mfma_f32_16x16x32_bf16 v[60:63], v[92:95], v[220:223], 0\n\tbuffer_load_dwordx4 v[196:199], off, %[rt], %[sa] offset:1024 sc1\n\t"
        "s_add_u32 %[sa], %[sa], 0x2000\n\tv_mfma_f32_16x16x32_bf16 v[64:67], v[96:99], v[216:219], 0\n\tds_read_b128 v[92:95], %[pb1] offset:1024\n\t"
        "v_mfma_f32_16x16x32_bf16 v[68:71], v[96:99], v[220:223], 0\n\ts_add_u32 %[hx], %[hd], 0x4000\n\t"
        "buffer_store_dwordx4 v[140:143], off, %[rt], %[hx] offset:0 sc1\n\ts_waitcnt vmcnt(21)\n\t"
        "v_mfma_f32_16x16x32_bf16 v[16:19], v[100:103], v[224:227], v[16:19]\n\tds_read_b128 v[96:99], %[pb1] offset:2048\n\t"
        "v_mfma_f32_16x16x32_bf16 v[20:23], v[100:103], v[228:231], v[20:23]\n\ts_waitcnt lgkmcnt(3)\n\t"
        "v_mfma_f32_16x16x32_bf16 v[24:27], v[72:75], v[224:227], v[24:27]\n\tds_read_b128 v[100:103], %[pb1] offset:3072\n\t"
        "v_mfma_f32_16x16x32_bf16 v[28:31], v[72:75], v[228:231], v[28:31]\n\tbuffer_load_dwordx4 v[200:203], off, %[rt], %[sa] offset:0 sc1\n\t"
        "v_mfma_f32_16x16x32_bf16 v[32:35], v[76:79], v[224:227], v[32:35]\n\tds_read_b128 v[72:75], %[pb1] offset:4096\n\t"
        "v_mfma_f32_16x16x32_bf16 v[36:39], v[76:79], v[228:231], v[36:39]\n\tv_mfma_f32_16x16x32_bf16 v[40:43], v[80:83], v[224:227], v[40:43]\n\t"
        "ds_read_b128 v[76:79], %[pb1] offset:5120\n\tv_mfma_f32_16x16x32_bf16 v[44:47], v[80:83], v[228:231], v[44:47]\n\t"
        "v_mfma_f32_16x16x32_bf16 v[48:51], v[84:87], v[224:227], v[48:51]\n\tds_read_b128 v[80:83], %[pb1] offset:6144\n\t"
        "v_mfma_f32_16x16x32_bf16 v[52:55], v[84:87], v[228:231], v[52:55]\n\tbuffer_load_dwordx4 v[204:207], off, %[rt], %[sa] offset:1024 sc1\n\t"
        "s_add_u32 %[sa], %[sa], 0x2000\n\ts_waitcnt lgkmcnt(3)\n\tv_mfma_f32_16x16x32_bf16 v[56:59], v[88:91], v[224:227], v[56:59]\n\t"
        "ds_read_b128 v[84:87], %[pb1] offset:7168\n\tv_mfma_f32_16x16x32_bf16 v[60:63], v[88:91], v[228:231], v[60:63]\n\t"
        "v_mfma_f32_16x16x32_bf16 v[64:67], v[92:95], v[224:227], v[64:67]\n\tds_read_b128 v[88:91], %[pb1] offset:8192\n\t"
        "v_mfma_f32_16x16x32_bf16 v[68:71], v[92:95], v[228:231], v[68:71]\n\ts_waitcnt vmcnt(21)\n\t"
        "v_mfma_f32_16x16x32_bf16 v[16:19], v[96:99], v[232:235], v[16:19]\n\tds_read_b128 v[92:95], %[pb1] offset:9216\n\t"
        "v_mfma_f32_16x16x32_bf16 v[20:23], v[96:99], v[236:239], v[20:23]\n\tbuffer_load_dwordx4 v[208:211], off, %[rt], %[sa] offset:0 sc1\n\t"
        "v_mfma_f32_16x16x32_bf16 v[24:27], v[100:103], v[232:235], v[24:27]\n\tds_read_b128 v[96:99], %[pb1] offset:10240\n\t"
        "v_mfma_f32_16x16x32_bf16 v[28:31], v[100:103], v[236:239], v[28:31]\n\ts_waitcnt lgkmcnt(3)\n\t"
        "v_mfma_f32_16x16x32_bf16 v[32:35], v[72:75], v[232:235], v[32:35]\n\tds_read_b128 v[100:103], %[pb1] offset:11264\n\t"
        "v_mfma_f32_16x16x32_bf16 v[36:39], v[72:75], v[236:239], v[36:39]\n\tv_mfma_f32_16x16x32_bf16 v[40:43], v[76:79], v[232:235], v[40:43]\n\t"
        "ds_read_b128 v[72:75], %[pb1] offset:12288\n\tv_mfma_f32_16x16x32_bf16 v[44:47], v[76:79], v[236:239], v[44:47]\n\t"
        "buffer_load_dwordx4 v[212:215], off, %[rt], %[sa] offset:1024 sc1\n\ts_add_u32 %[sa], %[sa], 0xfffda000\n\t"
        "v_mfma_f32_16x16x32_bf16 v[48:51], v[80:83], v[232:235], v[48:51]\n\tds_read_b128 v[76:79], %[pb1] offset:13312\n\t"
        "v_mfma_f32_16x16x32_bf16 v[52:55], v[80:83], v[236:239], v[52:55]\n\tbuffer_store_dwordx4 v[144:147], off, %[rt], %[hx] offset:1024 sc1\n\t"
        "v_mfma_f32_16x16x32_bf16 v[56:59], v[84:87], v[232:235], v[56:59]\n\tds_read_b128 v[80:83], %[pb1] offset:14336\n\t"
        "v_mfma_f32_16x16x32_bf16 v[60:63], v[84:87], v[236:239], v[60:63]\n\ts_waitcnt lgkmcnt(3)\n\t"
        "v_mfma_f32_16x16x32_bf16 v[64:67], v[88:91], v[232:235], v[64:67]\n\tds_read_b128 v[84:87], %[pb1] offset:15360\n\t"
        "v_mfma_f32_16x16x32_bf16 v[68:71], v[88:91], v[236:239], v[68:71]\n\tbuffer_load_dwordx4 v[216:219], off, %[rt], %[sa] offset:0 sc1\n\t"
        "s_waitcnt vmcnt(23)\n\tv_mfma_f32_16x16x32_bf16 v[16:19], v[92:95], v[240:243], v[16:19]\n\tds_read_b128 v[88:91], %[pb0] offset:0\n\t"
        "v_mfma_f32_16x16x32_bf16 v[20:23], v[92:95], v[244:247], v[20:23]\n\tv_mfma_f32_16x16x32_bf16 v[24:27], v[96:99], v[240:243], v[24:27]\n\t"
        "ds_read_b128 v[92:95], %[pb0] offset:1024\n\tv_mfma_f32_16x16x32_bf16 v[28:31], v[96:99], v[244:247], v[28:31]\n\t"
        "v_mfma_f32_16x16x32_bf16 v[32:35], v[100:103], v[240:243], v[32:35]\n\tds_read_b128 v[96:99], %[pb0] offset:2048\n\t"
        "v_mfma_f32_16x16x32_bf16 v[36:39], v[100:103], v[244:247], v[36:39]\n\tbuffer_load_dwordx4 v[220:223], off, %[rt], %[sa] offset:1024 sc1\n\t"
        "s_add_u32 %[sa], %[sa], 0x2000\n\ts_waitcnt lgkmcnt(3)\n\tv_mfma_f32_16x16x32_bf16 v[40:43], v[72:75], v[240:243], v[40:43]\n\t"
        "ds_read_b128 v[100:103], %[pb0] offset:3072\n\tv_mfma_f32_16x16x32_bf16 v[44:47], v[72:75], v[244:247], v[44:47]\n\t"
        "v_mfma_f32_16x16x32_bf16 v[48:51], v[76:79], v[240:243], v[48:51]\n\tds_read_b128 v[72:75], %[pb0] offset:4096\n\t"
        "v_mfma_f32_16x16x32_bf16 v[52:55], v[76:79], v[244:247], v[52:55]\n\tv_mfma_f32_16x16x32_bf16 v[56:59], v[80:83], v[240:243], v[56:59]\n\t"
        "ds_read_b128 v[76:79], %[pb0] offset:5120\n\tv_mfma_f32_16x16x32_bf16 v[60:63], v[80:83], v[244:247], v[60:63]\n\t"
        "buffer_load_dwordx4 v[224:227], off, %[rt], %[sa] offset:0 sc1\n\tv_mfma_f32_16x16x32_bf16 v[64:67], v[84:87], v[240:243], v[64:67]\n\t"
        "ds_read_b128 v[80:83], %[pb0] offset:6144\n\tv_mfma_f32_16x16x32_bf16 v[68:71], v[84:87], v[244:247], v[68:71]\n\ts_waitcnt vmcnt(23) lgkmcnt(3)\n\t"
        "v_mfma_f32_16x16x32_bf16 v[16:19], v[88:91], v[152:155], v[16:19]\n\tds_read_b128 v[84:87], %[pb0] offset:7168\n\t"
        "v_mfma_f32_16x16x32_bf16 v[20:23], v[88:91], v[156:159], v[20:23]\n\tv_mfma_f32_16x16x32_bf16 v[24:27], v[92:95], v[152:155], v[24:27]\n\t"
        "ds_read_b128 v[88:91], %[pb0] offset:8192\n\tv_mfma_f32_16x16x32_bf16 v[28:31], v[92:95], v[156:159], v[28:31]\n\t"
        "buffer_load_dwordx4 v[228:231], off, %[rt], %[sa] offset:1024 sc1\n\ts_add_u32 %[sa], %[sa], 0x2000\n\t"
        "v_mfma_f32_16x16x32_bf16 v[32:35], v[96:99], v[152:155], v[32:35]\n\tds_read_b128 v[92:95], %[pb0] offset:9216\n\t"
        "v_mfma_f32_16x16x32_bf16 v[36:39], v[96:99], v[156:159], v[36:39]\n\tbuffer_store_dwordx4 v[148:151], v15, %[rs], %[hd7] offen sc1\n\t"
        "v_mfma_f32_16x16x32_bf16 v[40:43], v[100:103], v[152:155], v[40:43]\n\tds_read_b128 v[96:99], %[pb0] offset:10240\n\t"
        "v_mfma_f32_16x16x32_bf16 v[44:47], v[100:103], v[156:159], v[44:47]\n\ts_waitcnt lgkmcnt(3)\n\t"
        "v_mfma_f32_16x16x32_bf16 v[48:51], v[72:75], v[152:155], v[48:51]\n\tds_read_b128 v[100:103], %[pb0] offset:11264\n\t"
        "v_mfma_f32_16x16x32_bf16 v[52:55], v[72:75], v[156:159], v[52:55]\n\tbuffer_load_dwordx4 v[232:235], off, %[rt], %[sa] offset:0 sc1\n\t"
        "v_mfma_f32_16x16x32_bf16 v[56:59], v[76:79], v[152:155], v[56:59]\n\tds_read_b128 v[72:75], %[pb0] offset:12288\n\t"
        "v_mfma_f32_16x16x32_bf16 v[60:63], v[76:79], v[156:159], v[60:63]\n\tv_mfma_f32_16x16x32_bf16 v[64:67], v[80:83], v[152:155], v[64:67]\n\t"
        "ds_read_b128 v[76:79], %[pb0] offset:13312\n\tv_mfma_f32_16x16x32_bf16 v[68:71], v[80:83], v[156:159], v[68:71]\n\ts_waitcnt vmcnt(24)\n\t"
        "v_mfma_f32_16x16x32_bf16 v[16:19], v[84:87], v[160:163], v[16:19]\n\tds_read_b128 v[80:83], %[pb0] offset:14336\n\t"
        "v_mfma_f32_16x16x32_bf16 v[20:23], v[84:87], v[164:167], v[20:23]\n\tbuffer_load_dwordx4 v[236:239], off, %[rt], %[sa] offset:1024 sc1\n\t"
        "s_add_u32 %[sa], %[sa], 0x2000\n\ts_waitcnt lgkmcnt(3)\n\tv_mfma_f32_16x16x32_bf16 v[24:27], v[88:91], v[160:163], v[24:27]\n\t"
        "ds_read_b128 v[84:87], %[pb0] offset:15360\n\tv_mfma_f32_16x16x32_bf16 v[28:31], v[88:91], v[164:167], v[28:31]\n\t"
        "v_mfma_f32_16x16x32_bf16 v[32:35], v[92:95], v[160:163], v[32:35]\n\tds_read_b128 v[88:91], %[pb0] offset:16384\n\t"
        "v_mfma_f32_16x16x32_bf16 v[36:39], v[92:95], v[164:167], v[36:39]\n\tv_mfma_f32_16x16x32_bf16 v[40:43], v[96:99], v[160:163], v[40:43]\n\t"
        "ds_read_b128 v[92:95], %[pb0] offset:17408\n\tv_mfma_f32_16x16x32_bf16 v[44:47], v[96:99], v[164:167], v[44:47]\n\t"
        "buffer_load_dwordx4 v[240:243], off, %[rt], %[sa] offset:0 sc1\n\tv_mfma_f32_16x16x32_bf16 v[48:51], v[100:103], v[160:163], v[48:51]\n\t"
        "ds_read_b128 v[96:99], %[pb0] offset:18432\n\tv_mfma_f32_16x16x32_bf16 v[52:55], v[100:103], v[164:167], v[52:55]\n\ts_waitcnt lgkmcnt(3)\n\t"
        "v_mfma_f32_16x16x32_bf16 v[56:59], v[72:75], v[160:163], v[56:59]\n\tds_read_b128 v[100:103], %[pb0] offset:19456\n\t"
        "v_mfma_f32_16x16x32_bf16 v[60:63], v[72:75], v[164:167], v[60:63]\n\tv_mfma_f32_16x16x32_bf16 v[64:67], v[76:79], v[160:163], v[64:67]\n\t"
        "ds_read_b128 v[72:75], %[pb0] offset:20480\n\tv_mfma_f32_16x16x32_bf16 v[68:71], v[76:79], v[164:167], v[68:71]\n\t"
        "buffer_load_dwordx4 v[244:247], off, %[rt], %[sa] offset:1024 sc1\n\ts_add_u32 %[sa], %[sa], 0x2000\n\ts_waitcnt vmcnt(25)\n\t"
        "v_mfma_f32_16x16x32_bf16 v[16:19], v[80:83], v[168:171], v[16:19]\n\tds_read_b128 v[76:79], %[pb0] offset:21504\n\t"
        "v_mfma_f32_16x16x32_bf16 v[20:23], v[80:83], v[172:175], v[20:23]\n\tv_mfma_f32_16x16x32_bf16 v[24:27], v[84:87], v[168:171], v[24:27]\n\t"
        "ds_read_b128 v[80:83], %[pb0] offset:22528\n\tv_mfma_f32_16x16x32_bf16 v[28:31], v[84:87], v[172:175], v[28:31]\n\ts_waitcnt lgkmcnt(3)\n\t"
        "v_mfma_f32_16x16x32_bf16 v[32:35], v[88:91], v[168:171], v[32:35]\n\tds_read_b128 v[84:87], %[pb0] offset:23552\n\t"
        "v_mfma_f32_16x16x32_bf16 v[36:39], v[88:91], v[172:175], v[36:39]\n\tbuffer_load_dwordx4 v[152:155], off, %[rt], %[sa] offset:0 sc1\n\t"
        "v_mfma_f32_16x16x32_bf16 v[40:43], v[92:95], v[168:171], v[40:43]\n\tds_read_b128 v[88:91], %[pb0] offset:24576\n\t"
        "v_mfma_f32_16x16x32_bf16 v[44:47], v[92:95], v[172:175], v[44:47]\n\tv_mfma_f32_16x16x32_bf16 v[48:51], v[96:99], v[168:171], v[48:51]\n\t"
        "ds_read_b128 v[92:95], %[pb0] offset:25600\n\tv_mfma_f32_16x16x32_bf16 v[52:55], v[96:99], v[172:175], v[52:55]\n\t"
        "v_mfma_f32_16x16x32_bf16 v[56:59], v[100:103], v[168:171], v[56:59]\n\tds_read_b128 v[96:99], %[pb0] offset:26624\n\t"
        "v_mfma_f32_16x16x32_bf16 v[60:63], v[100:103], v[172:175], v[60:63]\n\tbuffer_load_dwordx4 v[156:159], off, %[rt], %[sa] offset:1024 sc1\n\t"
        "s_add_u32 %[sa], %[sa], 0x2000\n\ts_waitcnt lgkmcnt(3)\n\tv_mfma_f32_16x16x32_bf16 v[64:67], v[72:75], v[168:171], v[64:67]\n\t"
        "ds_read_b128 v[100:103], %[pb0] offset:27648\n\tv_mfma_f32_16x16x32_bf16 v[68:71], v[72:75], v[172:175], v[68:71]\n\ts_waitcnt vmcnt(25)\n\t"
        "v_mfma_f32_16x16x32_bf16 v[16:19], v[76:79], v[176:179], v[16:19]\n\tds_read_b128 v[72:75], %[pb0] offset:28672\n\t"
        "v_mfma_f32_16x16x32_bf16 v[20:23], v[76:79], v[180:183], v[20:23]\n\tv_mfma_f32_16x16x32_bf16 v[24:27], v[80:83], v[176:179], v[24:27]\n\t"
        "ds_read_b128 v[76:79], %[pb0] offset:29696\n\tv_mfma_f32_16x16x32_bf16 v[28:31], v[80:83], v[180:183], v[28:31]\n\t"
        "buffer_load_dwordx4 v[160:163], off, %[rt], %[sa] offset:0 sc1\n\tv_mfma_f32_16x16x32_bf16 v[32:35], v[84:87], v[176:179], v[32:35]\n\t"
        "ds_read_b128 v[80:83], %[pb0] offset:30720\n\tv_mfma_f32_16x16x32_bf16 v[36:39], v[84:87], v[180:183], v[36:39]\n\ts_waitcnt lgkmcnt(3)\n\t"
        "v_mfma_f32_16x16x32_bf16 v[40:43], v[88:91], v[176:179], v[40:43]\n\tds_read_b128 v[84:87], %[pb0] offset:31744\n\t"
        "v_mfma_f32_16x16x32_bf16 v[44:47], v[88:91], v[180:183], v[44:47]\n\tv_mfma_f32_16x16x32_bf16 v[48:51], v[92:95], v[176:179], v[48:51]\n\t"
        "ds_read_b128 v[88:91], %[pb0] offset:32768\n\tv_mfma_f32_16x16x32_bf16 v[52:55], v[92:95], v[180:183], v[52:55]\n\t"
        "buffer_load_dwordx4 v[164:167], off, %[rt], %[sa] offset:1024 sc1\n\ts_add_u32 %[sa], %[sa], 0x2000\n\t"
        "v_mfma_f32_16x16x32_bf16 v[56:59], v[96:99], v[176:179], v[56:59]\n\tds_read_b128 v[92:95], %[pb0] offset:33792\n\t"
        "v_mfma_f32_16x16x32_bf16 v[60:63], v[96:99], v[180:183], v[60:63]\n\tv_mfma_f32_16x16x32_bf16 v[64:67], v[100:103], v[176:179], v[64:67]\n\t"
        "ds_read_b128 v[96:99], %[pb2] offset:8192\n\tv_mfma_f32_16x16x32_bf16 v[68:71], v[100:103], v[180:183], v[68:71]\n\ts_waitcnt vmcnt(25) lgkmcnt(3)\n\t"
        "v_mfma_f32_16x16x32_bf16 v[16:19], v[72:75], v[184:187], v[16:19]\n\tds_read_b128 v[100:103], %[pb0] offset:34816\n\t"
        "v_mfma_f32_16x16x32_bf16 v[20:23], v[72:75], v[188:191], v[20:23]\n\tbuffer_load_dwordx4 v[168:171], off, %[rt], %[sa] offset:0 sc1\n\t"
        "s_waitcnt vmcnt(9)\n\tbuffer_store_dwordx2 %[one], off, %[rs], %[fl] sc1\n\tv_mfma_f32_16x16x32_bf16 v[24:27], v[76:79], v[184:187], v[24:27]\n\t"
        "ds_read_b128 v[72:75], %[pb0] offset:35840\n\tv_mfma_f32_16x16x32_bf16 v[28:31], v[76:79], v[188:191], v[28:31]\n\t"
        "v_mfma_f32_16x16x32_bf16 v[32:35], v[80:83], v[184:187], v[32:35]\n\tds_read_b128 v[76:79], %[pb0] offset:36864\n\t"
        "v_mfma_f32_16x16x32_bf16 v[36:39], v[80:83], v[188:191], v[36:39]\n\tv_mfma_f32_16x16x32_bf16 v[40:43], v[84:87], v[184:187], v[40:43]\n\t"
        "ds_read_b128 v[80:83], %[pb0] offset:37888\n\tv_mfma_f32_16x16x32_bf16 v[44:47], v[84:87], v[188:191], v[44:47]\n\t"
        "buffer_load_dwordx4 v[172:175], off, %[rt], %[sa] offset:1024 sc1\n\ts_add_u32 %[sa], %[sa], 0x2000\n\ts_waitcnt lgkmcnt(3)\n\t"
        "v_mfma_f32_16x16x32_bf16 v[48:51], v[88:91], v[184:187], v[48:51]\n\tds_read_b128 v[84:87], %[pb0] offset:38912\n\t"
        "v_mfma_f32_16x16x32_bf16 v[52:55], v[88:91], v[188:191], v[52:55]\n\tv_mfma_f32_16x16x32_bf16 v[56:59], v[92:95], v[184:187], v[56:59]\n\t"
        "ds_read_b128 v[88:91], %[pb0] offset:39936\n\tv_mfma_f32_16x16x32_bf16 v[60:63], v[92:95], v[188:191], v[60:63]\n\t"
        "v_mfma_f32_16x16x32_bf16 v[64:67], v[96:99], v[184:187], v[64:67]\n\tds_read_b128 v[92:95], %[pb2] offset:9216\n\t"
        "v_mfma_f32_16x16x32_bf16 v[68:71], v[96:99], v[188:191], v[68:71]\n\tbuffer_load_dwordx4 v[176:179], off, %[rt], %[sa] offset:0 sc1\n\t"
        "s_waitcnt vmcnt(23)\n\tv_mfma_f32_16x16x32_bf16 v[16:19], v[100:103], v[192:195], v[16:19]\n\tds_read_b128 v[96:99], %[pb0] offset:40960\n\t"
        "v_mfma_f32_16x16x32_bf16 v[20:23], v[100:103], v[196:199], v[20:23]\n\ts_waitcnt lgkmcnt(3)\n\t"
        "v_mfma_f32_16x16x32_bf16 v[24:27], v[72:75], v[192:195], v[24:27]\n\tds_read_b128 v[100:103], %[pb0] offset:41984\n\t"
        "v_mfma_f32_16x16x32_bf16 v[28:31], v[72:75], v[196:199], v[28:31]\n\tv_mfma_f32_16x16x32_bf16 v[32:35], v[76:79], v[192:195], v[32:35]\n\t"
        "ds_read_b128 v[72:75], %[pb0] offset:43008\n\tv_mfma_f32_16x16x32_bf16 v[36:39], v[76:79], v[196:199], v[36:39]\n\t"
        "buffer_load_dwordx4 v[180:183], off, %[rt], %[sa] offset:1024 sc1\n\ts_add_u32 %[sa], %[sa], 0xfffda000\n\t"
        "v_mfma_f32_16x16x32_bf16 v[40:43], v[80:83], v[192:195], v[40:43]\n\tds_read_b128 v[76:79], %[pb0] offset:44032\n\t"
        "v_mfma_f32_16x16x32_bf16 v[44:47], v[80:83], v[196:199], v[44:47]\n\tv_mfma_f32_16x16x32_bf16 v[48:51], v[84:87], v[192:195], v[48:51]\n\t"
        "ds_read_b128 v[80:83], %[pb0] offset:45056\n\tv_mfma_f32_16x16x32_bf16 v[52:55], v[84:87], v[196:199], v[52:55]\n\ts_waitcnt lgkmcnt(3)\n\t"
        "v_mfma_f32_16x16x32_bf16 v[56:59], v[88:91], v[192:195], v[56:59]\n\tds_read_b128 v[84:87], %[pb0] offset:46080\n\t"
        "v_mfma_f32_16x16x32_bf16 v[60:63], v[88:91], v[196:199], v[60:63]\n\tbuffer_load_dwordx4 v[184:187], off, %[rt], %[sa] offset:0 sc1\n\t"
        "v_mfma_f32_16x16x32_bf16 v[64:67], v[92:95], v[192:195], v[64:67]\n\tds_read_b128 v[88:91], %[pb2] offset:10240\n\t"
        "v_mfma_f32_16x16x32_bf16 v[68:71], v[92:95], v[196:199], v[68:71]\n\ts_waitcnt vmcnt(22)\n\t"
        "v_mfma_f32_16x16x32_bf16 v[16:19], v[96:99], v[200:203], v[16:19]\n\tds_read_b128 v[92:95], %[pb0] offset:47104\n\t"
        "v_mfma_f32_16x16x32_bf16 v[20:23], v[96:99], v[204:207], v[20:23]\n\tv_mfma_f32_16x16x32_bf16 v[24:27], v[100:103], v[200:203], v[24:27]\n\t"
        "ds_read_b128 v[96:99], %[pb0] offset:48128\n\tv_mfma_f32_16x16x32_bf16 v[28:31], v[100:103], v[204:207], v[28:31]\n\t"
        "buffer_load_dwordx4 v[188:191], off, %[rt], %[sa] offset:1024 sc1\n\ts_add_u32 %[sa], %[sa], 0x2000\n\ts_waitcnt lgkmcnt(3)\n\t"
        "v_mfma_f32_16x16x32_bf16 v[32:35], v[72:75], v[200:203], v[32:35]\n\tds_read_b128 v[100:103], %[pb0] offset:49152\n\t"
        "v_mfma_f32_16x16x32_bf16 v[36:39], v[72:75], v[204:207], v[36:39]\n\tv_mfma_f32_16x16x32_bf16 v[40:43], v[76:79], v[200:203], v[40:43]\n\t"
        "ds_read_b128 v[72:75], %[pb0] offset:50176\n\tv_mfma_f32_16x16x32_bf16 v[44:47], v[76:79], v[204:207], v[44:47]\n\t"
        "v_mfma_f32_16x16x32_bf16 v[48:51], v[80:83], v[200:203], v[48:51]\n\tds_read_b128 v[76:79], %[pb0] offset:51200\n\t"
        "v_mfma_f32_16x16x32_bf16 v[52:55], v[80:83], v[204:207], v[52:55]\n\tbuffer_load_dwordx4 v[192:195], off, %[rt], %[sa] offset:0 sc1\n\t"
        "v_mfma_f32_16x16x32_bf16 v[56:59], v[84:87], v[200:203], v[56:59]\n\tds_read_b128 v[80:83], %[pb0] offset:52224\n\t"
        "v_mfma_f32_16x16x32_bf16 v[60:63], v[84:87], v[204:207], v[60:63]\n\ts_waitcnt lgkmcnt(3)\n\t"
        "v_mfma_f32_16x16x32_bf16 v[64:67], v[88:91], v[200:203], v[64:67]\n\tds_read_b128 v[84:87], %[pb2] offset:11264\n\t"
        "v_mfma_f32_16x16x32_bf16 v[68:71], v[88:91], v[204:207], v[68:71]\n\ts_waitcnt vmcnt(22)\n\t"
        "v_mfma_f32_16x16x32_bf16 v[16:19], v[92:95], v[208:211], v[16:19]\n\ts_barrier\n\tds_read_b128 v[88:91], %[pb1] offset:16384\n\t"
        "v_mfma_f32_16x16x32_bf16 v[20:23], v[92:95], v[212:215], v[20:23]\n\tbuffer_load_dwordx4 v[196:199], off, %[rt], %[sa] offset:1024 sc1\n\t"
        "s_add_u32 %[sa], %[sa], 0x2000\n\tv_mfma_f32_16x16x32_bf16 v[24:27], v[96:99], v[208:211], v[24:27]\n\tds_read_b128 v[92:95], %[pb1] offset:17408\n\t"
        "v_mfma_f32_16x16x32_bf16 v[28:31], v[96:99], v[212:215], v[28:31]\n\tv_mfma_f32_16x16x32_bf16 v[32:35], v[100:103], v[208:211], v[32:35]\n\t"
        "ds_read_b128 v[96:99], %[pb1] offset:18432\n\tv_mfma_f32_16x16x32_bf16 v[36:39], v[100:103], v[212:215], v[36:39]\n\ts_waitcnt lgkmcnt(3)\n\t"
        "v_mfma_f32_16x16x32_bf16 v[40:43], v[72:75], v[208:211], v[40:43]\n\tds_read_b128 v[100:103], %[pb1] offset:19456\n\t"
        "v_mfma_f32_16x16x32_bf16 v[44:47], v[72:75], v[212:215], v[44:47]\n\tbuffer_load_dwordx4 v[200:203], off, %[rt], %[sa] offset:0 sc1\n\t"
        "v_mfma_f32_16x16x32_bf16 v[48:51], v[76:79], v[208:211], v[48:51]\n\tds_read_b128 v[72:75], %[pb1] offset:20480\n\t"
        "v_mfma_f32_16x16x32_bf16 v[52:55], v[76:79], v[212:215], v[52:55]\n\tv_mfma_f32_16x16x32_bf16 v[56:59], v[80:83], v[208:211], v[56:59]\n\t"
        "ds_read_b128 v[76:79], %[pb1] offset:21504\n\tv_mfma_f32_16x16x32_bf16 v[60:63], v[80:83], v[212:215], v[60:63]\n\t"
        "v_mfma_f32_16x16x32_bf16 v[64:67], v[84:87], v[208:211], v[64:67]\n\tds_read_b128 v[80:83], %[pb1] offset:22528\n\t"
        "v_mfma_f32_16x16x32_bf16 v[68:71], v[84:87], v[212:215], v[68:71]\n\tbuffer_load_dwordx4 v[204:207], off, %[rt], %[sa] offset:1024 sc1\n\t"
        "s_add_u32 %[sa], %[sa], 0x2000\n\ts_waitcnt vmcnt(22) lgkmcnt(3)\n\tv_mfma_f32_16x16x32_bf16 v[16:19], v[88:91], v[216:219], v[16:19]\n\t"
        "ds_read_b128 v[84:87], %[pb1] offset:23552\n\tv_mfma_f32_16x16x32_bf16 v[20:23], v[88:91], v[220:223], v[20:23]\n\t"
        "v_mfma_f32_16x16x32_bf16 v[24:27], v[92:95], v[216:219], v[24:27]\n\tds_read_b128 v[88:91], %[pb1] offset:24576\n\t"
        "v_mfma_f32_16x16x32_bf16 v[28:31], v[92:95], v[220:223], v[28:31]\n\tv_mfma_f32_16x16x32_bf16 v[32:35], v[96:99], v[216:219], v[32:35]\n\t"
        "ds_read_b128 v[92:95], %[pb1] offset:25600\n\tv_mfma_f32_16x16x32_bf16 v[36:39], v[96:99], v[220:223], v[36:39]\n\t"
        "buffer_load_dwordx4 v[208:211], off, %[rt], %[sa] offset:0 sc1\n\tv_mfma_f32_16x16x32_bf16 v[40:43], v[100:103], v[216:219], v[40:43]\n\t"
        "ds_read_b128 v[96:99], %[pb1] offset:26624\n\tv_mfma_f32_16x16x32_bf16 v[44:47], v[100:103], v[220:223], v[44:47]\n\ts_waitcnt lgkmcnt(3)\n\t"
        "v_mfma_f32_16x16x32_bf16 v[48:51], v[72:75], v[216:219], v[48:51]\n\tds_read_b128 v[100:103], %[pb1] offset:27648\n\t"
        "v_mfma_f32_16x16x32_bf16 v[52:55], v[72:75], v[220:223], v[52:55]\n\tv_mfma_f32_16x16x32_bf16 v[56:59], v[76:79], v[216:219], v[56:59]\n\t"
        "ds_read_b128 v[72:75], %[pb1] offset:28672\n\tv_mfma_f32_16x16x32_bf16 v[60:63], v[76:79], v[220:223], v[60:63]\n\t"
        "buffer_load_dwordx4 v[212:215], off, %[rt], %[sa] offset:1024 sc1\n\ts_add_u32 %[sa], %[sa], 0x2000\n\t"
        "v_mfma_f32_16x16x32_bf16 v[64:67], v[80:83], v[216:219], v[64:67]\n\tds_read_b128 v[76:79], %[pb1] offset:29696\n\t"
        "v_mfma_f32_16x16x32_bf16 v[68:71], v[80:83], v[220:223], v[68:71]\n\ts_waitcnt vmcnt(22)\n\t"
        "v_mfma_f32_16x16x32_bf16 v[16:19], v[84:87], v[224:227], v[16:19]\n\tds_read_b128 v[80:83], %[pb1] offset:30720\n\t"
        "v_mfma_f32_16x16x32_bf16 v[20:23], v[84:87], v[228:231], v[20:23]\n\ts_waitcnt lgkmcnt(3)\n\t"
        "v_mfma_f32_16x16x32_bf16 v[24:27], v[88:91], v[224:227], v[24:27]\n\tds_read_b128 v[84:87], %[pb1] offset:31744\n\t"
        "v_mfma_f32_16x16x32_bf16 v[28:31], v[88:91], v[228:231], v[28:31]\n\tbuffer_load_dwordx4 v[216:219], off, %[rt], %[sa] offset:0 sc1\n\t"
        "v_mfma_f32_16x16x32_bf16 v[32:35], v[92:95], v[224:227], v[32:35]\n\tds_read_b128 v[88:91], %[pb1] offset:32768\n\t"
        "v_mfma_f32_16x16x32_bf16 v[36:39], v[92:95], v[228:231], v[36:39]\n\tv_mfma_f32_16x16x32_bf16 v[40:43], v[96:99], v[224:227], v[40:43]\n\t"
        "ds_read_b128 v[92:95], %[pb1] offset:33792\n\tv_mfma_f32_16x16x32_bf16 v[44:47], v[96:99], v[228:231], v[44:47]\n\t"
        "v_mfma_f32_16x16x32_bf16 v[48:51], v[100:103], v[224:227], v[48:51]\n\tds_read_b128 v[96:99], %[pb1] offset:34816\n\t"
        "v_mfma_f32_16x16x32_bf16 v[52:55], v[100:103], v[228:231], v[52:55]\n\tbuffer_load_dwordx4 v[220:223], off, %[rt], %[sa] offset:1024 sc1\n\t"
        "s_add_u32 %[sa], %[sa], 0x2000\n\ts_waitcnt lgkmcnt(3)\n\tv_mfma_f32_16x16x32_bf16 v[56:59], v[72:75], v[224:227], v[56:59]\n\t"
        "ds_read_b128 v[100:103], %[pb1] offset:35840\n\tv_mfma_f32_16x16x32_bf16 v[60:63], v[72:75], v[228:231], v[60:63]\n\t"
        "v_mfma_f32_16x16x32_bf16 v[64:67], v[76:79], v[224:227], v[64:67]\n\tds_read_b128 v[72:75], %[pb1] offset:36864\n\t"
        "v_mfma_f32_16x16x32_bf16 v[68:71], v[76:79], v[228:231], v[68:71]\n\ts_waitcnt vmcnt(21)\n\t"
        "v_mfma_f32_16x16x32_bf16 v[16:19], v[80:83], v[232:235], v[16:19]\n\tds_read_b128 v[76:79], %[pb1] offset:37888\n\t"
        "v_mfma_f32_16x16x32_bf16 v[20:23], v[80:83], v[236:239], v[20:23]\n\tbuffer_load_dwordx4 v[224:227], off, %[rt], %[sa] offset:0 sc1\n\t"
        "v_mfma_f32_16x16x32_bf16 v[24:27], v[84:87], v[232:235], v[24:27]\n\tds_read_b128 v[80:83], %[pb1] offset:38912\n\t"
        "v_mfma_f32_16x16x32_bf16 v[28:31], v[84:87], v[236:239], v[28:31]\n\t.p2align 12,,12\n\t.Lpg12:\n\ts_cbranch_execz .Lpgret\n\ts_waitcnt lgkmcnt(3)\n\t"
        "v_mfma_f32_16x16x32_bf16 v[32:35], v[88:91], v[232:235], v[32:35]\n\tds_read_b128 v[84:87], %[pb1] offset:39936\n\t"
        "v_mfma_f32_16x16x32_bf16 v[36:39], v[88:91], v[236:239], v[36:39]\n\tv_mfma_f32_16x16x32_bf16 v[40:43], v[92:95], v[232:235], v[40:43]\n\t"
        "ds_read_b128 v[88:91], %[pb1] offset:40960\n\tv_mfma_f32_16x16x32_bf16 v[44:47], v[92:95], v[236:239], v[44:47]\n\t"
        "buffer_load_dwordx4 v[228:231], off, %[rt], %[sa] offset:1024 sc1\n\ts_add_u32 %[sa], %[sa], 0x2000\n\t"
        "v_mfma_f32_16x16x32_bf16 v[48:51], v[96:99], v[232:235], v[48:51]\n\tds_read_b128 v[92:95], %[pb1] offset:41984\n\t"
        "v_mfma_f32_16x16x32_bf16 v[52:55], v[96:99], v[236:239], v[52:55]\n\tv_mfma_f32_16x16x32_bf16 v[56:59], v[100:103], v[232:235], v[56:59]\n\t"
        "ds_read_b128 v[96:99], %[pb1] offset:43008\n\tv_mfma_f32_16x16x32_bf16 v[60:63], v[100:103], v[236:239], v[60:63]\n\ts_waitcnt lgkmcnt(3)\n\t"
        "v_mfma_f32_16x16x32_bf16 v[64:67], v[72:75], v[232:235], v[64:67]\n\tds_read_b128 v[100:103], %[pb1] offset:44032\n\t"
        "v_mfma_f32_16x16x32_bf16 v[68:71], v[72:75], v[236:239], v[68:71]\n\tbuffer_load_dwordx4 v[232:235], off, %[rt], %[sa] offset:0 sc1\n\t"
        "s_waitcnt vmcnt(22)\n\tv_mfma_f32_16x16x32_bf16 v[16:19], v[76:79], v[240:243], v[16:19]\n\tds_read_b128 v[72:75], %[pb1] offset:45056\n\t"
        "v_mfma_f32_16x16x32_bf16 v[20:23], v[76:79], v[244:247], v[20:23]\n\tv_mfma_f32_16x16x32_bf16 v[24:27], v[80:83], v[240:243], v[24:27]\n\t"
        "ds_read_b128 v[76:79], %[pb1] offset:46080\n\tv_mfma_f32_16x16x32_bf16 v[28:31], v[80:83], v[244:247], v[28:31]\n\t"
        "v_mfma_f32_16x16x32_bf16 v[32:35], v[84:87], v[240:243], v[32:35]\n\tds_read_b128 v[80:83], %[pb1] offset:47104\n\t"
        "v_mfma_f32_16x16x32_bf16 v[36:39], v[84:87], v[244:247], v[36:39]\n\tbuffer_load_dwordx4 v[236:239], off, %[rt], %[sa] offset:1024 sc1\n\t"
        "s_add_u32 %[sa], %[sa], 0x2000\n\ts_waitcnt lgkmcnt(3)\n\tv_mfma_f32_16x16x32_bf16 v[40:43], v[88:91], v[240:243], v[40:43]\n\t"
        "ds_read_b128 v[84:87], %[pb1] offset:48128\n\tv_mfma_f32_16x16x32_bf16 v[44:47], v[88:91], v[244:247], v[44:47]\n\t"
        "v_mfma_f32_16x16x32_bf16 v[48:51], v[92:95], v[240:243], v[48:51]\n\tds_read_b128 v[88:91], %[pb1] offset:49152\n\t"
        "v_mfma_f32_16x16x32_bf16 v[52:55], v[92:95], v[244:247], v[52:55]\n\tv_mfma_f32_16x16x32_bf16 v[56:59], v[96:99], v[240:243], v[56:59]\n\t"
        "ds_read_b128 v[92:95], %[pb1] offset:50176\n\tv_mfma_f32_16x16x32_bf16 v[60:63], v[96:99], v[244:247], v[60:63]\n\t"
        "v_mfma_f32_16x16x32_bf16 v[64:67], v[100:103], v[240:243], v[64:67]\n\tds_read_b128 v[96:99], %[pb1] offset:51200\n\t"
        "v_mfma_f32_16x16x32_bf16 v[68:71], v[100:103], v[244:247], v[68:71]\n\tbuffer_load_dwordx4 v[240:243], off, %[rt], %[sa] offset:0 sc1\n\t"
        "s_waitcnt vmcnt(22) lgkmcnt(3)\n\tv_mfma_f32_16x16x32_bf16 v[16:19], v[72:75], v[152:155], v[16:19]\n\t"
        "ds_read_b128 v[100:103], %[pb1] offset:52224\n\tv_mfma_f32_16x16x32_bf16 v[20:23], v[72:75], v[156:159], v[20:23]\n\t"
        "v_mfma_f32_16x16x32_bf16 v[24:27], v[76:79], v[152:155], v[24:27]\n\tds_read_b128 v[72:75], %[pb1] offset:53248\n\t"
        "v_mfma_f32_16x16x32_bf16 v[28:31], v[76:79], v[156:159], v[28:31]\n\tv_mfma_f32_16x16x32_bf16 v[32:35], v[80:83], v[152:155], v[32:35]\n\t"
        "ds_read_b128 v[76:79], %[pb1] offset:54272\n\tv_mfma_f32_16x16x32_bf16 v[36:39], v[80:83], v[156:159], v[36:39]\n\t"
        "buffer_load_dwordx4 v[244:247], off, %[rt], %[sa] offset:1024 sc1\n\ts_add_u32 %[sa], %[sa], 0x2000\n\t"
        "v_mfma_f32_16x16x32_bf16 v[40:43], v[84:87], v[152:155], v[40:43]\n\tds_read_b128 v[80:83], %[pb1] offset:55296\n\t"
        "v_mfma_f32_16x16x32_bf16 v[44:47], v[84:87], v[156:159], v[44:47]\n\ts_waitcnt lgkmcnt(3)\n\t"
        "v_mfma_f32_16x16x32_bf16 v[48:51], v[88:91], v[152:155], v[48:51]\n\tds_read_b128 v[84:87], %[pb1] offset:56320\n\t"
        "v_mfma_f32_16x16x32_bf16 v[52:55], v[88:91], v[156:159], v[52:55]\n\tv_mfma_f32_16x16x32_bf16 v[56:59], v[92:95], v[152:155], v[56:59]\n\t"
        "ds_read_b128 v[88:91], %[pb1] offset:57344\n\tv_mfma_f32_16x16x32_bf16 v[60:63], v[92:95], v[156:159], v[60:63]\n\t"
        "v_mfma_f32_16x16x32_bf16 v[64:67], v[96:99], v[152:155], v[64:67]\n\tds_read_b128 v[92:95], %[pb1] offset:58368\n\t"
        "v_mfma_f32_16x16x32_bf16 v[68:71], v[96:99], v[156:159], v[68:71]\n\tbuffer_load_dwordx4 v[152:155], off, %[rt], %[sa] offset:0 sc1\n\t"
        "s_waitcnt vmcnt(22)\n\tv_mfma_f32_16x16x32_bf16 v[16:19], v[100:103], v[160:163], v[16:19]\n\tds_read_b128 v[96:99], %[pb1] offset:59392\n\t"
        "v_mfma_f32_16x16x32_bf16 v[20:23], v[100:103], v[164:167], v[20:23]\n\ts_waitcnt lgkmcnt(3)\n\t"
        "v_mfma_f32_16x16x32_bf16 v[24:27], v[72:75], v[160:163], v[24:27]\n\tds_read_b128 v[100:103], %[pb1] offset:60416\n\t"
        "v_mfma_f32_16x16x32_bf16 v[28:31], v[72:75], v[164:167], v[28:31]\n\tv_mfma_f32_16x16x32_bf16 v[32:35], v[76:79], v[160:163], v[32:35]\n\t"
        "ds_read_b128 v[72:75], %[pb1] offset:61440\n\tv_mfma_f32_16x16x32_bf16 v[36:39], v[76:79], v[164:167], v[36:39]\n\t"
        "buffer_load_dwordx4 v[156:159], off, %[rt], %[sa] offset:1024 sc1\n\ts_add_u32 %[sa], %[sa], 0x2000\n\t"
        "v_mfma_f32_16x16x32_bf16 v[40:43], v[80:83], v[160:163], v[40:43]\n\tds_read_b128 v[76:79], %[pb1] offset:62464\n\t"
        "v_mfma_f32_16x16x32_bf16 v[44:47], v[80:83], v[164:167], v[44:47]\n\tv_mfma_f32_16x16x32_bf16 v[48:51], v[84:87], v[160:163], v[48:51]\n\t"
        "ds_read_b128 v[80:83], %[pb1] offset:63488\n\tv_mfma_f32_16x16x32_bf16 v[52:55], v[84:87], v[164:167], v[52:55]\n\ts_waitcnt lgkmcnt(3)\n\t"
        "v_mfma_f32_16x16x32_bf16 v[56:59], v[88:91], v[160:163], v[56:59]\n\tds_read_b128 v[84:87], %[pb1] offset:64512\n\t"
        "v_mfma_f32_16x16x32_bf16 v[60:63], v[88:91], v[164:167], v[60:63]\n\tv_mfma_f32_16x16x32_bf16 v[64:67], v[92:95], v[160:163], v[64:67]\n\t"
        "ds_read_b128 v[88:91], %[pb2] offset:0\n\tv_mfma_f32_16x16x32_bf16 v[68:71], v[92:95], v[164:167], v[68:71]\n\t"
        "buffer_load_dwordx4 v[160:163], off, %[rt], %[sa] offset:0 sc1\n\ts_waitcnt vmcnt(21)\n\t"
        "v_mfma_f32_16x16x32_bf16 v[16:19], v[96:99], v[168:171], v[16:19]\n\tds_read_b128 v[92:95], %[pb2] offset:1024\n\t"
        "v_mfma_f32_16x16x32_bf16 v[20:23], v[96:99], v[172:175], v[20:23]\n\tv_mfma_f32_16x16x32_bf16 v[24:27], v[100:103], v[168:171], v[24:27]\n\t"
        "ds_read_b128 v[96:99], %[pb2] offset:2048\n\tv_mfma_f32_16x16x32_bf16 v[28:31], v[100:103], v[172:175], v[28:31]\n\ts_waitcnt lgkmcnt(3)\n\t"
        "v_mfma_f32_16x16x32_bf16 v[32:35], v[72:75], v[168:171], v[32:35]\n\tds_read_b128 v[100:103], %[pb2] offset:3072\n\t"
        "v_mfma_f32_16x16x32_bf16 v[36:39], v[72:75], v[172:175], v[36:39]\n\tbuffer_load_dwordx4 v[164:167], off, %[rt], %[sa] offset:1024 sc1\n\t"
        "s_add_u32 %[sa], %[sa], 0x2000\n\tv_mfma_f32_16x16x32_bf16 v[40:43], v[76:79], v[168:171], v[40:43]\n\tds_read_b128 v[72:75], %[pb2] offset:4096\n\t"
        "v_mfma_f32_16x16x32_bf16 v[44:47], v[76:79], v[172:175], v[44:47]\n\tv_mfma_f32_16x16x32_bf16 v[48:51], v[80:83], v[168:171], v[48:51]\n\t"
        "ds_read_b128 v[76:79], %[pb2] offset:5120\n\tv_mfma_f32_16x16x32_bf16 v[52:55], v[80:83], v[172:175], v[52:55]\n\t"
        "v_mfma_f32_16x16x32_bf16 v[56:59], v[84:87], v[168:171], v[56:59]\n\tds_read_b128 v[80:83], %[pb2] offset:6144\n\t"
        "v_mfma_f32_16x16x32_bf16 v[60:63], v[84:87], v[172:175], v[60:63]\n\ts_waitcnt lgkmcnt(3)\n\t"
        "v_mfma_f32_16x16x32_bf16 v[64:67], v[88:91], v[168:171], v[64:67]\n\tds_read_b128 v[84:87], %[pb2] offset:7168\n\t"
        "v_mfma_f32_16x16x32_bf16 v[68:71], v[88:91], v[172:175], v[68:71]\n\tbuffer_load_dwordx4 v[168:171], off, %[rt], %[sa] offset:0 sc1\n\t"
        "s_waitcnt vmcnt(21)\n\tv_mfma_f32_16x16x32_bf16 v[16:19], v[92:95], v[176:179], v[16:19]\n\ts_barrier\n\t"
        "ds_read_b128 v[88:91], %[pb0] offset:53248\n\tv_mfma_f32_16x16x32_bf16 v[20:23], v[92:95], v[180:183], v[20:23]\n\t"
        "v_mfma_f32_16x16x32_bf16 v[24:27], v[96:99], v[176:179], v[24:27]\n\tds_read_b128 v[92:95], %[pb0] offset:54272\n\t"
        "v_mfma_f32_16x16x32_bf16 v[28:31], v[96:99], v[180:183], v[28:31]\n\tv_mfma_f32_16x16x32_bf16 v[32:35], v[100:103], v[176:179], v[32:35]\n\t"
        "ds_read_b128 v[96:99], %[pb0] offset:55296\n\tv_mfma_f32_16x16x32_bf16 v[36:39], v[100:103], v[180:183], v[36:39]\n\t"
        "buffer_load_dwordx4 v[172:175], off, %[rt], %[sa] offset:1024 sc1\n\ts_add_u32 %[sa], %[sa], 0x2000\n\ts_waitcnt lgkmcnt(3)\n\t"
        "v_mfma_f32_16x16x32_bf16 v[40:43], v[72:75], v[176:179], v[40:43]\n\tds_read_b128 v[100:103], %[pb0] offset:56320\n\t"
        "v_mfma_f32_16x16x32_bf16 v[44:47], v[72:75], v[180:183], v[44:47]\n\tv_mfma_f32_16x16x32_bf16 v[48:51], v[76:79], v[176:179], v[48:51]\n\t"
        "ds_read_b128 v[72:75], %[pb0] offset:57344\n\tv_mfma_f32_16x16x32_bf16 v[52:55], v[76:79], v[180:183], v[52:55]\n\t"
        "v_mfma_f32_16x16x32_bf16 v[56:59], v[80:83], v[176:179], v[56:59]\n\tds_read_b128 v[76:79], %[pb0] offset:58368\n\t"
        "v_mfma_f32_16x16x32_bf16 v[60:63], v[80:83], v[180:183], v[60:63]\n\tv_mfma_f32_16x16x32_bf16 v[64:67], v[84:87], v[176:179], v[64:67]\n\t"
        "ds_read_b128 v[80:83], %[pb0] offset:59392\n\tv_mfma_f32_16x16x32_bf16 v[68:71], v[84:87], v[180:183], v[68:71]\n\t"
        "buffer_load_dwordx4 v[176:179], off, %[rt], %[sa] offset:0 sc1\n\ts_waitcnt vmcnt(21) lgkmcnt(3)\n\t"
        "v_mfma_f32_16x16x32_bf16 v[16:19], v[88:91], v[184:187], v[16:19]\n\tds_read_b128 v[84:87], %[pb0] offset:60416\n\t"
        "v_mfma_f32_16x16x32_bf16 v[20:23], v[88:91], v[188:191], v[20:23]\n\tv_mfma_f32_16x16x32_bf16 v[24:27], v[92:95], v[184:187], v[24:27]\n\t"
        "ds_read_b128 v[88:91], %[pb0] offset:61440\n\tv_mfma_f32_16x16x32_bf16 v[28:31], v[92:95], v[188:191], v[28:31]\n\t"
        "v_mfma_f32_16x16x32_bf16 v[32:35], v[96:99], v[184:187], v[32:35]\n\tds_read_b128 v[92:95], %[pb0] offset:62464\n\t"
        "v_mfma_f32_16x16x32_bf16 v[36:39], v[96:99], v[188:191], v[36:39]\n\tbuffer_load_dwordx4 v[180:183], off, %[rt], %[sa] offset:1024 sc1\n\t"
        "v_mfma_f32_16x16x32_bf16 v[40:43], v[100:103], v[184:187], v[40:43]\n\tds_read_b128 v[96:99], %[pb0] offset:63488\n\t"
        "v_mfma_f32_16x16x32_bf16 v[44:47], v[100:103], v[188:191], v[44:47]\n\ts_waitcnt lgkmcnt(3)\n\t"
        "v_mfma_f32_16x16x32_bf16 v[48:51], v[72:75], v[184:187], v[48:51]\n\tds_read_b128 v[100:103], %[pb0] offset:64512\n\t"
        "v_mfma_f32_16x16x32_bf16 v[52:55], v[72:75], v[188:191], v[52:55]\n\tv_mfma_f32_16x16x32_bf16 v[56:59], v[76:79], v[184:187], v[56:59]\n\t"
        "ds_read_b128 v[72:75], %[pb1] offset:0\n\tv_mfma_f32_16x16x32_bf16 v[60:63], v[76:79], v[188:191], v[60:63]\n\t"
        "v_mfma_f32_16x16x32_bf16 v[64:67], v[80:83], v[184:187], v[64:67]\n\tds_read_b128 v[76:79], %[pb1] offset:1024\n\t"
        "v_mfma_f32_16x16x32_bf16 v[68:71], v[80:83], v[188:191], v[68:71]\n\ts_waitcnt vmcnt(20)\n\t"
        "v_mfma_f32_16x16x32_bf16 v[16:19], v[84:87], v[192:195], v[16:19]\n\tds_read_b128 v[80:83], %[pb1] offset:2048\n\t"
        "v_mfma_f32_16x16x32_bf16 v[20:23], v[84:87], v[196:199], v[20:23]\n\ts_waitcnt lgkmcnt(3)\n\t"
        "v_mfma_f32_16x16x32_bf16 v[24:27], v[88:91], v[192:195], v[24:27]\n\tds_read_b128 v[84:87], %[pb1] offset:3072\n\t"
        "v_mfma_f32_16x16x32_bf16 v[28:31], v[88:91], v[196:199], v[28:31]\n\tv_mfma_f32_16x16x32_bf16 v[32:35], v[92:95], v[192:195], v[32:35]\n\t"
        "ds_read_b128 v[88:91], %[pb1] offset:4096\n\tv_mfma_f32_16x16x32_bf16 v[36:39], v[92:95], v[196:199], v[36:39]\n\t"
        "v_mfma_f32_16x16x32_bf16 v[40:43], v[96:99], v[192:195], v[40:43]\n\tds_read_b128 v[92:95], %[pb1] offset:5120\n\t"
        "v_mfma_f32_16x16x32_bf16 v[44:47], v[96:99], v[196:199], v[44:47]\n\tv_mfma_f32_16x16x32_bf16 v[48:51], v[100:103], v[192:195], v[48:51]\n\t"
        "ds_read_b128 v[96:99], %[pb1] offset:6144\n\tv_mfma_f32_16x16x32_bf16 v[52:55], v[100:103], v[196:199], v[52:55]\n\ts_waitcnt lgkmcnt(3)\n\t"
        "v_mfma_f32_16x16x32_bf16 v[56:59], v[72:75], v[192:195], v[56:59]\n\tds_read_b128 v[100:103], %[pb1] offset:7168\n\t"
        "v_mfma_f32_16x16x32_bf16 v[60:63], v[72:75], v[196:199], v[60:63]\n\tv_mfma_f32_16x16x32_bf16 v[64:67], v[76:79], v[192:195], v[64:67]\n\t"
        "ds_read_b128 v[72:75], %[pb1] offset:8192\n\tv_mfma_f32_16x16x32_bf16 v[68:71], v[76:79], v[196:199], v[68:71]\n\ts_waitcnt vmcnt(18)\n\t"
        "v_mfma_f32_16x16x32_bf16 v[16:19], v[80:83], v[200:203], v[16:19]\n\tds_read_b128 v[76:79], %[pb1] offset:9216\n\t"
        "v_mfma_f32_16x16x32_bf16 v[20:23], v[80:83], v[204:207], v[20:23]\n\tv_mfma_f32_16x16x32_bf16 v[24:27], v[84:87], v[200:203], v[24:27]\n\t"
        "ds_read_b128 v[80:83], %[pb1] offset:10240\n\tv_mfma_f32_16x16x32_bf16 v[28:31], v[84:87], v[204:207], v[28:31]\n\ts_waitcnt lgkmcnt(3)\n\t"
        "v_mfma_f32_16x16x32_bf16 v[32:35], v[88:91], v[200:203], v[32:35]\n\tds_read_b128 v[84:87], %[pb1] offset:11264\n\t"
        "v_mfma_f32_16x16x32_bf16 v[36:39], v[88:91], v[204:207], v[36:39]\n\tv_mfma_f32_16x16x32_bf16 v[40:43], v[92:95], v[200:203], v[40:43]\n\t"
        "ds_read_b128 v[88:91], %[pb1] offset:12288\n\tv_mfma_f32_16x16x32_bf16 v[44:47], v[92:95], v[204:207], v[44:47]\n\t"
        "v_mfma_f32_16x16x32_bf16 v[48:51], v[96:99], v[200:203], v[48:51]\n\tds_read_b128 v[92:95], %[pb1] offset:13312\n\t"
        "v_mfma_f32_16x16x32_bf16 v[52:55], v[96:99], v[204:207], v[52:55]\n\tv_mfma_f32_16x16x32_bf16 v[56:59], v[100:103], v[200:203], v[56:59]\n\t"
        "ds_read_b128 v[96:99], %[pb1] offset:14336\n\tv_mfma_f32_16x16x32_bf16 v[60:63], v[100:103], v[204:207], v[60:63]\n\ts_waitcnt lgkmcnt(3)\n\t"
        "v_mfma_f32_16x16x32_bf16 v[64:67], v[72:75], v[200:203], v[64:67]\n\tds_read_b128 v[100:103], %[pb1] offset:15360\n\t"
        "v_mfma_f32_16x16x32_bf16 v[68:71], v[72:75], v[204:207], v[68:71]\n\ts_waitcnt vmcnt(16)\n\t"
        "v_mfma_f32_16x16x32_bf16 v[16:19], v[76:79], v[208:211], v[16:19]\n\ts_barrier\n\tds_read_b128 v[72:75], %[pb0] offset:0\n\t"
        "buffer_load_dwordx4 v[184:187], off, %[rn], %[sn] offset:0 sc1\n\tv_mfma_f32_16x16x32_bf16 v[20:23], v[76:79], v[212:215], v[20:23]\n\t"
        "v_mfma_f32_16x16x32_bf16 v[24:27], v[80:83], v[208:211], v[24:27]\n\tds_read_b128 v[76:79], %[pb0] offset:1024\n\t"
        "v_mfma_f32_16x16x32_bf16 v[28:31], v[80:83], v[212:215], v[28:31]\n\tv_mfma_f32_16x16x32_bf16 v[32:35], v[84:87], v[208:211], v[32:35]\n\t"
        "ds_read_b128 v[80:83], %[pb0] offset:2048\n\tv_mfma_f32_16x16x32_bf16 v[36:39], v[84:87], v[212:215], v[36:39]\n\ts_waitcnt lgkmcnt(3)\n\t"
        "v_mfma_f32_16x16x32_bf16 v[40:43], v[88:91], v[208:211], v[40:43]\n\tds_read_b128 v[84:87], %[pb0] offset:3072\n\t"
        "buffer_load_dwordx4 v[188:191], off, %[rn], %[sn] offset:1024 sc1\n\ts_add_u32 %[sn], %[sn], 0x2000\n\t"
        "v_mfma_f32_16x16x32_bf16 v[44:47], v[88:91], v[212:215], v[44:47]\n\tv_mfma_f32_16x16x32_bf16 v[48:51], v[92:95], v[208:211], v[48:51]\n\t"
        "ds_read_b128 v[88:91], %[pb0] offset:4096\n\tv_mfma_f32_16x16x32_bf16 v[52:55], v[92:95], v[212:215], v[52:55]\n\t"
        "v_mfma_f32_16x16x32_bf16 v[56:59], v[96:99], v[208:211], v[56:59]\n\tds_read_b128 v[92:95], %[pb0] offset:5120\n\t"
        "v_mfma_f32_16x16x32_bf16 v[60:63], v[96:99], v[212:215], v[60:63]\n\tv_mfma_f32_16x16x32_bf16 v[64:67], v[100:103], v[208:211], v[64:67]\n\t"
        "ds_read_b128 v[96:99], %[pb0] offset:6144\n\tbuffer_load_dwordx4 v[192:195], off, %[rn], %[sn] offset:0 sc1\n\t"
        "v_mfma_f32_16x16x32_bf16 v[68:71], v[100:103], v[212:215], v[68:71]\n\ts_waitcnt vmcnt(17) lgkmcnt(3)\n\t"
        "v_mfma_f32_16x16x32_bf16 v[16:19], v[72:75], v[216:219], v[16:19]\n\tds_read_b128 v[100:103], %[pb0] offset:7168\n\t"
        "v_mfma_f32_16x16x32_bf16 v[20:23], v[72:75], v[220:223], v[20:23]\n\tv_mfma_f32_16x16x32_bf16 v[24:27], v[76:79], v[216:219], v[24:27]\n\t"
        "ds_read_b128 v[72:75], %[pb0] offset:8192\n\tv_mfma_f32_16x16x32_bf16 v[28:31], v[76:79], v[220:223], v[28:31]\n\t"
        "v_mfma_f32_16x16x32_bf16 v[32:35], v[80:83], v[216:219], v[32:35]\n\tds_read_b128 v[76:79], %[pb0] offset:9216\n\t"
        "buffer_load_dwordx4 v[196:199], off, %[rn], %[sn] offset:1024 sc1\n\ts_add_u32 %[sn], %[sn], 0x2000\n\t"
        "v_mfma_f32_16x16x32_bf16 v[36:39], v[80:83], v[220:223], v[36:39]\n\tv_mfma_f32_16x16x32_bf16 v[40:43], v[84:87], v[216:219], v[40:43]\n\t"
        "ds_read_b128 v[80:83], %[pb0] offset:10240\n\tv_mfma_f32_16x16x32_bf16 v[44:47], v[84:87], v[220:223], v[44:47]\n\ts_waitcnt lgkmcnt(3)\n\t"
        "v_mfma_f32_16x16x32_bf16 v[48:51], v[88:91], v[216:219], v[48:51]\n\tds_read_b128 v[84:87], %[pb0] offset:11264\n\t"
        "v_mfma_f32_16x16x32_bf16 v[52:55], v[88:91], v[220:223], v[52:55]\n\tv_mfma_f32_16x16x32_bf16 v[56:59], v[92:95], v[216:219], v[56:59]\n\t"
        "ds_read_b128 v[88:91], %[pb0] offset:12288\n\tbuffer_load_dwordx4 v[200:203], off, %[rn], %[sn] offset:0 sc1\n\t"
        "v_mfma_f32_16x16x32_bf16 v[60:63], v[92:95], v[220:223], v[60:63]\n\tv_mfma_f32_16x16x32_bf16 v[64:67], v[96:99], v[216:219], v[64:67]\n\t"
        "ds_read_b128 v[92:95], %[pb0] offset:13312\n\tv_mfma_f32_16x16x32_bf16 v[68:71], v[96:99], v[220:223], v[68:71]\n\ts_waitcnt vmcnt(17)\n\t"
        "v_mfma_f32_16x16x32_bf16 v[16:19], v[100:103], v[224:227], v[16:19]\n\tds_read_b128 v[96:99], %[pb0] offset:14336\n\t"
        "v_mfma_f32_16x16x32_bf16 v[20:23], v[100:103], v[228:231], v[20:23]\n\ts_waitcnt lgkmcnt(3)\n\t"
        "v_mfma_f32_16x16x32_bf16 v[24:27], v[72:75], v[224:227], v[24:27]\n\tds_read_b128 v[100:103], %[pb0] offset:15360\n\t"
        "buffer_load_dwordx4 v[204:207], off, %[rn], %[sn] offset:1024 sc1\n\ts_add_u32 %[sn], %[sn], 0x2000\n\t"
        "v_mfma_f32_16x16x32_bf16 v[28:31], v[72:75], v[228:231], v[28:31]\n\tv_mfma_f32_16x16x32_bf16 v[32:35], v[76:79], v[224:227], v[32:35]\n\t"
        "ds_read_b128 v[72:75], %[pb0] offset:16384\n\tv_mfma_f32_16x16x32_bf16 v[36:39], v[76:79], v[228:231], v[36:39]\n\t"
        "v_mfma_f32_16x16x32_bf16 v[40:43], v[80:83], v[224:227], v[40:43]\n\tds_read_b128 v[76:79], %[pb0] offset:17408\n\t"
        "v_mfma_f32_16x16x32_bf16 v[44:47], v[80:83], v[228:231], v[44:47]\n\tv_mfma_f32_16x16x32_bf16 v[48:51], v[84:87], v[224:227], v[48:51]\n\t"
        "ds_read_b128 v[80:83], %[pb0] offset:18432\n\tbuffer_load_dwordx4 v[208:211], off, %[rn], %[sn] offset:0 sc1\n\t"
        "v_mfma_f32_16x16x32_bf16 v[52:55], v[84:87], v[228:231], v[52:55]\n\ts_waitcnt lgkmcnt(3)\n\t"
        "v_mfma_f32_16x16x32_bf16 v[56:59], v[88:91], v[224:227], v[56:59]\n\tds_read_b128 v[84:87], %[pb0] offset:19456\n\t"
        "v_mfma_f32_16x16x32_bf16 v[60:63], v[88:91], v[228:231], v[60:63]\n\tv_mfma_f32_16x16x32_bf16 v[64:67], v[92:95], v[224:227], v[64:67]\n\t"
        "ds_read_b128 v[88:91], %[pb0] offset:20480\n\tv_mfma_f32_16x16x32_bf16 v[68:71], v[92:95], v[228:231], v[68:71]\n\ts_waitcnt vmcnt(17)\n\t"
        "v_mfma_f32_16x16x32_bf16 v[16:19], v[96:99], v[232:235], v[16:19]\n\tds_read_b128 v[92:95], %[pb0] offset:21504\n\t"
        "buffer_load_dwordx4 v[212:215], off, %[rn], %[sn] offset:1024 sc1\n\ts_add_u32 %[sn], %[sn], 0x2000\n\t"
        "v_mfma_f32_16x16x32_bf16 v[20:23], v[96:99], v[236:239], v[20:23]\n\tv_mfma_f32_16x16x32_bf16 v[24:27], v[100:103], v[232:235], v[24:27]\n\t"
        "ds_read_b128 v[96:99], %[pb0] offset:22528\n\tv_mfma_f32_16x16x32_bf16 v[28:31], v[100:103], v[236:239], v[28:31]\n\ts_waitcnt lgkmcnt(3)\n\t"
        "v_mfma_f32_16x16x32_bf16 v[32:35], v[72:75], v[232:235], v[32:35]\n\tds_read_b128 v[100:103], %[pb0] offset:23552\n\t"
        "v_mfma_f32_16x16x32_bf16 v[36:39], v[72:75], v[236:239], v[36:39]\n\tv_mfma_f32_16x16x32_bf16 v[40:43], v[76:79], v[232:235], v[40:43]\n\t"
        "ds_read_b128 v[72:75], %[pb0] offset:24576\n\tbuffer_load_dwordx4 v[216:219], off, %[rn], %[sn] offset:0 sc1\n\t"
        "v_mfma_f32_16x16x32_bf16 v[44:47], v[76:79], v[236:239], v[44:47]\n\tv_mfma_f32_16x16x32_bf16 v[48:51], v[80:83], v[232:235], v[48:51]\n\t"
        "ds_read_b128 v[76:79], %[pb0] offset:25600\n\tv_mfma_f32_16x16x32_bf16 v[52:55], v[80:83], v[236:239], v[52:55]\n\t"
        "v_mfma_f32_16x16x32_bf16 v[56:59], v[84:87], v[232:235], v[56:59]\n\tds_read_b128 v[80:83], %[pb0] offset:26624\n\t"
        "v_mfma_f32_16x16x32_bf16 v[60:63], v[84:87], v[236:239], v[60:63]\n\ts_waitcnt lgkmcnt(3)\n\t"
        "v_mfma_f32_16x16x32_bf16 v[64:67], v[88:91], v[232:235], v[64:67]\n\tds_read_b128 v[84:87], %[pb0] offset:27648\n\t"
        "buffer_load_dwordx4 v[220:223], off, %[rn], %[sn] offset:1024 sc1\n\ts_add_u32 %[sn], %[sn], 0x2000\n\t"
        "v_mfma_f32_16x16x32_bf16 v[68:71], v[88:91], v[236:239], v[68:71]\n\ts_waitcnt vmcnt(18)\n\t"
        "v_mfma_f32_16x16x32_bf16 v[16:19], v[92:95], v[240:243], v[16:19]\n\tds_read_b128 v[88:91], %[pb0] offset:28672\n\t"
        "v_mfma_f32_16x16x32_bf16 v[20:23], v[92:95], v[244:247], v[20:23]\n\tv_mfma_f32_16x16x32_bf16 v[24:27], v[96:99], v[240:243], v[24:27]\n\t"
        "ds_read_b128 v[92:95], %[pb0] offset:29696\n\tv_mfma_f32_16x16x32_bf16 v[28:31], v[96:99], v[244:247], v[28:31]\n\t"
        "v_mfma_f32_16x16x32_bf16 v[32:35], v[100:103], v[240:243], v[32:35]\n\tds_read_b128 v[96:99], %[pb0] offset:30720\n\t"
        "buffer_load_dwordx4 v[224:227], off, %[rn], %[sn] offset:0 sc1\n\tv_mfma_f32_16x16x32_bf16 v[36:39], v[100:103], v[244:247], v[36:39]\n\t"
        "s_waitcnt lgkmcnt(3)\n\tv_mfma_f32_16x16x32_bf16 v[40:43], v[72:75], v[240:243], v[40:43]\n\tds_read_b128 v[100:103], %[pb0] offset:31744\n\t"
        "v_mfma_f32_16x16x32_bf16 v[44:47], v[72:75], v[244:247], v[44:47]\n\tv_mfma_f32_16x16x32_bf16 v[48:51], v[76:79], v[240:243], v[48:51]\n\t"
        "ds_read_b128 v[72:75], %[pb0] offset:32768\n\tv_mfma_f32_16x16x32_bf16 v[52:55], v[76:79], v[244:247], v[52:55]\n\t"
        "v_mfma_f32_16x16x32_bf16 v[56:59], v[80:83], v[240:243], v[56:59]\n\tds_read_b128 v[76:79], %[pb0] offset:33792\n\t"
        "buffer_load_dwordx4 v[228:231], off, %[rn], %[sn] offset:1024 sc1\n\ts_add_u32 %[sn], %[sn], 0x2000\n\t"
        "v_mfma_f32_16x16x32_bf16 v[60:63], v[80:83], v[244:247], v[60:63]\n\tv_mfma_f32_16x16x32_bf16 v[64:67], v[84:87], v[240:243], v[64:67]\n\t"
        "ds_read_b128 v[80:83], %[pb2] offset:8192\n\tv_mfma_f32_16x16x32_bf16 v[68:71], v[84:87], v[244:247], v[68:71]\n\ts_waitcnt vmcnt(18) lgkmcnt(3)\n\t"
        "v_mfma_f32_16x16x32_bf16 v[16:19], v[88:91], v[152:155], v[16:19]\n\tds_read_b128 v[84:87], %[pb0] offset:34816\n\t"
        "v_mfma_f32_16x16x32_bf16 v[20:23], v[88:91], v[156:159], v[20:23]\n\tv_mfma_f32_16x16x32_bf16 v[24:27], v[92:95], v[152:155], v[24:27]\n\t"
        "ds_read_b128 v[88:91], %[pb0] offset:35840\n\tbuffer_load_dwordx4 v[232:235], off, %[rn], %[sn] offset:0 sc1\n\t"
        "v_mfma_f32_16x16x32_bf16 v[28:31], v[92:95], v[156:159], v[28:31]\n\tv_mfma_f32_16x16x32_bf16 v[32:35], v[96:99], v[152:155], v[32:35]\n\t"
        "ds_read_b128 v[92:95], %[pb0] offset:36864\n\tv_mfma_f32_16x16x32_bf16 v[36:39], v[96:99], v[156:159], v[36:39]\n\t"
        "v_mfma_f32_16x16x32_bf16 v[40:43], v[100:103], v[152:155], v[40:43]\n\tds_read_b128 v[96:99], %[pb0] offset:37888\n\t"
        "v_mfma_f32_16x16x32_bf16 v[44:47], v[100:103], v[156:159], v[44:47]\n\ts_waitcnt lgkmcnt(3)\n\t"
        "v_mfma_f32_16x16x32_bf16 v[48:51], v[72:75], v[152:155], v[48:51]\n\tds_read_b128 v[100:103], %[pb0] offset:38912\n\t"
        "buffer_load_dwordx4 v[236:239], off, %[rn], %[sn] offset:1024 sc1\n\ts_add_u32 %[sn], %[sn], 0x2000\n\t"
        "v_mfma_f32_16x16x32_bf16 v[52:55], v[72:75], v[156:159], v[52:55]\n\tv_mfma_f32_16x16x32_bf16 v[56:59], v[76:79], v[152:155], v[56:59]\n\t"
        "ds_read_b128 v[72:75], %[pb0] offset:39936\n\tv_mfma_f32_16x16x32_bf16 v[60:63], v[76:79], v[156:159], v[60:63]\n\t"
        "v_mfma_f32_16x16x32_bf16 v[64:67], v[80:83], v[152:155], v[64:67]\n\tds_read_b128 v[76:79], %[pb2] offset:9216\n\t"
        "v_mfma_f32_16x16x32_bf16 v[68:71], v[80:83], v[156:159], v[68:71]\n\ts_waitcnt vmcnt(18)\n\t"
        "v_mfma_f32_16x16x32_bf16 v[16:19], v[84:87], v[160:163], v[16:19]\n\tds_read_b128 v[80:83], %[pb0] offset:40960\n\t"
        "buffer_load_dwordx4 v[240:243], off, %[rn], %[sn] offset:0 sc1\n\tv_mfma_f32_16x16x32_bf16 v[20:23], v[84:87], v[164:167], v[20:23]\n\t"
        "s_waitcnt lgkmcnt(3)\n\tv_mfma_f32_16x16x32_bf16 v[24:27], v[88:91], v[160:163], v[24:27]\n\tds_read_b128 v[84:87], %[pb0] offset:41984\n\t"
        "v_mfma_f32_16x16x32_bf16 v[28:31], v[88:91], v[164:167], v[28:31]\n\tv_mfma_f32_16x16x32_bf16 v[32:35], v[92:95], v[160:163], v[32:35]\n\t"
        "ds_read_b128 v[88:91], %[pb0] offset:43008\n\tv_mfma_f32_16x16x32_bf16 v[36:39], v[92:95], v[164:167], v[36:39]\n\t"
        "v_mfma_f32_16x16x32_bf16 v[40:43], v[96:99], v[160:163], v[40:43]\n\tds_read_b128 v[92:95], %[pb0] offset:44032\n\t"
        "buffer_load_dwordx4 v[244:247], off, %[rn], %[sn] offset:1024 sc1\n\ts_add_u32 %[sn], %[sn], 0x2000\n\t"
        "v_mfma_f32_16x16x32_bf16 v[44:47], v[96:99], v[164:167], v[44:47]\n\tv_mfma_f32_16x16x32_bf16 v[48:51], v[100:103], v[160:163], v[48:51]\n\t"
        "ds_read_b128 v[96:99], %[pb0] offset:45056\n\tv_mfma_f32_16x16x32_bf16 v[52:55], v[100:103], v[164:167], v[52:55]\n\ts_waitcnt lgkmcnt(3)\n\t"
        "v_mfma_f32_16x16x32_bf16 v[56:59], v[72:75], v[160:163], v[56:59]\n\tds_read_b128 v[100:103], %[pb0] offset:46080\n\t"
        "v_mfma_f32_16x16x32_bf16 v[60:63], v[72:75], v[164:167], v[60:63]\n\tv_mfma_f32_16x16x32_bf16 v[64:67], v[76:79], v[160:163], v[64:67]\n\t"
        "ds_read_b128 v[72:75], %[pb2] offset:10240\n\tbuffer_load_dwordx4 v[152:155], off, %[rn], %[sn] offset:0 sc1\n\t"
        "v_mfma_f32_16x16x32_bf16 v[68:71], v[76:79], v[164:167], v[68:71]\n\ts_waitcnt vmcnt(19)\n\t"
        "v_mfma_f32_16x16x32_bf16 v[16:19], v[80:83], v[168:171], v[16:19]\n\tds_read_b128 v[76:79], %[pb0] offset:47104\n\t"
        "v_mfma_f32_16x16x32_bf16 v[20:23], v[80:83], v[172:175], v[20:23]\n\tv_mfma_f32_16x16x32_bf16 v[24:27], v[84:87], v[168:171], v[24:27]\n\t"
        "ds_read_b128 v[80:83], %[pb0] offset:48128\n\tv_mfma_f32_16x16x32_bf16 v[28:31], v[84:87], v[172:175], v[28:31]\n\ts_waitcnt lgkmcnt(3)\n\t"
        "v_mfma_f32_16x16x32_bf16 v[32:35], v[88:91], v[168:171], v[32:35]\n\tds_read_b128 v[84:87], %[pb0] offset:49152\n\t"
        "buffer_load_dwordx4 v[156:159], off, %[rn], %[sn] offset:1024 sc1\n\ts_add_u32 %[sn], %[sn], 0x2000\n\t"
        "v_mfma_f32_16x16x32_bf16 v[36:39], v[88:91], v[172:175], v[36:39]\n\tv_mfma_f32_16x16x32_bf16 v[40:43], v[92:95], v[168:171], v[40:43]\n\t"
        "ds_read_b128 v[88:91], %[pb0] offset:50176\n\tv_mfma_f32_16x16x32_bf16 v[44:47], v[92:95], v[172:175], v[44:47]\n\t"
        "v_mfma_f32_16x16x32_bf16 v[48:51], v[96:99], v[168:171], v[48:51]\n\tds_read_b128 v[92:95], %[pb0] offset:51200\n\t"
        "v_mfma_f32_16x16x32_bf16 v[52:55], v[96:99], v[172:175], v[52:55]\n\tv_mfma_f32_16x16x32_bf16 v[56:59], v[100:103], v[168:171], v[56:59]\n\t"
        "ds_read_b128 v[96:99], %[pb0] offset:52224\n\tbuffer_load_dwordx4 v[160:163], off, %[rn], %[sn] offset:0 sc1\n\t"
        "v_mfma_f32_16x16x32_bf16 v[60:63], v[100:103], v[172:175], v[60:63]\n\ts_waitcnt lgkmcnt(3)\n\t"
        "v_mfma_f32_16x16x32_bf16 v[64:67], v[72:75], v[168:171], v[64:67]\n\tds_read_b128 v[100:103], %[pb2] offset:11264\n\t"
        "v_mfma_f32_16x16x32_bf16 v[68:71], v[72:75], v[172:175], v[68:71]\n\ts_waitcnt vmcnt(19)\n\t"
        "v_mfma_f32_16x16x32_bf16 v[16:19], v[76:79], v[176:179], v[16:19]\n\tv_mfma_f32_16x16x32_bf16 v[20:23], v[76:79], v[180:183], v[20:23]\n\t"
        "s_waitcnt lgkmcnt(0)\n\ts_barrier\n\tv_mfma_f32_16x16x32_bf16 v[24:27], v[80:83], v[176:179], v[24:27]\n\t"
        "buffer_load_dwordx4 v[164:167], off, %[rn], %[sn] offset:1024 sc1\n\ts_add_u32 %[sn], %[sn], 0x2000\n\t"
        "v_mfma_f32_16x16x32_bf16 v[28:31], v[80:83], v[180:183], v[28:31]\n\tv_mov_b32 v8, 0x409d30fc\n\t"
        "v_mfma_f32_16x16x32_bf16 v[32:35], v[84:87], v[176:179], v[32:35]\n\tv_mfma_f32_16x16x32_bf16 v[36:39], v[84:87], v[180:183], v[36:39]\n\t"
        "v_mov_b32 v9, 0x3eeff558\n\tv_mfma_f32_16x16x32_bf16 v[40:43], v[88:91], v[176:179], v[40:43]\n\t"
        "v_mfma_f32_16x16x32_bf16 v[44:47], v[88:91], v[180:183], v[44:47]\n\tds_read_b32 v104, v253 offset:512\n\tds_read_b32 v105, v253 offset:576\n\t"
        "v_mfma_f32_16x16x32_bf16 v[48:51], v[92:95], v[176:179], v[48:51]\n\tbuffer_load_dwordx4 v[168:171], off, %[rn], %[sn] offset:0 sc1\n\t"
        "v_mfma_f32_16x16x32_bf16 v[52:55], v[92:95], v[180:183], v[52:55]\n\tds_read_b32 v106, v253 offset:1536\n\tds_read_b32 v107, v253 offset:1600\n\t"
        "v_mfma_f32_16x16x32_bf16 v[56:59], v[96:99], v[176:179], v[56:59]\n\tv_mfma_f32_16x16x32_bf16 v[60:63], v[96:99], v[180:183], v[60:63]\n\t"
        "ds_read_b128 v[108:111], v255 offset:2048\n\tds_read_b128 v[116:119], v255 offset:2560\n\t"
        "v_mfma_f32_16x16x32_bf16 v[64:67], v[100:103], v[176:179], v[64:67]\n\tv_mfma_f32_16x16x32_bf16 v[68:71], v[100:103], v[180:183], v[68:71]\n\t"
        "ds_read_b128 v[112:115], v255 offset:2112\n\tds_read_b128 v[120:123], v255 offset:2624\n\ts_waitcnt lgkmcnt(0)\n\ts_nop 0\n\t"
        "ds_read_b128 v[72:75], v255 offset:2176\n\tds_read_b128 v[80:83], v255 offset:2688\n\tds_read_b128 v[76:79], v255 offset:2240\n\t"
        "ds_read_b128 v[84:87], v255 offset:2752\n\tv_pk_fma_f32 v[88:89], v[106:107], v[108:109], v[116:117] op_sel:[0,0,0] op_sel_hi:[0,1,1]\n\t"
        "v_pk_fma_f32 v[90:91], v[106:107], v[110:111], v[118:119] op_sel:[0,0,0] op_sel_hi:[0,1,1]\n\t"
        "v_pk_fma_f32 v[92:93], v[106:107], v[112:113], v[120:121] op_sel:[0,0,0] op_sel_hi:[0,1,1]\n\t"
        "v_pk_fma_f32 v[94:95], v[106:107], v[114:115], v[122:123] op_sel:[0,0,0] op_sel_hi:[0,1,1]\n\t"
        "v_pk_fma_f32 v[16:17], v[16:17], v[104:105], v[88:89] op_sel:[0,0,0] op_sel_hi:[1,0,1]\n\t"
        "v_pk_fma_f32 v[18:19], v[18:19], v[104:105], v[90:91] op_sel:[0,0,0] op_sel_hi:[1,0,1]\n\t"
        "v_pk_fma_f32 v[24:25], v[24:25], v[104:105], v[92:93] op_sel:[0,0,0] op_sel_hi:[1,0,1]\n\t"
        "v_pk_fma_f32 v[26:27], v[26:27], v[104:105], v[94:95] op_sel:[0,0,0] op_sel_hi:[1,0,1]\n\t"
        "v_pk_fma_f32 v[88:89], v[16:17], v[16:17], v[8:9] op_sel_hi:[1,1,0]\n\tv_pk_fma_f32 v[90:91], v[18:19], v[18:19], v[8:9] op_sel_hi:[1,1,0]\n\t"
        "v_pk_fma_f32 v[92:93], v[24:25], v[24:25], v[8:9] op_sel_hi:[1,1,0]\n\tv_pk_fma_f32 v[94:95], v[26:27], v[26:27], v[8:9] op_sel_hi:[1,1,0]\n\t"
        "v_pk_mul_f32 v[88:89], v[16:17], v[88:89] neg_lo:[1,0] neg_hi:[1,0]\n\tv_pk_mul_f32 v[90:91], v[18:19], v[90:91] neg_lo:[1,0] neg_hi:[1,0]\n\t"
        "v_pk_mul_f32 v[92:93], v[24:25], v[92:93] neg_lo:[1,0] neg_hi:[1,0]\n\tv_pk_mul_f32 v[94:95], v[26:27], v[94:95] neg_lo:[1,0] neg_hi:[1,0]\n\t"
        "v_exp_f32 v88, v88\n\tv_exp_f32 v89, v89\n\tv_exp_f32 v90, v90\n\tv_exp_f32 v91, v91\n\tv_exp_f32 v92, v92\n\tv_exp_f32 v93, v93\n\t"
        "v_exp_f32 v94, v94\n\tv_exp_f32 v95, v95\n\tv_pk_fma_f32 v[88:89], v[88:89], v[8:9], v[8:9] op_sel:[0,1,1] op_sel_hi:[1,1,1]\n\t"
        "v_pk_fma_f32 v[90:91], v[90:91], v[8:9], v[8:9] op_sel:[0,1,1] op_sel_hi:[1,1,1]\n\t"
        "v_pk_fma_f32 v[92:93], v[92:93], v[8:9], v[8:9] op_sel:[0,1,1] op_sel_hi:[1,1,1]\n\t"
        "v_pk_fma_f32 v[94:95], v[94:95], v[8:9], v[8:9] op_sel:[0,1,1] op_sel_hi:[1,1,1]\n\tv_rcp_f32 v88, v88\n\tv_rcp_f32 v89, v89\n\tv_rcp_f32 v90, v90\n\t"
        "v_rcp_f32 v91, v91\n\tv_rcp_f32 v92, v92\n\tv_rcp_f32 v93, v93\n\tv_rcp_f32 v94, v94\n\tv_rcp_f32 v95, v95\n\t"
        "v_pk_mul_f32 v[16:17], v[16:17], v[88:89]\n\tv_pk_mul_f32 v[18:19], v[18:19], v[90:91]\n\tv_pk_mul_f32 v[24:25], v[24:25], v[92:93]\n\t"
        "v_pk_mul_f32 v[26:27], v[26:27], v[94:95]\n\tv_cvt_pk_bf16_f32 v124, v16, v17\n\tv_cvt_pk_bf16_f32 v125, v18, v19\n\t"
        "v_cvt_pk_bf16_f32 v126, v24, v25\n\tv_cvt_pk_bf16_f32 v127, v26, v27\n\ts_add_u32 %[hx], %[hd], 0x700000\n\t"
        "buffer_store_dwordx4 v[124:127], off, %[rt], %[hx] offset:0 sc1\n\t"
        "v_pk_fma_f32 v[88:89], v[106:107], v[108:109], v[116:117] op_sel:[1,0,0] op_sel_hi:[1,1,1]\n\t"
        "v_pk_fma_f32 v[90:91], v[106:107], v[110:111], v[118:119] op_sel:[1,0,0] op_sel_hi:[1,1,1]\n\t"
        "v_pk_fma_f32 v[92:93], v[106:107], v[112:113], v[120:121] op_sel:[1,0,0] op_sel_hi:[1,1,1]\n\t"
        "v_pk_fma_f32 v[94:95], v[106:107], v[114:115], v[122:123] op_sel:[1,0,0] op_sel_hi:[1,1,1]\n\t"
        "v_pk_fma_f32 v[20:21], v[20:21], v[104:105], v[88:89] op_sel:[0,1,0] op_sel_hi:[1,1,1]\n\t"
        "v_pk_fma_f32 v[22:23], v[22:23], v[104:105], v[90:91] op_sel:[0,1,0] op_sel_hi:[1,1,1]\n\t"
        "v_pk_fma_f32 v[28:29], v[28:29], v[104:105], v[92:93] op_sel:[0,1,0] op_sel_hi:[1,1,1]\n\t"
        "v_pk_fma_f32 v[30:31], v[30:31], v[104:105], v[94:95] op_sel:[0,1,0] op_sel_hi:[1,1,1]\n\t"
        "v_pk_fma_f32 v[88:89], v[20:21], v[20:21], v[8:9] op_sel_hi:[1,1,0]\n\tv_pk_fma_f32 v[90:91], v[22:23], v[22:23], v[8:9] op_sel_hi:[1,1,0]\n\t"
        "v_pk_fma_f32 v[92:93], v[28:29], v[28:29], v[8:9] op_sel_hi:[1,1,0]\n\tv_pk_fma_f32 v[94:95], v[30:31], v[30:31], v[8:9] op_sel_hi:[1,1,0]\n\t"
        "v_pk_mul_f32 v[88:89], v[20:21], v[88:89] neg_lo:[1,0] neg_hi:[1,0]\n\tv_pk_mul_f32 v[90:91], v[22:23], v[90:91] neg_lo:[1,0] neg_hi:[1,0]\n\t"
        "v_pk_mul_f32 v[92:93], v[28:29], v[92:93] neg_lo:[1,0] neg_hi:[1,0]\n\tv_pk_mul_f32 v[94:95], v[30:31], v[94:95] neg_lo:[1,0] neg_hi:[1,0]\n\t"
        "v_exp_f32 v88, v88\n\tv_exp_f32 v89, v89\n\tv_exp_f32 v90, v90\n\tv_exp_f32 v91, v91\n\tv_exp_f32 v92, v92\n\tv_exp_f32 v93, v93\n\t"
        "v_exp_f32 v94, v94\n\tv_exp_f32 v95, v95\n\t.p2align 12,,12\n\t.Lpg13:\n\ts_cbranch_execz .Lpgret\n\t"
        "v_pk_fma_f32 v[88:89], v[88:89], v[8:9], v[8:9] op_sel:[0,1,1] op_sel_hi:[1,1,1]\n\t"
        "v_pk_fma_f32 v[90:91], v[90:91], v[8:9], v[8:9] op_sel:[0,1,1] op_sel_hi:[1,1,1]\n\t"
        "v_pk_fma_f32 v[92:93], v[92:93], v[8:9], v[8:9] op_sel:[0,1,1] op_sel_hi:[1,1,1]\n\t"
        "v_pk_fma_f32 v[94:95], v[94:95], v[8:9], v[8:9] op_sel:[0,1,1] op_sel_hi:[1,1,1]\n\tv_rcp_f32 v88, v88\n\tv_rcp_f32 v89, v89\n\tv_rcp_f32 v90, v90\n\t"
        "v_rcp_f32 v91, v91\n\tv_rcp_f32 v92, v92\n\tv_rcp_f32 v93, v93\n\tv_rcp_f32 v94, v94\n\tv_rcp_f32 v95, v95\n\t"
        "v_pk_mul_f32 v[20:21], v[20:21], v[88:89]\n\tv_pk_mul_f32 v[22:23], v[22:23], v[90:91]\n\tv_pk_mul_f32 v[28:29], v[28:29], v[92:93]\n\t"
        "v_pk_mul_f32 v[30:31], v[30:31], v[94:95]\n\tv_cvt_pk_bf16_f32 v128, v20, v21\n\tv_cvt_pk_bf16_f32 v129, v22, v23\n\t"
        "v_cvt_pk_bf16_f32 v130, v28, v29\n\tv_cvt_pk_bf16_f32 v131, v30, v31\n\ts_waitcnt lgkmcnt(0)\n\tds_read_b128 v[108:111], v255 offset:2304\n\t"
        "ds_read_b128 v[116:119], v255 offset:2816\n\tds_read_b128 v[112:115], v255 offset:2368\n\tds_read_b128 v[120:123], v255 offset:2880\n\t"
        "v_pk_fma_f32 v[88:89], v[106:107], v[72:73], v[80:81] op_sel:[0,0,0] op_sel_hi:[0,1,1]\n\t"
        "v_pk_fma_f32 v[90:91], v[106:107], v[74:75], v[82:83] op_sel:[0,0,0] op_sel_hi:[0,1,1]\n\t"
        "v_pk_fma_f32 v[92:93], v[106:107], v[76:77], v[84:85] op_sel:[0,0,0] op_sel_hi:[0,1,1]\n\t"
        "v_pk_fma_f32 v[94:95], v[106:107], v[78:79], v[86:87] op_sel:[0,0,0] op_sel_hi:[0,1,1]\n\t"
        "v_pk_fma_f32 v[32:33], v[32:33], v[104:105], v[88:89] op_sel:[0,0,0] op_sel_hi:[1,0,1]\n\t"
        "v_pk_fma_f32 v[34:35], v[34:35], v[104:105], v[90:91] op_sel:[0,0,0] op_sel_hi:[1,0,1]\n\t"
        "v_pk_fma_f32 v[40:41], v[40:41], v[104:105], v[92:93] op_sel:[0,0,0] op_sel_hi:[1,0,1]\n\t"
        "v_pk_fma_f32 v[42:43], v[42:43], v[104:105], v[94:95] op_sel:[0,0,0] op_sel_hi:[1,0,1]\n\t"
        "v_pk_fma_f32 v[88:89], v[32:33], v[32:33], v[8:9] op_sel_hi:[1,1,0]\n\tv_pk_fma_f32 v[90:91], v[34:35], v[34:35], v[8:9] op_sel_hi:[1,1,0]\n\t"
        "v_pk_fma_f32 v[92:93], v[40:41], v[40:41], v[8:9] op_sel_hi:[1,1,0]\n\tv_pk_fma_f32 v[94:95], v[42:43], v[42:43], v[8:9] op_sel_hi:[1,1,0]\n\t"
        "v_pk_mul_f32 v[88:89], v[32:33], v[88:89] neg_lo:[1,0] neg_hi:[1,0]\n\tv_pk_mul_f32 v[90:91], v[34:35], v[90:91] neg_lo:[1,0] neg_hi:[1,0]\n\t"
        "v_pk_mul_f32 v[92:93], v[40:41], v[92:93] neg_lo:[1,0] neg_hi:[1,0]\n\tv_pk_mul_f32 v[94:95], v[42:43], v[94:95] neg_lo:[1,0] neg_hi:[1,0]\n\t"
        "v_exp_f32 v88, v88\n\tv_exp_f32 v89, v89\n\tv_exp_f32 v90, v90\n\tv_exp_f32 v91, v91\n\tv_exp_f32 v92, v92\n\tv_exp_f32 v93, v93\n\t"
        "v_exp_f32 v94, v94\n\tv_exp_f32 v95, v95\n\tv_pk_fma_f32 v[88:89], v[88:89], v[8:9], v[8:9] op_sel:[0,1,1] op_sel_hi:[1,1,1]\n\t"
        "v_pk_fma_f32 v[90:91], v[90:91], v[8:9], v[8:9] op_sel:[0,1,1] op_sel_hi:[1,1,1]\n\t"
        "v_pk_fma_f32 v[92:93], v[92:93], v[8:9], v[8:9] op_sel:[0,1,1] op_sel_hi:[1,1,1]\n\t"
        "v_pk_fma_f32 v[94:95], v[94:95], v[8:9], v[8:9] op_sel:[0,1,1] op_sel_hi:[1,1,1]\n\tv_rcp_f32 v88, v88\n\tv_rcp_f32 v89, v89\n\tv_rcp_f32 v90, v90\n\t"
        "v_rcp_f32 v91, v91\n\tv_rcp_f32 v92, v92\n\tv_rcp_f32 v93, v93\n\tv_rcp_f32 v94, v94\n\tv_rcp_f32 v95, v95\n\t"
        "v_pk_mul_f32 v[32:33], v[32:33], v[88:89]\n\tbuffer_store_dwordx4 v[128:131], off, %[rt], %[hx] offset:1024 sc1\n\t"
        "v_pk_mul_f32 v[34:35], v[34:35], v[90:91]\n\tv_pk_mul_f32 v[40:41], v[40:41], v[92:93]\n\tv_pk_mul_f32 v[42:43], v[42:43], v[94:95]\n\t"
        "v_cvt_pk_bf16_f32 v132, v32, v33\n\tv_cvt_pk_bf16_f32 v133, v34, v35\n\tv_cvt_pk_bf16_f32 v134, v40, v41\n\tv_cvt_pk_bf16_f32 v135, v42, v43\n\t"
        "v_pk_fma_f32 v[88:89], v[106:107], v[72:73], v[80:81] op_sel:[1,0,0] op_sel_hi:[1,1,1]\n\t"
        "v_pk_fma_f32 v[90:91], v[106:107], v[74:75], v[82:83] op_sel:[1,0,0] op_sel_hi:[1,1,1]\n\t"
        "v_pk_fma_f32 v[92:93], v[106:107], v[76:77], v[84:85] op_sel:[1,0,0] op_sel_hi:[1,1,1]\n\t"
        "v_pk_fma_f32 v[94:95], v[106:107], v[78:79], v[86:87] op_sel:[1,0,0] op_sel_hi:[1,1,1]\n\t"
        "v_pk_fma_f32 v[36:37], v[36:37], v[104:105], v[88:89] op_sel:[0,1,0] op_sel_hi:[1,1,1]\n\t"
        "v_pk_fma_f32 v[38:39], v[38:39], v[104:105], v[90:91] op_sel:[0,1,0] op_sel_hi:[1,1,1]\n\t"
        "v_pk_fma_f32 v[44:45], v[44:45], v[104:105], v[92:93] op_sel:[0,1,0] op_sel_hi:[1,1,1]\n\t"
        "v_pk_fma_f32 v[46:47], v[46:47], v[104:105], v[94:95] op_sel:[0,1,0] op_sel_hi:[1,1,1]\n\t"
        "v_pk_fma_f32 v[88:89], v[36:37], v[36:37], v[8:9] op_sel_hi:[1,1,0]\n\tv_pk_fma_f32 v[90:91], v[38:39], v[38:39], v[8:9] op_sel_hi:[1,1,0]\n\t"
        "v_pk_fma_f32 v[92:93], v[44:45], v[44:45], v[8:9] op_sel_hi:[1,1,0]\n\tv_pk_fma_f32 v[94:95], v[46:47], v[46:47], v[8:9] op_sel_hi:[1,1,0]\n\t"
        "v_pk_mul_f32 v[88:89], v[36:37], v[88:89] neg_lo:[1,0] neg_hi:[1,0]\n\tv_pk_mul_f32 v[90:91], v[38:39], v[90:91] neg_lo:[1,0] neg_hi:[1,0]\n\t"
        "v_pk_mul_f32 v[92:93], v[44:45], v[92:93] neg_lo:[1,0] neg_hi:[1,0]\n\tv_pk_mul_f32 v[94:95], v[46:47], v[94:95] neg_lo:[1,0] neg_hi:[1,0]\n\t"
        "v_exp_f32 v88, v88\n\tv_exp_f32 v89, v89\n\tv_exp_f32 v90, v90\n\tv_exp_f32 v91, v91\n\tv_exp_f32 v92, v92\n\tv_exp_f32 v93, v93\n\t"
        "v_exp_f32 v94, v94\n\tv_exp_f32 v95, v95\n\tv_pk_fma_f32 v[88:89], v[88:89], v[8:9], v[8:9] op_sel:[0,1,1] op_sel_hi:[1,1,1]\n\t"
        "v_pk_fma_f32 v[90:91], v[90:91], v[8:9], v[8:9] op_sel:[0,1,1] op_sel_hi:[1,1,1]\n\t"
        "v_pk_fma_f32 v[92:93], v[92:93], v[8:9], v[8:9] op_sel:[0,1,1] op_sel_hi:[1,1,1]\n\t"
        "v_pk_fma_f32 v[94:95], v[94:95], v[8:9], v[8:9] op_sel:[0,1,1] op_sel_hi:[1,1,1]\n\tv_rcp_f32 v88, v88\n\tv_rcp_f32 v89, v89\n\tv_rcp_f32 v90, v90\n\t"
        "v_rcp_f32 v91, v91\n\tv_rcp_f32 v92, v92\n\tv_rcp_f32 v93, v93\n\tv_rcp_f32 v94, v94\n\tv_rcp_f32 v95, v95\n\t"
        "v_pk_mul_f32 v[36:37], v[36:37], v[88:89]\n\tv_pk_mul_f32 v[38:39], v[38:39], v[90:91]\n\tv_pk_mul_f32 v[44:45], v[44:45], v[92:93]\n\t"
        "v_pk_mul_f32 v[46:47], v[46:47], v[94:95]\n\tv_cvt_pk_bf16_f32 v136, v36, v37\n\tv_cvt_pk_bf16_f32 v137, v38, v39\n\t"
        "v_cvt_pk_bf16_f32 v138, v44, v45\n\tv_cvt_pk_bf16_f32 v139, v46, v47\n\ts_waitcnt lgkmcnt(0)\n\ts_nop 0\n\tds_read_b128 v[72:75], v255 offset:2432\n\t"
        "ds_read_b128 v[80:83], v255 offset:2944\n\tv_pk_fma_f32 v[88:89], v[106:107], v[108:109], v[116:117] op_sel:[0,0,0] op_sel_hi:[0,1,1]\n\t"
        "v_pk_fma_f32 v[90:91], v[106:107], v[110:111], v[118:119] op_sel:[0,0,0] op_sel_hi:[0,1,1]\n\t"
        "v_pk_fma_f32 v[92:93], v[106:107], v[112:113], v[120:121] op_sel:[0,0,0] op_sel_hi:[0,1,1]\n\t"
        "v_pk_fma_f32 v[94:95], v[106:107], v[114:115], v[122:123] op_sel:[0,0,0] op_sel_hi:[0,1,1]\n\t"
        "v_pk_fma_f32 v[48:49], v[48:49], v[104:105], v[88:89] op_sel:[0,0,0] op_sel_hi:[1,0,1]\n\t"
        "v_pk_fma_f32 v[50:51], v[50:51], v[104:105], v[90:91] op_sel:[0,0,0] op_sel_hi:[1,0,1]\n\t"
        "v_pk_fma_f32 v[56:57], v[56:57], v[104:105], v[92:93] op_sel:[0,0,0] op_sel_hi:[1,0,1]\n\t"
        "v_pk_fma_f32 v[58:59], v[58:59], v[104:105], v[94:95] op_sel:[0,0,0] op_sel_hi:[1,0,1]\n\t"
        "v_pk_fma_f32 v[88:89], v[48:49], v[48:49], v[8:9] op_sel_hi:[1,1,0]\n\tv_pk_fma_f32 v[90:91], v[50:51], v[50:51], v[8:9] op_sel_hi:[1,1,0]\n\t"
        "v_pk_fma_f32 v[92:93], v[56:57], v[56:57], v[8:9] op_sel_hi:[1,1,0]\n\tv_pk_fma_f32 v[94:95], v[58:59], v[58:59], v[8:9] op_sel_hi:[1,1,0]\n\t"
        "v_pk_mul_f32 v[88:89], v[48:49], v[88:89] neg_lo:[1,0] neg_hi:[1,0]\n\tv_pk_mul_f32 v[90:91], v[50:51], v[90:91] neg_lo:[1,0] neg_hi:[1,0]\n\t"
        "v_pk_mul_f32 v[92:93], v[56:57], v[92:93] neg_lo:[1,0] neg_hi:[1,0]\n\tv_pk_mul_f32 v[94:95], v[58:59], v[94:95] neg_lo:[1,0] neg_hi:[1,0]\n\t"
        "v_exp_f32 v88, v88\n\tv_exp_f32 v89, v89\n\tv_exp_f32 v90, v90\n\tv_exp_f32 v91, v91\n\tv_exp_f32 v92, v92\n\tv_exp_f32 v93, v93\n\t"
        "v_exp_f32 v94, v94\n\tv_exp_f32 v95, v95\n\tv_pk_fma_f32 v[88:89], v[88:89], v[8:9], v[8:9] op_sel:[0,1,1] op_sel_hi:[1,1,1]\n\t"
        "v_pk_fma_f32 v[90:91], v[90:91], v[8:9], v[8:9] op_sel:[0,1,1] op_sel_hi:[1,1,1]\n\t"
        "v_pk_fma_f32 v[92:93], v[92:93], v[8:9], v[8:9] op_sel:[0,1,1] op_sel_hi:[1,1,1]\n\t"
        "v_pk_fma_f32 v[94:95], v[94:95], v[8:9], v[8:9] op_sel:[0,1,1] op_sel_hi:[1,1,1]\n\tv_rcp_f32 v88, v88\n\tv_rcp_f32 v89, v89\n\tv_rcp_f32 v90, v90\n\t"
        "v_rcp_f32 v91, v91\n\tv_rcp_f32 v92, v92\n\tv_rcp_f32 v93, v93\n\tv_rcp_f32 v94, v94\n\tv_rcp_f32 v95, v95\n\t"
        "v_pk_mul_f32 v[48:49], v[48:49], v[88:89]\n\ts_add_u32 %[hx], %[hd], 0x702000\n\tbuffer_store_dwordx4 v[132:135], off, %[rt], %[hx] offset:0 sc1\n\t"
        "v_pk_mul_f32 v[50:51], v[50:51], v[90:91]\n\tv_pk_mul_f32 v[56:57], v[56:57], v[92:93]\n\tv_pk_mul_f32 v[58:59], v[58:59], v[94:95]\n\t"
        "v_cvt_pk_bf16_f32 v140, v48, v49\n\tv_cvt_pk_bf16_f32 v141, v50, v51\n\tv_cvt_pk_bf16_f32 v142, v56, v57\n\tv_cvt_pk_bf16_f32 v143, v58, v59\n\t"
        "v_pk_fma_f32 v[88:89], v[106:107], v[108:109], v[116:117] op_sel:[1,0,0] op_sel_hi:[1,1,1]\n\t"
        "v_pk_fma_f32 v[90:91], v[106:107], v[110:111], v[118:119] op_sel:[1,0,0] op_sel_hi:[1,1,1]\n\t"
        "v_pk_fma_f32 v[92:93], v[106:107], v[112:113], v[120:121] op_sel:[1,0,0] op_sel_hi:[1,1,1]\n\t"
        "v_pk_fma_f32 v[94:95], v[106:107], v[114:115], v[122:123] op_sel:[1,0,0] op_sel_hi:[1,1,1]\n\t"
        "v_pk_fma_f32 v[52:53], v[52:53], v[104:105], v[88:89] op_sel:[0,1,0] op_sel_hi:[1,1,1]\n\t"
        "v_pk_fma_f32 v[54:55], v[54:55], v[104:105], v[90:91] op_sel:[0,1,0] op_sel_hi:[1,1,1]\n\t"
        "v_pk_fma_f32 v[60:61], v[60:61], v[104:105], v[92:93] op_sel:[0,1,0] op_sel_hi:[1,1,1]\n\t"
        "v_pk_fma_f32 v[62:63], v[62:63], v[104:105], v[94:95] op_sel:[0,1,0] op_sel_hi:[1,1,1]\n\t"
        "v_pk_fma_f32 v[88:89], v[52:53], v[52:53], v[8:9] op_sel_hi:[1,1,0]\n\tv_pk_fma_f32 v[90:91], v[54:55], v[54:55], v[8:9] op_sel_hi:[1,1,0]\n\t"
        "v_pk_fma_f32 v[92:93], v[60:61], v[60:61], v[8:9] op_sel_hi:[1,1,0]\n\tv_pk_fma_f32 v[94:95], v[62:63], v[62:63], v[8:9] op_sel_hi:[1,1,0]\n\t"
        "v_pk_mul_f32 v[88:89], v[52:53], v[88:89] neg_lo:[1,0] neg_hi:[1,0]\n\tv_pk_mul_f32 v[90:91], v[54:55], v[90:91] neg_lo:[1,0] neg_hi:[1,0]\n\t"
        "v_pk_mul_f32 v[92:93], v[60:61], v[92:93] neg_lo:[1,0] neg_hi:[1,0]\n\tv_pk_mul_f32 v[94:95], v[62:63], v[94:95] neg_lo:[1,0] neg_hi:[1,0]\n\t"
        "v_exp_f32 v88, v88\n\tv_exp_f32 v89, v89\n\tv_exp_f32 v90, v90\n\tv_exp_f32 v91, v91\n\tv_exp_f32 v92, v92\n\tv_exp_f32 v93, v93\n\t"
        "v_exp_f32 v94, v94\n\tv_exp_f32 v95, v95\n\tv_pk_fma_f32 v[88:89], v[88:89], v[8:9], v[8:9] op_sel:[0,1,1] op_sel_hi:[1,1,1]\n\t"
        "v_pk_fma_f32 v[90:91], v[90:91], v[8:9], v[8:9] op_sel:[0,1,1] op_sel_hi:[1,1,1]\n\t"
        "v_pk_fma_f32 v[92:93], v[92:93], v[8:9], v[8:9] op_sel:[0,1,1] op_sel_hi:[1,1,1]\n\t"
        "v_pk_fma_f32 v[94:95], v[94:95], v[8:9], v[8:9] op_sel:[0,1,1] op_sel_hi:[1,1,1]\n\tv_rcp_f32 v88, v88\n\tv_rcp_f32 v89, v89\n\tv_rcp_f32 v90, v90\n\t"
        "v_rcp_f32 v91, v91\n\tv_rcp_f32 v92, v92\n\tv_rcp_f32 v93, v93\n\tv_rcp_f32 v94, v94\n\tv_rcp_f32 v95, v95\n\t"
        "v_pk_mul_f32 v[52:53], v[52:53], v[88:89]\n\tv_pk_mul_f32 v[54:55], v[54:55], v[90:91]\n\tv_pk_mul_f32 v[60:61], v[60:61], v[92:93]\n\t"
        "v_pk_mul_f32 v[62:63], v[62:63], v[94:95]\n\tv_cvt_pk_bf16_f32 v144, v52, v53\n\tv_cvt_pk_bf16_f32 v145, v54, v55\n\t"
        "v_cvt_pk_bf16_f32 v146, v60, v61\n\tv_cvt_pk_bf16_f32 v147, v62, v63\n\ts_waitcnt lgkmcnt(0)\n\ts_nop 0\n\t"
        "v_pk_fma_f32 v[88:89], v[106:107], v[72:73], v[80:81] op_sel:[0,0,0] op_sel_hi:[0,1,1]\n\t"
        "v_pk_fma_f32 v[90:91], v[106:107], v[74:75], v[82:83] op_sel:[0,0,0] op_sel_hi:[0,1,1]\n\t"
        "v_pk_fma_f32 v[92:93], v[106:107], v[72:73], v[80:81] op_sel:[1,0,0] op_sel_hi:[1,1,1]\n\t"
        "v_pk_fma_f32 v[94:95], v[106:107], v[74:75], v[82:83] op_sel:[1,0,0] op_sel_hi:[1,1,1]\n\t"
        "v_pk_fma_f32 v[64:65], v[64:65], v[104:105], v[88:89] op_sel:[0,0,0] op_sel_hi:[1,0,1]\n\t"
        "v_pk_fma_f32 v[66:67], v[66:67], v[104:105], v[90:91] op_sel:[0,0,0] op_sel_hi:[1,0,1]\n\t"
        "v_pk_fma_f32 v[68:69], v[68:69], v[104:105], v[92:93] op_sel:[0,1,0] op_sel_hi:[1,1,1]\n\t"
        "v_pk_fma_f32 v[70:71], v[70:71], v[104:105], v[94:95] op_sel:[0,1,0] op_sel_hi:[1,1,1]\n\t"
        "v_pk_fma_f32 v[88:89], v[64:65], v[64:65], v[8:9] op_sel_hi:[1,1,0]\n\tv_pk_fma_f32 v[90:91], v[66:67], v[66:67], v[8:9] op_sel_hi:[1,1,0]\n\t"
        "v_pk_fma_f32 v[92:93], v[68:69], v[68:69], v[8:9] op_sel_hi:[1,1,0]\n\tv_pk_fma_f32 v[94:95], v[70:71], v[70:71], v[8:9] op_sel_hi:[1,1,0]\n\t"
        "v_pk_mul_f32 v[88:89], v[64:65], v[88:89] neg_lo:[1,0] neg_hi:[1,0]\n\tv_pk_mul_f32 v[90:91], v[66:67], v[90:91] neg_lo:[1,0] neg_hi:[1,0]\n\t"
        "v_pk_mul_f32 v[92:93], v[68:69], v[92:93] neg_lo:[1,0] neg_hi:[1,0]\n\tv_pk_mul_f32 v[94:95], v[70:71], v[94:95] neg_lo:[1,0] neg_hi:[1,0]\n\t"
        "v_exp_f32 v88, v88\n\tv_exp_f32 v89, v89\n\tv_exp_f32 v90, v90\n\tv_exp_f32 v91, v91\n\tv_exp_f32 v92, v92\n\tv_exp_f32 v93, v93\n\t"
        "v_exp_f32 v94, v94\n\tv_exp_f32 v95, v95\n\tv_pk_fma_f32 v[88:89], v[88:89], v[8:9], v[8:9] op_sel:[0,1,1] op_sel_hi:[1,1,1]\n\t"
        "v_pk_fma_f32 v[90:91], v[90:91], v[8:9], v[8:9] op_sel:[0,1,1] op_sel_hi:[1,1,1]\n\t"
        "v_pk_fma_f32 v[92:93], v[92:93], v[8:9], v[8:9] op_sel:[0,1,1] op_sel_hi:[1,1,1]\n\t"
        "v_pk_fma_f32 v[94:95], v[94:95], v[8:9], v[8:9] op_sel:[0,1,1] op_sel_hi:[1,1,1]\n\tv_rcp_f32 v88, v88\n\tv_rcp_f32 v89, v89\n\tv_rcp_f32 v90, v90\n\t"
        "v_rcp_f32 v91, v91\n\tv_rcp_f32 v92, v92\n\tv_rcp_f32 v93, v93\n\tv_rcp_f32 v94, v94\n\tv_rcp_f32 v95, v95\n\t"
        "v_pk_mul_f32 v[64:65], v[64:65], v[88:89]\n\tbuffer_store_dwordx4 v[136:139], off, %[rt], %[hx] offset:1024 sc1\n\t"
        "v_pk_mul_f32 v[66:67], v[66:67], v[90:91]\n\tv_pk_mul_f32 v[68:69], v[68:69], v[92:93]\n\tv_pk_mul_f32 v[70:71], v[70:71], v[94:95]\n\t"
        "v_cvt_pk_bf16_f32 v148, v64, v65\n\tv_cvt_pk_bf16_f32 v150, v68, v69\n\tv_cvt_pk_bf16_f32 v149, v66, v67\n\tv_cvt_pk_bf16_f32 v151, v70, v71\n\t"
        "v_permlane16_swap_b32 v148, v150\n\ts_nop 0\n\tv_permlane16_swap_b32 v149, v151\n\t.Lmk_u1_e:\n\t"
        : "+{v[152:183]}"(r0), "+{v[184:215]}"(r1), "+{v[216:247]}"(r2), "+{v[120:151]}"(ho), [hx] "+s"(hx), [sa] "+s"(sa), [sn] "+s"(sn)
        : [pb0] "{v250}"(n.pb0), [pb1] "{v251}"(n.pb1), [pb2] "{v252}"(n.pb2), [rt] "s"(rt4), [rn] "s"(rt4), [hd] "s"(hd), [hd7] "s"(hd7), "{v253}"(n.e1), "{v255}"(n.e3), "{v15}"(n.av), [rs] "s"(rs4), [one] "{v[12:13]}"(tok), [fl] "s"(fl)
        : "v72", "v73", "v74", "v75", "v76", "v77", "v78", "v79", "v80", "v81", "v82", "v83", "v84", "v85", "v86", "v87", "v88", "v89", "v90", "v91", "v92", "v93", "v94", "v95", "v96", "v97", "v98", "v99", "v100", "v101", "v102", "v103", "v8", "v9", "v16", "v17", "v18", "v19", "v20", "v21", "v22", "v23", "v24", "v25", "v26", "v27", "v28", "v29", "v30", "v31", "v32", "v33", "v34", "v35", "v36", "v37", "v38", "v39", "v40", "v41", "v42", "v43", "v44", "v45", "v46", "v47", "v48", "v49", "v50", "v51", "v52", "v53", "v54", "v55", "v56", "v57", "v58", "v59", "v60", "v61", "v62", "v63", "v64", "v65", "v66", "v67", "v68", "v69", "v70", "v71", "v104", "v105", "v106", "v107", "v108", "v109", "v110", "v111", "v112", "v113", "v114", "v115", "v116", "v117", "v118", "v119", "memory", "scc");
      STAMP(17);
    }
    {
      const unsigned fl = fh + 8192;
      const u32x4 rn4 = {rt4[0], rt4[1], layer0 ? 0u : WS_END, rt4[3]};
      sa = sn; sn = xq + (0 ? 0 : 8) * MB;
      sn += 0u;
      asm volatile(
        "; BLK v\n\t.Lmk_v_s:\n\ts_barrier\n\tds_read_b128 v[80:83], %[pb1] offset:0\n\tds_read_b128 v[84:87], %[pb1] offset:1024\n\t"
        "ds_read_b128 v[88:91], %[pb1] offset:2048\n\tds_read_b128 v[92:95], %[pb1] offset:3072\n\tds_read_b128 v[96:99], %[pb1] offset:4096\n\t"
        "ds_read_b128 v[100:103], %[pb1] offset:5120\n\tds_read_b128 v[104:107], %[pb1] offset:6144\n\ts_waitcnt vmcnt(23) lgkmcnt(3)\n\t"
        "v_mfma_f32_16x16x32_bf16 v[16:19], v[80:83], v[184:187], 0\n\tds_read_b128 v[108:111], %[pb1] offset:7168\n\t"
        "v_mfma_f32_16x16x32_bf16 v[20:23], v[80:83], v[188:191], 0\n\tv_mfma_f32_16x16x32_bf16 v[24:27], v[84:87], v[184:187], 0\n\t"
        "ds_read_b128 v[80:83], %[pb1] offset:8192\n\tv_mfma_f32_16x16x32_bf16 v[28:31], v[84:87], v[188:191], 0\n\t"
        "v_mfma_f32_16x16x32_bf16 v[32:35], v[88:91], v[184:187], 0\n\tds_read_b128 v[84:87], %[pb1] offset:9216\n\t"
        "v_mfma_f32_16x16x32_bf16 v[36:39], v[88:91], v[188:191], 0\n\tbuffer_load_dwordx4 v[172:175], off, %[rt], %[sa] offset:1024 sc1\n\t"
        "s_add_u32 %[sa], %[sa], 0x2000\n\tv_mfma_f32_16x16x32_bf16 v[40:43], v[92:95], v[184:187], 0\n\tds_read_b128 v[88:91], %[pb1] offset:10240\n\t"
        "v_mfma_f32_16x16x32_bf16 v[44:47], v[92:95], v[188:191], 0\n\ts_waitcnt lgkmcnt(3)\n\tv_mfma_f32_16x16x32_bf16 v[48:51], v[96:99], v[184:187], 0\n\t"
        "ds_read_b128 v[92:95], %[pb1] offset:11264\n\tv_mfma_f32_16x16x32_bf16 v[52:55], v[96:99], v[188:191], 0\n\t"
        "v_mfma_f32_16x16x32_bf16 v[56:59], v[100:103], v[184:187], 0\n\tds_read_b128 v[96:99], %[pb1] offset:12288\n\t"
        "v_mfma_f32_16x16x32_bf16 v[60:63], v[100:103], v[188:191], 0\n\tbuffer_load_dwordx4 v[176:179], off, %[rt], %[sa] offset:0 sc1\n\t"
        "v_mfma_f32_16x16x32_bf16 v[64:67], v[104:107], v[184:187], 0\n\tds_read_b128 v[100:103], %[pb1] offset:13312\n\t"
        "v_mfma_f32_16x16x32_bf16 v[68:71], v[104:107], v[188:191], 0\n\ts_add_u32 %[hx], %[hd], 0x704000\n\t"
        "buffer_store_dwordx4 v[140:143], off, %[rt], %[hx] offset:0 sc1\n\tv_mfma_f32_16x16x32_bf16 v[72:75], v[108:111], v[184:187], 0\n\t"
        "ds_read_b128 v[104:107], %[pb1] offset:14336\n\tv_mfma_f32_16x16x32_bf16 v[76:79], v[108:111], v[188:191], 0\n\ts_waitcnt vmcnt(24) lgkmcnt(3)\n\t"
        "v_mfma_f32_16x16x32_bf16 v[16:19], v[80:83], v[192:195], v[16:19]\n\tds_read_b128 v[108:111], %[pb1] offset:15360\n\t"
        "v_mfma_f32_16x16x32_bf16 v[20:23], v[80:83], v[196:199], v[20:23]\n\tbuffer_load_dwordx4 v[180:183], off, %[rt], %[sa] offset:1024 sc1\n\t"
        "s_add_u32 %[sa], %[sa], 0x2000\n\tv_mfma_f32_16x16x32_bf16 v[24:27], v[84:87], v[192:195], v[24:27]\n\tds_read_b128 v[80:83], %[pb1] offset:16384\n\t"
        "v_mfma_f32_16x16x32_bf16 v[28:31], v[84:87], v[196:199], v[28:31]\n\tv_mfma_f32_16x16x32_bf16 v[32:35], v[88:91], v[192:195], v[32:35]\n\t"
        "ds_read_b128 v[84:87], %[pb1] offset:17408\n\tv_mfma_f32_16x16x32_bf16 v[36:39], v[88:91], v[196:199], v[36:39]\n\t"
        "v_mfma_f32_16x16x32_bf16 v[40:43], v[92:95], v[192:195], v[40:43]\n\tds_read_b128 v[88:91], %[pb1] offset:18432\n\t"
        "v_mfma_f32_16x16x32_bf16 v[44:47], v[92:95], v[196:199], v[44:47]\n\tbuffer_load_dwordx4 v[184:187], off, %[rt], %[sa] offset:0 sc1\n\t"
        "s_waitcnt lgkmcnt(3)\n\tv_mfma_f32_16x16x32_bf16 v[48:51], v[96:99], v[192:195], v[48:51]\n\tds_read_b128 v[92:95], %[pb1] offset:19456\n\t"
        "v_mfma_f32_16x16x32_bf16 v[52:55], v[96:99], v[196:199], v[52:55]\n\tv_mfma_f32_16x16x32_bf16 v[56:59], v[100:103], v[192:195], v[56:59]\n\t"
        "ds_read_b128 v[96:99], %[pb1] offset:20480\n\tv_mfma_f32_16x16x32_bf16 v[60:63], v[100:103], v[196:199], v[60:63]\n\t"
        "v_mfma_f32_16x16x32_bf16 v[64:67], v[104:107], v[192:195], v[64:67]\n\tds_read_b128 v[100:103], %[pb1] offset:21504\n\t"
        "v_mfma_f32_16x16x32_bf16 v[68:71], v[104:107], v[196:199], v[68:71]\n\tbuffer_load_dwordx4 v[188:191], off, %[rt], %[sa] offset:1024 sc1\n\t"
        "s_add_u32 %[sa], %[sa], 0x2000\n\tv_mfma_f32_16x16x32_bf16 v[72:75], v[108:111], v[192:195], v[72:75]\n\t"
        "ds_read_b128 v[104:107], %[pb1] offset:22528\n\tv_mfma_f32_16x16x32_bf16 v[76:79], v[108:111], v[196:199], v[76:79]\n\t"
        "s_waitcnt vmcnt(25) lgkmcnt(3)\n\tv_mfma_f32_16x16x32_bf16 v[16:19], v[80:83], v[200:203], v[16:19]\n\t"
        "ds_read_b128 v[108:111], %[pb1] offset:23552\n\tv_mfma_f32_16x16x32_bf16 v[20:23], v[80:83], v[204:207], v[20:23]\n\t"
        "v_mfma_f32_16x16x32_bf16 v[24:27], v[84:87], v[200:203], v[24:27]\n\tds_read_b128 v[80:83], %[pb1] offset:24576\n\t"
        "v_mfma_f32_16x16x32_bf16 v[28:31], v[84:87], v[204:207], v[28:31]\n\tbuffer_load_dwordx4 v[192:195], off, %[rt], %[sa] offset:0 sc1\n\t"
        "v_mfma_f32_16x16x32_bf16 v[32:35], v[88:91], v[200:203], v[32:35]\n\tds_read_b128 v[84:87], %[pb1] offset:25600\n\t"
        "v_mfma_f32_16x16x32_bf16 v[36:39], v[88:91], v[204:207], v[36:39]\n\tbuffer_store_dwordx4 v[144:147], off, %[rt], %[hx] offset:1024 sc1\n\t"
        "v_mfma_f32_16x16x32_bf16 v[40:43], v[92:95], v[200:203], v[40:43]\n\tds_read_b128 v[88:91], %[pb1] offset:26624\n\t"
        "v_mfma_f32_16x16x32_bf16 v[44:47], v[92:95], v[204:207], v[44:47]\n\ts_waitcnt lgkmcnt(3)\n\t"
        "v_mfma_f32_16x16x32_bf16 v[48:51], v[96:99], v[200:203], v[48:51]\n\tds_read_b128 v[92:95], %[pb1] offset:27648\n\t"
        "v_mfma_f32_16x16x32_bf16 v[52:55], v[96:99], v[204:207], v[52:55]\n\tbuffer_load_dwordx4 v[196:199], off, %[rt], %[sa] offset:1024 sc1\n\t"
        "s_add_u32 %[sa], %[sa], 0x2000\n\tv_mfma_f32_16x16x32_bf16 v[56:59], v[100:103], v[200:203], v[56:59]\n\t"
        "ds_read_b128 v[96:99], %[pb1] offset:28672\n\tv_mfma_f32_16x16x32_bf16 v[60:63], v[100:103], v[204:207], v[60:63]\n\t"
        "v_mfma_f32_16x16x32_bf16 v[64:67], v[104:107], v[200:203], v[64:67]\n\tds_read_b128 v[100:103], %[pb1] offset:29696\n\t"
        "v_mfma_f32_16x16x32_bf16 v[68:71], v[104:107], v[204:207], v[68:71]\n\tv_mfma_f32_16x16x32_bf16 v[72:75], v[108:111], v[200:203], v[72:75]\n\t"
        "ds_read_b128 v[104:107], %[pb1] offset:30720\n\tv_mfma_f32_16x16x32_bf16 v[76:79], v[108:111], v[204:207], v[76:79]\n\t"
        "buffer_load_dwordx4 v[200:203], off, %[rt], %[sa] offset:0 sc1\n\ts_waitcnt vmcnt(27) lgkmcnt(3)\n\t"
        "v_mfma_f32_16x16x32_bf16 v[16:19], v[80:83], v[208:211], v[16:19]\n\tds_read_b128 v[108:111], %[pb1] offset:31744\n\t"
        "v_mfma_f32_16x16x32_bf16 v[20:23], v[80:83], v[212:215], v[20:23]\n\tv_mfma_f32_16x16x32_bf16 v[24:27], v[84:87], v[208:211], v[24:27]\n\t"
        "ds_read_b128 v[80:83], %[pb1] offset:32768\n\tv_mfma_f32_16x16x32_bf16 v[28:31], v[84:87], v[212:215], v[28:31]\n\t"
        "v_mfma_f32_16x16x32_bf16 v[32:35], v[88:91], v[208:211], v[32:35]\n\tds_read_b128 v[84:87], %[pb1] offset:33792\n\t"
        "v_mfma_f32_16x16x32_bf16 v[36:39], v[88:91], v[212:215], v[36:39]\n\tbuffer_load_dwordx4 v[204:207], off, %[rt], %[sa] offset:1024 sc1\n\t"
        "s_add_u32 %[sa], %[sa], 0x2000\n\tv_mfma_f32_16x16x32_bf16 v[40:43], v[92:95], v[208:211], v[40:43]\n\tds_read_b128 v[88:91], %[pb1] offset:34816\n\t"
        "v_mfma_f32_16x16x32_bf16 v[44:47], v[92:95], v[212:215], v[44:47]\n\ts_waitcnt lgkmcnt(3)\n\t"
        "v_mfma_f32_16x16x32_bf16 v[48:51], v[96:99], v[208:211], v[48:51]\n\tds_read_b128 v[92:95], %[pb1] offset:35840\n\t"
        "v_mfma_f32_16x16x32_bf16 v[52:55], v[96:99], v[212:215], v[52:55]\n\tv_mfma_f32_16x16x32_bf16 v[56:59], v[100:103], v[208:211], v[56:59]\n\t"
        "ds_read_b128 v[96:99], %[pb1] offset:36864\n\tv_mfma_f32_16x16x32_bf16 v[60:63], v[100:103], v[212:215], v[60:63]\n\t"
        "v_mfma_f32_16x16x32_bf16 v[64:67], v[104:107], v[208:211], v[64:67]\n\tds_read_b128 v[100:103], %[pb1] offset:37888\n\t"
        "v_mfma_f32_16x16x32_bf16 v[68:71], v[104:107], v[212:215], v[68:71]\n\ts_add_u32 %[hx], %[hd7], 0x700000\n\t"
        "buffer_store_dwordx4 v[148:151], v15, %[rs], %[hx] offen sc1\n\tv_mfma_f32_16x16x32_bf16 v[72:75], v[108:111], v[208:211], v[72:75]\n\t"
        "ds_read_b128 v[104:107], %[pb1] offset:38912\n\tv_mfma_f32_16x16x32_bf16 v[76:79], v[108:111], v[212:215], v[76:79]\n\t"
        "buffer_load_dwordx4 v[208:211], off, %[rt], %[sa] offset:0 sc1\n\ts_waitcnt vmcnt(28) lgkmcnt(3)\n\t"
        "v_mfma_f32_16x16x32_bf16 v[16:19], v[80:83], v[216:219], v[16:19]\n\tds_read_b128 v[108:111], %[pb1] offset:39936\n\t"
        "v_mfma_f32_16x16x32_bf16 v[20:23], v[80:83], v[220:223], v[20:23]\n\tv_mfma_f32_16x16x32_bf16 v[24:27], v[84:87], v[216:219], v[24:27]\n\t"
        "ds_read_b128 v[80:83], %[pb1] offset:40960\n\tv_mfma_f32_16x16x32_bf16 v[28:31], v[84:87], v[220:223], v[28:31]\n\t"
        "v_mfma_f32_16x16x32_bf16 v[32:35], v[88:91], v[216:219], v[32:35]\n\tds_read_b128 v[84:87], %[pb1] offset:41984\n\t"
        "v_mfma_f32_16x16x32_bf16 v[36:39], v[88:91], v[220:223], v[36:39]\n\tbuffer_load_dwordx4 v[212:215], off, %[rt], %[sa] offset:1024 sc1\n\t"
        "s_add_u32 %[sa], %[sa], 0x2000\n\tv_mfma_f32_16x16x32_bf16 v[40:43], v[92:95], v[216:219], v[40:43]\n\tds_read_b128 v[88:91], %[pb1] offset:43008\n\t"
        "v_mfma_f32_16x16x32_bf16 v[44:47], v[92:95], v[220:223], v[44:47]\n\ts_waitcnt lgkmcnt(3)\n\t"
        "v_mfma_f32_16x16x32_bf16 v[48:51], v[96:99], v[216:219], v[48:51]\n\tds_read_b128 v[92:95], %[pb1] offset:44032\n\t"
        "v_mfma_f32_16x16x32_bf16 v[52:55], v[96:99], v[220:223], v[52:55]\n\tv_mfma_f32_16x16x32_bf16 v[56:59], v[100:103], v[216:219], v[56:59]\n\t"
        "ds_read_b128 v[96:99], %[pb1] offset:45056\n\tv_mfma_f32_16x16x32_bf16 v[60:63], v[100:103], v[220:223], v[60:63]\n\t"
        "v_mfma_f32_16x16x32_bf16 v[64:67], v[104:107], v[216:219], v[64:67]\n\tds_read_b128 v[100:103], %[pb1] offset:46080\n\t"
        "v_mfma_f32_16x16x32_bf16 v[68:71], v[104:107], v[220:223], v[68:71]\n\tv_mfma_f32_16x16x32_bf16 v[72:75], v[108:111], v[216:219], v[72:75]\n\t"
        "ds_read_b128 v[104:107], %[pb1] offset:47104\n\tv_mfma_f32_16x16x32_bf16 v[76:79], v[108:111], v[220:223], v[76:79]\n\t"
        "buffer_load_dwordx4 v[216:219], off, %[rt], %[sa] offset:0 sc1\n\ts_waitcnt vmcnt(28) lgkmcnt(3)\n\t"
        "v_mfma_f32_16x16x32_bf16 v[16:19], v[80:83], v[224:227], v[16:19]\n\tds_read_b128 v[108:111], %[pb1] offset:48128\n\t"
        "v_mfma_f32_16x16x32_bf16 v[20:23], v[80:83], v[228:231], v[20:23]\n\tv_mfma_f32_16x16x32_bf16 v[24:27], v[84:87], v[224:227], v[24:27]\n\t"
        "ds_read_b128 v[80:83], %[pb1] offset:49152\n\tv_mfma_f32_16x16x32_bf16 v[28:31], v[84:87], v[228:231], v[28:31]\n\t"
        "v_mfma_f32_16x16x32_bf16 v[32:35], v[88:91], v[224:227], v[32:35]\n\tds_read_b128 v[84:87], %[pb1] offset:50176\n\t"
        "v_mfma_f32_16x16x32_bf16 v[36:39], v[88:91], v[228:231], v[36:39]\n\tbuffer_load_dwordx4 v[220:223], off, %[rt], %[sa] offset:1024 sc1\n\t"
        "s_add_u32 %[sa], %[sa], 0x2000\n\tv_mfma_f32_16x16x32_bf16 v[40:43], v[92:95], v[224:227], v[40:43]\n\tds_read_b128 v[88:91], %[pb1] offset:51200\n\t"
        "v_mfma_f32_16x16x32_bf16 v[44:47], v[92:95], v[228:231], v[44:47]\n\ts_waitcnt lgkmcnt(3)\n\t"
        "v_mfma_f32_16x16x32_bf16 v[48:51], v[96:99], v[224:227], v[48:51]\n\tds_read_b128 v[92:95], %[pb1] offset:52224\n\t"
        "v_mfma_f32_16x16x32_bf16 v[52:55], v[96:99], v[228:231], v[52:55]\n\tv_mfma_f32_16x16x32_bf16 v[56:59], v[100:103], v[224:227], v[56:59]\n\t"
        "ds_read_b128 v[96:99], %[pb1] offset:53248\n\tv_mfma_f32_16x16x32_bf16 v[60:63], v[100:103], v[228:231], v[60:63]\n\t"
        "v_mfma_f32_16x16x32_bf16 v[64:67], v[104:107], v[224:227], v[64:67]\n\tds_read_b128 v[100:103], %[pb1] offset:54272\n\t"
        "v_mfma_f32_16x16x32_bf16 v[68:71], v[104:107], v[228:231], v[68:71]\n\tv_mfma_f32_16x16x32_bf16 v[72:75], v[108:111], v[224:227], v[72:75]\n\t"
        "ds_read_b128 v[104:107], %[pb1] offset:55296\n\tv_mfma_f32_16x16x32_bf16 v[76:79], v[108:111], v[228:231], v[76:79]\n\t"
        "buffer_load_dwordx4 v[224:227], off, %[rt], %[sa] offset:0 sc1\n\ts_waitcnt vmcnt(28) lgkmcnt(3)\n\t"
        "v_mfma_f32_16x16x32_bf16 v[16:19], v[80:83], v[232:235], v[16:19]\n\tds_read_b128 v[108:111], %[pb1] offset:56320\n\t"
        "v_mfma_f32_16x16x32_bf16 v[20:23], v[80:83], v[236:239], v[20:23]\n\tv_mfma_f32_16x16x32_bf16 v[24:27], v[84:87], v[232:235], v[24:27]\n\t"
        "ds_read_b128 v[80:83], %[pb1] offset:57344\n\tv_mfma_f32_16x16x32_bf16 v[28:31], v[84:87], v[236:239], v[28:31]\n\t"
        "v_mfma_f32_16x16x32_bf16 v[32:35], v[88:91], v[232:235], v[32:35]\n\tds_read_b128 v[84:87], %[pb1] offset:58368\n\t"
        "v_mfma_f32_16x16x32_bf16 v[36:39], v[88:91], v[236:239], v[36:39]\n\tbuffer_load_dwordx4 v[228:231], off, %[rt], %[sa] offset:1024 sc1\n\t"
        "s_add_u32 %[sa], %[sa], 0x2000\n\tv_mfma_f32_16x16x32_bf16 v[40:43], v[92:95], v[232:235], v[40:43]\n\tds_read_b128 v[88:91], %[pb1] offset:59392\n\t"
        "v_mfma_f32_16x16x32_bf16 v[44:47], v[92:95], v[236:239], v[44:47]\n\ts_waitcnt lgkmcnt(3)\n\t"
        "v_mfma_f32_16x16x32_bf16 v[48:51], v[96:99], v[232:235], v[48:51]\n\tds_read_b128 v[92:95], %[pb1] offset:60416\n\t"
        "v_mfma_f32_16x16x32_bf16 v[52:55], v[96:99], v[236:239], v[52:55]\n\tv_mfma_f32_16x16x32_bf16 v[56:59], v[100:103], v[232:235], v[56:59]\n\t"
        "ds_read_b128 v[96:99], %[pb1] offset:61440\n\tv_mfma_f32_16x16x32_bf16 v[60:63], v[100:103], v[236:239], v[60:63]\n\t"
        "v_mfma_f32_16x16x32_bf16 v[64:67], v[104:107], v[232:235], v[64:67]\n\tds_read_b128 v[100:103], %[pb1] offset:62464\n\t"
        "v_mfma_f32_16x16x32_bf16 v[68:71], v[104:107], v[236:239], v[68:71]\n\tv_mfma_f32_16x16x32_bf16 v[72:75], v[108:111], v[232:235], v[72:75]\n\t"
        "ds_read_b128 v[104:107], %[pb1] offset:63488\n\tv_mfma_f32_16x16x32_bf16 v[76:79], v[108:111], v[236:239], v[76:79]\n\t"
        "buffer_load_dwordx4 v[232:235], off, %[rt], %[sa] offset:0 sc1\n\ts_waitcnt vmcnt(28) lgkmcnt(3)\n\t"
        "v_mfma_f32_16x16x32_bf16 v[16:19], v[80:83], v[240:243], v[16:19]\n\tds_read_b128 v[108:111], %[pb1] offset:64512\n\t"
        "v_mfma_f32_16x16x32_bf16 v[20:23], v[80:83], v[244:247], v[20:23]\n\ts_waitcnt vmcnt(7)\n\tbuffer_store_dwordx2 %[one], off, %[rs], %[fl] sc1\n\t"
        "v_mfma_f32_16x16x32_bf16 v[24:27], v[84:87], v[240:243], v[24:27]\n\ts_barrier\n\tds_read_b128 v[80:83], %[pb2] offset:0\n\t"
        "v_mfma_f32_16x16x32_bf16 v[28:31], v[84:87], v[244:247], v[28:31]\n\tv_mfma_f32_16x16x32_bf16 v[32:35], v[88:91], v[240:243], v[32:35]\n\t"
        "ds_read_b128 v[84:87], %[pb2] offset:1024\n\tv_mfma_f32_16x16x32_bf16 v[36:39], v[88:91], v[244:247], v[36:39]\n\t"
        "buffer_load_dwordx4 v[236:239], off, %[rt], %[sa] offset:1024 sc1\n\ts_add_u32 %[sa], %[sa], 0x2000\n\t"
        "v_mfma_f32_16x16x32_bf16 v[40:43], v[92:95], v[240:243], v[40:43]\n\tds_read_b128 v[88:91], %[pb2] offset:2048\n\t"
        "v_mfma_f32_16x16x32_bf16 v[44:47], v[92:95], v[244:247], v[44:47]\n\ts_waitcnt lgkmcnt(3)\n\t"
        "v_mfma_f32_16x16x32_bf16 v[48:51], v[96:99], v[240:243], v[48:51]\n\tds_read_b128 v[92:95], %[pb2] offset:3072\n\t"
        "v_mfma_f32_16x16x32_bf16 v[52:55], v[96:99], v[244:247], v[52:55]\n\tv_mfma_f32_16x16x32_bf16 v[56:59], v[100:103], v[240:243], v[56:59]\n\t"
        "ds_read_b128 v[96:99], %[pb2] offset:4096\n\tv_mfma_f32_16x16x32_bf16 v[60:63], v[100:103], v[244:247], v[60:63]\n\t"
        "v_mfma_f32_16x16x32_bf16 v[64:67], v[104:107], v[240:243], v[64:67]\n\tds_read_b128 v[100:103], %[pb2] offset:5120\n\t"
        "v_mfma_f32_16x16x32_bf16 v[68:71], v[104:107], v[244:247], v[68:71]\n\tv_mfma_f32_16x16x32_bf16 v[72:75], v[108:111], v[240:243], v[72:75]\n\t"
        "ds_read_b128 v[104:107], %[pb2] offset:6144\n\tv_mfma_f32_16x16x32_bf16 v[76:79], v[108:111], v[244:247], v[76:79]\n\t"
        "buffer_load_dwordx4 v[240:243], off, %[rt], %[sa] offset:0 sc1\n\ts_waitcnt vmcnt(29) lgkmcnt(3)\n\t"
        "v_mfma_f32_16x16x32_bf16 v[16:19], v[80:83], v[152:155], v[16:19]\n\tds_read_b128 v[108:111], %[pb2] offset:7168\n\t"
        "v_mfma_f32_16x16x32_bf16 v[20:23], v[80:83], v[156:159], v[20:23]\n\tv_mfma_f32_16x16x32_bf16 v[24:27], v[84:87], v[152:155], v[24:27]\n\t"
        "ds_read_b128 v[80:83], %[pb2] offset:8192\n\tv_mfma_f32_16x16x32_bf16 v[28:31], v[84:87], v[156:159], v[28:31]\n\t"
        "v_mfma_f32_16x16x32_bf16 v[32:35], v[88:91], v[152:155], v[32:35]\n\tds_read_b128 v[84:87], %[pb2] offset:9216\n\t"
        "v_mfma_f32_16x16x32_bf16 v[36:39], v[88:91], v[156:159], v[36:39]\n\tbuffer_load_dwordx4 v[244:247], off, %[rt], %[sa] offset:1024 sc1\n\t"
        "s_add_u32 %[sa], %[sa], 0x2000\n\tv_mfma_f32_16x16x32_bf16 v[40:43], v[92:95], v[152:155], v[40:43]\n\tds_read_b128 v[88:91], %[pb2] offset:10240\n\t"
        "v_mfma_f32_16x16x32_bf16 v[44:47], v[92:95], v[156:159], v[44:47]\n\ts_waitcnt lgkmcnt(3)\n\t"
        "v_mfma_f32_16x16x32_bf16 v[48:51], v[96:99], v[152:155], v[48:51]\n\tds_read_b128 v[92:95], %[pb2] offset:11264\n\t"
        "v_mfma_f32_16x16x32_bf16 v[52:55], v[96:99], v[156:159], v[52:55]\n\tv_mfma_f32_16x16x32_bf16 v[56:59], v[100:103], v[152:155], v[56:59]\n\t"
        "ds_read_b128 v[96:99], %[pb2] offset:12288\n\tv_mfma_f32_16x16x32_bf16 v[60:63], v[100:103], v[156:159], v[60:63]\n\t"
        "v_mfma_f32_16x16x32_bf16 v[64:67], v[104:107], v[152:155], v[64:67]\n\tds_read_b128 v[100:103], %[pb2] offset:13312\n\t"
        "v_mfma_f32_16x16x32_bf16 v[68:71], v[104:107], v[156:159], v[68:71]\n\tv_mfma_f32_16x16x32_bf16 v[72:75], v[108:111], v[152:155], v[72:75]\n\t"
        "ds_read_b128 v[104:107], %[pb2] offset:14336\n\tv_mfma_f32_16x16x32_bf16 v[76:79], v[108:111], v[156:159], v[76:79]\n\t"
        "buffer_load_dwordx4 v[152:155], off, %[rt], %[sa] offset:0 sc1\n\ts_waitcnt vmcnt(29) lgkmcnt(3)\n\t"
        "v_mfma_f32_16x16x32_bf16 v[16:19], v[80:83], v[160:163], v[16:19]\n\tds_read_b128 v[108:111], %[pb2] offset:15360\n\t"
        "v_mfma_f32_16x16x32_bf16 v[20:23], v[80:83], v[164:167], v[20:23]\n\tv_mfma_f32_16x16x32_bf16 v[24:27], v[84:87], v[160:163], v[24:27]\n\t"
        "ds_read_b128 v[80:83], %[pb0] offset:0\n\tv_mfma_f32_16x16x32_bf16 v[28:31], v[84:87], v[164:167], v[28:31]\n\t"
        "v_mfma_f32_16x16x32_bf16 v[32:35], v[88:91], v[160:163], v[32:35]\n\tds_read_b128 v[84:87], %[pb0] offset:1024\n\t"
        "v_mfma_f32_16x16x32_bf16 v[36:39], v[88:91], v[164:167], v[36:39]\n\tbuffer_load_dwordx4 v[156:159], off, %[rt], %[sa] offset:1024 sc1\n\t"
        "s_add_u32 %[sa], %[sa], 0x2000\n\tv_mfma_f32_16x16x32_bf16 v[40:43], v[92:95], v[160:163], v[40:43]\n\tds_read_b128 v[88:91], %[pb0] offset:2048\n\t"
        "v_mfma_f32_16x16x32_bf16 v[44:47], v[92:95], v[164:167], v[44:47]\n\ts_waitcnt lgkmcnt(3)\n\t"
        "v_mfma_f32_16x16x32_bf16 v[48:51], v[96:99], v[160:163], v[48:51]\n\tds_read_b128 v[92:95], %[pb0] offset:3072\n\t"
        "v_mfma_f32_16x16x32_bf16 v[52:55], v[96:99], v[164:167], v[52:55]\n\tv_mfma_f32_16x16x32_bf16 v[56:59], v[100:103], v[160:163], v[56:59]\n\t"
        "ds_read_b128 v[96:99], %[pb0] offset:4096\n\tv_mfma_f32_16x16x32_bf16 v[60:63], v[100:103], v[164:167], v[60:63]\n\t"
        "v_mfma_f32_16x16x32_bf16 v[64:67], v[104:107], v[160:163], v[64:67]\n\tds_read_b128 v[100:103], %[pb0] offset:5120\n\t"
        "v_mfma_f32_16x16x32_bf16 v[68:71], v[104:107], v[164:167], v[68:71]\n\tv_mfma_f32_16x16x32_bf16 v[72:75], v[108:111], v[160:163], v[72:75]\n\t"
        "ds_read_b128 v[104:107], %[pb0] offset:6144\n\tv_mfma_f32_16x16x32_bf16 v[76:79], v[108:111], v[164:167], v[76:79]\n\t"
        "buffer_load_dwordx4 v[160:163], off, %[rt], %[sa] offset:0 sc1\n\ts_waitcnt vmcnt(25) lgkmcnt(3)\n\t.p2align 12,,12\n\t.Lpg14:\n\t"
        "s_cbranch_execz .Lpgret\n\tv_mfma_f32_16x16x32_bf16 v[16:19], v[80:83], v[168:171], v[16:19]\n\tds_read_b128 v[108:111], %[pb0] offset:7168\n\t"
        "v_mfma_f32_16x16x32_bf16 v[20:23], v[80:83], v[172:175], v[20:23]\n\tv_mfma_f32_16x16x32_bf16 v[24:27], v[84:87], v[168:171], v[24:27]\n\t"
        "ds_read_b128 v[80:83], %[pb0] offset:8192\n\tv_mfma_f32_16x16x32_bf16 v[28:31], v[84:87], v[172:175], v[28:31]\n\t"
        "v_mfma_f32_16x16x32_bf16 v[32:35], v[88:91], v[168:171], v[32:35]\n\tds_read_b128 v[84:87], %[pb0] offset:9216\n\t"
        "v_mfma_f32_16x16x32_bf16 v[36:39], v[88:91], v[172:175], v[36:39]\n\tbuffer_load_dwordx4 v[164:167], off, %[rt], %[sa] offset:1024 sc1\n\t"
        "s_add_u32 %[sa], %[sa], 0x2000\n\tv_mfma_f32_16x16x32_bf16 v[40:43], v[92:95], v[168:171], v[40:43]\n\tds_read_b128 v[88:91], %[pb0] offset:10240\n\t"
        "v_mfma_f32_16x16x32_bf16 v[44:47], v[92:95], v[172:175], v[44:47]\n\ts_waitcnt lgkmcnt(3)\n\t"
        "v_mfma_f32_16x16x32_bf16 v[48:51], v[96:99], v[168:171], v[48:51]\n\tds_read_b128 v[92:95], %[pb0] offset:11264\n\t"
        "v_mfma_f32_16x16x32_bf16 v[52:55], v[96:99], v[172:175], v[52:55]\n\tv_mfma_f32_16x16x32_bf16 v[56:59], v[100:103], v[168:171], v[56:59]\n\t"
        "ds_read_b128 v[96:99], %[pb0] offset:12288\n\tv_mfma_f32_16x16x32_bf16 v[60:63], v[100:103], v[172:175], v[60:63]\n\t"
        "v_mfma_f32_16x16x32_bf16 v[64:67], v[104:107], v[168:171], v[64:67]\n\tds_read_b128 v[100:103], %[pb0] offset:13312\n\t"
        "v_mfma_f32_16x16x32_bf16 v[68:71], v[104:107], v[172:175], v[68:71]\n\tv_mfma_f32_16x16x32_bf16 v[72:75], v[108:111], v[168:171], v[72:75]\n\t"
        "ds_read_b128 v[104:107], %[pb0] offset:14336\n\tv_mfma_f32_16x16x32_bf16 v[76:79], v[108:111], v[172:175], v[76:79]\n\t"
        "buffer_load_dwordx4 v[168:171], off, %[rt], %[sa] offset:0 sc1\n\ts_waitcnt vmcnt(24) lgkmcnt(3)\n\t"
        "v_mfma_f32_16x16x32_bf16 v[16:19], v[80:83], v[176:179], v[16:19]\n\tds_read_b128 v[108:111], %[pb0] offset:15360\n\t"
        "v_mfma_f32_16x16x32_bf16 v[20:23], v[80:83], v[180:183], v[20:23]\n\tv_mfma_f32_16x16x32_bf16 v[24:27], v[84:87], v[176:179], v[24:27]\n\t"
        "ds_read_b128 v[80:83], %[pb0] offset:16384\n\tv_mfma_f32_16x16x32_bf16 v[28:31], v[84:87], v[180:183], v[28:31]\n\t"
        "v_mfma_f32_16x16x32_bf16 v[32:35], v[88:91], v[176:179], v[32:35]\n\tds_read_b128 v[84:87], %[pb0] offset:17408\n\t"
        "v_mfma_f32_16x16x32_bf16 v[36:39], v[88:91], v[180:183], v[36:39]\n\tbuffer_load_dwordx4 v[172:175], off, %[rt], %[sa] offset:1024 sc1\n\t"
        "s_add_u32 %[sa], %[sa], 0x2000\n\tv_mfma_f32_16x16x32_bf16 v[40:43], v[92:95], v[176:179], v[40:43]\n\tds_read_b128 v[88:91], %[pb0] offset:18432\n\t"
        "v_mfma_f32_16x16x32_bf16 v[44:47], v[92:95], v[180:183], v[44:47]\n\ts_waitcnt lgkmcnt(3)\n\t"
        "v_mfma_f32_16x16x32_bf16 v[48:51], v[96:99], v[176:179], v[48:51]\n\tds_read_b128 v[92:95], %[pb0] offset:19456\n\t"
        "v_mfma_f32_16x16x32_bf16 v[52:55], v[96:99], v[180:183], v[52:55]\n\tv_mfma_f32_16x16x32_bf16 v[56:59], v[100:103], v[176:179], v[56:59]\n\t"
        "ds_read_b128 v[96:99], %[pb0] offset:20480\n\tv_mfma_f32_16x16x32_bf16 v[60:63], v[100:103], v[180:183], v[60:63]\n\t"
        "v_mfma_f32_16x16x32_bf16 v[64:67], v[104:107], v[176:179], v[64:67]\n\tds_read_b128 v[100:103], %[pb0] offset:21504\n\t"
        "v_mfma_f32_16x16x32_bf16 v[68:71], v[104:107], v[180:183], v[68:71]\n\tv_mfma_f32_16x16x32_bf16 v[72:75], v[108:111], v[176:179], v[72:75]\n\t"
        "ds_read_b128 v[104:107], %[pb0] offset:22528\n\tv_mfma_f32_16x16x32_bf16 v[76:79], v[108:111], v[180:183], v[76:79]\n\t"
        "buffer_load_dwordx4 v[176:179], off, %[rt], %[sa] offset:0 sc1\n\ts_waitcnt vmcnt(24) lgkmcnt(3)\n\t"
        "v_mfma_f32_16x16x32_bf16 v[16:19], v[80:83], v[184:187], v[16:19]\n\tds_read_b128 v[108:111], %[pb0] offset:23552\n\t"
        "v_mfma_f32_16x16x32_bf16 v[20:23], v[80:83], v[188:191], v[20:23]\n\tv_mfma_f32_16x16x32_bf16 v[24:27], v[84:87], v[184:187], v[24:27]\n\t"
        "ds_read_b128 v[80:83], %[pb0] offset:24576\n\tv_mfma_f32_16x16x32_bf16 v[28:31], v[84:87], v[188:191], v[28:31]\n\t"
        "v_mfma_f32_16x16x32_bf16 v[32:35], v[88:91], v[184:187], v[32:35]\n\tds_read_b128 v[84:87], %[pb0] offset:25600\n\t"
        "v_mfma_f32_16x16x32_bf16 v[36:39], v[88:91], v[188:191], v[36:39]\n\tbuffer_load_dwordx4 v[180:183], off, %[rt], %[sa] offset:1024 sc1\n\t"
        "s_add_u32 %[sa], %[sa], 0x2000\n\tv_mfma_f32_16x16x32_bf16 v[40:43], v[92:95], v[184:187], v[40:43]\n\tds_read_b128 v[88:91], %[pb0] offset:26624\n\t"
        "v_mfma_f32_16x16x32_bf16 v[44:47], v[92:95], v[188:191], v[44:47]\n\ts_waitcnt lgkmcnt(3)\n\t"
        "v_mfma_f32_16x16x32_bf16 v[48:51], v[96:99], v[184:187], v[48:51]\n\tds_read_b128 v[92:95], %[pb0] offset:27648\n\t"
        "v_mfma_f32_16x16x32_bf16 v[52:55], v[96:99], v[188:191], v[52:55]\n\tv_mfma_f32_16x16x32_bf16 v[56:59], v[100:103], v[184:187], v[56:59]\n\t"
        "ds_read_b128 v[96:99], %[pb0] offset:28672\n\tv_mfma_f32_16x16x32_bf16 v[60:63], v[100:103], v[188:191], v[60:63]\n\t"
        "v_mfma_f32_16x16x32_bf16 v[64:67], v[104:107], v[184:187], v[64:67]\n\tds_read_b128 v[100:103], %[pb0] offset:29696\n\t"
        "v_mfma_f32_16x16x32_bf16 v[68:71], v[104:107], v[188:191], v[68:71]\n\tv_mfma_f32_16x16x32_bf16 v[72:75], v[108:111], v[184:187], v[72:75]\n\t"
        "ds_read_b128 v[104:107], %[pb0] offset:30720\n\tv_mfma_f32_16x16x32_bf16 v[76:79], v[108:111], v[188:191], v[76:79]\n\t"
        "buffer_load_dwordx4 v[184:187], off, %[rt], %[sa] offset:0 sc1\n\ts_waitcnt vmcnt(23) lgkmcnt(3)\n\t"
        "v_mfma_f32_16x16x32_bf16 v[16:19], v[80:83], v[192:195], v[16:19]\n\tds_read_b128 v[108:111], %[pb0] offset:31744\n\t"
        "v_mfma_f32_16x16x32_bf16 v[20:23], v[80:83], v[196:199], v[20:23]\n\tv_mfma_f32_16x16x32_bf16 v[24:27], v[84:87], v[192:195], v[24:27]\n\t"
        "s_barrier\n\tds_read_b128 v[80:83], %[pb0] offset:32768\n\tv_mfma_f32_16x16x32_bf16 v[28:31], v[84:87], v[196:199], v[28:31]\n\t"
        "v_mfma_f32_16x16x32_bf16 v[32:35], v[88:91], v[192:195], v[32:35]\n\tds_read_b128 v[84:87], %[pb0] offset:33792\n\t"
        "v_mfma_f32_16x16x32_bf16 v[36:39], v[88:91], v[196:199], v[36:39]\n\tbuffer_load_dwordx4 v[188:191], off, %[rt], %[sa] offset:1024 sc1\n\t"
        "s_add_u32 %[sa], %[sa], 0x2000\n\tv_mfma_f32_16x16x32_bf16 v[40:43], v[92:95], v[192:195], v[40:43]\n\tds_read_b128 v[88:91], %[pb0] offset:34816\n\t"
        "v_mfma_f32_16x16x32_bf16 v[44:47], v[92:95], v[196:199], v[44:47]\n\ts_waitcnt lgkmcnt(3)\n\t"
        "v_mfma_f32_16x16x32_bf16 v[48:51], v[96:99], v[192:195], v[48:51]\n\tds_read_b128 v[92:95], %[pb0] offset:35840\n\t"
        "v_mfma_f32_16x16x32_bf16 v[52:55], v[96:99], v[196:199], v[52:55]\n\tv_mfma_f32_16x16x32_bf16 v[56:59], v[100:103], v[192:195], v[56:59]\n\t"
        "ds_read_b128 v[96:99], %[pb0] offset:36864\n\tv_mfma_f32_16x16x32_bf16 v[60:63], v[100:103], v[196:199], v[60:63]\n\t"
        "v_mfma_f32_16x16x32_bf16 v[64:67], v[104:107], v[192:195], v[64:67]\n\tds_read_b128 v[100:103], %[pb0] offset:37888\n\t"
        "v_mfma_f32_16x16x32_bf16 v[68:71], v[104:107], v[196:199], v[68:71]\n\tv_mfma_f32_16x16x32_bf16 v[72:75], v[108:111], v[192:195], v[72:75]\n\t"
        "ds_read_b128 v[104:107], %[pb0] offset:38912\n\tv_mfma_f32_16x16x32_bf16 v[76:79], v[108:111], v[196:199], v[76:79]\n\t"
        "buffer_load_dwordx4 v[192:195], off, %[rt], %[sa] offset:0 sc1\n\ts_waitcnt vmcnt(23) lgkmcnt(3)\n\t"
        "v_mfma_f32_16x16x32_bf16 v[16:19], v[80:83], v[200:203], v[16:19]\n\tds_read_b128 v[108:111], %[pb0] offset:39936\n\t"
        "v_mfma_f32_16x16x32_bf16 v[20:23], v[80:83], v[204:207], v[20:23]\n\tv_mfma_f32_16x16x32_bf16 v[24:27], v[84:87], v[200:203], v[24:27]\n\t"
        "ds_read_b128 v[80:83], %[pb0] offset:40960\n\tv_mfma_f32_16x16x32_bf16 v[28:31], v[84:87], v[204:207], v[28:31]\n\t"
        "v_mfma_f32_16x16x32_bf16 v[32:35], v[88:91], v[200:203], v[32:35]\n\tds_read_b128 v[84:87], %[pb0] offset:41984\n\t"
        "v_mfma_f32_16x16x32_bf16 v[36:39], v[88:91], v[204:207], v[36:39]\n\tbuffer_load_dwordx4 v[196:199], off, %[rt], %[sa] offset:1024 sc1\n\t"
        "s_add_u32 %[sa], %[sa], 0x2000\n\tv_mfma_f32_16x16x32_bf16 v[40:43], v[92:95], v[200:203], v[40:43]\n\tds_read_b128 v[88:91], %[pb0] offset:43008\n\t"
        "v_mfma_f32_16x16x32_bf16 v[44:47], v[92:95], v[204:207], v[44:47]\n\ts_waitcnt lgkmcnt(3)\n\t"
        "v_mfma_f32_16x16x32_bf16 v[48:51], v[96:99], v[200:203], v[48:51]\n\tds_read_b128 v[92:95], %[pb0] offset:44032\n\t"
        "v_mfma_f32_16x16x32_bf16 v[52:55], v[96:99], v[204:207], v[52:55]\n\tv_mfma_f32_16x16x32_bf16 v[56:59], v[100:103], v[200:203], v[56:59]\n\t"
        "ds_read_b128 v[96:99], %[pb0] offset:45056\n\tv_mfma_f32_16x16x32_bf16 v[60:63], v[100:103], v[204:207], v[60:63]\n\t"
        "v_mfma_f32_16x16x32_bf16 v[64:67], v[104:107], v[200:203], v[64:67]\n\tds_read_b128 v[100:103], %[pb0] offset:46080\n\t"
        "v_mfma_f32_16x16x32_bf16 v[68:71], v[104:107], v[204:207], v[68:71]\n\tv_mfma_f32_16x16x32_bf16 v[72:75], v[108:111], v[200:203], v[72:75]\n\t"
        "ds_read_b128 v[104:107], %[pb0] offset:47104\n\tv_mfma_f32_16x16x32_bf16 v[76:79], v[108:111], v[204:207], v[76:79]\n\t"
        "buffer_load_dwordx4 v[200:203], off, %[rt], %[sa] offset:0 sc1\n\ts_waitcnt vmcnt(22) lgkmcnt(3)\n\t"
        "v_mfma_f32_16x16x32_bf16 v[16:19], v[80:83], v[208:211], v[16:19]\n\tds_read_b128 v[108:111], %[pb0] offset:48128\n\t"
        "v_mfma_f32_16x16x32_bf16 v[20:23], v[80:83], v[212:215], v[20:23]\n\tv_mfma_f32_16x16x32_bf16 v[24:27], v[84:87], v[208:211], v[24:27]\n\t"
        "ds_read_b128 v[80:83], %[pb0] offset:49152\n\tv_mfma_f32_16x16x32_bf16 v[28:31], v[84:87], v[212:215], v[28:31]\n\t"
        "v_mfma_f32_16x16x32_bf16 v[32:35], v[88:91], v[208:211], v[32:35]\n\tds_read_b128 v[84:87], %[pb0] offset:50176\n\t"
        "v_mfma_f32_16x16x32_bf16 v[36:39], v[88:91], v[212:215], v[36:39]\n\tbuffer_load_dwordx4 v[204:207], off, %[rt], %[sa] offset:1024 sc1\n\t"
        "s_add_u32 %[sa], %[sa], 0x2000\n\tv_mfma_f32_16x16x32_bf16 v[40:43], v[92:95], v[208:211], v[40:43]\n\tds_read_b128 v[88:91], %[pb0] offset:51200\n\t"
        "v_mfma_f32_16x16x32_bf16 v[44:47], v[92:95], v[212:215], v[44:47]\n\ts_waitcnt lgkmcnt(3)\n\t"
        "v_mfma_f32_16x16x32_bf16 v[48:51], v[96:99], v[208:211], v[48:51]\n\tds_read_b128 v[92:95], %[pb0] offset:52224\n\t"
        "v_mfma_f32_16x16x32_bf16 v[52:55], v[96:99], v[212:215], v[52:55]\n\tv_mfma_f32_16x16x32_bf16 v[56:59], v[100:103], v[208:211], v[56:59]\n\t"
        "ds_read_b128 v[96:99], %[pb0] offset:53248\n\tv_mfma_f32_16x16x32_bf16 v[60:63], v[100:103], v[212:215], v[60:63]\n\t"
        "v_mfma_f32_16x16x32_bf16 v[64:67], v[104:107], v[208:211], v[64:67]\n\tds_read_b128 v[100:103], %[pb0] offset:54272\n\t"
        "v_mfma_f32_16x16x32_bf16 v[68:71], v[104:107], v[212:215], v[68:71]\n\tv_mfma_f32_16x16x32_bf16 v[72:75], v[108:111], v[208:211], v[72:75]\n\t"
        "ds_read_b128 v[104:107], %[pb0] offset:55296\n\tv_mfma_f32_16x16x32_bf16 v[76:79], v[108:111], v[212:215], v[76:79]\n\t"
        "buffer_load_dwordx4 v[208:211], off, %[rt], %[sa] offset:0 sc1\n\ts_waitcnt vmcnt(22) lgkmcnt(3)\n\t"
        "v_mfma_f32_16x16x32_bf16 v[16:19], v[80:83], v[216:219], v[16:19]\n\tds_read_b128 v[108:111], %[pb0] offset:56320\n\t"
        "v_mfma_f32_16x16x32_bf16 v[20:23], v[80:83], v[220:223], v[20:23]\n\tv_mfma_f32_16x16x32_bf16 v[24:27], v[84:87], v[216:219], v[24:27]\n\t"
        "ds_read_b128 v[80:83], %[pb0] offset:57344\n\tv_mfma_f32_16x16x32_bf16 v[28:31], v[84:87], v[220:223], v[28:31]\n\t"
        "v_mfma_f32_16x16x32_bf16 v[32:35], v[88:91], v[216:219], v[32:35]\n\tds_read_b128 v[84:87], %[pb0] offset:58368\n\t"
        "v_mfma_f32_16x16x32_bf16 v[36:39], v[88:91], v[220:223], v[36:39]\n\tbuffer_load_dwordx4 v[212:215], off, %[rt], %[sa] offset:1024 sc1\n\t"
        "s_add_u32 %[sa], %[sa], 0x6ee000\n\tv_mfma_f32_16x16x32_bf16 v[40:43], v[92:95], v[216:219], v[40:43]\n\t"
        "ds_read_b128 v[88:91], %[pb0] offset:59392\n\tv_mfma_f32_16x16x32_bf16 v[44:47], v[92:95], v[220:223], v[44:47]\n\ts_waitcnt lgkmcnt(3)\n\t"
        "v_mfma_f32_16x16x32_bf16 v[48:51], v[96:99], v[216:219], v[48:51]\n\tds_read_b128 v[92:95], %[pb0] offset:60416\n\t"
        "v_mfma_f32_16x16x32_bf16 v[52:55], v[96:99], v[220:223], v[52:55]\n\tv_mfma_f32_16x16x32_bf16 v[56:59], v[100:103], v[216:219], v[56:59]\n\t"
        "ds_read_b128 v[96:99], %[pb0] offset:61440\n\tv_mfma_f32_16x16x32_bf16 v[60:63], v[100:103], v[220:223], v[60:63]\n\t"
        "v_mfma_f32_16x16x32_bf16 v[64:67], v[104:107], v[216:219], v[64:67]\n\tds_read_b128 v[100:103], %[pb0] offset:62464\n\t"
        "v_mfma_f32_16x16x32_bf16 v[68:71], v[104:107], v[220:223], v[68:71]\n\tv_mfma_f32_16x16x32_bf16 v[72:75], v[108:111], v[216:219], v[72:75]\n\t"
        "ds_read_b128 v[104:107], %[pb0] offset:63488\n\tv_mfma_f32_16x16x32_bf16 v[76:79], v[108:111], v[220:223], v[76:79]\n\t"
        "s_waitcnt vmcnt(21) lgkmcnt(3)\n\tv_mfma_f32_16x16x32_bf16 v[16:19], v[80:83], v[224:227], v[16:19]\n\t"
        "ds_read_b128 v[108:111], %[pb0] offset:64512\n\tv_mfma_f32_16x16x32_bf16 v[20:23], v[80:83], v[228:231], v[20:23]\n\t"
        "v_mfma_f32_16x16x32_bf16 v[24:27], v[84:87], v[224:227], v[24:27]\n\ts_barrier\n\tds_read_b128 v[80:83], %[pb1] offset:0\n\t"
        "buffer_load_dwordx4 v[216:219], off, %[rt], %[sa] offset:0 sc1\n\tv_mfma_f32_16x16x32_bf16 v[28:31], v[84:87], v[228:231], v[28:31]\n\t"
        "v_mfma_f32_16x16x32_bf16 v[32:35], v[88:91], v[224:227], v[32:35]\n\tds_read_b128 v[84:87], %[pb1] offset:1024\n\t"
        "v_mfma_f32_16x16x32_bf16 v[36:39], v[88:91], v[228:231], v[36:39]\n\tv_mfma_f32_16x16x32_bf16 v[40:43], v[92:95], v[224:227], v[40:43]\n\t"
        "ds_read_b128 v[88:91], %[pb1] offset:2048\n\tv_mfma_f32_16x16x32_bf16 v[44:47], v[92:95], v[228:231], v[44:47]\n\ts_waitcnt lgkmcnt(3)\n\t"
        "v_mfma_f32_16x16x32_bf16 v[48:51], v[96:99], v[224:227], v[48:51]\n\tds_read_b128 v[92:95], %[pb1] offset:3072\n\t"
        "buffer_load_dwordx4 v[220:223], off, %[rt], %[sa] offset:1024 sc1\n\ts_add_u32 %[sa], %[sa], 0x2000\n\t"
        "v_mfma_f32_16x16x32_bf16 v[52:55], v[96:99], v[228:231], v[52:55]\n\tv_mfma_f32_16x16x32_bf16 v[56:59], v[100:103], v[224:227], v[56:59]\n\t"
        "ds_read_b128 v[96:99], %[pb1] offset:4096\n\tv_mfma_f32_16x16x32_bf16 v[60:63], v[100:103], v[228:231], v[60:63]\n\t"
        "v_mfma_f32_16x16x32_bf16 v[64:67], v[104:107], v[224:227], v[64:67]\n\tds_read_b128 v[100:103], %[pb1] offset:5120\n\t"
        "v_mfma_f32_16x16x32_bf16 v[68:71], v[104:107], v[228:231], v[68:71]\n\tv_mfma_f32_16x16x32_bf16 v[72:75], v[108:111], v[224:227], v[72:75]\n\t"
        "ds_read_b128 v[104:107], %[pb1] offset:6144\n\tv_mfma_f32_16x16x32_bf16 v[76:79], v[108:111], v[228:231], v[76:79]\n\t"
        "buffer_load_dwordx4 v[224:227], off, %[rt], %[sa] offset:0 sc1\n\ts_waitcnt vmcnt(21) lgkmcnt(3)\n\t"
        "v_mfma_f32_16x16x32_bf16 v[16:19], v[80:83], v[232:235], v[16:19]\n\tds_read_b128 v[108:111], %[pb1] offset:7168\n\t"
        "v_mfma_f32_16x16x32_bf16 v[20:23], v[80:83], v[236:239], v[20:23]\n\tv_mfma_f32_16x16x32_bf16 v[24:27], v[84:87], v[232:235], v[24:27]\n\t"
        "ds_read_b128 v[80:83], %[pb1] offset:8192\n\tv_mfma_f32_16x16x32_bf16 v[28:31], v[84:87], v[236:239], v[28:31]\n\t"
        "v_mfma_f32_16x16x32_bf16 v[32:35], v[88:91], v[232:235], v[32:35]\n\tds_read_b128 v[84:87], %[pb1] offset:9216\n\t"
        "v_mfma_f32_16x16x32_bf16 v[36:39], v[88:91], v[236:239], v[36:39]\n\tbuffer_load_dwordx4 v[228:231], off, %[rt], %[sa] offset:1024 sc1\n\t"
        "s_add_u32 %[sa], %[sa], 0x2000\n\tv_mfma_f32_16x16x32_bf16 v[40:43], v[92:95], v[232:235], v[40:43]\n\tds_read_b128 v[88:91], %[pb1] offset:10240\n\t"
        "v_mfma_f32_16x16x32_bf16 v[44:47], v[92:95], v[236:239], v[44:47]\n\ts_waitcnt lgkmcnt(3)\n\t"
        "v_mfma_f32_16x16x32_bf16 v[48:51], v[96:99], v[232:235], v[48:51]\n\tds_read_b128 v[92:95], %[pb1] offset:11264\n\t"
        "v_mfma_f32_16x16x32_bf16 v[52:55], v[96:99], v[236:239], v[52:55]\n\tv_mfma_f32_16x16x32_bf16 v[56:59], v[100:103], v[232:235], v[56:59]\n\t"
        "ds_read_b128 v[96:99], %[pb1] offset:12288\n\tv_mfma_f32_16x16x32_bf16 v[60:63], v[100:103], v[236:239], v[60:63]\n\t"
        "v_mfma_f32_16x16x32_bf16 v[64:67], v[104:107], v[232:235], v[64:67]\n\tds_read_b128 v[100:103], %[pb1] offset:13312\n\t"
        "v_mfma_f32_16x16x32_bf16 v[68:71], v[104:107], v[236:239], v[68:71]\n\tv_mfma_f32_16x16x32_bf16 v[72:75], v[108:111], v[232:235], v[72:75]\n\t"
        "ds_read_b128 v[104:107], %[pb1] offset:14336\n\tv_mfma_f32_16x16x32_bf16 v[76:79], v[108:111], v[236:239], v[76:79]\n\t"
        "buffer_load_dwordx4 v[232:235], off, %[rt], %[sa] offset:0 sc1\n\ts_waitcnt vmcnt(21) lgkmcnt(3)\n\t"
        "v_mfma_f32_16x16x32_bf16 v[16:19], v[80:83], v[240:243], v[16:19]\n\tds_read_b128 v[108:111], %[pb1] offset:15360\n\t"
        "v_mfma_f32_16x16x32_bf16 v[20:23], v[80:83], v[244:247], v[20:23]\n\tv_mfma_f32_16x16x32_bf16 v[24:27], v[84:87], v[240:243], v[24:27]\n\t"
        "ds_read_b128 v[80:83], %[pb1] offset:16384\n\tv_mfma_f32_16x16x32_bf16 v[28:31], v[84:87], v[244:247], v[28:31]\n\t"
        "v_mfma_f32_16x16x32_bf16 v[32:35], v[88:91], v[240:243], v[32:35]\n\tds_read_b128 v[84:87], %[pb1] offset:17408\n\t"
        "v_mfma_f32_16x16x32_bf16 v[36:39], v[88:91], v[244:247], v[36:39]\n\tbuffer_load_dwordx4 v[236:239], off, %[rt], %[sa] offset:1024 sc1\n\t"
        "s_add_u32 %[sa], %[sa], 0x2000\n\tv_mfma_f32_16x16x32_bf16 v[40:43], v[92:95], v[240:243], v[40:43]\n\tds_read_b128 v[88:91], %[pb1] offset:18432\n\t"
        "v_mfma_f32_16x16x32_bf16 v[44:47], v[92:95], v[244:247], v[44:47]\n\ts_waitcnt lgkmcnt(3)\n\t"
        "v_mfma_f32_16x16x32_bf16 v[48:51], v[96:99], v[240:243], v[48:51]\n\tds_read_b128 v[92:95], %[pb1] offset:19456\n\t"
        "v_mfma_f32_16x16x32_bf16 v[52:55], v[96:99], v[244:247], v[52:55]\n\tv_mfma_f32_16x16x32_bf16 v[56:59], v[100:103], v[240:243], v[56:59]\n\t"
        "ds_read_b128 v[96:99], %[pb1] offset:20480\n\tv_mfma_f32_16x16x32_bf16 v[60:63], v[100:103], v[244:247], v[60:63]\n\t"
        "v_mfma_f32_16x16x32_bf16 v[64:67], v[104:107], v[240:243], v[64:67]\n\tds_read_b128 v[100:103], %[pb1] offset:21504\n\t"
        "v_mfma_f32_16x16x32_bf16 v[68:71], v[104:107], v[244:247], v[68:71]\n\tv_mfma_f32_16x16x32_bf16 v[72:75], v[108:111], v[240:243], v[72:75]\n\t"
        "ds_read_b128 v[104:107], %[pb1] offset:22528\n\tv_mfma_f32_16x16x32_bf16 v[76:79], v[108:111], v[244:247], v[76:79]\n\t"
        "buffer_load_dwordx4 v[240:243], off, %[rt], %[sa] offset:0 sc1\n\ts_waitcnt vmcnt(21) lgkmcnt(3)\n\t"
        "v_mfma_f32_16x16x32_bf16 v[16:19], v[80:83], v[152:155], v[16:19]\n\tds_read_b128 v[108:111], %[pb1] offset:23552\n\t"
        "v_mfma_f32_16x16x32_bf16 v[20:23], v[80:83], v[156:159], v[20:23]\n\tv_mfma_f32_16x16x32_bf16 v[24:27], v[84:87], v[152:155], v[24:27]\n\t"
        "ds_read_b128 v[80:83], %[pb1] offset:24576\n\tv_mfma_f32_16x16x32_bf16 v[28:31], v[84:87], v[156:159], v[28:31]\n\t"
        "v_mfma_f32_16x16x32_bf16 v[32:35], v[88:91], v[152:155], v[32:35]\n\tds_read_b128 v[84:87], %[pb1] offset:25600\n\t"
        "v_mfma_f32_16x16x32_bf16 v[36:39], v[88:91], v[156:159], v[36:39]\n\tbuffer_load_dwordx4 v[244:247], off, %[rt], %[sa] offset:1024 sc1\n\t"
        "s_add_u32 %[sa], %[sa], 0x2000\n\tv_mfma_f32_16x16x32_bf16 v[40:43], v[92:95], v[152:155], v[40:43]\n\tds_read_b128 v[88:91], %[pb1] offset:26624\n\t"
        "v_mfma_f32_16x16x32_bf16 v[44:47], v[92:95], v[156:159], v[44:47]\n\ts_waitcnt lgkmcnt(3)\n\t"
        "v_mfma_f32_16x16x32_bf16 v[48:51], v[96:99], v[152:155], v[48:51]\n\tds_read_b128 v[92:95], %[pb1] offset:27648\n\t"
        "v_mfma_f32_16x16x32_bf16 v[52:55], v[96:99], v[156:159], v[52:55]\n\tv_mfma_f32_16x16x32_bf16 v[56:59], v[100:103], v[152:155], v[56:59]\n\t"
        "ds_read_b128 v[96:99], %[pb1] offset:28672\n\tv_mfma_f32_16x16x32_bf16 v[60:63], v[100:103], v[156:159], v[60:63]\n\t"
        "v_mfma_f32_16x16x32_bf16 v[64:67], v[104:107], v[152:155], v[64:67]\n\tds_read_b128 v[100:103], %[pb1] offset:29696\n\t"
        "v_mfma_f32_16x16x32_bf16 v[68:71], v[104:107], v[156:159], v[68:71]\n\tv_mfma_f32_16x16x32_bf16 v[72:75], v[108:111], v[152:155], v[72:75]\n\t"
        "ds_read_b128 v[104:107], %[pb1] offset:30720\n\tv_mfma_f32_16x16x32_bf16 v[76:79], v[108:111], v[156:159], v[76:79]\n\t"
        "buffer_load_dwordx4 v[152:155], off, %[rt], %[sa] offset:0 sc1\n\ts_waitcnt vmcnt(21) lgkmcnt(3)\n\t"
        "v_mfma_f32_16x16x32_bf16 v[16:19], v[80:83], v[160:163], v[16:19]\n\tds_read_b128 v[108:111], %[pb1] offset:31744\n\t"
        "v_mfma_f32_16x16x32_bf16 v[20:23], v[80:83], v[164:167], v[20:23]\n\tv_mfma_f32_16x16x32_bf16 v[24:27], v[84:87], v[160:163], v[24:27]\n\t"
        "ds_read_b128 v[80:83], %[pb1] offset:32768\n\tv_mfma_f32_16x16x32_bf16 v[28:31], v[84:87], v[164:167], v[28:31]\n\t"
        "v_mfma_f32_16x16x32_bf16 v[32:35], v[88:91], v[160:163], v[32:35]\n\tds_read_b128 v[84:87], %[pb1] offset:40960\n\t"
        "v_mfma_f32_16x16x32_bf16 v[36:39], v[88:91], v[164:167], v[36:39]\n\tbuffer_load_dwordx4 v[156:159], off, %[rt], %[sa] offset:1024 sc1\n\t"
        "s_add_u32 %[sa], %[sa], 0x2000\n\tv_mfma_f32_16x16x32_bf16 v[40:43], v[92:95], v[160:163], v[40:43]\n\tds_read_b128 v[88:91], %[pb1] offset:49152\n\t"
        "v_mfma_f32_16x16x32_bf16 v[44:47], v[92:95], v[164:167], v[44:47]\n\ts_waitcnt lgkmcnt(3)\n\t"
        "v_mfma_f32_16x16x32_bf16 v[48:51], v[96:99], v[160:163], v[48:51]\n\tds_read_b128 v[92:95], %[pb1] offset:57344\n\t"
        "v_mfma_f32_16x16x32_bf16 v[52:55], v[96:99], v[164:167], v[52:55]\n\tv_mfma_f32_16x16x32_bf16 v[56:59], v[100:103], v[160:163], v[56:59]\n\t"
        "ds_read_b128 v[96:99], %[pb2] offset:0\n\tv_mfma_f32_16x16x32_bf16 v[60:63], v[100:103], v[164:167], v[60:63]\n\t"
        "v_mfma_f32_16x16x32_bf16 v[64:67], v[104:107], v[160:163], v[64:67]\n\tds_read_b128 v[100:103], %[pb2] offset:8192\n\t"
        "v_mfma_f32_16x16x32_bf16 v[68:71], v[104:107], v[164:167], v[68:71]\n\tv_mfma_f32_16x16x32_bf16 v[72:75], v[108:111], v[160:163], v[72:75]\n\t"
        "ds_read_b128 v[104:107], %[pb1] offset:33792\n\tv_mfma_f32_16x16x32_bf16 v[76:79], v[108:111], v[164:167], v[76:79]\n\t"
        "buffer_load_dwordx4 v[160:163], off, %[rt], %[sa] offset:0 sc1\n\ts_waitcnt vmcnt(21) lgkmcnt(3)\n\t"
        "v_mfma_f32_16x16x32_bf16 v[16:19], v[80:83], v[168:171], v[16:19]\n\tds_read_b128 v[108:111], %[pb1] offset:41984\n\t"
        "v_mfma_f32_16x16x32_bf16 v[20:23], v[80:83], v[172:175], v[20:23]\n\ts_waitcnt vmcnt(19)\n\t"
        "v_mfma_f32_16x16x32_bf16 v[16:19], v[84:87], v[176:179], v[16:19]\n\tds_read_b128 v[80:83], %[pb1] offset:50176\n\t"
        "v_mfma_f32_16x16x32_bf16 v[20:23], v[84:87], v[180:183], v[20:23]\n\ts_waitcnt vmcnt(17)\n\t"
        "v_mfma_f32_16x16x32_bf16 v[16:19], v[88:91], v[184:187], v[16:19]\n\tds_read_b128 v[84:87], %[pb1] offset:58368\n\t"
        "v_mfma_f32_16x16x32_bf16 v[20:23], v[88:91], v[188:191], v[20:23]\n\tbuffer_load_dwordx4 v[164:167], off, %[rt], %[sa] offset:1024 sc1\n\t"
        "s_add_u32 %[sa], %[sa], 0x2000\n\ts_waitcnt vmcnt(16)\n\tv_mfma_f32_16x16x32_bf16 v[16:19], v[92:95], v[192:195], v[16:19]\n\t"
        "ds_read_b128 v[88:91], %[pb2] offset:1024\n\tv_mfma_f32_16x16x32_bf16 v[20:23], v[92:95], v[196:199], v[20:23]\n\ts_waitcnt vmcnt(14) lgkmcnt(3)\n\t"
        "v_mfma_f32_16x16x32_bf16 v[16:19], v[96:99], v[200:203], v[16:19]\n\tds_read_b128 v[92:95], %[pb2] offset:9216\n\t"
        "v_mfma_f32_16x16x32_bf16 v[20:23], v[96:99], v[204:207], v[20:23]\n\ts_waitcnt vmcnt(12)\n\t"
        "v_mfma_f32_16x16x32_bf16 v[16:19], v[100:103], v[208:211], v[16:19]\n\tds_read_b128 v[96:99], %[pb1] offset:34816\n\t"
        "v_mfma_f32_16x16x32_bf16 v[20:23], v[100:103], v[212:215], v[20:23]\n\tv_mfma_f32_16x16x32_bf16 v[24:27], v[104:107], v[168:171], v[24:27]\n\t"
        "ds_read_b128 v[100:103], %[pb1] offset:43008\n\tv_mfma_f32_16x16x32_bf16 v[28:31], v[104:107], v[172:175], v[28:31]\n\t"
        "v_mfma_f32_16x16x32_bf16 v[24:27], v[108:111], v[176:179], v[24:27]\n\tds_read_b128 v[104:107], %[pb1] offset:51200\n\t"
        "v_mfma_f32_16x16x32_bf16 v[28:31], v[108:111], v[180:183], v[28:31]\n\tv_cvt_pk_bf16_f32 v112, v16, v17\n\tv_cvt_pk_bf16_f32 v113, v18, v19\n\t"
        "s_waitcnt lgkmcnt(3)\n\tv_mfma_f32_16x16x32_bf16 v[24:27], v[80:83], v[184:187], v[24:27]\n\tds_read_b128 v[108:111], %[pb1] offset:59392\n\t"
        "v_mfma_f32_16x16x32_bf16 v[28:31], v[80:83], v[188:191], v[28:31]\n\tv_cvt_pk_bf16_f32 v116, v20, v21\n\tv_cvt_pk_bf16_f32 v117, v22, v23\n\t"
        "v_mfma_f32_16x16x32_bf16 v[24:27], v[84:87], v[192:195], v[24:27]\n\tds_read_b128 v[80:83], %[pb2] offset:2048\n\t"
        "v_mfma_f32_16x16x32_bf16 v[28:31], v[84:87], v[196:199], v[28:31]\n\tv_mfma_f32_16x16x32_bf16 v[24:27], v[88:91], v[200:203], v[24:27]\n\t"
        "ds_read_b128 v[84:87], %[pb2] offset:10240\n\tv_mfma_f32_16x16x32_bf16 v[28:31], v[88:91], v[204:207], v[28:31]\n\t"
        "v_mfma_f32_16x16x32_bf16 v[24:27], v[92:95], v[208:211], v[24:27]\n\tds_read_b128 v[88:91], %[pb1] offset:35840\n\t"
        "v_mfma_f32_16x16x32_bf16 v[28:31], v[92:95], v[212:215], v[28:31]\n\ts_waitcnt lgkmcnt(3)\n\t"
        "v_mfma_f32_16x16x32_bf16 v[32:35], v[96:99], v[168:171], v[32:35]\n\tds_read_b128 v[92:95], %[pb1] offset:44032\n\t"
        "v_mfma_f32_16x16x32_bf16 v[36:39], v[96:99], v[172:175], v[36:39]\n\tv_mfma_f32_16x16x32_bf16 v[32:35], v[100:103], v[176:179], v[32:35]\n\t"
        "ds_read_b128 v[96:99], %[pb1] offset:52224\n\tv_mfma_f32_16x16x32_bf16 v[36:39], v[100:103], v[180:183], v[36:39]\n\t"
        "v_cvt_pk_bf16_f32 v114, v24, v25\n\tv_cvt_pk_bf16_f32 v115, v26, v27\n\tv_mfma_f32_16x16x32_bf16 v[32:35], v[104:107], v[184:187], v[32:35]\n\t"
        "ds_read_b128 v[100:103], %[pb1] offset:60416\n\tv_mfma_f32_16x16x32_bf16 v[36:39], v[104:107], v[188:191], v[36:39]\n\t"
        "buffer_store_dwordx4 v[112:115], off, %[rt], %[sx0] offset:0 sc1\n\tv_cvt_pk_bf16_f32 v118, v28, v29\n\tv_cvt_pk_bf16_f32 v119, v30, v31\n\t"
        "v_mfma_f32_16x16x32_bf16 v[32:35], v[108:111], v[192:195], v[32:35]\n\tds_read_b128 v[104:107], %[pb2] offset:3072\n\t"
        "v_mfma_f32_16x16x32_bf16 v[36:39], v[108:111], v[196:199], v[36:39]\n\ts_waitcnt lgkmcnt(3)\n\t"
        "v_mfma_f32_16x16x32_bf16 v[32:35], v[80:83], v[200:203], v[32:35]\n\tds_read_b128 v[108:111], %[pb2] offset:11264\n\t"
        "v_mfma_f32_16x16x32_bf16 v[36:39], v[80:83], v[204:207], v[36:39]\n\tv_mfma_f32_16x16x32_bf16 v[32:35], v[84:87], v[208:211], v[32:35]\n\t"
        "ds_read_b128 v[80:83], %[pb1] offset:36864\n\tv_mfma_f32_16x16x32_bf16 v[36:39], v[84:87], v[212:215], v[36:39]\n\t"
        "buffer_store_dwordx4 v[116:119], off, %[rt], %[sx0] offset:1024 sc1\n\ts_add_u32 %[sx0], %[sx0], 0x800000\n\t"
        "v_mfma_f32_16x16x32_bf16 v[40:43], v[88:91], v[168:171], v[40:43]\n\tds_read_b128 v[84:87], %[pb1] offset:45056\n\t"
        "v_mfma_f32_16x16x32_bf16 v[44:47], v[88:91], v[172:175], v[44:47]\n\tv_mfma_f32_16x16x32_bf16 v[40:43], v[92:95], v[176:179], v[40:43]\n\t"
        "ds_read_b128 v[88:91], %[pb1] offset:53248\n\tv_mfma_f32_16x16x32_bf16 v[44:47], v[92:95], v[180:183], v[44:47]\n\t"
        "v_cvt_pk_bf16_f32 v120, v32, v33\n\tv_cvt_pk_bf16_f32 v121, v34, v35\n\ts_waitcnt lgkmcnt(3)\n\t"
        "v_mfma_f32_16x16x32_bf16 v[40:43], v[96:99], v[184:187], v[40:43]\n\tds_read_b128 v[92:95], %[pb1] offset:61440\n\t"
        "v_mfma_f32_16x16x32_bf16 v[44:47], v[96:99], v[188:191], v[44:47]\n\tv_cvt_pk_bf16_f32 v124, v36, v37\n\tv_cvt_pk_bf16_f32 v125, v38, v39\n\t"
        "v_mfma_f32_16x16x32_bf16 v[40:43], v[100:103], v[192:195], v[40:43]\n\tds_read_b128 v[96:99], %[pb2] offset:4096\n\t"
        "v_mfma_f32_16x16x32_bf16 v[44:47], v[100:103], v[196:199], v[44:47]\n\tv_mfma_f32_16x16x32_bf16 v[40:43], v[104:107], v[200:203], v[40:43]\n\t"
        "ds_read_b128 v[100:103], %[pb2] offset:12288\n\tv_mfma_f32_16x16x32_bf16 v[44:47], v[104:107], v[204:207], v[44:47]\n\t"
        "v_mfma_f32_16x16x32_bf16 v[40:43], v[108:111], v[208:211], v[40:43]\n\tds_read_b128 v[104:107], %[pb1] offset:37888\n\t"
        "v_mfma_f32_16x16x32_bf16 v[44:47], v[108:111], v[212:215], v[44:47]\n\ts_waitcnt lgkmcnt(3)\n\t"
        "v_mfma_f32_16x16x32_bf16 v[48:51], v[80:83], v[168:171], v[48:51]\n\tds_read_b128 v[108:111], %[pb1] offset:46080\n\t"
        "v_mfma_f32_16x16x32_bf16 v[52:55], v[80:83], v[172:175], v[52:55]\n\tv_mfma_f32_16x16x32_bf16 v[48:51], v[84:87], v[176:179], v[48:51]\n\t"
        "ds_read_b128 v[80:83], %[pb1] offset:54272\n\tv_mfma_f32_16x16x32_bf16 v[52:55], v[84:87], v[180:183], v[52:55]\n\t"
        "v_cvt_pk_bf16_f32 v122, v40, v41\n\tv_cvt_pk_bf16_f32 v123, v42, v43\n\tv_mfma_f32_16x16x32_bf16 v[48:51], v[88:91], v[184:187], v[48:51]\n\t"
        "ds_read_b128 v[84:87], %[pb1] offset:62464\n\tv_mfma_f32_16x16x32_bf16 v[52:55], v[88:91], v[188:191], v[52:55]\n\t"
        "buffer_store_dwordx4 v[120:123], off, %[rt], %[sx1] offset:0 sc1\n\tv_cvt_pk_bf16_f32 v126, v44, v45\n\tv_cvt_pk_bf16_f32 v127, v46, v47\n\t"
        "v_mfma_f32_16x16x32_bf16 v[48:51], v[92:95], v[192:195], v[48:51]\n\tds_read_b128 v[88:91], %[pb2] offset:5120\n\t"
        "v_mfma_f32_16x16x32_bf16 v[52:55], v[92:95], v[196:199], v[52:55]\n\ts_waitcnt lgkmcnt(3)\n\t"
        "v_mfma_f32_16x16x32_bf16 v[48:51], v[96:99], v[200:203], v[48:51]\n\tds_read_b128 v[92:95], %[pb2] offset:13312\n\t"
        "v_mfma_f32_16x16x32_bf16 v[52:55], v[96:99], v[204:207], v[52:55]\n\tv_mfma_f32_16x16x32_bf16 v[48:51], v[100:103], v[208:211], v[48:51]\n\t"
        "ds_read_b128 v[96:99], %[pb1] offset:38912\n\tv_mfma_f32_16x16x32_bf16 v[52:55], v[100:103], v[212:215], v[52:55]\n\t"
        "buffer_store_dwordx4 v[124:127], off, %[rt], %[sx1] offset:1024 sc1\n\ts_add_u32 %[sx1], %[sx1], 0x800000\n\t"
        "v_mfma_f32_16x16x32_bf16 v[56:59], v[104:107], v[168:171], v[56:59]\n\tds_read_b128 v[100:103], %[pb1] offset:47104\n\t"
        "v_mfma_f32_16x16x32_bf16 v[60:63], v[104:107], v[172:175], v[60:63]\n\tv_mfma_f32_16x16x32_bf16 v[56:59], v[108:111], v[176:179], v[56:59]\n\t"
        "ds_read_b128 v[104:107], %[pb1] offset:55296\n\tv_mfma_f32_16x16x32_bf16 v[60:63], v[108:111], v[180:183], v[60:63]\n\t"
        "v_cvt_pk_bf16_f32 v128, v48, v49\n\tv_cvt_pk_bf16_f32 v129, v50, v51\n\ts_waitcnt lgkmcnt(3)\n\t"
        "v_mfma_f32_16x16x32_bf16 v[56:59], v[80:83], v[184:187], v[56:59]\n\tds_read_b128 v[108:111], %[pb1] offset:63488\n\t"
        "v_mfma_f32_16x16x32_bf16 v[60:63], v[80:83], v[188:191], v[60:63]\n\tv_cvt_pk_bf16_f32 v132, v52, v53\n\tv_cvt_pk_bf16_f32 v133, v54, v55\n\t"
        "v_mfma_f32_16x16x32_bf16 v[56:59], v[84:87], v[192:195], v[56:59]\n\tds_read_b128 v[80:83], %[pb2] offset:6144\n\t"
        "v_mfma_f32_16x16x32_bf16 v[60:63], v[84:87], v[196:199], v[60:63]\n\tv_mfma_f32_16x16x32_bf16 v[56:59], v[88:91], v[200:203], v[56:59]\n\t"
        "ds_read_b128 v[84:87], %[pb2] offset:14336\n\tv_mfma_f32_16x16x32_bf16 v[60:63], v[88:91], v[204:207], v[60:63]\n\t"
        "v_mfma_f32_16x16x32_bf16 v[56:59], v[92:95], v[208:211], v[56:59]\n\tds_read_b128 v[88:91], %[pb1] offset:39936\n\t"
        "v_mfma_f32_16x16x32_bf16 v[60:63], v[92:95], v[212:215], v[60:63]\n\ts_waitcnt lgkmcnt(3)\n\t"
        "v_mfma_f32_16x16x32_bf16 v[64:67], v[96:99], v[168:171], v[64:67]\n\tds_read_b128 v[92:95], %[pb1] offset:48128\n\t"
        "v_mfma_f32_16x16x32_bf16 v[68:71], v[96:99], v[172:175], v[68:71]\n\tv_mfma_f32_16x16x32_bf16 v[64:67], v[100:103], v[176:179], v[64:67]\n\t"
        "ds_read_b128 v[96:99], %[pb1] offset:56320\n\tv_mfma_f32_16x16x32_bf16 v[68:71], v[100:103], v[180:183], v[68:71]\n\t"
        "v_cvt_pk_bf16_f32 v130, v56, v57\n\tv_cvt_pk_bf16_f32 v131, v58, v59\n\tv_mfma_f32_16x16x32_bf16 v[64:67], v[104:107], v[184:187], v[64:67]\n\t"
        "ds_read_b128 v[100:103], %[pb1] offset:64512\n\tv_mfma_f32_16x16x32_bf16 v[68:71], v[104:107], v[188:191], v[68:71]\n\t"
        "buffer_store_dwordx4 v[128:131], off, %[rt], %[sx2] offset:0 sc1\n\tv_cvt_pk_bf16_f32 v134, v60, v61\n\tv_cvt_pk_bf16_f32 v135, v62, v63\n\t"
        "v_mfma_f32_16x16x32_bf16 v[64:67], v[108:111], v[192:195], v[64:67]\n\tds_read_b128 v[104:107], %[pb2] offset:7168\n\t"
        "v_mfma_f32_16x16x32_bf16 v[68:71], v[108:111], v[196:199], v[68:71]\n\ts_waitcnt lgkmcnt(3)\n\t"
        "v_mfma_f32_16x16x32_bf16 v[64:67], v[80:83], v[200:203], v[64:67]\n\tds_read_b128 v[108:111], %[pb2] offset:15360\n\t"
        "v_mfma_f32_16x16x32_bf16 v[68:71], v[80:83], v[204:207], v[68:71]\n\tv_mfma_f32_16x16x32_bf16 v[64:67], v[84:87], v[208:211], v[64:67]\n\t"
        "ds_read_b128 v[80:83], %[pb1] offset:0\n\tv_mfma_f32_16x16x32_bf16 v[68:71], v[84:87], v[212:215], v[68:71]\n\t"
        "buffer_store_dwordx4 v[132:135], off, %[rt], %[sx2] offset:1024 sc1\n\ts_add_u32 %[sx2], %[sx2], 0x800000\n\t"
        "v_mfma_f32_16x16x32_bf16 v[72:75], v[88:91], v[168:171], v[72:75]\n\tds_read_b128 v[84:87], %[pb1] offset:1024\n\t"
        "v_mfma_f32_16x16x32_bf16 v[76:79], v[88:91], v[172:175], v[76:79]\n\tbuffer_load_dwordx4 v[168:171], off, %[rt], %[sa] offset:0 sc1\n\t"
        "v_mfma_f32_16x16x32_bf16 v[72:75], v[92:95], v[176:179], v[72:75]\n\tds_read_b128 v[88:91], %[pb1] offset:2048\n\t.p2align 12,,12\n\t.Lpg15:\n\t"
        "s_cbranch_execz .Lpgret\n\tv_mfma_f32_16x16x32_bf16 v[76:79], v[92:95], v[180:183], v[76:79]\n\tds_write_b128 %[px], v[64:67] offset:0\n\t"
        "s_waitcnt lgkmcnt(4)\n\tv_mfma_f32_16x16x32_bf16 v[72:75], v[96:99], v[184:187], v[72:75]\n\tds_read_b128 v[92:95], %[pb1] offset:3072\n\t"
        "v_mfma_f32_16x16x32_bf16 v[76:79], v[96:99], v[188:191], v[76:79]\n\tds_write_b128 %[px], v[68:71] offset:1024\n\t"
        "v_mfma_f32_16x16x32_bf16 v[72:75], v[100:103], v[192:195], v[72:75]\n\tds_read_b128 v[96:99], %[pb1] offset:4096\n\t"
        "v_mfma_f32_16x16x32_bf16 v[76:79], v[100:103], v[196:199], v[76:79]\n\tbuffer_load_dwordx4 v[172:175], off, %[rt], %[sa] offset:1024 sc1\n\t"
        "s_add_u32 %[sa], %[sa], 0x2000\n\tv_mfma_f32_16x16x32_bf16 v[72:75], v[104:107], v[200:203], v[72:75]\n\t"
        "ds_read_b128 v[100:103], %[pb1] offset:5120\n\tv_mfma_f32_16x16x32_bf16 v[76:79], v[104:107], v[204:207], v[76:79]\n\t"
        "v_mfma_f32_16x16x32_bf16 v[72:75], v[108:111], v[208:211], v[72:75]\n\tds_read_b128 v[104:107], %[pb1] offset:6144\n\t"
        "v_mfma_f32_16x16x32_bf16 v[76:79], v[108:111], v[212:215], v[76:79]\n\ts_waitcnt vmcnt(18) lgkmcnt(4)\n\t"
        "v_mfma_f32_16x16x32_bf16 v[16:19], v[80:83], v[216:219], 0\n\tds_read_b128 v[108:111], %[pb1] offset:7168\n\t"
        "v_mfma_f32_16x16x32_bf16 v[20:23], v[80:83], v[220:223], 0\n\tbuffer_load_dwordx4 v[176:179], off, %[rt], %[sa] offset:0 sc1\n\t"
        "v_mfma_f32_16x16x32_bf16 v[24:27], v[84:87], v[216:219], 0\n\tds_read_b128 v[80:83], %[pb1] offset:8192\n\t"
        "v_mfma_f32_16x16x32_bf16 v[28:31], v[84:87], v[220:223], 0\n\tds_write_b128 %[px], v[72:75] offset:2048\n\t"
        "v_mfma_f32_16x16x32_bf16 v[32:35], v[88:91], v[216:219], 0\n\tds_read_b128 v[84:87], %[pb1] offset:9216\n\t"
        "v_mfma_f32_16x16x32_bf16 v[36:39], v[88:91], v[220:223], 0\n\tds_write_b128 %[px], v[76:79] offset:3072\n\t"
        "v_mfma_f32_16x16x32_bf16 v[40:43], v[92:95], v[216:219], 0\n\tds_read_b128 v[88:91], %[pb1] offset:10240\n\t"
        "v_mfma_f32_16x16x32_bf16 v[44:47], v[92:95], v[220:223], 0\n\tbuffer_load_dwordx4 v[180:183], off, %[rt], %[sa] offset:1024 sc1\n\t"
        "s_add_u32 %[sa], %[sa], 0x2000\n\ts_waitcnt lgkmcnt(5)\n\tv_mfma_f32_16x16x32_bf16 v[48:51], v[96:99], v[216:219], 0\n\t"
        "ds_read_b128 v[92:95], %[pb1] offset:11264\n\tv_mfma_f32_16x16x32_bf16 v[52:55], v[96:99], v[220:223], 0\n\t"
        "v_mfma_f32_16x16x32_bf16 v[56:59], v[100:103], v[216:219], 0\n\tds_read_b128 v[96:99], %[pb1] offset:12288\n\t"
        "v_mfma_f32_16x16x32_bf16 v[60:63], v[100:103], v[220:223], 0\n\tv_mfma_f32_16x16x32_bf16 v[64:67], v[104:107], v[216:219], 0\n\t"
        "ds_read_b128 v[100:103], %[pb1] offset:13312\n\tv_mfma_f32_16x16x32_bf16 v[68:71], v[104:107], v[220:223], 0\n\t"
        "buffer_load_dwordx4 v[184:187], off, %[rt], %[sa] offset:0 sc1\n\tv_mfma_f32_16x16x32_bf16 v[72:75], v[108:111], v[216:219], 0\n\t"
        "ds_read_b128 v[104:107], %[pb1] offset:14336\n\tv_mfma_f32_16x16x32_bf16 v[76:79], v[108:111], v[220:223], 0\n\ts_waitcnt vmcnt(19) lgkmcnt(3)\n\t"
        "v_mfma_f32_16x16x32_bf16 v[16:19], v[80:83], v[224:227], v[16:19]\n\tds_read_b128 v[108:111], %[pb1] offset:15360\n\t"
        "v_mfma_f32_16x16x32_bf16 v[20:23], v[80:83], v[228:231], v[20:23]\n\tv_mfma_f32_16x16x32_bf16 v[24:27], v[84:87], v[224:227], v[24:27]\n\t"
        "ds_read_b128 v[80:83], %[pb1] offset:16384\n\tv_mfma_f32_16x16x32_bf16 v[28:31], v[84:87], v[228:231], v[28:31]\n\t"
        "buffer_load_dwordx4 v[188:191], off, %[rt], %[sa] offset:1024 sc1\n\ts_add_u32 %[sa], %[sa], 0x2000\n\t"
        "v_mfma_f32_16x16x32_bf16 v[32:35], v[88:91], v[224:227], v[32:35]\n\tds_read_b128 v[84:87], %[pb1] offset:17408\n\t"
        "v_mfma_f32_16x16x32_bf16 v[36:39], v[88:91], v[228:231], v[36:39]\n\tv_mfma_f32_16x16x32_bf16 v[40:43], v[92:95], v[224:227], v[40:43]\n\t"
        "ds_read_b128 v[88:91], %[pb1] offset:18432\n\tv_mfma_f32_16x16x32_bf16 v[44:47], v[92:95], v[228:231], v[44:47]\n\ts_waitcnt lgkmcnt(3)\n\t"
        "v_mfma_f32_16x16x32_bf16 v[48:51], v[96:99], v[224:227], v[48:51]\n\tds_read_b128 v[92:95], %[pb1] offset:19456\n\t"
        "v_mfma_f32_16x16x32_bf16 v[52:55], v[96:99], v[228:231], v[52:55]\n\tbuffer_load_dwordx4 v[192:195], off, %[rt], %[sa] offset:0 sc1\n\t"
        "v_mfma_f32_16x16x32_bf16 v[56:59], v[100:103], v[224:227], v[56:59]\n\tds_read_b128 v[96:99], %[pb1] offset:20480\n\t"
        "v_mfma_f32_16x16x32_bf16 v[60:63], v[100:103], v[228:231], v[60:63]\n\tv_mfma_f32_16x16x32_bf16 v[64:67], v[104:107], v[224:227], v[64:67]\n\t"
        "ds_read_b128 v[100:103], %[pb1] offset:21504\n\tv_mfma_f32_16x16x32_bf16 v[68:71], v[104:107], v[228:231], v[68:71]\n\t"
        "v_mfma_f32_16x16x32_bf16 v[72:75], v[108:111], v[224:227], v[72:75]\n\tds_read_b128 v[104:107], %[pb1] offset:22528\n\t"
        "v_mfma_f32_16x16x32_bf16 v[76:79], v[108:111], v[228:231], v[76:79]\n\tbuffer_load_dwordx4 v[196:199], off, %[rt], %[sa] offset:1024 sc1\n\t"
        "s_add_u32 %[sa], %[sa], 0xfffde000\n\ts_waitcnt vmcnt(20) lgkmcnt(3)\n\tv_mfma_f32_16x16x32_bf16 v[16:19], v[80:83], v[232:235], v[16:19]\n\t"
        "ds_read_b128 v[108:111], %[pb1] offset:23552\n\tv_mfma_f32_16x16x32_bf16 v[20:23], v[80:83], v[236:239], v[20:23]\n\t"
        "v_mfma_f32_16x16x32_bf16 v[24:27], v[84:87], v[232:235], v[24:27]\n\tds_read_b128 v[80:83], %[pb1] offset:24576\n\t"
        "v_mfma_f32_16x16x32_bf16 v[28:31], v[84:87], v[236:239], v[28:31]\n\tv_mfma_f32_16x16x32_bf16 v[32:35], v[88:91], v[232:235], v[32:35]\n\t"
        "ds_read_b128 v[84:87], %[pb1] offset:25600\n\tv_mfma_f32_16x16x32_bf16 v[36:39], v[88:91], v[236:239], v[36:39]\n\t"
        "buffer_load_dwordx4 v[200:203], off, %[rt], %[sa] offset:0 sc1\n\tv_mfma_f32_16x16x32_bf16 v[40:43], v[92:95], v[232:235], v[40:43]\n\t"
        "ds_read_b128 v[88:91], %[pb1] offset:26624\n\tv_mfma_f32_16x16x32_bf16 v[44:47], v[92:95], v[236:239], v[44:47]\n\ts_waitcnt lgkmcnt(3)\n\t"
        "v_mfma_f32_16x16x32_bf16 v[48:51], v[96:99], v[232:235], v[48:51]\n\tds_read_b128 v[92:95], %[pb1] offset:27648\n\t"
        "v_mfma_f32_16x16x32_bf16 v[52:55], v[96:99], v[236:239], v[52:55]\n\tv_mfma_f32_16x16x32_bf16 v[56:59], v[100:103], v[232:235], v[56:59]\n\t"
        "ds_read_b128 v[96:99], %[pb1] offset:28672\n\tv_mfma_f32_16x16x32_bf16 v[60:63], v[100:103], v[236:239], v[60:63]\n\t"
        "buffer_load_dwordx4 v[204:207], off, %[rt], %[sa] offset:1024 sc1\n\ts_add_u32 %[sa], %[sa], 0x2000\n\t"
        "v_mfma_f32_16x16x32_bf16 v[64:67], v[104:107], v[232:235], v[64:67]\n\tds_read_b128 v[100:103], %[pb1] offset:29696\n\t"
        "v_mfma_f32_16x16x32_bf16 v[68:71], v[104:107], v[236:239], v[68:71]\n\tv_mfma_f32_16x16x32_bf16 v[72:75], v[108:111], v[232:235], v[72:75]\n\t"
        "ds_read_b128 v[104:107], %[pb1] offset:30720\n\tv_mfma_f32_16x16x32_bf16 v[76:79], v[108:111], v[236:239], v[76:79]\n\t"
        "s_waitcnt vmcnt(20) lgkmcnt(3)\n\tv_mfma_f32_16x16x32_bf16 v[16:19], v[80:83], v[240:243], v[16:19]\n\t"
        "ds_read_b128 v[108:111], %[pb1] offset:31744\n\tv_mfma_f32_16x16x32_bf16 v[20:23], v[80:83], v[244:247], v[20:23]\n\t"
        "buffer_load_dwordx4 v[208:211], off, %[rt], %[sa] offset:0 sc1\n\tv_mfma_f32_16x16x32_bf16 v[24:27], v[84:87], v[240:243], v[24:27]\n\ts_barrier\n\t"
        "ds_read_b128 v[80:83], %[pb1] offset:32768\n\tv_mfma_f32_16x16x32_bf16 v[28:31], v[84:87], v[244:247], v[28:31]\n\t"
        "v_mfma_f32_16x16x32_bf16 v[32:35], v[88:91], v[240:243], v[32:35]\n\tds_read_b128 v[84:87], %[pb1] offset:33792\n\t"
        "v_mfma_f32_16x16x32_bf16 v[36:39], v[88:91], v[244:247], v[36:39]\n\tv_mfma_f32_16x16x32_bf16 v[40:43], v[92:95], v[240:243], v[40:43]\n\t"
        "ds_read_b128 v[88:91], %[pb1] offset:34816\n\tv_mfma_f32_16x16x32_bf16 v[44:47], v[92:95], v[244:247], v[44:47]\n\t"
        "buffer_load_dwordx4 v[212:215], off, %[rt], %[sa] offset:1024 sc1\n\ts_add_u32 %[sa], %[sa], 0x2000\n\ts_waitcnt lgkmcnt(3)\n\t"
        "v_mfma_f32_16x16x32_bf16 v[48:51], v[96:99], v[240:243], v[48:51]\n\tds_read_b128 v[92:95], %[pb1] offset:35840\n\t"
        "v_mfma_f32_16x16x32_bf16 v[52:55], v[96:99], v[244:247], v[52:55]\n\tv_mfma_f32_16x16x32_bf16 v[56:59], v[100:103], v[240:243], v[56:59]\n\t"
        "ds_read_b128 v[96:99], %[pb1] offset:36864\n\tv_mfma_f32_16x16x32_bf16 v[60:63], v[100:103], v[244:247], v[60:63]\n\t"
        "v_mfma_f32_16x16x32_bf16 v[64:67], v[104:107], v[240:243], v[64:67]\n\tds_read_b128 v[100:103], %[pb1] offset:37888\n\t"
        "v_mfma_f32_16x16x32_bf16 v[68:71], v[104:107], v[244:247], v[68:71]\n\tbuffer_load_dwordx4 v[216:219], off, %[rt], %[sa] offset:0 sc1\n\t"
        "v_mfma_f32_16x16x32_bf16 v[72:75], v[108:111], v[240:243], v[72:75]\n\tds_read_b128 v[104:107], %[pb1] offset:38912\n\t"
        "v_mfma_f32_16x16x32_bf16 v[76:79], v[108:111], v[244:247], v[76:79]\n\ts_waitcnt vmcnt(21) lgkmcnt(3)\n\t"
        "v_mfma_f32_16x16x32_bf16 v[16:19], v[80:83], v[152:155], v[16:19]\n\tds_read_b128 v[108:111], %[pb1] offset:39936\n\t"
        "v_mfma_f32_16x16x32_bf16 v[20:23], v[80:83], v[156:159], v[20:23]\n\tv_mfma_f32_16x16x32_bf16 v[24:27], v[84:87], v[152:155], v[24:27]\n\t"
        "ds_read_b128 v[80:83], %[pb1] offset:40960\n\tv_mfma_f32_16x16x32_bf16 v[28:31], v[84:87], v[156:159], v[28:31]\n\t"
        "buffer_load_dwordx4 v[220:223], off, %[rt], %[sa] offset:1024 sc1\n\ts_add_u32 %[sa], %[sa], 0x2000\n\t"
        "v_mfma_f32_16x16x32_bf16 v[32:35], v[88:91], v[152:155], v[32:35]\n\tds_read_b128 v[84:87], %[pb1] offset:41984\n\t"
        "v_mfma_f32_16x16x32_bf16 v[36:39], v[88:91], v[156:159], v[36:39]\n\tv_mfma_f32_16x16x32_bf16 v[40:43], v[92:95], v[152:155], v[40:43]\n\t"
        "ds_read_b128 v[88:91], %[pb1] offset:43008\n\tv_mfma_f32_16x16x32_bf16 v[44:47], v[92:95], v[156:159], v[44:47]\n\ts_waitcnt lgkmcnt(3)\n\t"
        "v_mfma_f32_16x16x32_bf16 v[48:51], v[96:99], v[152:155], v[48:51]\n\tds_read_b128 v[92:95], %[pb1] offset:44032\n\t"
        "v_mfma_f32_16x16x32_bf16 v[52:55], v[96:99], v[156:159], v[52:55]\n\tbuffer_load_dwordx4 v[224:227], off, %[rt], %[sa] offset:0 sc1\n\t"
        "v_mfma_f32_16x16x32_bf16 v[56:59], v[100:103], v[152:155], v[56:59]\n\tds_read_b128 v[96:99], %[pb1] offset:45056\n\t"
        "v_mfma_f32_16x16x32_bf16 v[60:63], v[100:103], v[156:159], v[60:63]\n\tv_mfma_f32_16x16x32_bf16 v[64:67], v[104:107], v[152:155], v[64:67]\n\t"
        "ds_read_b128 v[100:103], %[pb1] offset:46080\n\tv_mfma_f32_16x16x32_bf16 v[68:71], v[104:107], v[156:159], v[68:71]\n\t"
        "v_mfma_f32_16x16x32_bf16 v[72:75], v[108:111], v[152:155], v[72:75]\n\tds_read_b128 v[104:107], %[pb1] offset:47104\n\t"
        "v_mfma_f32_16x16x32_bf16 v[76:79], v[108:111], v[156:159], v[76:79]\n\tbuffer_load_dwordx4 v[228:231], off, %[rt], %[sa] offset:1024 sc1\n\t"
        "s_add_u32 %[sa], %[sa], 0x2000\n\ts_waitcnt vmcnt(22) lgkmcnt(3)\n\tv_mfma_f32_16x16x32_bf16 v[16:19], v[80:83], v[160:163], v[16:19]\n\t"
        "ds_read_b128 v[108:111], %[pb1] offset:48128\n\tv_mfma_f32_16x16x32_bf16 v[20:23], v[80:83], v[164:167], v[20:23]\n\t"
        "v_mfma_f32_16x16x32_bf16 v[24:27], v[84:87], v[160:163], v[24:27]\n\tds_read_b128 v[80:83], %[pb1] offset:49152\n\t"
        "v_mfma_f32_16x16x32_bf16 v[28:31], v[84:87], v[164:167], v[28:31]\n\tv_mfma_f32_16x16x32_bf16 v[32:35], v[88:91], v[160:163], v[32:35]\n\t"
        "ds_read_b128 v[84:87], %[pb1] offset:50176\n\tv_mfma_f32_16x16x32_bf16 v[36:39], v[88:91], v[164:167], v[36:39]\n\t"
        "buffer_load_dwordx4 v[232:235], off, %[rt], %[sa] offset:0 sc1\n\tv_mfma_f32_16x16x32_bf16 v[40:43], v[92:95], v[160:163], v[40:43]\n\t"
        "ds_read_b128 v[88:91], %[pb1] offset:51200\n\tv_mfma_f32_16x16x32_bf16 v[44:47], v[92:95], v[164:167], v[44:47]\n\ts_waitcnt lgkmcnt(3)\n\t"
        "v_mfma_f32_16x16x32_bf16 v[48:51], v[96:99], v[160:163], v[48:51]\n\tds_read_b128 v[92:95], %[pb1] offset:52224\n\t"
        "v_mfma_f32_16x16x32_bf16 v[52:55], v[96:99], v[164:167], v[52:55]\n\tv_mfma_f32_16x16x32_bf16 v[56:59], v[100:103], v[160:163], v[56:59]\n\t"
        "ds_read_b128 v[96:99], %[pb1] offset:53248\n\tv_mfma_f32_16x16x32_bf16 v[60:63], v[100:103], v[164:167], v[60:63]\n\t"
        "buffer_load_dwordx4 v[236:239], off, %[rt], %[sa] offset:1024 sc1\n\ts_add_u32 %[sa], %[sa], 0x2000\n\t"
        "v_mfma_f32_16x16x32_bf16 v[64:67], v[104:107], v[160:163], v[64:67]\n\tds_read_b128 v[100:103], %[pb1] offset:54272\n\t"
        "v_mfma_f32_16x16x32_bf16 v[68:71], v[104:107], v[164:167], v[68:71]\n\tv_mfma_f32_16x16x32_bf16 v[72:75], v[108:111], v[160:163], v[72:75]\n\t"
        "ds_read_b128 v[104:107], %[pb1] offset:55296\n\tv_mfma_f32_16x16x32_bf16 v[76:79], v[108:111], v[164:167], v[76:79]\n\t"
        "s_waitcnt vmcnt(16) lgkmcnt(3)\n\tv_mfma_f32_16x16x32_bf16 v[16:19], v[80:83], v[168:171], v[16:19]\n\t"
        "ds_read_b128 v[108:111], %[pb1] offset:56320\n\tv_mfma_f32_16x16x32_bf16 v[20:23], v[80:83], v[172:175], v[20:23]\n\t"
        "buffer_load_dwordx4 v[240:243], off, %[rt], %[sa] offset:0 sc1\n\tv_mfma_f32_16x16x32_bf16 v[24:27], v[84:87], v[168:171], v[24:27]\n\t"
        "ds_read_b128 v[80:83], %[pb1] offset:57344\n\tv_mfma_f32_16x16x32_bf16 v[28:31], v[84:87], v[172:175], v[28:31]\n\t"
        "v_mfma_f32_16x16x32_bf16 v[32:35], v[88:91], v[168:171], v[32:35]\n\tds_read_b128 v[84:87], %[pb1] offset:58368\n\t"
        "v_mfma_f32_16x16x32_bf16 v[36:39], v[88:91], v[172:175], v[36:39]\n\tv_mfma_f32_16x16x32_bf16 v[40:43], v[92:95], v[168:171], v[40:43]\n\t"
        "ds_read_b128 v[88:91], %[pb1] offset:59392\n\tv_mfma_f32_16x16x32_bf16 v[44:47], v[92:95], v[172:175], v[44:47]\n\t"
        "buffer_load_dwordx4 v[244:247], off, %[rt], %[sa] offset:1024 sc1\n\ts_add_u32 %[sa], %[sa], 0x2000\n\ts_waitcnt lgkmcnt(3)\n\t"
        "v_mfma_f32_16x16x32_bf16 v[48:51], v[96:99], v[168:171], v[48:51]\n\tds_read_b128 v[92:95], %[pb1] offset:60416\n\t"
        "v_mfma_f32_16x16x32_bf16 v[52:55], v[96:99], v[172:175], v[52:55]\n\tv_mfma_f32_16x16x32_bf16 v[56:59], v[100:103], v[168:171], v[56:59]\n\t"
        "ds_read_b128 v[96:99], %[pb1] offset:61440\n\tv_mfma_f32_16x16x32_bf16 v[60:63], v[100:103], v[172:175], v[60:63]\n\t"
        "v_mfma_f32_16x16x32_bf16 v[64:67], v[104:107], v[168:171], v[64:67]\n\tds_read_b128 v[100:103], %[pb1] offset:62464\n\t"
        "v_mfma_f32_16x16x32_bf16 v[68:71], v[104:107], v[172:175], v[68:71]\n\tbuffer_load_dwordx4 v[152:155], off, %[rt], %[sa] offset:0 sc1\n\t"
        "v_mfma_f32_16x16x32_bf16 v[72:75], v[108:111], v[168:171], v[72:75]\n\tds_read_b128 v[104:107], %[pb1] offset:63488\n\t"
        "v_mfma_f32_16x16x32_bf16 v[76:79], v[108:111], v[172:175], v[76:79]\n\ts_waitcnt vmcnt(17) lgkmcnt(3)\n\t"
        "v_mfma_f32_16x16x32_bf16 v[16:19], v[80:83], v[176:179], v[16:19]\n\tds_read_b128 v[108:111], %[pb1] offset:64512\n\t"
        "v_mfma_f32_16x16x32_bf16 v[20:23], v[80:83], v[180:183], v[20:23]\n\tv_mfma_f32_16x16x32_bf16 v[24:27], v[84:87], v[176:179], v[24:27]\n\t"
        "ds_read_b128 v[80:83], %[pb2] offset:0\n\tv_mfma_f32_16x16x32_bf16 v[28:31], v[84:87], v[180:183], v[28:31]\n\t"
        "buffer_load_dwordx4 v[156:159], off, %[rt], %[sa] offset:1024 sc1\n\ts_add_u32 %[sa], %[sa], 0x2000\n\t"
        "v_mfma_f32_16x16x32_bf16 v[32:35], v[88:91], v[176:179], v[32:35]\n\tds_read_b128 v[84:87], %[pb2] offset:1024\n\t"
        "v_mfma_f32_16x16x32_bf16 v[36:39], v[88:91], v[180:183], v[36:39]\n\tv_mfma_f32_16x16x32_bf16 v[40:43], v[92:95], v[176:179], v[40:43]\n\t"
        "ds_read_b128 v[88:91], %[pb2] offset:2048\n\tv_mfma_f32_16x16x32_bf16 v[44:47], v[92:95], v[180:183], v[44:47]\n\ts_waitcnt lgkmcnt(3)\n\t"
        "v_mfma_f32_16x16x32_bf16 v[48:51], v[96:99], v[176:179], v[48:51]\n\tds_read_b128 v[92:95], %[pb2] offset:3072\n\t"
        "v_mfma_f32_16x16x32_bf16 v[52:55], v[96:99], v[180:183], v[52:55]\n\tbuffer_load_dwordx4 v[160:163], off, %[rt], %[sa] offset:0 sc1\n\t"
        "v_mfma_f32_16x16x32_bf16 v[56:59], v[100:103], v[176:179], v[56:59]\n\tds_read_b128 v[96:99], %[pb2] offset:4096\n\t"
        "v_mfma_f32_16x16x32_bf16 v[60:63], v[100:103], v[180:183], v[60:63]\n\tv_mfma_f32_16x16x32_bf16 v[64:67], v[104:107], v[176:179], v[64:67]\n\t"
        "ds_read_b128 v[100:103], %[pb2] offset:5120\n\tv_mfma_f32_16x16x32_bf16 v[68:71], v[104:107], v[180:183], v[68:71]\n\t"
        "v_mfma_f32_16x16x32_bf16 v[72:75], v[108:111], v[176:179], v[72:75]\n\tds_read_b128 v[104:107], %[pb2] offset:6144\n\t"
        "v_mfma_f32_16x16x32_bf16 v[76:79], v[108:111], v[180:183], v[76:79]\n\tbuffer_load_dwordx4 v[164:167], off, %[rt], %[sa] offset:1024 sc1\n\t"
        "s_add_u32 %[sa], %[sa], 0xfffde000\n\ts_waitcnt vmcnt(18) lgkmcnt(3)\n\tv_mfma_f32_16x16x32_bf16 v[16:19], v[80:83], v[184:187], v[16:19]\n\t"
        "ds_read_b128 v[108:111], %[pb2] offset:7168\n\tv_mfma_f32_16x16x32_bf16 v[20:23], v[80:83], v[188:191], v[20:23]\n\t"
        "v_mfma_f32_16x16x32_bf16 v[24:27], v[84:87], v[184:187], v[24:27]\n\tds_read_b128 v[80:83], %[pb2] offset:8192\n\t"
        "v_mfma_f32_16x16x32_bf16 v[28:31], v[84:87], v[188:191], v[28:31]\n\tv_mfma_f32_16x16x32_bf16 v[32:35], v[88:91], v[184:187], v[32:35]\n\t"
        "ds_read_b128 v[84:87], %[pb2] offset:9216\n\tv_mfma_f32_16x16x32_bf16 v[36:39], v[88:91], v[188:191], v[36:39]\n\t"
        "buffer_load_dwordx4 v[168:171], off, %[rt], %[sa] offset:0 sc1\n\tv_mfma_f32_16x16x32_bf16 v[40:43], v[92:95], v[184:187], v[40:43]\n\t"
        "ds_read_b128 v[88:91], %[pb2] offset:10240\n\tv_mfma_f32_16x16x32_bf16 v[44:47], v[92:95], v[188:191], v[44:47]\n\ts_waitcnt lgkmcnt(3)\n\t"
        "v_mfma_f32_16x16x32_bf16 v[48:51], v[96:99], v[184:187], v[48:51]\n\tds_read_b128 v[92:95], %[pb2] offset:11264\n\t"
        "v_mfma_f32_16x16x32_bf16 v[52:55], v[96:99], v[188:191], v[52:55]\n\tv_mfma_f32_16x16x32_bf16 v[56:59], v[100:103], v[184:187], v[56:59]\n\t"
        "ds_read_b128 v[96:99], %[pb2] offset:12288\n\tv_mfma_f32_16x16x32_bf16 v[60:63], v[100:103], v[188:191], v[60:63]\n\t"
        "buffer_load_dwordx4 v[172:175], off, %[rt], %[sa] offset:1024 sc1\n\ts_add_u32 %[sa], %[sa], 0x2000\n\t"
        "v_mfma_f32_16x16x32_bf16 v[64:67], v[104:107], v[184:187], v[64:67]\n\tds_read_b128 v[100:103], %[pb2] offset:13312\n\t"
        "v_mfma_f32_16x16x32_bf16 v[68:71], v[104:107], v[188:191], v[68:71]\n\tv_mfma_f32_16x16x32_bf16 v[72:75], v[108:111], v[184:187], v[72:75]\n\t"
        "ds_read_b128 v[104:107], %[pb2] offset:14336\n\tv_mfma_f32_16x16x32_bf16 v[76:79], v[108:111], v[188:191], v[76:79]\n\t"
        "s_waitcnt vmcnt(18) lgkmcnt(3)\n\tv_mfma_f32_16x16x32_bf16 v[16:19], v[80:83], v[192:195], v[16:19]\n\t"
        "ds_read_b128 v[108:111], %[pb2] offset:15360\n\tv_mfma_f32_16x16x32_bf16 v[20:23], v[80:83], v[196:199], v[20:23]\n\t"
        "buffer_load_dwordx4 v[176:179], off, %[rt], %[sa] offset:0 sc1\n\tv_mfma_f32_16x16x32_bf16 v[24:27], v[84:87], v[192:195], v[24:27]\n\t"
        "ds_read_b128 v[80:83], %[pb0] offset:0\n\tv_mfma_f32_16x16x32_bf16 v[28:31], v[84:87], v[196:199], v[28:31]\n\t"
        "v_mfma_f32_16x16x32_bf16 v[32:35], v[88:91], v[192:195], v[32:35]\n\tds_read_b128 v[84:87], %[pb0] offset:1024\n\t"
        "v_mfma_f32_16x16x32_bf16 v[36:39], v[88:91], v[196:199], v[36:39]\n\tv_mfma_f32_16x16x32_bf16 v[40:43], v[92:95], v[192:195], v[40:43]\n\t"
        "ds_read_b128 v[88:91], %[pb0] offset:2048\n\tv_mfma_f32_16x16x32_bf16 v[44:47], v[92:95], v[196:199], v[44:47]\n\t"
        "buffer_load_dwordx4 v[180:183], off, %[rt], %[sa] offset:1024 sc1\n\ts_add_u32 %[sa], %[sa], 0x2000\n\ts_waitcnt lgkmcnt(3)\n\t"
        "v_mfma_f32_16x16x32_bf16 v[48:51], v[96:99], v[192:195], v[48:51]\n\tds_read_b128 v[92:95], %[pb0] offset:3072\n\t"
        "v_mfma_f32_16x16x32_bf16 v[52:55], v[96:99], v[196:199], v[52:55]\n\tv_mfma_f32_16x16x32_bf16 v[56:59], v[100:103], v[192:195], v[56:59]\n\t"
        "ds_read_b128 v[96:99], %[pb0] offset:4096\n\tv_mfma_f32_16x16x32_bf16 v[60:63], v[100:103], v[196:199], v[60:63]\n\t"
        "v_mfma_f32_16x16x32_bf16 v[64:67], v[104:107], v[192:195], v[64:67]\n\tds_read_b128 v[100:103], %[pb0] offset:5120\n\t"
        "v_mfma_f32_16x16x32_bf16 v[68:71], v[104:107], v[196:199], v[68:71]\n\tbuffer_load_dwordx4 v[184:187], off, %[rt], %[sa] offset:0 sc1\n\t"
        "v_mfma_f32_16x16x32_bf16 v[72:75], v[108:111], v[192:195], v[72:75]\n\tds_read_b128 v[104:107], %[pb0] offset:6144\n\t"
        "v_mfma_f32_16x16x32_bf16 v[76:79], v[108:111], v[196:199], v[76:79]\n\ts_waitcnt vmcnt(19) lgkmcnt(3)\n\t"
        "v_mfma_f32_16x16x32_bf16 v[16:19], v[80:83], v[200:203], v[16:19]\n\tds_read_b128 v[108:111], %[pb0] offset:7168\n\t"
        "v_mfma_f32_16x16x32_bf16 v[20:23], v[80:83], v[204:207], v[20:23]\n\tv_mfma_f32_16x16x32_bf16 v[24:27], v[84:87], v[200:203], v[24:27]\n\t"
        "ds_read_b128 v[80:83], %[pb0] offset:8192\n\tv_mfma_f32_16x16x32_bf16 v[28:31], v[84:87], v[204:207], v[28:31]\n\t"
        "buffer_load_dwordx4 v[188:191], off, %[rt], %[sa] offset:1024 sc1\n\ts_add_u32 %[sa], %[sa], 0x2000\n\t"
        "v_mfma_f32_16x16x32_bf16 v[32:35], v[88:91], v[200:203], v[32:35]\n\tds_read_b128 v[84:87], %[pb0] offset:9216\n\t"
        "v_mfma_f32_16x16x32_bf16 v[36:39], v[88:91], v[204:207], v[36:39]\n\tv_mfma_f32_16x16x32_bf16 v[40:43], v[92:95], v[200:203], v[40:43]\n\t"
        "ds_read_b128 v[88:91], %[pb0] offset:10240\n\tv_mfma_f32_16x16x32_bf16 v[44:47], v[92:95], v[204:207], v[44:47]\n\ts_waitcnt lgkmcnt(3)\n\t"
        "v_mfma_f32_16x16x32_bf16 v[48:51], v[96:99], v[200:203], v[48:51]\n\tds_read_b128 v[92:95], %[pb0] offset:11264\n\t"
        "v_mfma_f32_16x16x32_bf16 v[52:55], v[96:99], v[204:207], v[52:55]\n\tbuffer_load_dwordx4 v[192:195], off, %[rt], %[sa] offset:0 sc1\n\t"
        "v_mfma_f32_16x16x32_bf16 v[56:59], v[100:103], v[200:203], v[56:59]\n\tds_read_b128 v[96:99], %[pb0] offset:12288\n\t"
        "v_mfma_f32_16x16x32_bf16 v[60:63], v[100:103], v[204:207], v[60:63]\n\tv_mfma_f32_16x16x32_bf16 v[64:67], v[104:107], v[200:203], v[64:67]\n\t"
        "ds_read_b128 v[100:103], %[pb0] offset:13312\n\tv_mfma_f32_16x16x32_bf16 v[68:71], v[104:107], v[204:207], v[68:71]\n\t"
        "v_mfma_f32_16x16x32_bf16 v[72:75], v[108:111], v[200:203], v[72:75]\n\tds_read_b128 v[104:107], %[pb0] offset:14336\n\t"
        "v_mfma_f32_16x16x32_bf16 v[76:79], v[108:111], v[204:207], v[76:79]\n\tbuffer_load_dwordx4 v[196:199], off, %[rt], %[sa] offset:1024 sc1\n\t"
        "s_add_u32 %[sa], %[sa], 0x2000\n\ts_waitcnt vmcnt(20) lgkmcnt(3)\n\tv_mfma_f32_16x16x32_bf16 v[16:19], v[80:83], v[208:211], v[16:19]\n\t"
        "ds_read_b128 v[108:111], %[pb0] offset:15360\n\tv_mfma_f32_16x16x32_bf16 v[20:23], v[80:83], v[212:215], v[20:23]\n\t"
        "v_mfma_f32_16x16x32_bf16 v[24:27], v[84:87], v[208:211], v[24:27]\n\ts_barrier\n\tds_read_b128 v[80:83], %[pb0] offset:16384\n\t"
        "v_mfma_f32_16x16x32_bf16 v[28:31], v[84:87], v[212:215], v[28:31]\n\tv_mfma_f32_16x16x32_bf16 v[32:35], v[88:91], v[208:211], v[32:35]\n\t"
        "ds_read_b128 v[84:87], %[pb0] offset:17408\n\tv_mfma_f32_16x16x32_bf16 v[36:39], v[88:91], v[212:215], v[36:39]\n\t"
        "buffer_load_dwordx4 v[200:203], off, %[rt], %[sa] offset:0 sc1\n\tv_mfma_f32_16x16x32_bf16 v[40:43], v[92:95], v[208:211], v[40:43]\n\t"
        "ds_read_b128 v[88:91], %[pb0] offset:18432\n\tv_mfma_f32_16x16x32_bf16 v[44:47], v[92:95], v[212:215], v[44:47]\n\ts_waitcnt lgkmcnt(3)\n\t"
        "v_mfma_f32_16x16x32_bf16 v[48:51], v[96:99], v[208:211], v[48:51]\n\tds_read_b128 v[92:95], %[pb0] offset:19456\n\t"
        "v_mfma_f32_16x16x32_bf16 v[52:55], v[96:99], v[212:215], v[52:55]\n\tv_mfma_f32_16x16x32_bf16 v[56:59], v[100:103], v[208:211], v[56:59]\n\t"
        "ds_read_b128 v[96:99], %[pb0] offset:20480\n\tv_mfma_f32_16x16x32_bf16 v[60:63], v[100:103], v[212:215], v[60:63]\n\t"
        "buffer_load_dwordx4 v[204:207], off, %[rt], %[sa] offset:1024 sc1\n\ts_add_u32 %[sa], %[sa], 0x2000\n\t"
        "v_mfma_f32_16x16x32_bf16 v[64:67], v[104:107], v[208:211], v[64:67]\n\tds_read_b128 v[100:103], %[pb0] offset:21504\n\t"
        "v_mfma_f32_16x16x32_bf16 v[68:71], v[104:107], v[212:215], v[68:71]\n\tv_mfma_f32_16x16x32_bf16 v[72:75], v[108:111], v[208:211], v[72:75]\n\t"
        "ds_read_b128 v[104:107], %[pb0] offset:22528\n\tv_mfma_f32_16x16x32_bf16 v[76:79], v[108:111], v[212:215], v[76:79]\n\t"
        "s_waitcnt vmcnt(20) lgkmcnt(3)\n\tv_mfma_f32_16x16x32_bf16 v[16:19], v[80:83], v[216:219], v[16:19]\n\t"
        "ds_read_b128 v[108:111], %[pb0] offset:23552\n\tv_mfma_f32_16x16x32_bf16 v[20:23], v[80:83], v[220:223], v[20:23]\n\t"
        "buffer_load_dwordx4 v[208:211], off, %[rt], %[sa] offset:0 sc1\n\tv_mfma_f32_16x16x32_bf16 v[24:27], v[84:87], v[216:219], v[24:27]\n\t"
        "ds_read_b128 v[80:83], %[pb0] offset:24576\n\tv_mfma_f32_16x16x32_bf16 v[28:31], v[84:87], v[220:223], v[28:31]\n\t"
        "v_mfma_f32_16x16x32_bf16 v[32:35], v[88:91], v[216:219], v[32:35]\n\tds_read_b128 v[84:87], %[pb0] offset:25600\n\t"
        "v_mfma_f32_16x16x32_bf16 v[36:39], v[88:91], v[220:223], v[36:39]\n\tv_mfma_f32_16x16x32_bf16 v[40:43], v[92:95], v[216:219], v[40:43]\n\t"
        "ds_read_b128 v[88:91], %[pb0] offset:26624\n\tv_mfma_f32_16x16x32_bf16 v[44:47], v[92:95], v[220:223], v[44:47]\n\t"
        "buffer_load_dwordx4 v[212:215], off, %[rt], %[sa] offset:1024 sc1\n\ts_add_u32 %[sa], %[sa], 0x2000\n\ts_waitcnt lgkmcnt(3)\n\t"
        "v_mfma_f32_16x16x32_bf16 v[48:51], v[96:99], v[216:219], v[48:51]\n\tds_read_b128 v[92:95], %[pb0] offset:27648\n\t"
        "v_mfma_f32_16x16x32_bf16 v[52:55], v[96:99], v[220:223], v[52:55]\n\tv_mfma_f32_16x16x32_bf16 v[56:59], v[100:103], v[216:219], v[56:59]\n\t"
        "ds_read_b128 v[96:99], %[pb0] offset:28672\n\tv_mfma_f32_16x16x32_bf16 v[60:63], v[100:103], v[220:223], v[60:63]\n\t"
        "v_mfma_f32_16x16x32_bf16 v[64:67], v[104:107], v[216:219], v[64:67]\n\tds_read_b128 v[100:103], %[pb0] offset:29696\n\t"
        "v_mfma_f32_16x16x32_bf16 v[68:71], v[104:107], v[220:223], v[68:71]\n\tv_mfma_f32_16x16x32_bf16 v[72:75], v[108:111], v[216:219], v[72:75]\n\t"
        "ds_read_b128 v[104:107], %[pb0] offset:30720\n\tv_mfma_f32_16x16x32_bf16 v[76:79], v[108:111], v[220:223], v[76:79]\n\t"
        "buffer_load_dwordx4 v[216:219], off, %[rt], %[sa] offset:0 sc1\n\ts_waitcnt vmcnt(21) lgkmcnt(3)\n\t"
        "v_mfma_f32_16x16x32_bf16 v[16:19], v[80:83], v[224:227], v[16:19]\n\tds_read_b128 v[108:111], %[pb0] offset:31744\n\t"
        "v_mfma_f32_16x16x32_bf16 v[20:23], v[80:83], v[228:231], v[20:23]\n\tv_mfma_f32_16x16x32_bf16 v[24:27], v[84:87], v[224:227], v[24:27]\n\t"
        "ds_read_b128 v[80:83], %[pb0] offset:32768\n\tv_mfma_f32_16x16x32_bf16 v[28:31], v[84:87], v[228:231], v[28:31]\n\t"
        "v_mfma_f32_16x16x32_bf16 v[32:35], v[88:91], v[224:227], v[32:35]\n\tds_read_b128 v[84:87], %[pb0] offset:33792\n\t"
        "v_mfma_f32_16x16x32_bf16 v[36:39], v[88:91], v[228:231], v[36:39]\n\tbuffer_load_dwordx4 v[220:223], off, %[rt], %[sa] offset:1024 sc1\n\t"
        "s_add_u32 %[sa], %[sa], 0x2000\n\tv_mfma_f32_16x16x32_bf16 v[40:43], v[92:95], v[224:227], v[40:43]\n\tds_read_b128 v[88:91], %[pb0] offset:34816\n\t"
        "v_mfma_f32_16x16x32_bf16 v[44:47], v[92:95], v[228:231], v[44:47]\n\ts_waitcnt lgkmcnt(3)\n\t"
        "v_mfma_f32_16x16x32_bf16 v[48:51], v[96:99], v[224:227], v[48:51]\n\tds_read_b128 v[92:95], %[pb0] offset:35840\n\t"
        "v_mfma_f32_16x16x32_bf16 v[52:55], v[96:99], v[228:231], v[52:55]\n\tv_mfma_f32_16x16x32_bf16 v[56:59], v[100:103], v[224:227], v[56:59]\n\t"
        "ds_read_b128 v[96:99], %[pb0] offset:36864\n\tv_mfma_f32_16x16x32_bf16 v[60:63], v[100:103], v[228:231], v[60:63]\n\t"
        "v_mfma_f32_16x16x32_bf16 v[64:67], v[104:107], v[224:227], v[64:67]\n\tds_read_b128 v[100:103], %[pb0] offset:37888\n\t"
        "v_mfma_f32_16x16x32_bf16 v[68:71], v[104:107], v[228:231], v[68:71]\n\tv_mfma_f32_16x16x32_bf16 v[72:75], v[108:111], v[224:227], v[72:75]\n\t"
        "ds_read_b128 v[104:107], %[pb0] offset:38912\n\tv_mfma_f32_16x16x32_bf16 v[76:79], v[108:111], v[228:231], v[76:79]\n\t"
        "buffer_load_dwordx4 v[224:227], off, %[rt], %[sa] offset:0 sc1\n\ts_waitcnt vmcnt(21) lgkmcnt(3)\n\t"
        "v_mfma_f32_16x16x32_bf16 v[16:19], v[80:83], v[232:235], v[16:19]\n\tds_read_b128 v[108:111], %[pb0] offset:39936\n\t"
        "v_mfma_f32_16x16x32_bf16 v[20:23], v[80:83], v[236:239], v[20:23]\n\tv_mfma_f32_16x16x32_bf16 v[24:27], v[84:87], v[232:235], v[24:27]\n\t"
        "ds_read_b128 v[80:83], %[pb0] offset:40960\n\tv_mfma_f32_16x16x32_bf16 v[28:31], v[84:87], v[236:239], v[28:31]\n\t"
        "v_mfma_f32_16x16x32_bf16 v[32:35], v[88:91], v[232:235], v[32:35]\n\tds_read_b128 v[84:87], %[pb0] offset:41984\n\t"
        "v_mfma_f32_16x16x32_bf16 v[36:39], v[88:91], v[236:239], v[36:39]\n\tbuffer_load_dwordx4 v[228:231], off, %[rt], %[sa] offset:1024 sc1\n\t"
        "s_add_u32 %[sa], %[sa], 0x2000\n\tv_mfma_f32_16x16x32_bf16 v[40:43], v[92:95], v[232:235], v[40:43]\n\tds_read_b128 v[88:91], %[pb0] offset:43008\n\t"
        "v_mfma_f32_16x16x32_bf16 v[44:47], v[92:95], v[236:239], v[44:47]\n\ts_waitcnt lgkmcnt(3)\n\t"
        "v_mfma_f32_16x16x32_bf16 v[48:51], v[96:99], v[232:235], v[48:51]\n\tds_read_b128 v[92:95], %[pb0] offset:44032\n\t"
        "v_mfma_f32_16x16x32_bf16 v[52:55], v[96:99], v[236:239], v[52:55]\n\tv_mfma_f32_16x16x32_bf16 v[56:59], v[100:103], v[232:235], v[56:59]\n\t"
        "ds_read_b128 v[96:99], %[pb0] offset:45056\n\tv_mfma_f32_16x16x32_bf16 v[60:63], v[100:103], v[236:239], v[60:63]\n\t"
        "v_mfma_f32_16x16x32_bf16 v[64:67], v[104:107], v[232:235], v[64:67]\n\tds_read_b128 v[100:103], %[pb0] offset:46080\n\t"
        "v_mfma_f32_16x16x32_bf16 v[68:71], v[104:107], v[236:239], v[68:71]\n\tv_mfma_f32_16x16x32_bf16 v[72:75], v[108:111], v[232:235], v[72:75]\n\t"
        "ds_read_b128 v[104:107], %[pb0] offset:47104\n\tv_mfma_f32_16x16x32_bf16 v[76:79], v[108:111], v[236:239], v[76:79]\n\t"
        "buffer_load_dwordx4 v[232:235], off, %[rt], %[sa] offset:0 sc1\n\ts_waitcnt vmcnt(21) lgkmcnt(3)\n\t"
        "v_mfma_f32_16x16x32_bf16 v[16:19], v[80:83], v[240:243], v[16:19]\n\tds_read_b128 v[108:111], %[pb0] offset:48128\n\t"
        "v_mfma_f32_16x16x32_bf16 v[20:23], v[80:83], v[244:247], v[20:23]\n\tv_mfma_f32_16x16x32_bf16 v[24:27], v[84:87], v[240:243], v[24:27]\n\t"
        "ds_read_b128 v[80:83], %[pb0] offset:49152\n\tv_mfma_f32_16x16x32_bf16 v[28:31], v[84:87], v[244:247], v[28:31]\n\t"
        "v_mfma_f32_16x16x32_bf16 v[32:35], v[88:91], v[240:243], v[32:35]\n\tds_read_b128 v[84:87], %[pb0] offset:50176\n\t"
        "v_mfma_f32_16x16x32_bf16 v[36:39], v[88:91], v[244:247], v[36:39]\n\tbuffer_load_dwordx4 v[236:239], off, %[rt], %[sa] offset:1024 sc1\n\t"
        "s_add_u32 %[sa], %[sa], 0x2000\n\tv_mfma_f32_16x16x32_bf16 v[40:43], v[92:95], v[240:243], v[40:43]\n\tds_read_b128 v[88:91], %[pb0] offset:51200\n\t"
        "v_mfma_f32_16x16x32_bf16 v[44:47], v[92:95], v[244:247], v[44:47]\n\ts_waitcnt lgkmcnt(3)\n\t"
        "v_mfma_f32_16x16x32_bf16 v[48:51], v[96:99], v[240:243], v[48:51]\n\tds_read_b128 v[92:95], %[pb0] offset:52224\n\t"
        "v_mfma_f32_16x16x32_bf16 v[52:55], v[96:99], v[244:247], v[52:55]\n\tv_mfma_f32_16x16x32_bf16 v[56:59], v[100:103], v[240:243], v[56:59]\n\t"
        "ds_read_b128 v[96:99], %[pb0] offset:53248\n\tv_mfma_f32_16x16x32_bf16 v[60:63], v[100:103], v[244:247], v[60:63]\n\t"
        "v_mfma_f32_16x16x32_bf16 v[64:67], v[104:107], v[240:243], v[64:67]\n\tds_read_b128 v[100:103], %[pb0] offset:54272\n\t"
        "v_mfma_f32_16x16x32_bf16 v[68:71], v[104:107], v[244:247], v[68:71]\n\tv_mfma_f32_16x16x32_bf16 v[72:75], v[108:111], v[240:243], v[72:75]\n\t"
        "ds_read_b128 v[104:107], %[pb0] offset:55296\n\tv_mfma_f32_16x16x32_bf16 v[76:79], v[108:111], v[244:247], v[76:79]\n\t"
        "buffer_load_dwordx4 v[240:243], off, %[rt], %[sa] offset:0 sc1\n\ts_waitcnt vmcnt(21) lgkmcnt(3)\n\t"
        "v_mfma_f32_16x16x32_bf16 v[16:19], v[80:83], v[152:155], v[16:19]\n\tds_read_b128 v[108:111], %[pb0] offset:56320\n\t"
        "v_mfma_f32_16x16x32_bf16 v[20:23], v[80:83], v[156:159], v[20:23]\n\tv_mfma_f32_16x16x32_bf16 v[24:27], v[84:87], v[152:155], v[24:27]\n\t"
        "ds_read_b128 v[80:83], %[pb0] offset:57344\n\tv_mfma_f32_16x16x32_bf16 v[28:31], v[84:87], v[156:159], v[28:31]\n\t"
        "v_mfma_f32_16x16x32_bf16 v[32:35], v[88:91], v[152:155], v[32:35]\n\tds_read_b128 v[84:87], %[pb0] offset:58368\n\t"
        "v_mfma_f32_16x16x32_bf16 v[36:39], v[88:91], v[156:159], v[36:39]\n\tbuffer_load_dwordx4 v[244:247], off, %[rt], %[sa] offset:1024 sc1\n\t"
        "v_mfma_f32_16x16x32_bf16 v[40:43], v[92:95], v[152:155], v[40:43]\n\tds_read_b128 v[88:91], %[pb0] offset:59392\n\t"
        "v_mfma_f32_16x16x32_bf16 v[44:47], v[92:95], v[156:159], v[44:47]\n\ts_waitcnt lgkmcnt(3)\n\t"
        "v_mfma_f32_16x16x32_bf16 v[48:51], v[96:99], v[152:155], v[48:51]\n\tds_read_b128 v[92:95], %[pb0] offset:60416\n\t"
        "v_mfma_f32_16x16x32_bf16 v[52:55], v[96:99], v[156:159], v[52:55]\n\tv_mfma_f32_16x16x32_bf16 v[56:59], v[100:103], v[152:155], v[56:59]\n\t"
        "ds_read_b128 v[96:99], %[pb0] offset:61440\n\tv_mfma_f32_16x16x32_bf16 v[60:63], v[100:103], v[156:159], v[60:63]\n\t"
        "v_mfma_f32_16x16x32_bf16 v[64:67], v[104:107], v[152:155], v[64:67]\n\tds_read_b128 v[100:103], %[pb0] offset:62464\n\t"
        "v_mfma_f32_16x16x32_bf16 v[68:71], v[104:107], v[156:159], v[68:71]\n\tv_mfma_f32_16x16x32_bf16 v[72:75], v[108:111], v[152:155], v[72:75]\n\t"
        "ds_read_b128 v[104:107], %[pb0] offset:63488\n\tv_mfma_f32_16x16x32_bf16 v[76:79], v[108:111], v[156:159], v[76:79]\n\t"
        "s_waitcnt vmcnt(20) lgkmcnt(3)\n\tv_mfma_f32_16x16x32_bf16 v[16:19], v[80:83], v[160:163], v[16:19]\n\t"
        "ds_read_b128 v[108:111], %[pb0] offset:64512\n\tv_mfma_f32_16x16x32_bf16 v[20:23], v[80:83], v[164:167], v[20:23]\n\t.p2align 12,,12\n\t.Lpg16:\n\t"
        "s_cbranch_execz .Lpgret\n\tv_mfma_f32_16x16x32_bf16 v[24:27], v[84:87], v[160:163], v[24:27]\n\tds_read_b128 v[80:83], %[pb1] offset:0\n\t"
        "v_mfma_f32_16x16x32_bf16 v[28:31], v[84:87], v[164:167], v[28:31]\n\tv_mfma_f32_16x16x32_bf16 v[32:35], v[88:91], v[160:163], v[32:35]\n\t"
        "ds_read_b128 v[84:87], %[pb1] offset:1024\n\tv_mfma_f32_16x16x32_bf16 v[36:39], v[88:91], v[164:167], v[36:39]\n\t"
        "v_mfma_f32_16x16x32_bf16 v[40:43], v[92:95], v[160:163], v[40:43]\n\tds_read_b128 v[88:91], %[pb1] offset:2048\n\t"
        "v_mfma_f32_16x16x32_bf16 v[44:47], v[92:95], v[164:167], v[44:47]\n\ts_waitcnt lgkmcnt(3)\n\t"
        "v_mfma_f32_16x16x32_bf16 v[48:51], v[96:99], v[160:163], v[48:51]\n\tds_read_b128 v[92:95], %[pb1] offset:3072\n\t"
        "v_mfma_f32_16x16x32_bf16 v[52:55], v[96:99], v[164:167], v[52:55]\n\tv_mfma_f32_16x16x32_bf16 v[56:59], v[100:103], v[160:163], v[56:59]\n\t"
        "ds_read_b128 v[96:99], %[pb1] offset:4096\n\tv_mfma_f32_16x16x32_bf16 v[60:63], v[100:103], v[164:167], v[60:63]\n\t"
        "v_mfma_f32_16x16x32_bf16 v[64:67], v[104:107], v[160:163], v[64:67]\n\tds_read_b128 v[100:103], %[pb1] offset:5120\n\t"
        "v_mfma_f32_16x16x32_bf16 v[68:71], v[104:107], v[164:167], v[68:71]\n\tv_mfma_f32_16x16x32_bf16 v[72:75], v[108:111], v[160:163], v[72:75]\n\t"
        "ds_read_b128 v[104:107], %[pb1] offset:6144\n\tv_mfma_f32_16x16x32_bf16 v[76:79], v[108:111], v[164:167], v[76:79]\n\t"
        "s_waitcnt vmcnt(18) lgkmcnt(3)\n\tv_mfma_f32_16x16x32_bf16 v[16:19], v[80:83], v[168:171], v[16:19]\n\tds_read_b128 v[108:111], %[pb1] offset:7168\n\t"
        "v_mfma_f32_16x16x32_bf16 v[20:23], v[80:83], v[172:175], v[20:23]\n\tv_mfma_f32_16x16x32_bf16 v[24:27], v[84:87], v[168:171], v[24:27]\n\t"
        "ds_read_b128 v[80:83], %[pb1] offset:8192\n\tv_mfma_f32_16x16x32_bf16 v[28:31], v[84:87], v[172:175], v[28:31]\n\t"
        "v_mfma_f32_16x16x32_bf16 v[32:35], v[88:91], v[168:171], v[32:35]\n\tds_read_b128 v[84:87], %[pb1] offset:9216\n\t"
        "v_mfma_f32_16x16x32_bf16 v[36:39], v[88:91], v[172:175], v[36:39]\n\tv_mfma_f32_16x16x32_bf16 v[40:43], v[92:95], v[168:171], v[40:43]\n\t"
        "ds_read_b128 v[88:91], %[pb1] offset:10240\n\tv_mfma_f32_16x16x32_bf16 v[44:47], v[92:95], v[172:175], v[44:47]\n\ts_waitcnt lgkmcnt(3)\n\t"
        "v_mfma_f32_16x16x32_bf16 v[48:51], v[96:99], v[168:171], v[48:51]\n\tds_read_b128 v[92:95], %[pb1] offset:11264\n\t"
        "v_mfma_f32_16x16x32_bf16 v[52:55], v[96:99], v[172:175], v[52:55]\n\tv_mfma_f32_16x16x32_bf16 v[56:59], v[100:103], v[168:171], v[56:59]\n\t"
        "ds_read_b128 v[96:99], %[pb1] offset:12288\n\tv_mfma_f32_16x16x32_bf16 v[60:63], v[100:103], v[172:175], v[60:63]\n\t"
        "v_mfma_f32_16x16x32_bf16 v[64:67], v[104:107], v[168:171], v[64:67]\n\tds_read_b128 v[100:103], %[pb1] offset:13312\n\t"
        "v_mfma_f32_16x16x32_bf16 v[68:71], v[104:107], v[172:175], v[68:71]\n\tv_mfma_f32_16x16x32_bf16 v[72:75], v[108:111], v[168:171], v[72:75]\n\t"
        "ds_read_b128 v[104:107], %[pb1] offset:14336\n\tv_mfma_f32_16x16x32_bf16 v[76:79], v[108:111], v[172:175], v[76:79]\n\t"
        "s_waitcnt vmcnt(16) lgkmcnt(3)\n\tv_mfma_f32_16x16x32_bf16 v[16:19], v[80:83], v[176:179], v[16:19]\n\t"
        "ds_read_b128 v[108:111], %[pb1] offset:15360\n\tv_mfma_f32_16x16x32_bf16 v[20:23], v[80:83], v[180:183], v[20:23]\n\t"
        "v_mfma_f32_16x16x32_bf16 v[24:27], v[84:87], v[176:179], v[24:27]\n\ts_barrier\n\tds_read_b128 v[80:83], %[pb1] offset:16384\n\t"
        "v_mfma_f32_16x16x32_bf16 v[28:31], v[84:87], v[180:183], v[28:31]\n\tv_mfma_f32_16x16x32_bf16 v[32:35], v[88:91], v[176:179], v[32:35]\n\t"
        "ds_read_b128 v[84:87], %[pb1] offset:17408\n\tv_mfma_f32_16x16x32_bf16 v[36:39], v[88:91], v[180:183], v[36:39]\n\t"
        "v_mfma_f32_16x16x32_bf16 v[40:43], v[92:95], v[176:179], v[40:43]\n\tds_read_b128 v[88:91], %[pb1] offset:18432\n\t"
        "v_mfma_f32_16x16x32_bf16 v[44:47], v[92:95], v[180:183], v[44:47]\n\ts_waitcnt lgkmcnt(3)\n\t"
        "v_mfma_f32_16x16x32_bf16 v[48:51], v[96:99], v[176:179], v[48:51]\n\tds_read_b128 v[92:95], %[pb1] offset:19456\n\t"
        "v_mfma_f32_16x16x32_bf16 v[52:55], v[96:99], v[180:183], v[52:55]\n\tv_mfma_f32_16x16x32_bf16 v[56:59], v[100:103], v[176:179], v[56:59]\n\t"
        "ds_read_b128 v[96:99], %[pb1] offset:20480\n\tv_mfma_f32_16x16x32_bf16 v[60:63], v[100:103], v[180:183], v[60:63]\n\t"
        "v_mfma_f32_16x16x32_bf16 v[64:67], v[104:107], v[176:179], v[64:67]\n\tds_read_b128 v[100:103], %[pb1] offset:21504\n\t"
        "v_mfma_f32_16x16x32_bf16 v[68:71], v[104:107], v[180:183], v[68:71]\n\tv_mfma_f32_16x16x32_bf16 v[72:75], v[108:111], v[176:179], v[72:75]\n\t"
        "ds_read_b128 v[104:107], %[pb1] offset:22528\n\tv_mfma_f32_16x16x32_bf16 v[76:79], v[108:111], v[180:183], v[76:79]\n\t"
        "s_waitcnt vmcnt(14) lgkmcnt(3)\n\tv_mfma_f32_16x16x32_bf16 v[16:19], v[80:83], v[184:187], v[16:19]\n\t"
        "ds_read_b128 v[108:111], %[pb1] offset:23552\n\tv_mfma_f32_16x16x32_bf16 v[20:23], v[80:83], v[188:191], v[20:23]\n\t"
        "v_mfma_f32_16x16x32_bf16 v[24:27], v[84:87], v[184:187], v[24:27]\n\tds_read_b128 v[80:83], %[pb1] offset:24576\n\t"
        "v_mfma_f32_16x16x32_bf16 v[28:31], v[84:87], v[188:191], v[28:31]\n\tv_mfma_f32_16x16x32_bf16 v[32:35], v[88:91], v[184:187], v[32:35]\n\t"
        "ds_read_b128 v[84:87], %[pb1] offset:25600\n\tv_mfma_f32_16x16x32_bf16 v[36:39], v[88:91], v[188:191], v[36:39]\n\t"
        "v_mfma_f32_16x16x32_bf16 v[40:43], v[92:95], v[184:187], v[40:43]\n\tds_read_b128 v[88:91], %[pb1] offset:26624\n\t"
        "v_mfma_f32_16x16x32_bf16 v[44:47], v[92:95], v[188:191], v[44:47]\n\ts_waitcnt lgkmcnt(3)\n\t"
        "v_mfma_f32_16x16x32_bf16 v[48:51], v[96:99], v[184:187], v[48:51]\n\tds_read_b128 v[92:95], %[pb1] offset:27648\n\t"
        "v_mfma_f32_16x16x32_bf16 v[52:55], v[96:99], v[188:191], v[52:55]\n\tv_mfma_f32_16x16x32_bf16 v[56:59], v[100:103], v[184:187], v[56:59]\n\t"
        "ds_read_b128 v[96:99], %[pb1] offset:28672\n\tv_mfma_f32_16x16x32_bf16 v[60:63], v[100:103], v[188:191], v[60:63]\n\t"
        "v_mfma_f32_16x16x32_bf16 v[64:67], v[104:107], v[184:187], v[64:67]\n\tds_read_b128 v[100:103], %[pb1] offset:29696\n\t"
        "v_mfma_f32_16x16x32_bf16 v[68:71], v[104:107], v[188:191], v[68:71]\n\tv_mfma_f32_16x16x32_bf16 v[72:75], v[108:111], v[184:187], v[72:75]\n\t"
        "ds_read_b128 v[104:107], %[pb1] offset:30720\n\tv_mfma_f32_16x16x32_bf16 v[76:79], v[108:111], v[188:191], v[76:79]\n\t"
        "s_waitcnt vmcnt(12) lgkmcnt(3)\n\tv_mfma_f32_16x16x32_bf16 v[16:19], v[80:83], v[192:195], v[16:19]\n\t"
        "ds_read_b128 v[108:111], %[pb1] offset:31744\n\tv_mfma_f32_16x16x32_bf16 v[20:23], v[80:83], v[196:199], v[20:23]\n\t"
        "v_mfma_f32_16x16x32_bf16 v[24:27], v[84:87], v[192:195], v[24:27]\n\ts_barrier\n\tds_read_b128 v[80:83], %[pb1] offset:32768\n\t"
        "v_mfma_f32_16x16x32_bf16 v[28:31], v[84:87], v[196:199], v[28:31]\n\tv_mfma_f32_16x16x32_bf16 v[32:35], v[88:91], v[192:195], v[32:35]\n\t"
        "ds_read_b128 v[84:87], %[pb1] offset:40960\n\tv_mfma_f32_16x16x32_bf16 v[36:39], v[88:91], v[196:199], v[36:39]\n\t"
        "v_mfma_f32_16x16x32_bf16 v[40:43], v[92:95], v[192:195], v[40:43]\n\tds_read_b128 v[88:91], %[pb1] offset:49152\n\t"
        "v_mfma_f32_16x16x32_bf16 v[44:47], v[92:95], v[196:199], v[44:47]\n\ts_waitcnt lgkmcnt(3)\n\t"
        "v_mfma_f32_16x16x32_bf16 v[48:51], v[96:99], v[192:195], v[48:51]\n\tds_read_b128 v[92:95], %[pb1] offset:57344\n\t"
        "v_mfma_f32_16x16x32_bf16 v[52:55], v[96:99], v[196:199], v[52:55]\n\tv_mfma_f32_16x16x32_bf16 v[56:59], v[100:103], v[192:195], v[56:59]\n\t"
        "ds_read_b128 v[96:99], %[pb2] offset:0\n\tv_mfma_f32_16x16x32_bf16 v[60:63], v[100:103], v[196:199], v[60:63]\n\t"
        "v_mfma_f32_16x16x32_bf16 v[64:67], v[104:107], v[192:195], v[64:67]\n\tds_read_b128 v[100:103], %[pb2] offset:8192\n\t"
        "v_mfma_f32_16x16x32_bf16 v[68:71], v[104:107], v[196:199], v[68:71]\n\tv_mfma_f32_16x16x32_bf16 v[72:75], v[108:111], v[192:195], v[72:75]\n\t"
        "ds_read_b128 v[104:107], %[pb1] offset:33792\n\tv_mfma_f32_16x16x32_bf16 v[76:79], v[108:111], v[196:199], v[76:79]\n\t"
        "s_waitcnt vmcnt(10) lgkmcnt(3)\n\tv_mfma_f32_16x16x32_bf16 v[16:19], v[80:83], v[200:203], v[16:19]\n\t"
        "ds_read_b128 v[108:111], %[pb1] offset:41984\n\tv_mfma_f32_16x16x32_bf16 v[20:23], v[80:83], v[204:207], v[20:23]\n\ts_waitcnt vmcnt(8)\n\t"
        "v_mfma_f32_16x16x32_bf16 v[16:19], v[84:87], v[208:211], v[16:19]\n\tds_read_b128 v[80:83], %[pb1] offset:50176\n\t"
        "v_mfma_f32_16x16x32_bf16 v[20:23], v[84:87], v[212:215], v[20:23]\n\ts_waitcnt vmcnt(6)\n\t"
        "v_mfma_f32_16x16x32_bf16 v[16:19], v[88:91], v[216:219], v[16:19]\n\tds_read_b128 v[84:87], %[pb1] offset:58368\n\t"
        "v_mfma_f32_16x16x32_bf16 v[20:23], v[88:91], v[220:223], v[20:23]\n\ts_waitcnt vmcnt(4)\n\t"
        "v_mfma_f32_16x16x32_bf16 v[16:19], v[92:95], v[224:227], v[16:19]\n\tds_read_b128 v[88:91], %[pb2] offset:1024\n\t"
        "v_mfma_f32_16x16x32_bf16 v[20:23], v[92:95], v[228:231], v[20:23]\n\ts_waitcnt vmcnt(2) lgkmcnt(3)\n\t"
        "v_mfma_f32_16x16x32_bf16 v[16:19], v[96:99], v[232:235], v[16:19]\n\tds_read_b128 v[92:95], %[pb2] offset:9216\n\t"
        "v_mfma_f32_16x16x32_bf16 v[20:23], v[96:99], v[236:239], v[20:23]\n\ts_waitcnt vmcnt(0)\n\t"
        "v_mfma_f32_16x16x32_bf16 v[16:19], v[100:103], v[240:243], v[16:19]\n\tds_read_b128 v[96:99], %[pb1] offset:34816\n\t"
        "v_mfma_f32_16x16x32_bf16 v[20:23], v[100:103], v[244:247], v[20:23]\n\tv_mfma_f32_16x16x32_bf16 v[24:27], v[104:107], v[200:203], v[24:27]\n\t"
        "ds_read_b128 v[100:103], %[pb1] offset:43008\n\tv_mfma_f32_16x16x32_bf16 v[28:31], v[104:107], v[204:207], v[28:31]\n\t"
        "v_mfma_f32_16x16x32_bf16 v[24:27], v[108:111], v[208:211], v[24:27]\n\tds_read_b128 v[104:107], %[pb1] offset:51200\n\t"
        "v_mfma_f32_16x16x32_bf16 v[28:31], v[108:111], v[212:215], v[28:31]\n\tv_cvt_pk_bf16_f32 v112, v16, v17\n\tv_cvt_pk_bf16_f32 v113, v18, v19\n\t"
        "s_waitcnt lgkmcnt(3)\n\tv_mfma_f32_16x16x32_bf16 v[24:27], v[80:83], v[216:219], v[24:27]\n\tds_read_b128 v[108:111], %[pb1] offset:59392\n\t"
        "v_mfma_f32_16x16x32_bf16 v[28:31], v[80:83], v[220:223], v[28:31]\n\tv_cvt_pk_bf16_f32 v116, v20, v21\n\tv_cvt_pk_bf16_f32 v117, v22, v23\n\t"
        "v_mfma_f32_16x16x32_bf16 v[24:27], v[84:87], v[224:227], v[24:27]\n\tds_read_b128 v[80:83], %[pb2] offset:2048\n\t"
        "v_mfma_f32_16x16x32_bf16 v[28:31], v[84:87], v[228:231], v[28:31]\n\tv_mfma_f32_16x16x32_bf16 v[24:27], v[88:91], v[232:235], v[24:27]\n\t"
        "ds_read_b128 v[84:87], %[pb2] offset:10240\n\tv_mfma_f32_16x16x32_bf16 v[28:31], v[88:91], v[236:239], v[28:31]\n\t"
        "v_mfma_f32_16x16x32_bf16 v[24:27], v[92:95], v[240:243], v[24:27]\n\tds_read_b128 v[88:91], %[pb1] offset:35840\n\t"
        "v_mfma_f32_16x16x32_bf16 v[28:31], v[92:95], v[244:247], v[28:31]\n\ts_waitcnt lgkmcnt(3)\n\t"
        "v_mfma_f32_16x16x32_bf16 v[32:35], v[96:99], v[200:203], v[32:35]\n\tds_read_b128 v[92:95], %[pb1] offset:44032\n\t"
        "v_mfma_f32_16x16x32_bf16 v[36:39], v[96:99], v[204:207], v[36:39]\n\tv_mfma_f32_16x16x32_bf16 v[32:35], v[100:103], v[208:211], v[32:35]\n\t"
        "ds_read_b128 v[96:99], %[pb1] offset:52224\n\tv_mfma_f32_16x16x32_bf16 v[36:39], v[100:103], v[212:215], v[36:39]\n\t"
        "v_cvt_pk_bf16_f32 v114, v24, v25\n\tv_cvt_pk_bf16_f32 v115, v26, v27\n\tv_mfma_f32_16x16x32_bf16 v[32:35], v[104:107], v[216:219], v[32:35]\n\t"
        "ds_read_b128 v[100:103], %[pb1] offset:60416\n\tv_mfma_f32_16x16x32_bf16 v[36:39], v[104:107], v[220:223], v[36:39]\n\t"
        "buffer_store_dwordx4 v[112:115], off, %[rt], %[sx0] offset:0 sc1\n\tv_cvt_pk_bf16_f32 v118, v28, v29\n\tv_cvt_pk_bf16_f32 v119, v30, v31\n\t"
        "v_mfma_f32_16x16x32_bf16 v[32:35], v[108:111], v[224:227], v[32:35]\n\tds_read_b128 v[104:107], %[pb2] offset:3072\n\t"
        "v_mfma_f32_16x16x32_bf16 v[36:39], v[108:111], v[228:231], v[36:39]\n\ts_waitcnt lgkmcnt(3)\n\t"
        "v_mfma_f32_16x16x32_bf16 v[32:35], v[80:83], v[232:235], v[32:35]\n\tds_read_b128 v[108:111], %[pb2] offset:11264\n\t"
        "v_mfma_f32_16x16x32_bf16 v[36:39], v[80:83], v[236:239], v[36:39]\n\tv_mfma_f32_16x16x32_bf16 v[32:35], v[84:87], v[240:243], v[32:35]\n\t"
        "ds_read_b128 v[80:83], %[pb1] offset:36864\n\tv_mfma_f32_16x16x32_bf16 v[36:39], v[84:87], v[244:247], v[36:39]\n\t"
        "buffer_store_dwordx4 v[116:119], off, %[rt], %[sx0] offset:1024 sc1\n\ts_add_u32 %[sx0], %[sx0], 0xff800000\n\t"
        "v_mfma_f32_16x16x32_bf16 v[40:43], v[88:91], v[200:203], v[40:43]\n\tds_read_b128 v[84:87], %[pb1] offset:45056\n\t"
        "v_mfma_f32_16x16x32_bf16 v[44:47], v[88:91], v[204:207], v[44:47]\n\tv_mfma_f32_16x16x32_bf16 v[40:43], v[92:95], v[208:211], v[40:43]\n\t"
        "ds_read_b128 v[88:91], %[pb1] offset:53248\n\tv_mfma_f32_16x16x32_bf16 v[44:47], v[92:95], v[212:215], v[44:47]\n\t"
        "v_cvt_pk_bf16_f32 v120, v32, v33\n\tv_cvt_pk_bf16_f32 v121, v34, v35\n\ts_waitcnt lgkmcnt(3)\n\t"
        "v_mfma_f32_16x16x32_bf16 v[40:43], v[96:99], v[216:219], v[40:43]\n\tds_read_b128 v[92:95], %[pb1] offset:61440\n\t"
        "v_mfma_f32_16x16x32_bf16 v[44:47], v[96:99], v[220:223], v[44:47]\n\tv_cvt_pk_bf16_f32 v124, v36, v37\n\tv_cvt_pk_bf16_f32 v125, v38, v39\n\t"
        "v_mfma_f32_16x16x32_bf16 v[40:43], v[100:103], v[224:227], v[40:43]\n\tds_read_b128 v[96:99], %[pb2] offset:4096\n\t"
        "v_mfma_f32_16x16x32_bf16 v[44:47], v[100:103], v[228:231], v[44:47]\n\tv_mfma_f32_16x16x32_bf16 v[40:43], v[104:107], v[232:235], v[40:43]\n\t"
        "ds_read_b128 v[100:103], %[pb2] offset:12288\n\tv_mfma_f32_16x16x32_bf16 v[44:47], v[104:107], v[236:239], v[44:47]\n\t"
        "v_mfma_f32_16x16x32_bf16 v[40:43], v[108:111], v[240:243], v[40:43]\n\tds_read_b128 v[104:107], %[pb1] offset:37888\n\t"
        "v_mfma_f32_16x16x32_bf16 v[44:47], v[108:111], v[244:247], v[44:47]\n\ts_waitcnt lgkmcnt(3)\n\t"
        "v_mfma_f32_16x16x32_bf16 v[48:51], v[80:83], v[200:203], v[48:51]\n\tds_read_b128 v[108:111], %[pb1] offset:46080\n\t"
        "v_mfma_f32_16x16x32_bf16 v[52:55], v[80:83], v[204:207], v[52:55]\n\tv_mfma_f32_16x16x32_bf16 v[48:51], v[84:87], v[208:211], v[48:51]\n\t"
        "ds_read_b128 v[80:83], %[pb1] offset:54272\n\tv_mfma_f32_16x16x32_bf16 v[52:55], v[84:87], v[212:215], v[52:55]\n\t"
        "v_cvt_pk_bf16_f32 v122, v40, v41\n\tv_cvt_pk_bf16_f32 v123, v42, v43\n\tv_mfma_f32_16x16x32_bf16 v[48:51], v[88:91], v[216:219], v[48:51]\n\t"
        "ds_read_b128 v[84:87], %[pb1] offset:62464\n\tv_mfma_f32_16x16x32_bf16 v[52:55], v[88:91], v[220:223], v[52:55]\n\t"
        "buffer_store_dwordx4 v[120:123], off, %[rt], %[sx1] offset:0 sc1\n\tv_cvt_pk_bf16_f32 v126, v44, v45\n\tv_cvt_pk_bf16_f32 v127, v46, v47\n\t"
        "v_mfma_f32_16x16x32_bf16 v[48:51], v[92:95], v[224:227], v[48:51]\n\tds_read_b128 v[88:91], %[pb2] offset:5120\n\t"
        "v_mfma_f32_16x16x32_bf16 v[52:55], v[92:95], v[228:231], v[52:55]\n\ts_waitcnt lgkmcnt(3)\n\t"
        "v_mfma_f32_16x16x32_bf16 v[48:51], v[96:99], v[232:235], v[48:51]\n\tds_read_b128 v[92:95], %[pb2] offset:13312\n\t"
        "v_mfma_f32_16x16x32_bf16 v[52:55], v[96:99], v[236:239], v[52:55]\n\tv_mfma_f32_16x16x32_bf16 v[48:51], v[100:103], v[240:243], v[48:51]\n\t"
        "ds_read_b128 v[96:99], %[pb1] offset:38912\n\tv_mfma_f32_16x16x32_bf16 v[52:55], v[100:103], v[244:247], v[52:55]\n\t"
        "buffer_store_dwordx4 v[124:127], off, %[rt], %[sx1] offset:1024 sc1\n\ts_add_u32 %[sx1], %[sx1], 0xff800000\n\t"
        "v_mfma_f32_16x16x32_bf16 v[56:59], v[104:107], v[200:203], v[56:59]\n\tds_read_b128 v[100:103], %[pb1] offset:47104\n\t"
        "v_mfma_f32_16x16x32_bf16 v[60:63], v[104:107], v[204:207], v[60:63]\n\tv_mfma_f32_16x16x32_bf16 v[56:59], v[108:111], v[208:211], v[56:59]\n\t"
        "ds_read_b128 v[104:107], %[pb1] offset:55296\n\tv_mfma_f32_16x16x32_bf16 v[60:63], v[108:111], v[212:215], v[60:63]\n\t"
        "v_cvt_pk_bf16_f32 v128, v48, v49\n\tv_cvt_pk_bf16_f32 v129, v50, v51\n\ts_waitcnt lgkmcnt(3)\n\t"
        "v_mfma_f32_16x16x32_bf16 v[56:59], v[80:83], v[216:219], v[56:59]\n\tds_read_b128 v[108:111], %[pb1] offset:63488\n\t"
        "v_mfma_f32_16x16x32_bf16 v[60:63], v[80:83], v[220:223], v[60:63]\n\tv_cvt_pk_bf16_f32 v132, v52, v53\n\tv_cvt_pk_bf16_f32 v133, v54, v55\n\t"
        "v_mfma_f32_16x16x32_bf16 v[56:59], v[84:87], v[224:227], v[56:59]\n\tds_read_b128 v[80:83], %[pb2] offset:6144\n\t"
        "v_mfma_f32_16x16x32_bf16 v[60:63], v[84:87], v[228:231], v[60:63]\n\tv_mfma_f32_16x16x32_bf16 v[56:59], v[88:91], v[232:235], v[56:59]\n\t"
        "ds_read_b128 v[84:87], %[pb2] offset:14336\n\tv_mfma_f32_16x16x32_bf16 v[60:63], v[88:91], v[236:239], v[60:63]\n\t"
        "v_mfma_f32_16x16x32_bf16 v[56:59], v[92:95], v[240:243], v[56:59]\n\tds_read_b128 v[88:91], %[pb1] offset:39936\n\t"
        "v_mfma_f32_16x16x32_bf16 v[60:63], v[92:95], v[244:247], v[60:63]\n\ts_waitcnt lgkmcnt(3)\n\t"
        "v_mfma_f32_16x16x32_bf16 v[64:67], v[96:99], v[200:203], v[64:67]\n\tds_read_b128 v[92:95], %[pb1] offset:48128\n\t"
        "v_mfma_f32_16x16x32_bf16 v[68:71], v[96:99], v[204:207], v[68:71]\n\tv_mfma_f32_16x16x32_bf16 v[64:67], v[100:103], v[208:211], v[64:67]\n\t"
        "ds_read_b128 v[96:99], %[pb1] offset:56320\n\tv_mfma_f32_16x16x32_bf16 v[68:71], v[100:103], v[212:215], v[68:71]\n\t"
        "v_cvt_pk_bf16_f32 v130, v56, v57\n\tv_cvt_pk_bf16_f32 v131, v58, v59\n\tv_mfma_f32_16x16x32_bf16 v[64:67], v[104:107], v[216:219], v[64:67]\n\t"
        "ds_read_b128 v[100:103], %[pb1] offset:64512\n\tv_mfma_f32_16x16x32_bf16 v[68:71], v[104:107], v[220:223], v[68:71]\n\t"
        "buffer_store_dwordx4 v[128:131], off, %[rt], %[sx2] offset:0 sc1\n\tv_cvt_pk_bf16_f32 v134, v60, v61\n\tv_cvt_pk_bf16_f32 v135, v62, v63\n\t"
        "v_mfma_f32_16x16x32_bf16 v[64:67], v[108:111], v[224:227], v[64:67]\n\tds_read_b128 v[104:107], %[pb2] offset:7168\n\t"
        "v_mfma_f32_16x16x32_bf16 v[68:71], v[108:111], v[228:231], v[68:71]\n\ts_waitcnt lgkmcnt(3)\n\t"
        "v_mfma_f32_16x16x32_bf16 v[64:67], v[80:83], v[232:235], v[64:67]\n\tds_read_b128 v[108:111], %[pb2] offset:15360\n\t"
        "v_mfma_f32_16x16x32_bf16 v[68:71], v[80:83], v[236:239], v[68:71]\n\tv_mfma_f32_16x16x32_bf16 v[64:67], v[84:87], v[240:243], v[64:67]\n\t"
        "v_mfma_f32_16x16x32_bf16 v[68:71], v[84:87], v[244:247], v[68:71]\n\tbuffer_store_dwordx4 v[132:135], off, %[rt], %[sx2] offset:1024 sc1\n\t"
        "s_add_u32 %[sx2], %[sx2], 0xff800000\n\ts_waitcnt lgkmcnt(0)\n\ts_barrier\n\tv_mfma_f32_16x16x32_bf16 v[72:75], v[88:91], v[200:203], v[72:75]\n\t"
        "buffer_load_dwordx4 v[152:155], off, %[rn], %[sn] offset:0 sc1\n\tv_mfma_f32_16x16x32_bf16 v[76:79], v[88:91], v[204:207], v[76:79]\n\t"
        "v_mfma_f32_16x16x32_bf16 v[72:75], v[92:95], v[208:211], v[72:75]\n\tv_mfma_f32_16x16x32_bf16 v[76:79], v[92:95], v[212:215], v[76:79]\n\t"
        "ds_write_b128 %[px], v[64:67] offset:0\n\tv_mfma_f32_16x16x32_bf16 v[72:75], v[96:99], v[216:219], v[72:75]\n\t"
        "v_mfma_f32_16x16x32_bf16 v[76:79], v[96:99], v[220:223], v[76:79]\n\tds_write_b128 %[px], v[68:71] offset:1024\n\t"
        "v_mfma_f32_16x16x32_bf16 v[72:75], v[100:103], v[224:227], v[72:75]\n\tbuffer_load_dwordx4 v[156:159], off, %[rn], %[sn] offset:1024 sc1\n\t"
        "s_add_u32 %[sn], %[sn], 0x2000\n\tv_mfma_f32_16x16x32_bf16 v[76:79], v[100:103], v[228:231], v[76:79]\n\t"
        "v_mfma_f32_16x16x32_bf16 v[72:75], v[104:107], v[232:235], v[72:75]\n\tv_mfma_f32_16x16x32_bf16 v[76:79], v[104:107], v[236:239], v[76:79]\n\t"
        "v_mfma_f32_16x16x32_bf16 v[72:75], v[108:111], v[240:243], v[72:75]\n\tv_mfma_f32_16x16x32_bf16 v[76:79], v[108:111], v[244:247], v[76:79]\n\t"
        "s_nop 7\n\tds_write_b128 %[px], v[72:75] offset:2048\n\tds_write_b128 %[px], v[76:79] offset:3072\n\ts_waitcnt lgkmcnt(0)\n\ts_barrier\n\t"
        "buffer_load_dwordx4 v[160:163], off, %[rn], %[sn] offset:0 sc1\n\tbuffer_load_dwordx4 v[164:167], off, %[rn], %[sn] offset:1024 sc1\n\t"
        "s_add_u32 %[sn], %[sn], 0x2000\n\tbuffer_load_dwordx4 v[168:171], off, %[rn], %[sn] offset:0 sc1\n\t"
        "buffer_load_dwordx4 v[172:175], off, %[rn], %[sn] offset:1024 sc1\n\ts_add_u32 %[sn], %[sn], 0x2000\n\t"
        "buffer_load_dwordx4 v[176:179], off, %[rn], %[sn] offset:0 sc1\n\tbuffer_load_dwordx4 v[180:183], off, %[rn], %[sn] offset:1024 sc1\n\t"
        "s_add_u32 %[sn], %[sn], 0x2000\n\t.Lmk_v_e:\n\t"
        : "+{v[152:183]}"(r0), "+{v[184:215]}"(r1), "+{v[216:247]}"(r2), [sx0] "+s"(sx0), [sx1] "+s"(sx1), [sx2] "+s"(sx2), "+{v[120:151]}"(ho), [hx] "+s"(hx), [sa] "+s"(sa), [sn] "+s"(sn)
        : [pb0] "{v250}"(n.pb0), [pb1] "{v251}"(n.pb1), [pb2] "{v252}"(n.pb2), [rt] "s"(rt4), [rn] "s"(rn4), [px] "v"(n.pb0 + n.own), [hd] "s"(hd), [hd7] "s"(hd7), "{v253}"(n.e1), "{v255}"(n.e3), "{v15}"(n.av), [rs] "s"(rs4), [one] "{v[12:13]}"(tok), [fl] "s"(fl)
        : "v80", "v81", "v82", "v83", "v84", "v85", "v86", "v87", "v88", "v89", "v90", "v91", "v92", "v93", "v94", "v95", "v96", "v97", "v98", "v99", "v100", "v101", "v102", "v103", "v104", "v105", "v106", "v107", "v108", "v109", "v110", "v111", "v16", "v17", "v18", "v19", "v20", "v21", "v22", "v23", "v24", "v25", "v26", "v27", "v28", "v29", "v30", "v31", "v32", "v33", "v34", "v35", "v36", "v37", "v38", "v39", "v40", "v41", "v42", "v43", "v44", "v45", "v46", "v47", "v48", "v49", "v50", "v51", "v52", "v53", "v54", "v55", "v56", "v57", "v58", "v59", "v60", "v61", "v62", "v63", "v64", "v65", "v66", "v67", "v68", "v69", "v70", "v71", "v72", "v73", "v74", "v75", "v76", "v77", "v78", "v79", "v112", "v113", "v114", "v115", "v116", "v117", "v118", "v119", "memory", "scc");
    }
    STAMP(18);
    STAMP_OUT();
    asm volatile("s_cmp_eq_u32 %0, 0\n\ts_cbranch_scc1 7f\n\ts_endpgm\n7:" ::"s"(layer0) : "scc", "memory");
    STAMP(0);
    sa = sn; sn = xq + (0 ? 2 : 10) * MB;
      sn += 196608u;
      asm volatile(
        "; BLK q0\n\t.Lmk_q0_s:\n\tds_read_b128 v[64:67], %[pb0] offset:0\n\tds_read_b128 v[68:71], %[pb0] offset:1024\n\t"
        "ds_read_b128 v[72:75], %[pb0] offset:2048\n\tds_read_b128 v[76:79], %[pb0] offset:3072\n\tds_read_b128 v[80:83], %[pb0] offset:4096\n\t"
        "ds_read_b128 v[84:87], %[pb0] offset:5120\n\tds_read_b128 v[88:91], %[pb0] offset:6144\n\ts_waitcnt vmcnt(6) lgkmcnt(3)\n\t"
        "v_mfma_f32_16x16x32_bf16 v[16:19], v[64:67], v[152:155], 0\n\tds_read_b128 v[92:95], %[pb0] offset:7168\n\t"
        "v_mfma_f32_16x16x32_bf16 v[20:23], v[64:67], v[156:159], 0\n\tv_mfma_f32_16x16x32_bf16 v[24:27], v[68:71], v[152:155], 0\n\t"
        "ds_read_b128 v[64:67], %[pb0] offset:8192\n\tv_mfma_f32_16x16x32_bf16 v[28:31], v[68:71], v[156:159], 0\n\t"
        "v_mfma_f32_16x16x32_bf16 v[32:35], v[72:75], v[152:155], 0\n\tds_read_b128 v[68:71], %[pb0] offset:9216\n\t"
        "buffer_load_dwordx4 v[184:187], off, %[rt], %[sa] offset:0 sc1\n\tv_mfma_f32_16x16x32_bf16 v[36:39], v[72:75], v[156:159], 0\n\t"
        "v_mfma_f32_16x16x32_bf16 v[40:43], v[76:79], v[152:155], 0\n\tds_read_b128 v[72:75], %[pb0] offset:10240\n\t"
        "v_mfma_f32_16x16x32_bf16 v[44:47], v[76:79], v[156:159], 0\n\ts_waitcnt lgkmcnt(3)\n\tv_mfma_f32_16x16x32_bf16 v[48:51], v[152:155], v[80:83], 0\n\t"
        "ds_read_b128 v[76:79], %[pb0] offset:11264\n\tv_mfma_f32_16x16x32_bf16 v[52:55], v[156:159], v[80:83], 0\n\t"
        "buffer_load_dwordx4 v[188:191], off, %[rt], %[sa] offset:1024 sc1\n\ts_add_u32 %[sa], %[sa], 0x2000\n\t"
        "v_mfma_f32_16x16x32_bf16 v[56:59], v[152:155], v[84:87], 0\n\tds_read_b128 v[80:83], %[pb0] offset:12288\n\t"
        "v_mfma_f32_16x16x32_bf16 v[60:63], v[156:159], v[84:87], 0\n\ts_waitcnt vmcnt(6)\n\t"
        "v_mfma_f32_16x16x32_bf16 v[16:19], v[88:91], v[160:163], v[16:19]\n\tds_read_b128 v[84:87], %[pb0] offset:13312\n\t"
        "v_mfma_f32_16x16x32_bf16 v[20:23], v[88:91], v[164:167], v[20:23]\n\tv_mfma_f32_16x16x32_bf16 v[24:27], v[92:95], v[160:163], v[24:27]\n\t"
        "ds_read_b128 v[88:91], %[pb0] offset:14336\n\tbuffer_load_dwordx4 v[192:195], off, %[rt], %[sa] offset:0 sc1\n\t"
        "v_mfma_f32_16x16x32_bf16 v[28:31], v[92:95], v[164:167], v[28:31]\n\ts_waitcnt lgkmcnt(3)\n\t"
        "v_mfma_f32_16x16x32_bf16 v[32:35], v[64:67], v[160:163], v[32:35]\n\tds_read_b128 v[92:95], %[pb0] offset:15360\n\t"
        "v_mfma_f32_16x16x32_bf16 v[36:39], v[64:67], v[164:167], v[36:39]\n\tv_mfma_f32_16x16x32_bf16 v[40:43], v[68:71], v[160:163], v[40:43]\n\t"
        "ds_read_b128 v[64:67], %[pb0] offset:16384\n\tv_mfma_f32_16x16x32_bf16 v[44:47], v[68:71], v[164:167], v[44:47]\n\t"
        "buffer_load_dwordx4 v[196:199], off, %[rt], %[sa] offset:1024 sc1\n\ts_add_u32 %[sa], %[sa], 0x2000\n\t"
        "v_mfma_f32_16x16x32_bf16 v[48:51], v[160:163], v[72:75], v[48:51]\n\tds_read_b128 v[68:71], %[pb0] offset:17408\n\t"
        "v_mfma_f32_16x16x32_bf16 v[52:55], v[164:167], v[72:75], v[52:55]\n\tv_mfma_f32_16x16x32_bf16 v[56:59], v[160:163], v[76:79], v[56:59]\n\t"
        "ds_read_b128 v[72:75], %[pb0] offset:18432\n\tv_mfma_f32_16x16x32_bf16 v[60:63], v[164:167], v[76:79], v[60:63]\n\ts_waitcnt vmcnt(6) lgkmcnt(3)\n\t"
        "v_mfma_f32_16x16x32_bf16 v[16:19], v[80:83], v[168:171], v[16:19]\n\tds_read_b128 v[76:79], %[pb0] offset:19456\n\t"
        "buffer_load_dwordx4 v[200:203], off, %[rt], %[sa] offset:0 sc1\n\tv_mfma_f32_16x16x32_bf16 v[20:23], v[80:83], v[172:175], v[20:23]\n\t"
        "v_mfma_f32_16x16x32_bf16 v[24:27], v[84:87], v[168:171], v[24:27]\n\tds_read_b128 v[80:83], %[pb0] offset:20480\n\t"
        "v_mfma_f32_16x16x32_bf16 v[28:31], v[84:87], v[172:175], v[28:31]\n\tv_mfma_f32_16x16x32_bf16 v[32:35], v[88:91], v[168:171], v[32:35]\n\t"
        "ds_read_b128 v[84:87], %[pb0] offset:21504\n\tv_mfma_f32_16x16x32_bf16 v[36:39], v[88:91], v[172:175], v[36:39]\n\t"
        "buffer_load_dwordx4 v[204:207], off, %[rt], %[sa] offset:1024 sc1\n\ts_add_u32 %[sa], %[sa], 0x2000\n\t"
        "v_mfma_f32_16x16x32_bf16 v[40:43], v[92:95], v[168:171], v[40:43]\n\tds_read_b128 v[88:91], %[pb0] offset:22528\n\t"
        "v_mfma_f32_16x16x32_bf16 v[44:47], v[92:95], v[172:175], v[44:47]\n\ts_waitcnt lgkmcnt(3)\n\t"
        "v_mfma_f32_16x16x32_bf16 v[48:51], v[168:171], v[64:67], v[48:51]\n\tds_read_b128 v[92:95], %[pb0] offset:23552\n\t"
        "v_mfma_f32_16x16x32_bf16 v[52:55], v[172:175], v[64:67], v[52:55]\n\tv_mfma_f32_16x16x32_bf16 v[56:59], v[168:171], v[68:71], v[56:59]\n\t"
        "ds_read_b128 v[64:67], %[pb0] offset:24576\n\tbuffer_load_dwordx4 v[208:211], off, %[rt], %[sa] offset:0 sc1\n\t"
        "v_mfma_f32_16x16x32_bf16 v[60:63], v[172:175], v[68:71], v[60:63]\n\ts_waitcnt vmcnt(7)\n\t"
        "v_mfma_f32_16x16x32_bf16 v[16:19], v[72:75], v[176:179], v[16:19]\n\tds_read_b128 v[68:71], %[pb0] offset:25600\n\t"
        "v_mfma_f32_16x16x32_bf16 v[20:23], v[72:75], v[180:183], v[20:23]\n\tv_mfma_f32_16x16x32_bf16 v[24:27], v[76:79], v[176:179], v[24:27]\n\t"
        "ds_read_b128 v[72:75], %[pb0] offset:26624\n\tv_mfma_f32_16x16x32_bf16 v[28:31], v[76:79], v[180:183], v[28:31]\n\t"
        "buffer_load_dwordx4 v[212:215], off, %[rt], %[sa] offset:1024 sc1\n\ts_add_u32 %[sa], %[sa], 0x2000\n\ts_waitcnt lgkmcnt(3)\n\t"
        "v_mfma_f32_16x16x32_bf16 v[32:35], v[80:83], v[176:179], v[32:35]\n\tds_read_b128 v[76:79], %[pb0] offset:27648\n\t"
        "v_mfma_f32_16x16x32_bf16 v[36:39], v[80:83], v[180:183], v[36:39]\n\tv_mfma_f32_16x16x32_bf16 v[40:43], v[84:87], v[176:179], v[40:43]\n\t"
        "ds_read_b128 v[80:83], %[pb0] offset:28672\n\tv_mfma_f32_16x16x32_bf16 v[44:47], v[84:87], v[180:183], v[44:47]\n\t"
        "v_mfma_f32_16x16x32_bf16 v[48:51], v[176:179], v[88:91], v[48:51]\n\tds_read_b128 v[84:87], %[pb0] offset:29696\n\t"
        "buffer_load_dwordx4 v[216:219], off, %[rt], %[sa] offset:0 sc1\n\tv_mfma_f32_16x16x32_bf16 v[52:55], v[180:183], v[88:91], v[52:55]\n\t"
        "v_mfma_f32_16x16x32_bf16 v[56:59], v[176:179], v[92:95], v[56:59]\n\tds_read_b128 v[88:91], %[pb0] offset:30720\n\t"
        "v_mfma_f32_16x16x32_bf16 v[60:63], v[180:183], v[92:95], v[60:63]\n\ts_waitcnt vmcnt(7) lgkmcnt(3)\n\t"
        "v_mfma_f32_16x16x32_bf16 v[16:19], v[64:67], v[184:187], v[16:19]\n\tds_read_b128 v[92:95], %[pb0] offset:31744\n\t"
        "v_mfma_f32_16x16x32_bf16 v[20:23], v[64:67], v[188:191], v[20:23]\n\tbuffer_load_dwordx4 v[220:223], off, %[rt], %[sa] offset:1024 sc1\n\t"
        "s_add_u32 %[sa], %[sa], 0x2000\n\tv_mfma_f32_16x16x32_bf16 v[24:27], v[68:71], v[184:187], v[24:27]\n\tds_read_b128 v[64:67], %[pb0] offset:32768\n\t"
        "v_mfma_f32_16x16x32_bf16 v[28:31], v[68:71], v[188:191], v[28:31]\n\tv_mfma_f32_16x16x32_bf16 v[32:35], v[72:75], v[184:187], v[32:35]\n\t"
        "ds_read_b128 v[68:71], %[pb0] offset:33792\n\tv_mfma_f32_16x16x32_bf16 v[36:39], v[72:75], v[188:191], v[36:39]\n\t"
        "v_mfma_f32_16x16x32_bf16 v[40:43], v[76:79], v[184:187], v[40:43]\n\tds_read_b128 v[72:75], %[pb0] offset:34816\n\t"
        "buffer_load_dwordx4 v[224:227], off, %[rt], %[sa] offset:0 sc1\n\tv_mfma_f32_16x16x32_bf16 v[44:47], v[76:79], v[188:191], v[44:47]\n\t"
        "s_waitcnt lgkmcnt(3)\n\tv_mfma_f32_16x16x32_bf16 v[48:51], v[184:187], v[80:83], v[48:51]\n\tds_read_b128 v[76:79], %[pb0] offset:35840\n\t"
        "v_mfma_f32_16x16x32_bf16 v[52:55], v[188:191], v[80:83], v[52:55]\n\tv_mfma_f32_16x16x32_bf16 v[56:59], v[184:187], v[84:87], v[56:59]\n\t"
        "ds_read_b128 v[80:83], %[pb0] offset:36864\n\tv_mfma_f32_16x16x32_bf16 v[60:63], v[188:191], v[84:87], v[60:63]\n\t"
        "buffer_load_dwordx4 v[228:231], off, %[rt], %[sa] offset:1024 sc1\n\ts_add_u32 %[sa], %[sa], 0x2000\n\ts_waitcnt vmcnt(8)\n\t"
        "v_mfma_f32_16x16x32_bf16 v[16:19], v[88:91], v[192:195], v[16:19]\n\tds_read_b128 v[84:87], %[pb0] offset:37888\n\t"
        "v_mfma_f32_16x16x32_bf16 v[20:23], v[88:91], v[196:199], v[20:23]\n\tv_mfma_f32_16x16x32_bf16 v[24:27], v[92:95], v[192:195], v[24:27]\n\t"
        "ds_read_b128 v[88:91], %[pb0] offset:38912\n\tv_mfma_f32_16x16x32_bf16 v[28:31], v[92:95], v[196:199], v[28:31]\n\ts_waitcnt lgkmcnt(3)\n\t"
        "v_mfma_f32_16x16x32_bf16 v[32:35], v[64:67], v[192:195], v[32:35]\n\tds_read_b128 v[92:95], %[pb0] offset:39936\n\t"
        "buffer_load_dwordx4 v[232:235], off, %[rt], %[sa] offset:0 sc1\n\tv_mfma_f32_16x16x32_bf16 v[36:39], v[64:67], v[196:199], v[36:39]\n\t"
        "v_mfma_f32_16x16x32_bf16 v[40:43], v[68:71], v[192:195], v[40:43]\n\tds_read_b128 v[64:67], %[pb0] offset:40960\n\t"
        "v_mfma_f32_16x16x32_bf16 v[44:47], v[68:71], v[196:199], v[44:47]\n\tv_mfma_f32_16x16x32_bf16 v[48:51], v[192:195], v[72:75], v[48:51]\n\t"
        "ds_read_b128 v[68:71], %[pb0] offset:41984\n\tv_mfma_f32_16x16x32_bf16 v[52:55], v[196:199], v[72:75], v[52:55]\n\t"
        "buffer_load_dwordx4 v[236:239], off, %[rt], %[sa] offset:1024 sc1\n\ts_add_u32 %[sa], %[sa], 0x2000\n\t"
        "v_mfma_f32_16x16x32_bf16 v[56:59], v[192:195], v[76:79], v[56:59]\n\tds_read_b128 v[72:75], %[pb0] offset:43008\n\t"
        "v_mfma_f32_16x16x32_bf16 v[60:63], v[196:199], v[76:79], v[60:63]\n\ts_waitcnt vmcnt(8) lgkmcnt(3)\n\t"
        "v_mfma_f32_16x16x32_bf16 v[16:19], v[80:83], v[200:203], v[16:19]\n\tds_read_b128 v[76:79], %[pb0] offset:44032\n\t"
        "v_mfma_f32_16x16x32_bf16 v[20:23], v[80:83], v[204:207], v[20:23]\n\tv_mfma_f32_16x16x32_bf16 v[24:27], v[84:87], v[200:203], v[24:27]\n\t"
        "ds_read_b128 v[80:83], %[pb0] offset:45056\n\tbuffer_load_dwordx4 v[240:243], off, %[rt], %[sa] offset:0 sc1\n\t"
        "v_mfma_f32_16x16x32_bf16 v[28:31], v[84:87], v[204:207], v[28:31]\n\tv_mfma_f32_16x16x32_bf16 v[32:35], v[88:91], v[200:203], v[32:35]\n\t"
        "ds_read_b128 v[84:87], %[pb0] offset:46080\n\tv_mfma_f32_16x16x32_bf16 v[36:39], v[88:91], v[204:207], v[36:39]\n\t"
        "v_mfma_f32_16x16x32_bf16 v[40:43], v[92:95], v[200:203], v[40:43]\n\tds_read_b128 v[88:91], %[pb0] offset:47104\n\t"
        "v_mfma_f32_16x16x32_bf16 v[44:47], v[92:95], v[204:207], v[44:47]\n\tbuffer_load_dwordx4 v[244:247], off, %[rt], %[sa] offset:1024 sc1\n\t"
        "s_add_u32 %[sa], %[sa], 0x2000\n\ts_waitcnt lgkmcnt(3)\n\tv_mfma_f32_16x16x32_bf16 v[48:51], v[200:203], v[64:67], v[48:51]\n\t"
        "ds_read_b128 v[92:95], %[pb0] offset:48128\n\tv_mfma_f32_16x16x32_bf16 v[52:55], v[204:207], v[64:67], v[52:55]\n\t"
        "v_mfma_f32_16x16x32_bf16 v[56:59], v[200:203], v[68:71], v[56:59]\n\tds_read_b128 v[64:67], %[pb0] offset:49152\n\t"
        "v_mfma_f32_16x16x32_bf16 v[60:63], v[204:207], v[68:71], v[60:63]\n\ts_waitcnt vmcnt(8)\n\t"
        "v_mfma_f32_16x16x32_bf16 v[16:19], v[72:75], v[208:211], v[16:19]\n\tds_read_b128 v[68:71], %[pb0] offset:50176\n\t"
        "buffer_load_dwordx4 v[152:155], off, %[rt], %[sa] offset:0 sc1\n\tv_mfma_f32_16x16x32_bf16 v[20:23], v[72:75], v[212:215], v[20:23]\n\t"
        "v_mfma_f32_16x16x32_bf16 v[24:27], v[76:79], v[208:211], v[24:27]\n\tds_read_b128 v[72:75], %[pb0] offset:51200\n\t"
        "v_mfma_f32_16x16x32_bf16 v[28:31], v[76:79], v[212:215], v[28:31]\n\ts_waitcnt lgkmcnt(3)\n\t"
        "v_mfma_f32_16x16x32_bf16 v[32:35], v[80:83], v[208:211], v[32:35]\n\tds_read_b128 v[76:79], %[pb0] offset:52224\n\t"
        "v_mfma_f32_16x16x32_bf16 v[36:39], v[80:83], v[212:215], v[36:39]\n\tbuffer_load_dwordx4 v[156:159], off, %[rt], %[sa] offset:1024 sc1\n\t"
        "s_add_u32 %[sa], %[sa], 0x2000\n\tv_mfma_f32_16x16x32_bf16 v[40:43], v[84:87], v[208:211], v[40:43]\n\tds_read_b128 v[80:83], %[pb0] offset:53248\n\t"
        "v_mfma_f32_16x16x32_bf16 v[44:47], v[84:87], v[212:215], v[44:47]\n\tv_mfma_f32_16x16x32_bf16 v[48:51], v[208:211], v[88:91], v[48:51]\n\t"
        "ds_read_b128 v[84:87], %[pb0] offset:54272\n\tv_mfma_f32_16x16x32_bf16 v[52:55], v[212:215], v[88:91], v[52:55]\n\t"
        "v_mfma_f32_16x16x32_bf16 v[56:59], v[208:211], v[92:95], v[56:59]\n\tds_read_b128 v[88:91], %[pb0] offset:55296\n\t"
        "buffer_load_dwordx4 v[160:163], off, %[rt], %[sa] offset:0 sc1\n\tv_mfma_f32_16x16x32_bf16 v[60:63], v[212:215], v[92:95], v[60:63]\n\t"
        "s_waitcnt vmcnt(9) lgkmcnt(3)\n\tv_mfma_f32_16x16x32_bf16 v[16:19], v[64:67], v[216:219], v[16:19]\n\tds_read_b128 v[92:95], %[pb0] offset:56320\n\t"
        "v_mfma_f32_16x16x32_bf16 v[20:23], v[64:67], v[220:223], v[20:23]\n\t.p2align 12,,12\n\t.Lpg17:\n\ts_cbranch_execz .Lpgret\n\t"
        "v_mfma_f32_16x16x32_bf16 v[24:27], v[68:71], v[216:219], v[24:27]\n\tds_read_b128 v[64:67], %[pb0] offset:57344\n\t"
        "v_mfma_f32_16x16x32_bf16 v[28:31], v[68:71], v[220:223], v[28:31]\n\tbuffer_load_dwordx4 v[164:167], off, %[rt], %[sa] offset:1024 sc1\n\t"
        "s_add_u32 %[sa], %[sa], 0x2000\n\ts_add_u32 %[kvo], %[kvo], 0x800000\n\tv_mfma_f32_16x16x32_bf16 v[32:35], v[72:75], v[216:219], v[32:35]\n\t"
        "ds_read_b128 v[68:71], %[pb0] offset:58368\n\tv_mfma_f32_16x16x32_bf16 v[36:39], v[72:75], v[220:223], v[36:39]\n\t"
        "v_mfma_f32_16x16x32_bf16 v[40:43], v[76:79], v[216:219], v[40:43]\n\tds_read_b128 v[72:75], %[pb0] offset:59392\n\t"
        "v_mfma_f32_16x16x32_bf16 v[44:47], v[76:79], v[220:223], v[44:47]\n\ts_waitcnt lgkmcnt(3)\n\t"
        "v_mfma_f32_16x16x32_bf16 v[48:51], v[216:219], v[80:83], v[48:51]\n\tds_read_b128 v[76:79], %[pb0] offset:60416\n\t"
        "buffer_load_dwordx4 v[168:171], off, %[rt], %[sa] offset:0 sc1\n\tv_mfma_f32_16x16x32_bf16 v[52:55], v[220:223], v[80:83], v[52:55]\n\t"
        "v_mfma_f32_16x16x32_bf16 v[56:59], v[216:219], v[84:87], v[56:59]\n\tds_read_b128 v[80:83], %[pb0] offset:61440\n\t"
        "v_mfma_f32_16x16x32_bf16 v[60:63], v[220:223], v[84:87], v[60:63]\n\ts_add_u32 %[fkv], %[fkv], 0x4000\n\ts_waitcnt vmcnt(9)\n\t"
        "v_mfma_f32_16x16x32_bf16 v[16:19], v[88:91], v[224:227], v[16:19]\n\tds_read_b128 v[84:87], %[pb0] offset:62464\n\t"
        "v_mfma_f32_16x16x32_bf16 v[20:23], v[88:91], v[228:231], v[20:23]\n\tbuffer_load_dwordx4 v[172:175], off, %[rt], %[sa] offset:1024 sc1\n\t"
        "s_add_u32 %[sa], %[sa], 0x2000\n\tv_mfma_f32_16x16x32_bf16 v[24:27], v[92:95], v[224:227], v[24:27]\n\tds_read_b128 v[88:91], %[pb0] offset:63488\n\t"
        "v_mfma_f32_16x16x32_bf16 v[28:31], v[92:95], v[228:231], v[28:31]\n\ts_waitcnt lgkmcnt(3)\n\t"
        "v_mfma_f32_16x16x32_bf16 v[32:35], v[64:67], v[224:227], v[32:35]\n\tds_read_b128 v[92:95], %[pb0] offset:64512\n\t"
        "v_mfma_f32_16x16x32_bf16 v[36:39], v[64:67], v[228:231], v[36:39]\n\tv_mfma_f32_16x16x32_bf16 v[40:43], v[68:71], v[224:227], v[40:43]\n\t"
        "ds_read_b128 v[64:67], %[pb1] offset:0\n\tbuffer_load_dwordx4 v[176:179], off, %[rt], %[sa] offset:0 sc1\n\t"
        "v_mfma_f32_16x16x32_bf16 v[44:47], v[68:71], v[228:231], v[44:47]\n\ts_add_u32 %[fo], %[fo], 0x10000\n\t"
        "v_mfma_f32_16x16x32_bf16 v[48:51], v[224:227], v[72:75], v[48:51]\n\tds_read_b128 v[68:71], %[pb1] offset:1024\n\t"
        "v_mfma_f32_16x16x32_bf16 v[52:55], v[228:231], v[72:75], v[52:55]\n\tv_mfma_f32_16x16x32_bf16 v[56:59], v[224:227], v[76:79], v[56:59]\n\t"
        "ds_read_b128 v[72:75], %[pb1] offset:2048\n\tv_mfma_f32_16x16x32_bf16 v[60:63], v[228:231], v[76:79], v[60:63]\n\t"
        "buffer_load_dwordx4 v[180:183], off, %[rt], %[sa] offset:1024 sc1\n\ts_add_u32 %[sa], %[sa], 0x2000\n\ts_waitcnt vmcnt(10) lgkmcnt(3)\n\t"
        "v_mfma_f32_16x16x32_bf16 v[16:19], v[80:83], v[232:235], v[16:19]\n\tds_read_b128 v[76:79], %[pb1] offset:3072\n\t"
        "v_mfma_f32_16x16x32_bf16 v[20:23], v[80:83], v[236:239], v[20:23]\n\tv_mfma_f32_16x16x32_bf16 v[24:27], v[84:87], v[232:235], v[24:27]\n\t"
        "ds_read_b128 v[80:83], %[pb1] offset:4096\n\tv_mfma_f32_16x16x32_bf16 v[28:31], v[84:87], v[236:239], v[28:31]\n\ts_add_u32 %[xr], %[xr], 0x800000\n\t"
        "v_mfma_f32_16x16x32_bf16 v[32:35], v[88:91], v[232:235], v[32:35]\n\tds_read_b128 v[84:87], %[pb1] offset:5120\n\t"
        "buffer_load_dwordx4 v[184:187], off, %[rt], %[sa] offset:0 sc1\n\tv_mfma_f32_16x16x32_bf16 v[36:39], v[88:91], v[236:239], v[36:39]\n\t"
        "v_mfma_f32_16x16x32_bf16 v[40:43], v[92:95], v[232:235], v[40:43]\n\tds_read_b128 v[88:91], %[pb1] offset:6144\n\t"
        "v_mfma_f32_16x16x32_bf16 v[44:47], v[92:95], v[236:239], v[44:47]\n\ts_waitcnt lgkmcnt(3)\n\t"
        "v_mfma_f32_16x16x32_bf16 v[48:51], v[232:235], v[64:67], v[48:51]\n\tds_read_b128 v[92:95], %[pb1] offset:7168\n\t"
        "v_mfma_f32_16x16x32_bf16 v[52:55], v[236:239], v[64:67], v[52:55]\n\tbuffer_load_dwordx4 v[188:191], off, %[rt], %[sa] offset:1024 sc1\n\t"
        "s_add_u32 %[sa], %[sa], 0x2000\n\tv_mfma_f32_16x16x32_bf16 v[56:59], v[232:235], v[68:71], v[56:59]\n\ts_barrier\n\t"
        "ds_read_b128 v[64:67], %[pb1] offset:8192\n\tv_mfma_f32_16x16x32_bf16 v[60:63], v[236:239], v[68:71], v[60:63]\n\ts_add_u32 %[so], %[so], 0x80000\n\t"
        "s_waitcnt vmcnt(10)\n\tv_mfma_f32_16x16x32_bf16 v[16:19], v[72:75], v[240:243], v[16:19]\n\tds_read_b128 v[68:71], %[pb1] offset:9216\n\t"
        "v_mfma_f32_16x16x32_bf16 v[20:23], v[72:75], v[244:247], v[20:23]\n\tv_mfma_f32_16x16x32_bf16 v[24:27], v[76:79], v[240:243], v[24:27]\n\t"
        "ds_read_b128 v[72:75], %[pb1] offset:10240\n\tbuffer_load_dwordx4 v[192:195], off, %[rt], %[sa] offset:0 sc1\n\t"
        "v_mfma_f32_16x16x32_bf16 v[28:31], v[76:79], v[244:247], v[28:31]\n\ts_waitcnt lgkmcnt(3)\n\t"
        "v_mfma_f32_16x16x32_bf16 v[32:35], v[80:83], v[240:243], v[32:35]\n\tds_read_b128 v[76:79], %[pb1] offset:11264\n\t"
        "v_mfma_f32_16x16x32_bf16 v[36:39], v[80:83], v[244:247], v[36:39]\n\tv_mfma_f32_16x16x32_bf16 v[40:43], v[84:87], v[240:243], v[40:43]\n\t"
        "ds_read_b128 v[80:83], %[pb1] offset:12288\n\tv_mfma_f32_16x16x32_bf16 v[44:47], v[84:87], v[244:247], v[44:47]\n\t"
        "buffer_load_dwordx4 v[196:199], off, %[rt], %[sa] offset:1024 sc1\n\ts_add_u32 %[sa], %[sa], 0x2000\n\ts_add_u32 %[fx], %[fx], 0x10000\n\t"
        "v_mfma_f32_16x16x32_bf16 v[48:51], v[240:243], v[88:91], v[48:51]\n\tds_read_b128 v[84:87], %[pb1] offset:13312\n\t"
        "v_mfma_f32_16x16x32_bf16 v[52:55], v[244:247], v[88:91], v[52:55]\n\tv_mfma_f32_16x16x32_bf16 v[56:59], v[240:243], v[92:95], v[56:59]\n\t"
        "ds_read_b128 v[88:91], %[pb1] offset:14336\n\tv_mfma_f32_16x16x32_bf16 v[60:63], v[244:247], v[92:95], v[60:63]\n\ts_waitcnt vmcnt(10) lgkmcnt(3)\n\t"
        "v_mfma_f32_16x16x32_bf16 v[16:19], v[64:67], v[152:155], v[16:19]\n\tds_read_b128 v[92:95], %[pb1] offset:15360\n\t"
        "buffer_load_dwordx4 v[200:203], off, %[rt], %[sa] offset:0 sc1\n\tv_mfma_f32_16x16x32_bf16 v[20:23], v[64:67], v[156:159], v[20:23]\n\t"
        "v_mfma_f32_16x16x32_bf16 v[24:27], v[68:71], v[152:155], v[24:27]\n\tds_read_b128 v[64:67], %[pb1] offset:16384\n\t"
        "v_mfma_f32_16x16x32_bf16 v[28:31], v[68:71], v[156:159], v[28:31]\n\ts_add_u32 %[xu], %[xu], 0x800000\n\t"
        "v_mfma_f32_16x16x32_bf16 v[32:35], v[72:75], v[152:155], v[32:35]\n\tds_read_b128 v[68:71], %[pb1] offset:17408\n\t"
        "v_mfma_f32_16x16x32_bf16 v[36:39], v[72:75], v[156:159], v[36:39]\n\tbuffer_load_dwordx4 v[204:207], off, %[rt], %[sa] offset:1024 sc1\n\t"
        "s_add_u32 %[sa], %[sa], 0x2000\n\tv_mfma_f32_16x16x32_bf16 v[40:43], v[76:79], v[152:155], v[40:43]\n\tds_read_b128 v[72:75], %[pb1] offset:18432\n\t"
        "v_mfma_f32_16x16x32_bf16 v[44:47], v[76:79], v[156:159], v[44:47]\n\ts_waitcnt lgkmcnt(3)\n\t"
        "v_mfma_f32_16x16x32_bf16 v[48:51], v[152:155], v[80:83], v[48:51]\n\tds_read_b128 v[76:79], %[pb1] offset:19456\n\t"
        "v_mfma_f32_16x16x32_bf16 v[52:55], v[156:159], v[80:83], v[52:55]\n\tv_mfma_f32_16x16x32_bf16 v[56:59], v[152:155], v[84:87], v[56:59]\n\t"
        "ds_read_b128 v[80:83], %[pb1] offset:20480\n\tbuffer_load_dwordx4 v[208:211], off, %[rt], %[sa] offset:0 sc1\n\t"
        "v_mfma_f32_16x16x32_bf16 v[60:63], v[156:159], v[84:87], v[60:63]\n\ts_add_u32 %[fh], %[fh], 0x10000\n\ts_waitcnt vmcnt(11)\n\t"
        "v_mfma_f32_16x16x32_bf16 v[16:19], v[88:91], v[160:163], v[16:19]\n\tds_read_b128 v[84:87], %[pb1] offset:21504\n\t"
        "v_mfma_f32_16x16x32_bf16 v[20:23], v[88:91], v[164:167], v[20:23]\n\tv_mfma_f32_16x16x32_bf16 v[24:27], v[92:95], v[160:163], v[24:27]\n\t"
        "ds_read_b128 v[88:91], %[pb1] offset:22528\n\tv_mfma_f32_16x16x32_bf16 v[28:31], v[92:95], v[164:167], v[28:31]\n\t"
        "buffer_load_dwordx4 v[212:215], off, %[rt], %[sa] offset:1024 sc1\n\ts_add_u32 %[sa], %[sa], 0x2000\n\ts_waitcnt lgkmcnt(3)\n\t"
        "v_mfma_f32_16x16x32_bf16 v[32:35], v[64:67], v[160:163], v[32:35]\n\tds_read_b128 v[92:95], %[pb1] offset:23552\n\t"
        "v_mfma_f32_16x16x32_bf16 v[36:39], v[64:67], v[164:167], v[36:39]\n\tv_mfma_f32_16x16x32_bf16 v[40:43], v[68:71], v[160:163], v[40:43]\n\t"
        "ds_read_b128 v[64:67], %[pb1] offset:24576\n\tv_mfma_f32_16x16x32_bf16 v[44:47], v[68:71], v[164:167], v[44:47]\n\t"
        "v_mfma_f32_16x16x32_bf16 v[48:51], v[160:163], v[72:75], v[48:51]\n\tds_read_b128 v[68:71], %[pb1] offset:25600\n\t"
        "buffer_load_dwordx4 v[216:219], off, %[rt], %[sa] offset:0 sc1\n\tv_mfma_f32_16x16x32_bf16 v[52:55], v[164:167], v[72:75], v[52:55]\n\t"
        "v_mfma_f32_16x16x32_bf16 v[56:59], v[160:163], v[76:79], v[56:59]\n\tds_read_b128 v[72:75], %[pb1] offset:26624\n\t"
        "v_mfma_f32_16x16x32_bf16 v[60:63], v[164:167], v[76:79], v[60:63]\n\ts_waitcnt vmcnt(11) lgkmcnt(3)\n\t"
        "v_mfma_f32_16x16x32_bf16 v[16:19], v[80:83], v[168:171], v[16:19]\n\tds_read_b128 v[76:79], %[pb1] offset:27648\n\t"
        "v_mfma_f32_16x16x32_bf16 v[20:23], v[80:83], v[172:175], v[20:23]\n\tbuffer_load_dwordx4 v[220:223], off, %[rt], %[sa] offset:1024 sc1\n\t"
        "s_add_u32 %[sa], %[sa], 0x2000\n\tv_mfma_f32_16x16x32_bf16 v[24:27], v[84:87], v[168:171], v[24:27]\n\tds_read_b128 v[80:83], %[pb1] offset:28672\n\t"
        "v_mfma_f32_16x16x32_bf16 v[28:31], v[84:87], v[172:175], v[28:31]\n\tv_mfma_f32_16x16x32_bf16 v[32:35], v[88:91], v[168:171], v[32:35]\n\t"
        "ds_read_b128 v[84:87], %[pb1] offset:29696\n\tv_mfma_f32_16x16x32_bf16 v[36:39], v[88:91], v[172:175], v[36:39]\n\t"
        "v_mfma_f32_16x16x32_bf16 v[40:43], v[92:95], v[168:171], v[40:43]\n\tds_read_b128 v[88:91], %[pb1] offset:30720\n\t"
        "buffer_load_dwordx4 v[224:227], off, %[rt], %[sa] offset:0 sc1\n\tv_mfma_f32_16x16x32_bf16 v[44:47], v[92:95], v[172:175], v[44:47]\n\t"
        "s_waitcnt lgkmcnt(3)\n\tv_mfma_f32_16x16x32_bf16 v[48:51], v[168:171], v[64:67], v[48:51]\n\tds_read_b128 v[92:95], %[pb1] offset:31744\n\t"
        "v_mfma_f32_16x16x32_bf16 v[52:55], v[172:175], v[64:67], v[52:55]\n\tv_mfma_f32_16x16x32_bf16 v[56:59], v[168:171], v[68:71], v[56:59]\n\t"
        "ds_read_b128 v[64:67], %[pb1] offset:32768\n\tv_mfma_f32_16x16x32_bf16 v[60:63], v[172:175], v[68:71], v[60:63]\n\t"
        "buffer_load_dwordx4 v[228:231], off, %[rt], %[sa] offset:1024 sc1\n\ts_add_u32 %[sa], %[sa], 0x2000\n\ts_waitcnt vmcnt(12)\n\t"
        "v_mfma_f32_16x16x32_bf16 v[16:19], v[72:75], v[176:179], v[16:19]\n\tds_read_b128 v[68:71], %[pb1] offset:33792\n\t"
        "v_mfma_f32_16x16x32_bf16 v[20:23], v[72:75], v[180:183], v[20:23]\n\tv_mfma_f32_16x16x32_bf16 v[24:27], v[76:79], v[176:179], v[24:27]\n\t"
        "ds_read_b128 v[72:75], %[pb1] offset:34816\n\tv_mfma_f32_16x16x32_bf16 v[28:31], v[76:79], v[180:183], v[28:31]\n\ts_waitcnt lgkmcnt(3)\n\t"
        "v_mfma_f32_16x16x32_bf16 v[32:35], v[80:83], v[176:179], v[32:35]\n\tds_read_b128 v[76:79], %[pb1] offset:35840\n\t"
        "buffer_load_dwordx4 v[232:235], off, %[rt], %[sa] offset:0 sc1\n\tv_mfma_f32_16x16x32_bf16 v[36:39], v[80:83], v[180:183], v[36:39]\n\t"
        "v_mfma_f32_16x16x32_bf16 v[40:43], v[84:87], v[176:179], v[40:43]\n\tds_read_b128 v[80:83], %[pb1] offset:36864\n\t"
        "v_mfma_f32_16x16x32_bf16 v[44:47], v[84:87], v[180:183], v[44:47]\n\tv_mfma_f32_16x16x32_bf16 v[48:51], v[176:179], v[88:91], v[48:51]\n\t"
        "ds_read_b128 v[84:87], %[pb1] offset:37888\n\tv_mfma_f32_16x16x32_bf16 v[52:55], v[180:183], v[88:91], v[52:55]\n\t"
        "buffer_load_dwordx4 v[236:239], off, %[rt], %[sa] offset:1024 sc1\n\ts_add_u32 %[sa], %[sa], 0x2000\n\t"
        "v_mfma_f32_16x16x32_bf16 v[56:59], v[176:179], v[92:95], v[56:59]\n\tds_read_b128 v[88:91], %[pb1] offset:38912\n\t"
        "v_mfma_f32_16x16x32_bf16 v[60:63], v[180:183], v[92:95], v[60:63]\n\ts_waitcnt vmcnt(12) lgkmcnt(3)\n\t"
        "v_mfma_f32_16x16x32_bf16 v[16:19], v[64:67], v[184:187], v[16:19]\n\tds_read_b128 v[92:95], %[pb1] offset:39936\n\t"
        "v_mfma_f32_16x16x32_bf16 v[20:23], v[64:67], v[188:191], v[20:23]\n\tv_mfma_f32_16x16x32_bf16 v[24:27], v[68:71], v[184:187], v[24:27]\n\t"
        "ds_read_b128 v[64:67], %[pb1] offset:40960\n\tbuffer_load_dwordx4 v[240:243], off, %[rt], %[sa] offset:0 sc1\n\t"
        "v_mfma_f32_16x16x32_bf16 v[28:31], v[68:71], v[188:191], v[28:31]\n\tv_mfma_f32_16x16x32_bf16 v[32:35], v[72:75], v[184:187], v[32:35]\n\t"
        "ds_read_b128 v[68:71], %[pb1] offset:41984\n\tv_mfma_f32_16x16x32_bf16 v[36:39], v[72:75], v[188:191], v[36:39]\n\t"
        "v_mfma_f32_16x16x32_bf16 v[40:43], v[76:79], v[184:187], v[40:43]\n\tds_read_b128 v[72:75], %[pb1] offset:43008\n\t"
        "v_mfma_f32_16x16x32_bf16 v[44:47], v[76:79], v[188:191], v[44:47]\n\tbuffer_load_dwordx4 v[244:247], off, %[rt], %[sa] offset:1024 sc1\n\t"
        "s_add_u32 %[sa], %[sa], 0x2000\n\ts_waitcnt lgkmcnt(3)\n\tv_mfma_f32_16x16x32_bf16 v[48:51], v[184:187], v[80:83], v[48:51]\n\t"
        "ds_read_b128 v[76:79], %[pb1] offset:44032\n\tv_mfma_f32_16x16x32_bf16 v[52:55], v[188:191], v[80:83], v[52:55]\n\t"
        "v_mfma_f32_16x16x32_bf16 v[56:59], v[184:187], v[84:87], v[56:59]\n\tds_read_b128 v[80:83], %[pb1] offset:45056\n\t"
        "v_mfma_f32_16x16x32_bf16 v[60:63], v[188:191], v[84:87], v[60:63]\n\ts_waitcnt vmcnt(12)\n\t"
        "v_mfma_f32_16x16x32_bf16 v[16:19], v[88:91], v[192:195], v[16:19]\n\tds_read_b128 v[84:87], %[pb1] offset:46080\n\t"
        "buffer_load_dwordx4 v[152:155], off, %[rt], %[sa] offset:0 sc1\n\tv_mfma_f32_16x16x32_bf16 v[20:23], v[88:91], v[196:199], v[20:23]\n\t"
        "v_mfma_f32_16x16x32_bf16 v[24:27], v[92:95], v[192:195], v[24:27]\n\tds_read_b128 v[88:91], %[pb1] offset:47104\n\t"
        "v_mfma_f32_16x16x32_bf16 v[28:31], v[92:95], v[196:199], v[28:31]\n\ts_waitcnt lgkmcnt(3)\n\t"
        "v_mfma_f32_16x16x32_bf16 v[32:35], v[64:67], v[192:195], v[32:35]\n\tds_read_b128 v[92:95], %[pb1] offset:48128\n\t"
        "v_mfma_f32_16x16x32_bf16 v[36:39], v[64:67], v[196:199], v[36:39]\n\tbuffer_load_dwordx4 v[156:159], off, %[rt], %[sa] offset:1024 sc1\n\t"
        "s_add_u32 %[sa], %[sa], 0x2000\n\tv_mfma_f32_16x16x32_bf16 v[40:43], v[68:71], v[192:195], v[40:43]\n\tds_read_b128 v[64:67], %[pb1] offset:49152\n\t"
        "v_mfma_f32_16x16x32_bf16 v[44:47], v[68:71], v[196:199], v[44:47]\n\tv_mfma_f32_16x16x32_bf16 v[48:51], v[192:195], v[72:75], v[48:51]\n\t"
        "ds_read_b128 v[68:71], %[pb1] offset:50176\n\tv_mfma_f32_16x16x32_bf16 v[52:55], v[196:199], v[72:75], v[52:55]\n\t"
        "v_mfma_f32_16x16x32_bf16 v[56:59], v[192:195], v[76:79], v[56:59]\n\tds_read_b128 v[72:75], %[pb1] offset:51200\n\t"
        "buffer_load_dwordx4 v[160:163], off, %[rt], %[sa] offset:0 sc1\n\tv_mfma_f32_16x16x32_bf16 v[60:63], v[196:199], v[76:79], v[60:63]\n\t"
        "s_waitcnt vmcnt(13) lgkmcnt(3)\n\tv_mfma_f32_16x16x32_bf16 v[16:19], v[80:83], v[200:203], v[16:19]\n\tds_read_b128 v[76:79], %[pb1] offset:52224\n\t"
        "v_mfma_f32_16x16x32_bf16 v[20:23], v[80:83], v[204:207], v[20:23]\n\tv_mfma_f32_16x16x32_bf16 v[24:27], v[84:87], v[200:203], v[24:27]\n\t"
        "ds_read_b128 v[80:83], %[pb1] offset:53248\n\tv_mfma_f32_16x16x32_bf16 v[28:31], v[84:87], v[204:207], v[28:31]\n\t"
        "buffer_load_dwordx4 v[164:167], off, %[rt], %[sa] offset:1024 sc1\n\ts_add_u32 %[sa], %[sa], 0x2000\n\t"
        "v_mfma_f32_16x16x32_bf16 v[32:35], v[88:91], v[200:203], v[32:35]\n\tds_read_b128 v[84:87], %[pb1] offset:54272\n\t"
        "v_mfma_f32_16x16x32_bf16 v[36:39], v[88:91], v[204:207], v[36:39]\n\tv_mfma_f32_16x16x32_bf16 v[40:43], v[92:95], v[200:203], v[40:43]\n\t"
        "ds_read_b128 v[88:91], %[pb1] offset:55296\n\tv_mfma_f32_16x16x32_bf16 v[44:47], v[92:95], v[204:207], v[44:47]\n\ts_waitcnt lgkmcnt(3)\n\t"
        "v_mfma_f32_16x16x32_bf16 v[48:51], v[200:203], v[64:67], v[48:51]\n\tds_read_b128 v[92:95], %[pb1] offset:56320\n\t"
        "buffer_load_dwordx4 v[168:171], off, %[rt], %[sa] offset:0 sc1\n\tv_mfma_f32_16x16x32_bf16 v[52:55], v[204:207], v[64:67], v[52:55]\n\t"
        "v_mfma_f32_16x16x32_bf16 v[56:59], v[200:203], v[68:71], v[56:59]\n\tds_read_b128 v[64:67], %[pb1] offset:57344\n\t"
        "v_mfma_f32_16x16x32_bf16 v[60:63], v[204:207], v[68:71], v[60:63]\n\ts_waitcnt vmcnt(13)\n\t"
        "v_mfma_f32_16x16x32_bf16 v[16:19], v[72:75], v[208:211], v[16:19]\n\tds_read_b128 v[68:71], %[pb1] offset:58368\n\t"
        "v_mfma_f32_16x16x32_bf16 v[20:23], v[72:75], v[212:215], v[20:23]\n\tbuffer_load_dwordx4 v[172:175], off, %[rt], %[sa] offset:1024 sc1\n\t"
        "s_add_u32 %[sa], %[sa], 0x2000\n\tv_mfma_f32_16x16x32_bf16 v[24:27], v[76:79], v[208:211], v[24:27]\n\tds_read_b128 v[72:75], %[pb1] offset:59392\n\t"
        "v_mfma_f32_16x16x32_bf16 v[28:31], v[76:79], v[212:215], v[28:31]\n\ts_waitcnt lgkmcnt(3)\n\t"
        "v_mfma_f32_16x16x32_bf16 v[32:35], v[80:83], v[208:211], v[32:35]\n\tds_read_b128 v[76:79], %[pb1] offset:60416\n\t"
        "v_mfma_f32_16x16x32_bf16 v[36:39], v[80:83], v[212:215], v[36:39]\n\tv_mfma_f32_16x16x32_bf16 v[40:43], v[84:87], v[208:211], v[40:43]\n\t"
        "ds_read_b128 v[80:83], %[pb1] offset:61440\n\tbuffer_load_dwordx4 v[176:179], off, %[rt], %[sa] offset:0 sc1\n\t"
        "v_mfma_f32_16x16x32_bf16 v[44:47], v[84:87], v[212:215], v[44:47]\n\tv_mfma_f32_16x16x32_bf16 v[48:51], v[208:211], v[88:91], v[48:51]\n\t"
        "ds_read_b128 v[84:87], %[pb1] offset:62464\n\tv_mfma_f32_16x16x32_bf16 v[52:55], v[212:215], v[88:91], v[52:55]\n\t"
        "v_mfma_f32_16x16x32_bf16 v[56:59], v[208:211], v[92:95], v[56:59]\n\tds_read_b128 v[88:91], %[pb1] offset:63488\n\t"
        "v_mfma_f32_16x16x32_bf16 v[60:63], v[212:215], v[92:95], v[60:63]\n\tbuffer_load_dwordx4 v[180:183], off, %[rt], %[sa] offset:1024 sc1\n\t"
        "s_add_u32 %[sa], %[sa], 0x2000\n\ts_waitcnt vmcnt(14) lgkmcnt(3)\n\tv_mfma_f32_16x16x32_bf16 v[16:19], v[64:67], v[216:219], v[16:19]\n\t"
        "ds_read_b128 v[92:95], %[pb1] offset:64512\n\tv_mfma_f32_16x16x32_bf16 v[20:23], v[64:67], v[220:223], v[20:23]\n\t"
        "v_mfma_f32_16x16x32_bf16 v[24:27], v[68:71], v[216:219], v[24:27]\n\tds_read_b128 v[64:67], %[pb2] offset:0\n\t"
        "v_mfma_f32_16x16x32_bf16 v[28:31], v[68:71], v[220:223], v[28:31]\n\tv_mfma_f32_16x16x32_bf16 v[32:35], v[72:75], v[216:219], v[32:35]\n\t"
        "ds_read_b128 v[68:71], %[pb2] offset:1024\n\tbuffer_load_dwordx4 v[184:187], off, %[rt], %[sa] offset:0 sc1\n\t"
        "v_mfma_f32_16x16x32_bf16 v[36:39], v[72:75], v[220:223], v[36:39]\n\tv_mfma_f32_16x16x32_bf16 v[40:43], v[76:79], v[216:219], v[40:43]\n\t"
        "ds_read_b128 v[72:75], %[pb2] offset:2048\n\tv_mfma_f32_16x16x32_bf16 v[44:47], v[76:79], v[220:223], v[44:47]\n\ts_waitcnt lgkmcnt(3)\n\t"
        "v_mfma_f32_16x16x32_bf16 v[48:51], v[216:219], v[80:83], v[48:51]\n\tds_read_b128 v[76:79], %[pb2] offset:3072\n\t"
        "v_mfma_f32_16x16x32_bf16 v[52:55], v[220:223], v[80:83], v[52:55]\n\tbuffer_load_dwordx4 v[188:191], off, %[rt], %[sa] offset:1024 sc1\n\t"
        "s_add_u32 %[sa], %[sa], 0x2000\n\tv_mfma_f32_16x16x32_bf16 v[56:59], v[216:219], v[84:87], v[56:59]\n\tds_read_b128 v[80:83], %[pb2] offset:4096\n\t"
        "v_mfma_f32_16x16x32_bf16 v[60:63], v[220:223], v[84:87], v[60:63]\n\ts_waitcnt vmcnt(14)\n\t"
        "v_mfma_f32_16x16x32_bf16 v[16:19], v[88:91], v[224:227], v[16:19]\n\tds_read_b128 v[84:87], %[pb2] offset:5120\n\t"
        "v_mfma_f32_16x16x32_bf16 v[20:23], v[88:91], v[228:231], v[20:23]\n\tv_mfma_f32_16x16x32_bf16 v[24:27], v[92:95], v[224:227], v[24:27]\n\t"
        "ds_read_b128 v[88:91], %[pb2] offset:6144\n\tbuffer_load_dwordx4 v[192:195], off, %[rt], %[sa] offset:0 sc1\n\t"
        "v_mfma_f32_16x16x32_bf16 v[28:31], v[92:95], v[228:231], v[28:31]\n\ts_waitcnt lgkmcnt(3)\n\t"
        "v_mfma_f32_16x16x32_bf16 v[32:35], v[64:67], v[224:227], v[32:35]\n\tds_read_b128 v[92:95], %[pb2] offset:7168\n\t"
        "v_mfma_f32_16x16x32_bf16 v[36:39], v[64:67], v[228:231], v[36:39]\n\tv_mfma_f32_16x16x32_bf16 v[40:43], v[68:71], v[224:227], v[40:43]\n\t"
        "ds_read_b128 v[64:67], %[pb2] offset:8192\n\tv_mfma_f32_16x16x32_bf16 v[44:47], v[68:71], v[228:231], v[44:47]\n\t"
        "buffer_load_dwordx4 v[196:199], off, %[rt], %[sa] offset:1024 sc1\n\ts_add_u32 %[sa], %[sa], 0x2000\n\t"
        "v_mfma_f32_16x16x32_bf16 v[48:51], v[224:227], v[72:75], v[48:51]\n\tds_read_b128 v[68:71], %[pb2] offset:9216\n\t"
        "v_mfma_f32_16x16x32_bf16 v[52:55], v[228:231], v[72:75], v[52:55]\n\tv_mfma_f32_16x16x32_bf16 v[56:59], v[224:227], v[76:79], v[56:59]\n\t"
        "ds_read_b128 v[72:75], %[pb2] offset:10240\n\tv_mfma_f32_16x16x32_bf16 v[60:63], v[228:231], v[76:79], v[60:63]\n\ts_waitcnt vmcnt(14) lgkmcnt(3)\n\t"
        "v_mfma_f32_16x16x32_bf16 v[16:19], v[80:83], v[232:235], v[16:19]\n\tds_read_b128 v[76:79], %[pb2] offset:11264\n\t"
        "buffer_load_dwordx4 v[200:203], off, %[rt], %[sa] offset:0 sc1\n\tv_mfma_f32_16x16x32_bf16 v[20:23], v[80:83], v[236:239], v[20:23]\n\t"
        "v_mfma_f32_16x16x32_bf16 v[24:27], v[84:87], v[232:235], v[24:27]\n\tds_read_b128 v[80:83], %[pb2] offset:12288\n\t"
        "v_mfma_f32_16x16x32_bf16 v[28:31], v[84:87], v[236:239], v[28:31]\n\tv_mfma_f32_16x16x32_bf16 v[32:35], v[88:91], v[232:235], v[32:35]\n\t"
        "ds_read_b128 v[84:87], %[pb2] offset:13312\n\tv_mfma_f32_16x16x32_bf16 v[36:39], v[88:91], v[236:239], v[36:39]\n\t"
        "buffer_load_dwordx4 v[204:207], off, %[rt], %[sa] offset:1024 sc1\n\ts_add_u32 %[sa], %[sa], 0x2000\n\t"
        "v_mfma_f32_16x16x32_bf16 v[40:43], v[92:95], v[232:235], v[40:43]\n\tds_read_b128 v[88:91], %[pb2] offset:14336\n\t"
        "v_mfma_f32_16x16x32_bf16 v[44:47], v[92:95], v[236:239], v[44:47]\n\ts_waitcnt lgkmcnt(3)\n\t"
        "v_mfma_f32_16x16x32_bf16 v[48:51], v[232:235], v[64:67], v[48:51]\n\tds_read_b128 v[92:95], %[pb2] offset:15360\n\t"
        "v_mfma_f32_16x16x32_bf16 v[52:55], v[236:239], v[64:67], v[52:55]\n\tv_mfma_f32_16x16x32_bf16 v[56:59], v[232:235], v[68:71], v[56:59]\n\t"
        "s_barrier\n\tds_read_b128 v[64:67], %[pb0] offset:0\n\tbuffer_load_dwordx4 v[208:211], off, %[rt], %[sa] offset:0 sc1\n\t"
        "v_mfma_f32_16x16x32_bf16 v[60:63], v[236:239], v[68:71], v[60:63]\n\ts_waitcnt vmcnt(15)\n\t"
        "v_mfma_f32_16x16x32_bf16 v[16:19], v[72:75], v[240:243], v[16:19]\n\tds_read_b128 v[68:71], %[pb0] offset:1024\n\t"
        "v_mfma_f32_16x16x32_bf16 v[20:23], v[72:75], v[244:247], v[20:23]\n\tv_mfma_f32_16x16x32_bf16 v[24:27], v[76:79], v[240:243], v[24:27]\n\t"
        "ds_read_b128 v[72:75], %[pb0] offset:2048\n\tv_mfma_f32_16x16x32_bf16 v[28:31], v[76:79], v[244:247], v[28:31]\n\t"
        "buffer_load_dwordx4 v[212:215], off, %[rt], %[sa] offset:1024 sc1\n\ts_waitcnt lgkmcnt(3)\n\t"
        "v_mfma_f32_16x16x32_bf16 v[32:35], v[80:83], v[240:243], v[32:35]\n\tds_read_b128 v[76:79], %[pb0] offset:3072\n\t"
        "v_mfma_f32_16x16x32_bf16 v[36:39], v[80:83], v[244:247], v[36:39]\n\tv_mfma_f32_16x16x32_bf16 v[40:43], v[84:87], v[240:243], v[40:43]\n\t"
        "ds_read_b128 v[80:83], %[pb0] offset:4096\n\tv_mfma_f32_16x16x32_bf16 v[44:47], v[84:87], v[244:247], v[44:47]\n\t"
        "v_mfma_f32_16x16x32_bf16 v[48:51], v[240:243], v[88:91], v[48:51]\n\tds_read_b128 v[84:87], %[pb0] offset:5120\n\t"
        "v_mfma_f32_16x16x32_bf16 v[52:55], v[244:247], v[88:91], v[52:55]\n\tv_mfma_f32_16x16x32_bf16 v[56:59], v[240:243], v[92:95], v[56:59]\n\t"
        "ds_read_b128 v[88:91], %[pb0] offset:6144\n\tv_mfma_f32_16x16x32_bf16 v[60:63], v[244:247], v[92:95], v[60:63]\n\ts_waitcnt vmcnt(14) lgkmcnt(3)\n\t"
        "v_mfma_f32_16x16x32_bf16 v[16:19], v[64:67], v[152:155], v[16:19]\n\tds_read_b128 v[92:95], %[pb0] offset:7168\n\t"
        "v_mfma_f32_16x16x32_bf16 v[20:23], v[64:67], v[156:159], v[20:23]\n\tv_mfma_f32_16x16x32_bf16 v[24:27], v[68:71], v[152:155], v[24:27]\n\t"
        "ds_read_b128 v[64:67], %[pb0] offset:8192\n\tv_mfma_f32_16x16x32_bf16 v[28:31], v[68:71], v[156:159], v[28:31]\n\t"
        "v_mfma_f32_16x16x32_bf16 v[32:35], v[72:75], v[152:155], v[32:35]\n\tds_read_b128 v[68:71], %[pb0] offset:9216\n\t"
        "v_mfma_f32_16x16x32_bf16 v[36:39], v[72:75], v[156:159], v[36:39]\n\tv_mfma_f32_16x16x32_bf16 v[40:43], v[76:79], v[152:155], v[40:43]\n\t"
        "ds_read_b128 v[72:75], %[pb0] offset:10240\n\tv_mfma_f32_16x16x32_bf16 v[44:47], v[76:79], v[156:159], v[44:47]\n\ts_waitcnt lgkmcnt(3)\n\t"
        "v_mfma_f32_16x16x32_bf16 v[48:51], v[152:155], v[80:83], v[48:51]\n\tds_read_b128 v[76:79], %[pb0] offset:11264\n\t"
        "v_mfma_f32_16x16x32_bf16 v[52:55], v[156:159], v[80:83], v[52:55]\n\tv_mfma_f32_16x16x32_bf16 v[56:59], v[152:155], v[84:87], v[56:59]\n\t"
        "ds_read_b128 v[80:83], %[pb0] offset:12288\n\tv_mfma_f32_16x16x32_bf16 v[60:63], v[156:159], v[84:87], v[60:63]\n\ts_waitcnt vmcnt(12)\n\t"
        "v_mfma_f32_16x16x32_bf16 v[16:19], v[88:91], v[160:163], v[16:19]\n\tds_read_b128 v[84:87], %[pb0] offset:13312\n\tv_subrev_u32 v10, %[w128], v253\n\t"
        "v_mfma_f32_16x16x32_bf16 v[20:23], v[88:91], v[164:167], v[20:23]\n\tds_read_b32 v8, v253 offset:0\n\tds_read_b32 v9, v253 offset:64\n\t"
        "v_mfma_f32_16x16x32_bf16 v[24:27], v[92:95], v[160:163], v[24:27]\n\tds_read_b128 v[88:91], %[pb0] offset:14336\n\t"
        "ds_read_b32 v2, v253 offset:1024\n\tv_mfma_f32_16x16x32_bf16 v[28:31], v[92:95], v[164:167], v[28:31]\n\tds_read_b32 v3, v253 offset:1088\n\t"
        "s_waitcnt lgkmcnt(7)\n\tv_mfma_f32_16x16x32_bf16 v[32:35], v[64:67], v[160:163], v[32:35]\n\tds_read_b128 v[92:95], %[pb0] offset:15360\n\t"
        "ds_read_b128 v[96:99], v255 offset:2176\n\tv_mfma_f32_16x16x32_bf16 v[36:39], v[64:67], v[164:167], v[36:39]\n\t"
        "ds_read_b128 v[100:103], v255 offset:2240\n\tds_read_b128 v[104:107], v255 offset:2048\n\t"
        "v_mfma_f32_16x16x32_bf16 v[40:43], v[68:71], v[160:163], v[40:43]\n\tds_read_b128 v[64:67], %[pb0] offset:16384\n\t"
        "ds_read_b128 v[108:111], v255 offset:2112\n\tv_mfma_f32_16x16x32_bf16 v[44:47], v[68:71], v[164:167], v[44:47]\n\tds_read_b32 v0, v10 offset:2304\n\t"
        "ds_read_b32 v1, v10 offset:2368\n\tv_mfma_f32_16x16x32_bf16 v[48:51], v[160:163], v[72:75], v[48:51]\n\tds_read_b128 v[68:71], %[pb0] offset:17408\n\t"
        "ds_read_b128 v[120:123], v254 offset:1024\n\tv_mfma_f32_16x16x32_bf16 v[52:55], v[164:167], v[72:75], v[52:55]\n\t"
        "ds_read_b128 v[124:127], v254 offset:1088\n\tds_read_b128 v[112:115], v254 offset:0\n\t"
        "v_mfma_f32_16x16x32_bf16 v[56:59], v[160:163], v[76:79], v[56:59]\n\tds_read_b128 v[72:75], %[pb0] offset:18432\n\t"
        "ds_read_b128 v[116:119], v254 offset:64\n\tv_mfma_f32_16x16x32_bf16 v[60:63], v[164:167], v[76:79], v[60:63]\n\ts_waitcnt vmcnt(10) lgkmcnt(13)\n\t"
        "v_mfma_f32_16x16x32_bf16 v[16:19], v[80:83], v[168:171], v[16:19]\n\tds_read_b128 v[76:79], %[pb0] offset:19456\n\t"
        "v_mfma_f32_16x16x32_bf16 v[20:23], v[80:83], v[172:175], v[20:23]\n\tv_mfma_f32_16x16x32_bf16 v[24:27], v[84:87], v[168:171], v[24:27]\n\t"
        "ds_read_b128 v[80:83], %[pb0] offset:20480\n\tv_mfma_f32_16x16x32_bf16 v[28:31], v[84:87], v[172:175], v[28:31]\n\t"
        "v_mfma_f32_16x16x32_bf16 v[32:35], v[88:91], v[168:171], v[32:35]\n\tds_read_b128 v[84:87], %[pb0] offset:21504\n\t"
        "v_mfma_f32_16x16x32_bf16 v[36:39], v[88:91], v[172:175], v[36:39]\n\tv_mfma_f32_16x16x32_bf16 v[40:43], v[92:95], v[168:171], v[40:43]\n\t"
        "ds_read_b128 v[88:91], %[pb0] offset:22528\n\tv_mfma_f32_16x16x32_bf16 v[44:47], v[92:95], v[172:175], v[44:47]\n\ts_waitcnt lgkmcnt(3)\n\t"
        "v_mfma_f32_16x16x32_bf16 v[48:51], v[168:171], v[64:67], v[48:51]\n\tds_read_b128 v[92:95], %[pb0] offset:23552\n\tv_mul_f32 v4, 0x3e8293ee, v8\n\t"
        "v_mfma_f32_16x16x32_bf16 v[52:55], v[172:175], v[64:67], v[52:55]\n\tv_mul_f32 v5, 0x3e8293ee, v9\n\tv_mul_f32 v6, 0x3e8293ee, v2\n\t"
        "v_mfma_f32_16x16x32_bf16 v[56:59], v[168:171], v[68:71], v[56:59]\n\tds_read_b128 v[64:67], %[pb0] offset:24576\n\tv_mul_f32 v7, 0x3e8293ee, v3\n\t"
        "v_mfma_f32_16x16x32_bf16 v[60:63], v[172:175], v[68:71], v[60:63]\n\tv_mul_f32 v128, v2, v96\n\ts_waitcnt vmcnt(8)\n\t"
        "v_mfma_f32_16x16x32_bf16 v[16:19], v[72:75], v[176:179], v[16:19]\n\tds_read_b128 v[68:71], %[pb0] offset:25600\n\tv_mul_f32 v129, v2, v97\n\t"
        "v_mfma_f32_16x16x32_bf16 v[20:23], v[72:75], v[180:183], v[20:23]\n\tv_mul_f32 v130, v2, v98\n\tv_mul_f32 v131, v2, v99\n\t"
        "v_mfma_f32_16x16x32_bf16 v[24:27], v[76:79], v[176:179], v[24:27]\n\tds_read_b128 v[72:75], %[pb0] offset:26624\n\tv_mul_f32 v132, v2, v100\n\t"
        "v_mfma_f32_16x16x32_bf16 v[28:31], v[76:79], v[180:183], v[28:31]\n\tv_mul_f32 v133, v2, v101\n\ts_waitcnt lgkmcnt(3)\n\t"
        "v_mfma_f32_16x16x32_bf16 v[32:35], v[80:83], v[176:179], v[32:35]\n\tds_read_b128 v[76:79], %[pb0] offset:27648\n\tv_mul_f32 v134, v2, v102\n\t"
        "v_mfma_f32_16x16x32_bf16 v[36:39], v[80:83], v[180:183], v[36:39]\n\tv_mul_f32 v135, v2, v103\n\tv_mul_f32 v96, v3, v96\n\t"
        "v_mfma_f32_16x16x32_bf16 v[40:43], v[84:87], v[176:179], v[40:43]\n\tds_read_b128 v[80:83], %[pb0] offset:28672\n\tv_mul_f32 v97, v3, v97\n\t"
        "v_mfma_f32_16x16x32_bf16 v[44:47], v[84:87], v[180:183], v[44:47]\n\tv_mul_f32 v98, v3, v98\n\tv_mul_f32 v99, v3, v99\n\t"
        "v_mfma_f32_16x16x32_bf16 v[48:51], v[176:179], v[88:91], v[48:51]\n\tds_read_b128 v[84:87], %[pb0] offset:29696\n\tv_mul_f32 v100, v3, v100\n\t"
        "v_mfma_f32_16x16x32_bf16 v[52:55], v[180:183], v[88:91], v[52:55]\n\tv_mul_f32 v101, v3, v101\n\tv_mul_f32 v102, v3, v102\n\t"
        "v_mfma_f32_16x16x32_bf16 v[56:59], v[176:179], v[92:95], v[56:59]\n\ts_barrier\n\tds_read_b128 v[88:91], %[pb0] offset:30720\n\t"
        "buffer_load_dwordx4 v[216:219], off, %[rn], %[sn] offset:0 sc1\n\tv_mfma_f32_16x16x32_bf16 v[60:63], v[180:183], v[92:95], v[60:63]\n\t"
        "v_mul_f32 v103, v3, v103\n\ts_waitcnt vmcnt(7) lgkmcnt(3)\n\tv_mfma_f32_16x16x32_bf16 v[16:19], v[64:67], v[184:187], v[16:19]\n\t"
        "ds_read_b128 v[92:95], %[pb0] offset:31744\n\tv_mul_f32 v136, v6, v104\n\tv_mfma_f32_16x16x32_bf16 v[20:23], v[64:67], v[188:191], v[20:23]\n\t"
        "v_mul_f32 v137, v6, v105\n\tv_mul_f32 v138, v6, v106\n\tv_mfma_f32_16x16x32_bf16 v[24:27], v[68:71], v[184:187], v[24:27]\n\t"
        "ds_read_b128 v[64:67], %[pb0] offset:32768\n\tv_mul_f32 v139, v6, v107\n\tv_mfma_f32_16x16x32_bf16 v[28:31], v[68:71], v[188:191], v[28:31]\n\t"
        "buffer_load_dwordx4 v[220:223], off, %[rn], %[sn] offset:1024 sc1\n\ts_add_u32 %[sn], %[sn], 0x2000\n\t"
        "v_mfma_f32_16x16x32_bf16 v[32:35], v[72:75], v[184:187], v[32:35]\n\tds_read_b128 v[68:71], %[pb0] offset:33792\n\tv_mul_f32 v140, v6, v108\n\t"
        "v_mfma_f32_16x16x32_bf16 v[36:39], v[72:75], v[188:191], v[36:39]\n\tv_mul_f32 v141, v6, v109\n\tv_mul_f32 v142, v6, v110\n\t"
        "v_mfma_f32_16x16x32_bf16 v[40:43], v[76:79], v[184:187], v[40:43]\n\tds_read_b128 v[72:75], %[pb0] offset:34816\n\tv_mul_f32 v143, v6, v111\n\t"
        "v_mfma_f32_16x16x32_bf16 v[44:47], v[76:79], v[188:191], v[44:47]\n\tv_mul_f32 v104, v7, v104\n\ts_waitcnt lgkmcnt(3)\n\t"
        "v_mfma_f32_16x16x32_bf16 v[48:51], v[184:187], v[80:83], v[48:51]\n\tds_read_b128 v[76:79], %[pb0] offset:35840\n\t"
        "buffer_load_dwordx4 v[224:227], off, %[rn], %[sn] offset:0 sc1\n\tv_mfma_f32_16x16x32_bf16 v[52:55], v[188:191], v[80:83], v[52:55]\n\t"
        "v_mul_f32 v105, v7, v105\n\tv_mul_f32 v106, v7, v106\n\tv_mfma_f32_16x16x32_bf16 v[56:59], v[184:187], v[84:87], v[56:59]\n\t"
        "ds_read_b128 v[80:83], %[pb0] offset:36864\n\tv_mul_f32 v107, v7, v107\n\tv_mfma_f32_16x16x32_bf16 v[60:63], v[188:191], v[84:87], v[60:63]\n\t"
        "v_mul_f32 v108, v7, v108\n\ts_waitcnt vmcnt(7)\n\tv_mfma_f32_16x16x32_bf16 v[16:19], v[88:91], v[192:195], v[16:19]\n\t"
        "ds_read_b128 v[84:87], %[pb0] offset:37888\n\tv_mul_f32 v109, v7, v109\n\tv_mfma_f32_16x16x32_bf16 v[20:23], v[88:91], v[196:199], v[20:23]\n\t"
        "buffer_load_dwordx4 v[228:231], off, %[rn], %[sn] offset:1024 sc1\n\ts_add_u32 %[sn], %[sn], 0x2000\n\t"
        "v_mfma_f32_16x16x32_bf16 v[24:27], v[92:95], v[192:195], v[24:27]\n\tds_read_b128 v[88:91], %[pb0] offset:38912\n\tv_mul_f32 v110, v7, v110\n\t"
        "v_mfma_f32_16x16x32_bf16 v[28:31], v[92:95], v[196:199], v[28:31]\n\tv_mul_f32 v111, v7, v111\n\ts_waitcnt lgkmcnt(3)\n\t"
        "v_mfma_f32_16x16x32_bf16 v[32:35], v[64:67], v[192:195], v[32:35]\n\tds_read_b128 v[92:95], %[pb0] offset:39936\n\tv_mul_f32 v144, v0, v120\n\t"
        "v_mfma_f32_16x16x32_bf16 v[36:39], v[64:67], v[196:199], v[36:39]\n\tv_mul_f32 v145, v0, v121\n\t.p2align 12,,12\n\t.Lpg18:\n\t"
        "s_cbranch_execz .Lpgret\n\tv_mul_f32 v146, v0, v122\n\tv_mfma_f32_16x16x32_bf16 v[40:43], v[68:71], v[192:195], v[40:43]\n\t"
        "ds_read_b128 v[64:67], %[pb0] offset:40960\n\tbuffer_load_dwordx4 v[232:235], off, %[rn], %[sn] offset:0 sc1\n\t"
        "v_mfma_f32_16x16x32_bf16 v[44:47], v[68:71], v[196:199], v[44:47]\n\tv_mul_f32 v147, v0, v123\n\tv_mul_f32 v148, v0, v124\n\t"
        "v_mfma_f32_16x16x32_bf16 v[48:51], v[192:195], v[72:75], v[48:51]\n\tds_read_b128 v[68:71], %[pb0] offset:41984\n\tv_mul_f32 v149, v0, v125\n\t"
        "v_mfma_f32_16x16x32_bf16 v[52:55], v[196:199], v[72:75], v[52:55]\n\tv_mul_f32 v150, v0, v126\n\tv_mul_f32 v151, v0, v127\n\t"
        "v_mfma_f32_16x16x32_bf16 v[56:59], v[192:195], v[76:79], v[56:59]\n\tds_read_b128 v[72:75], %[pb0] offset:43008\n\tv_mul_f32 v120, v1, v120\n\t"
        "v_mfma_f32_16x16x32_bf16 v[60:63], v[196:199], v[76:79], v[60:63]\n\tbuffer_load_dwordx4 v[236:239], off, %[rn], %[sn] offset:1024 sc1\n\t"
        "s_add_u32 %[sn], %[sn], 0x2000\n\ts_waitcnt vmcnt(8) lgkmcnt(3)\n\tv_mfma_f32_16x16x32_bf16 v[16:19], v[80:83], v[200:203], v[16:19]\n\t"
        "ds_read_b128 v[76:79], %[pb0] offset:44032\n\tv_mul_f32 v121, v1, v121\n\tv_mfma_f32_16x16x32_bf16 v[20:23], v[80:83], v[204:207], v[20:23]\n\t"
        "v_mul_f32 v122, v1, v122\n\tv_mul_f32 v123, v1, v123\n\tv_mfma_f32_16x16x32_bf16 v[24:27], v[84:87], v[200:203], v[24:27]\n\t"
        "ds_read_b128 v[80:83], %[pb0] offset:45056\n\tv_mul_f32 v124, v1, v124\n\tv_mfma_f32_16x16x32_bf16 v[28:31], v[84:87], v[204:207], v[28:31]\n\t"
        "v_mul_f32 v125, v1, v125\n\tv_mul_f32 v126, v1, v126\n\tv_mfma_f32_16x16x32_bf16 v[32:35], v[88:91], v[200:203], v[32:35]\n\t"
        "ds_read_b128 v[84:87], %[pb0] offset:46080\n\tbuffer_load_dwordx4 v[240:243], off, %[rn], %[sn] offset:0 sc1\n\t"
        "v_mfma_f32_16x16x32_bf16 v[36:39], v[88:91], v[204:207], v[36:39]\n\tv_mul_f32 v127, v1, v127\n\t"
        "v_mfma_f32_16x16x32_bf16 v[40:43], v[92:95], v[200:203], v[40:43]\n\tds_read_b128 v[88:91], %[pb0] offset:47104\n\t"
        "v_mfma_f32_16x16x32_bf16 v[44:47], v[92:95], v[204:207], v[44:47]\n\ts_waitcnt lgkmcnt(3)\n\t"
        "v_mfma_f32_16x16x32_bf16 v[48:51], v[200:203], v[64:67], v[48:51]\n\tds_read_b128 v[92:95], %[pb0] offset:48128\n\t"
        "v_mfma_f32_16x16x32_bf16 v[52:55], v[204:207], v[64:67], v[52:55]\n\tbuffer_load_dwordx4 v[244:247], off, %[rn], %[sn] offset:1024 sc1\n\t"
        "s_add_u32 %[sn], %[sn], 0x2000\n\tv_mfma_f32_16x16x32_bf16 v[56:59], v[200:203], v[68:71], v[56:59]\n\t"
        "v_mfma_f32_16x16x32_bf16 v[60:63], v[204:207], v[68:71], v[60:63]\n\ts_waitcnt vmcnt(8)\n\t"
        "v_mfma_f32_16x16x32_bf16 v[16:19], v[72:75], v[208:211], v[16:19]\n\tv_mfma_f32_16x16x32_bf16 v[20:23], v[72:75], v[212:215], v[20:23]\n\t"
        "v_mfma_f32_16x16x32_bf16 v[24:27], v[76:79], v[208:211], v[24:27]\n\tbuffer_load_dwordx4 v[152:155], off, %[rn], %[sn] offset:0 sc1\n\t"
        "v_mfma_f32_16x16x32_bf16 v[28:31], v[76:79], v[212:215], v[28:31]\n\ts_waitcnt lgkmcnt(0)\n\t"
        "v_mfma_f32_16x16x32_bf16 v[32:35], v[80:83], v[208:211], v[32:35]\n\tv_mfma_f32_16x16x32_bf16 v[36:39], v[80:83], v[212:215], v[36:39]\n\t"
        "v_mfma_f32_16x16x32_bf16 v[40:43], v[84:87], v[208:211], v[40:43]\n\tv_mfma_f32_16x16x32_bf16 v[44:47], v[84:87], v[212:215], v[44:47]\n\t"
        "buffer_load_dwordx4 v[156:159], off, %[rn], %[sn] offset:1024 sc1\n\ts_add_u32 %[sn], %[sn], 0x2000\n\t"
        "v_mfma_f32_16x16x32_bf16 v[48:51], v[208:211], v[88:91], v[48:51]\n\tv_mfma_f32_16x16x32_bf16 v[52:55], v[212:215], v[88:91], v[52:55]\n\t"
        "v_mfma_f32_16x16x32_bf16 v[56:59], v[208:211], v[92:95], v[56:59]\n\tv_mfma_f32_16x16x32_bf16 v[60:63], v[212:215], v[92:95], v[60:63]\n\ts_nop 0\n\t"
        "v_pk_fma_f32 v[32:33], v[32:33], v[8:9], v[128:129] op_sel:[0,0,0] op_sel_hi:[1,0,1]\n\t"
        "v_pk_fma_f32 v[34:35], v[34:35], v[8:9], v[130:131] op_sel:[0,0,0] op_sel_hi:[1,0,1]\n\t"
        "v_pk_fma_f32 v[40:41], v[40:41], v[8:9], v[132:133] op_sel:[0,0,0] op_sel_hi:[1,0,1]\n\t"
        "v_pk_fma_f32 v[42:43], v[42:43], v[8:9], v[134:135] op_sel:[0,0,0] op_sel_hi:[1,0,1]\n\t"
        "v_pk_fma_f32 v[36:37], v[36:37], v[8:9], v[96:97] op_sel:[0,1,0] op_sel_hi:[1,1,1]\n\t"
        "v_pk_fma_f32 v[38:39], v[38:39], v[8:9], v[98:99] op_sel:[0,1,0] op_sel_hi:[1,1,1]\n\t"
        "v_pk_fma_f32 v[44:45], v[44:45], v[8:9], v[100:101] op_sel:[0,1,0] op_sel_hi:[1,1,1]\n\t"
        "v_pk_fma_f32 v[46:47], v[46:47], v[8:9], v[102:103] op_sel:[0,1,0] op_sel_hi:[1,1,1]\n\tv_cvt_pk_bf16_f32 v32, v32, v33\n\t"
        "v_cvt_pk_bf16_f32 v33, v34, v35\n\tv_cvt_pk_bf16_f32 v34, v40, v41\n\tv_cvt_pk_bf16_f32 v35, v42, v43\n\t"
        "buffer_store_dwordx4 v[32:35], off, %[rt], %[kvo] offset:0 sc1\n\tv_cvt_pk_bf16_f32 v36, v36, v37\n\tv_cvt_pk_bf16_f32 v37, v38, v39\n\t"
        "v_cvt_pk_bf16_f32 v38, v44, v45\n\tv_cvt_pk_bf16_f32 v39, v46, v47\n\tbuffer_store_dwordx4 v[36:39], off, %[rt], %[kvo] offset:1024 sc1\n\t"
        "v_pk_fma_f32 v[16:17], v[16:17], v[4:5], v[136:137] op_sel:[0,0,0] op_sel_hi:[1,0,1]\n\t"
        "v_pk_fma_f32 v[18:19], v[18:19], v[4:5], v[138:139] op_sel:[0,0,0] op_sel_hi:[1,0,1]\n\t"
        "v_pk_fma_f32 v[24:25], v[24:25], v[4:5], v[140:141] op_sel:[0,0,0] op_sel_hi:[1,0,1]\n\t"
        "v_pk_fma_f32 v[26:27], v[26:27], v[4:5], v[142:143] op_sel:[0,0,0] op_sel_hi:[1,0,1]\n\t"
        "v_pk_fma_f32 v[20:21], v[20:21], v[4:5], v[104:105] op_sel:[0,1,0] op_sel_hi:[1,1,1]\n\t"
        "v_pk_fma_f32 v[22:23], v[22:23], v[4:5], v[106:107] op_sel:[0,1,0] op_sel_hi:[1,1,1]\n\t"
        "v_pk_fma_f32 v[28:29], v[28:29], v[4:5], v[108:109] op_sel:[0,1,0] op_sel_hi:[1,1,1]\n\t"
        "v_pk_fma_f32 v[30:31], v[30:31], v[4:5], v[110:111] op_sel:[0,1,0] op_sel_hi:[1,1,1]\n\tv_pk_fma_f32 v[48:49], v[48:49], v[112:113], v[144:145]\n\t"
        "v_pk_fma_f32 v[50:51], v[50:51], v[114:115], v[146:147]\n\tv_pk_fma_f32 v[52:53], v[52:53], v[116:117], v[148:149]\n\t"
        "v_pk_fma_f32 v[54:55], v[54:55], v[118:119], v[150:151]\n\tv_pk_fma_f32 v[56:57], v[56:57], v[112:113], v[120:121]\n\t"
        "v_pk_fma_f32 v[58:59], v[58:59], v[114:115], v[122:123]\n\tv_pk_fma_f32 v[60:61], v[60:61], v[116:117], v[124:125]\n\t"
        "v_pk_fma_f32 v[62:63], v[62:63], v[118:119], v[126:127]\n\tv_cvt_pk_bf16_f32 v0, v16, v17\n\tv_cvt_pk_bf16_f32 v1, v18, v19\n\t"
        "v_cvt_pk_bf16_f32 v2, v24, v25\n\tv_cvt_pk_bf16_f32 v3, v26, v27\n\tv_cvt_pk_bf16_f32 v4, v20, v21\n\tv_cvt_pk_bf16_f32 v5, v22, v23\n\t"
        "v_cvt_pk_bf16_f32 v6, v28, v29\n\tv_cvt_pk_bf16_f32 v7, v30, v31\n\tv_cvt_pk_bf16_f32 v48, v48, v49\n\tv_cvt_pk_bf16_f32 v49, v50, v51\n\t"
        "v_mfma_f32_16x16x32_bf16 v[40:43], v[32:35], v[0:3], 0\n\tv_mfma_f32_16x16x32_bf16 v[44:47], v[36:39], v[4:7], 0\n\t"
        "v_cvt_pk_bf16_f32 v50, v52, v53\n\tv_cvt_pk_bf16_f32 v51, v54, v55\n\tbuffer_store_dwordx4 v[48:51], off, %[rt], %[kvo] offset:2048 sc1\n\t"
        "v_cvt_pk_bf16_f32 v56, v56, v57\n\tv_cvt_pk_bf16_f32 v57, v58, v59\n\tv_cvt_pk_bf16_f32 v58, v60, v61\n\tv_cvt_pk_bf16_f32 v59, v62, v63\n\t"
        "buffer_store_dwordx4 v[56:59], off, %[rt], %[kvo] offset:3072 sc1\n\tv_max3_f32 v40, v40, v41, v42\n\tv_max3_f32 v44, v44, v45, v46\n\t"
        "v_max_f32 v40, v40, v43\n\tv_max_f32 v44, v44, v47\n\tv_permlane16_swap_b32 v0, v4\n\tv_permlane16_swap_b32 v1, v5\n\t"
        "v_permlane16_swap_b32 v40, v44\n\tv_max_f32 v40, v40, v44\n\tv_mov_b32 v44, v40\n\tv_permlane16_swap_b32 v2, v6\n\tv_permlane16_swap_b32 v3, v7\n\t"
        "v_permlane32_swap_b32 v40, v44\n\tv_min_f32_e64 v8, -v40, -v44\n\t.Lmk_q0_e:\n\t"
        : "=&{v[16:31]}"(acc[0]), "=&{v[32:47]}"(acc[1]), "=&{v[48:63]}"(acc[2]), "+{v[152:183]}"(r0), "+{v[184:215]}"(r1), "+{v[216:247]}"(r2), [kvo] "+s"(kvo), [fkv] "+s"(fkv), [fo] "+s"(fo), [xr] "+s"(xr), [so] "+s"(so), [fx] "+s"(fx), [xu] "+s"(xu), [fh] "+s"(fh), "=&{v[0:3]}"(qa0), "=&{v[4:7]}"(qa1), "=&{v8}"(nma), [sa] "+s"(sa), [sn] "+s"(sn)
        : [pb0] "{v250}"(n.pb0), [pb1] "{v251}"(n.pb1), [pb2] "{v252}"(n.pb2), [rt] "s"(rt4), [rn] "s"(rt4), [w128] "s"(n.w128), "{v253}"(n.e1), "{v254}"(n.e2), "{v255}"(n.e3)
        : "v64", "v65", "v66", "v67", "v68", "v69", "v70", "v71", "v72", "v73", "v74", "v75", "v76", "v77", "v78", "v79", "v80", "v81", "v82", "v83", "v84", "v85", "v86", "v87", "v88", "v89", "v90", "v91", "v92", "v93", "v94", "v95", "v9", "v10", "v96", "v97", "v98", "v99", "v100", "v101", "v102", "v103", "v104", "v105", "v106", "v107", "v108", "v109", "v110", "v111", "v112", "v113", "v114", "v115", "v116", "v117", "v118", "v119", "v120", "v121", "v122", "v123", "v124", "v125", "v126", "v127", "v128", "v129", "v130", "v131", "v132", "v133", "v134", "v135", "v136", "v137", "v138", "v139", "v140", "v141", "v142", "v143", "v144", "v145", "v146", "v147", "v148", "v149", "v150", "v151", "memory", "scc");
    STAMP(2);
    const unsigned fl = fkv;
    sa = sn;
      sn += 0u;
      asm volatile(
        "; BLK q1\n\t.Lmk_q1_s:\n\tds_read_b128 v[64:67], %[pb0] offset:0\n\tds_read_b128 v[68:71], %[pb0] offset:1024\n\t"
        "ds_read_b128 v[72:75], %[pb0] offset:2048\n\tds_read_b128 v[76:79], %[pb0] offset:3072\n\tds_read_b128 v[80:83], %[pb0] offset:4096\n\t"
        "ds_read_b128 v[84:87], %[pb0] offset:5120\n\tds_read_b128 v[88:91], %[pb0] offset:6144\n\ts_waitcnt vmcnt(12) lgkmcnt(3)\n\t"
        "v_mfma_f32_16x16x32_bf16 v[16:19], v[64:67], v[216:219], 0\n\tds_read_b128 v[92:95], %[pb0] offset:7168\n\t"
        "v_mfma_f32_16x16x32_bf16 v[20:23], v[64:67], v[220:223], 0\n\tv_mfma_f32_16x16x32_bf16 v[24:27], v[68:71], v[216:219], 0\n\t"
        "ds_read_b128 v[64:67], %[pb0] offset:8192\n\tv_mfma_f32_16x16x32_bf16 v[28:31], v[68:71], v[220:223], 0\n\t"
        "v_mfma_f32_16x16x32_bf16 v[32:35], v[72:75], v[216:219], 0\n\tds_read_b128 v[68:71], %[pb0] offset:9216\n\t"
        "buffer_load_dwordx4 v[160:163], off, %[rt], %[sa] offset:0 sc1\n\tv_mfma_f32_16x16x32_bf16 v[36:39], v[72:75], v[220:223], 0\n\t"
        "v_mfma_f32_16x16x32_bf16 v[40:43], v[76:79], v[216:219], 0\n\tds_read_b128 v[72:75], %[pb0] offset:10240\n\t"
        "v_mfma_f32_16x16x32_bf16 v[44:47], v[76:79], v[220:223], 0\n\ts_waitcnt lgkmcnt(3)\n\tv_mfma_f32_16x16x32_bf16 v[48:51], v[216:219], v[80:83], 0\n\t"
        "ds_read_b128 v[76:79], %[pb0] offset:11264\n\tv_mfma_f32_16x16x32_bf16 v[52:55], v[220:223], v[80:83], 0\n\t"
        "buffer_load_dwordx4 v[164:167], off, %[rt], %[sa] offset:1024 sc1\n\ts_add_u32 %[sa], %[sa], 0x2000\n\t"
        "v_mfma_f32_16x16x32_bf16 v[56:59], v[216:219], v[84:87], 0\n\tds_read_b128 v[80:83], %[pb0] offset:12288\n\t"
        "v_mfma_f32_16x16x32_bf16 v[60:63], v[220:223], v[84:87], 0\n\ts_waitcnt vmcnt(12)\n\t"
        "v_mfma_f32_16x16x32_bf16 v[16:19], v[88:91], v[224:227], v[16:19]\n\tds_read_b128 v[84:87], %[pb0] offset:13312\n\t"
        "v_mfma_f32_16x16x32_bf16 v[20:23], v[88:91], v[228:231], v[20:23]\n\tv_mfma_f32_16x16x32_bf16 v[24:27], v[92:95], v[224:227], v[24:27]\n\t"
        "ds_read_b128 v[88:91], %[pb0] offset:14336\n\tbuffer_load_dwordx4 v[168:171], off, %[rt], %[sa] offset:0 sc1\n\t"
        "v_mfma_f32_16x16x32_bf16 v[28:31], v[92:95], v[228:231], v[28:31]\n\ts_waitcnt lgkmcnt(3)\n\t"
        "v_mfma_f32_16x16x32_bf16 v[32:35], v[64:67], v[224:227], v[32:35]\n\tds_read_b128 v[92:95], %[pb0] offset:15360\n\t"
        "v_mfma_f32_16x16x32_bf16 v[36:39], v[64:67], v[228:231], v[36:39]\n\tv_mfma_f32_16x16x32_bf16 v[40:43], v[68:71], v[224:227], v[40:43]\n\t"
        "ds_read_b128 v[64:67], %[pb0] offset:16384\n\tv_mfma_f32_16x16x32_bf16 v[44:47], v[68:71], v[228:231], v[44:47]\n\t"
        "buffer_load_dwordx4 v[172:175], off, %[rt], %[sa] offset:1024 sc1\n\ts_add_u32 %[sa], %[sa], 0x2000\n\t"
        "v_mfma_f32_16x16x32_bf16 v[48:51], v[224:227], v[72:75], v[48:51]\n\tds_read_b128 v[68:71], %[pb0] offset:17408\n\t"
        "v_mfma_f32_16x16x32_bf16 v[52:55], v[228:231], v[72:75], v[52:55]\n\tv_mfma_f32_16x16x32_bf16 v[56:59], v[224:227], v[76:79], v[56:59]\n\t"
        "ds_read_b128 v[72:75], %[pb0] offset:18432\n\tv_mfma_f32_16x16x32_bf16 v[60:63], v[228:231], v[76:79], v[60:63]\n\ts_waitcnt vmcnt(12) lgkmcnt(3)\n\t"
        "v_mfma_f32_16x16x32_bf16 v[16:19], v[80:83], v[232:235], v[16:19]\n\tds_read_b128 v[76:79], %[pb0] offset:19456\n\t"
        "buffer_load_dwordx4 v[176:179], off, %[rt], %[sa] offset:0 sc1\n\tv_mfma_f32_16x16x32_bf16 v[20:23], v[80:83], v[236:239], v[20:23]\n\t"
        "v_mfma_f32_16x16x32_bf16 v[24:27], v[84:87], v[232:235], v[24:27]\n\tds_read_b128 v[80:83], %[pb0] offset:20480\n\t"
        "v_mfma_f32_16x16x32_bf16 v[28:31], v[84:87], v[236:239], v[28:31]\n\tv_mfma_f32_16x16x32_bf16 v[32:35], v[88:91], v[232:235], v[32:35]\n\t"
        "ds_read_b128 v[84:87], %[pb0] offset:21504\n\tv_mfma_f32_16x16x32_bf16 v[36:39], v[88:91], v[236:239], v[36:39]\n\t"
        "buffer_load_dwordx4 v[180:183], off, %[rt], %[sa] offset:1024 sc1\n\ts_add_u32 %[sa], %[sa], 0xfffd2000\n\t"
        "v_mfma_f32_16x16x32_bf16 v[40:43], v[92:95], v[232:235], v[40:43]\n\tds_read_b128 v[88:91], %[pb0] offset:22528\n\t"
        "v_mfma_f32_16x16x32_bf16 v[44:47], v[92:95], v[236:239], v[44:47]\n\ts_waitcnt lgkmcnt(3)\n\t"
        "v_mfma_f32_16x16x32_bf16 v[48:51], v[232:235], v[64:67], v[48:51]\n\tds_read_b128 v[92:95], %[pb0] offset:23552\n\t"
        "v_mfma_f32_16x16x32_bf16 v[52:55], v[236:239], v[64:67], v[52:55]\n\tv_mfma_f32_16x16x32_bf16 v[56:59], v[232:235], v[68:71], v[56:59]\n\t"
        "ds_read_b128 v[64:67], %[pb0] offset:24576\n\tbuffer_load_dwordx4 v[184:187], off, %[rt], %[sa] offset:0 sc1\n\t"
        "v_mfma_f32_16x16x32_bf16 v[60:63], v[236:239], v[68:71], v[60:63]\n\ts_waitcnt vmcnt(13)\n\t"
        "v_mfma_f32_16x16x32_bf16 v[16:19], v[72:75], v[240:243], v[16:19]\n\tds_read_b128 v[68:71], %[pb0] offset:25600\n\t"
        "v_mfma_f32_16x16x32_bf16 v[20:23], v[72:75], v[244:247], v[20:23]\n\tv_mfma_f32_16x16x32_bf16 v[24:27], v[76:79], v[240:243], v[24:27]\n\t"
        "ds_read_b128 v[72:75], %[pb0] offset:26624\n\tv_mfma_f32_16x16x32_bf16 v[28:31], v[76:79], v[244:247], v[28:31]\n\t"
        "buffer_load_dwordx4 v[188:191], off, %[rt], %[sa] offset:1024 sc1\n\ts_add_u32 %[sa], %[sa], 0x2000\n\ts_waitcnt lgkmcnt(3)\n\t"
        "v_mfma_f32_16x16x32_bf16 v[32:35], v[80:83], v[240:243], v[32:35]\n\tds_read_b128 v[76:79], %[pb0] offset:27648\n\t"
        "v_mfma_f32_16x16x32_bf16 v[36:39], v[80:83], v[244:247], v[36:39]\n\tv_mfma_f32_16x16x32_bf16 v[40:43], v[84:87], v[240:243], v[40:43]\n\t"
        "ds_read_b128 v[80:83], %[pb0] offset:28672\n\tv_mfma_f32_16x16x32_bf16 v[44:47], v[84:87], v[244:247], v[44:47]\n\t"
        "v_mfma_f32_16x16x32_bf16 v[48:51], v[240:243], v[88:91], v[48:51]\n\tds_read_b128 v[84:87], %[pb0] offset:29696\n\t"
        "buffer_load_dwordx4 v[192:195], off, %[rt], %[sa] offset:0 sc1\n\tv_mfma_f32_16x16x32_bf16 v[52:55], v[244:247], v[88:91], v[52:55]\n\t"
        "v_mfma_f32_16x16x32_bf16 v[56:59], v[240:243], v[92:95], v[56:59]\n\tds_read_b128 v[88:91], %[pb0] offset:30720\n\t"
        "v_mfma_f32_16x16x32_bf16 v[60:63], v[244:247], v[92:95], v[60:63]\n\ts_waitcnt vmcnt(13) lgkmcnt(3)\n\t"
        "v_mfma_f32_16x16x32_bf16 v[16:19], v[64:67], v[152:155], v[16:19]\n\tds_read_b128 v[92:95], %[pb0] offset:31744\n\t"
        "v_mfma_f32_16x16x32_bf16 v[20:23], v[64:67], v[156:159], v[20:23]\n\tbuffer_load_dwordx4 v[196:199], off, %[rt], %[sa] offset:1024 sc1\n\t"
        "s_add_u32 %[sa], %[sa], 0x2000\n\tv_mfma_f32_16x16x32_bf16 v[24:27], v[68:71], v[152:155], v[24:27]\n\tds_read_b128 v[64:67], %[pb0] offset:32768\n\t"
        "v_mfma_f32_16x16x32_bf16 v[28:31], v[68:71], v[156:159], v[28:31]\n\tv_mfma_f32_16x16x32_bf16 v[32:35], v[72:75], v[152:155], v[32:35]\n\t"
        "ds_read_b128 v[68:71], %[pb0] offset:33792\n\tv_mfma_f32_16x16x32_bf16 v[36:39], v[72:75], v[156:159], v[36:39]\n\t"
        "v_mfma_f32_16x16x32_bf16 v[40:43], v[76:79], v[152:155], v[40:43]\n\tds_read_b128 v[72:75], %[pb0] offset:34816\n\t"
        "buffer_load_dwordx4 v[200:203], off, %[rt], %[sa] offset:0 sc1\n\tv_mfma_f32_16x16x32_bf16 v[44:47], v[76:79], v[156:159], v[44:47]\n\t"
        "s_waitcnt lgkmcnt(3)\n\tv_mfma_f32_16x16x32_bf16 v[48:51], v[152:155], v[80:83], v[48:51]\n\tds_read_b128 v[76:79], %[pb0] offset:35840\n\t"
        "v_mfma_f32_16x16x32_bf16 v[52:55], v[156:159], v[80:83], v[52:55]\n\tv_mfma_f32_16x16x32_bf16 v[56:59], v[152:155], v[84:87], v[56:59]\n\t"
        "ds_read_b128 v[80:83], %[pb0] offset:36864\n\tv_mfma_f32_16x16x32_bf16 v[60:63], v[156:159], v[84:87], v[60:63]\n\t"
        "buffer_load_dwordx4 v[204:207], off, %[rt], %[sa] offset:1024 sc1\n\ts_add_u32 %[sa], %[sa], 0x2000\n\ts_waitcnt vmcnt(10)\n\t"
        "v_mfma_f32_16x16x32_bf16 v[16:19], v[88:91], v[160:163], v[16:19]\n\tds_read_b128 v[84:87], %[pb0] offset:37888\n\t"
        "v_mfma_f32_16x16x32_bf16 v[20:23], v[88:91], v[164:167], v[20:23]\n\tv_mfma_f32_16x16x32_bf16 v[24:27], v[92:95], v[160:163], v[24:27]\n\t"
        "ds_read_b128 v[88:91], %[pb0] offset:38912\n\tv_mfma_f32_16x16x32_bf16 v[28:31], v[92:95], v[164:167], v[28:31]\n\ts_waitcnt lgkmcnt(3)\n\t"
        "v_mfma_f32_16x16x32_bf16 v[32:35], v[64:67], v[160:163], v[32:35]\n\tds_read_b128 v[92:95], %[pb0] offset:39936\n\t"
        "buffer_load_dwordx4 v[208:211], off, %[rt], %[sa] offset:0 sc1\n\tv_mfma_f32_16x16x32_bf16 v[36:39], v[64:67], v[164:167], v[36:39]\n\t"
        "v_mfma_f32_16x16x32_bf16 v[40:43], v[68:71], v[160:163], v[40:43]\n\tds_read_b128 v[64:67], %[pb0] offset:40960\n\t"
        "v_mfma_f32_16x16x32_bf16 v[44:47], v[68:71], v[164:167], v[44:47]\n\tv_mfma_f32_16x16x32_bf16 v[48:51], v[160:163], v[72:75], v[48:51]\n\t"
        "ds_read_b128 v[68:71], %[pb0] offset:41984\n\tv_mfma_f32_16x16x32_bf16 v[52:55], v[164:167], v[72:75], v[52:55]\n\t"
        "buffer_load_dwordx4 v[212:215], off, %[rt], %[sa] offset:1024 sc1\n\ts_add_u32 %[sa], %[sa], 0x2000\n\t"
        "v_mfma_f32_16x16x32_bf16 v[56:59], v[160:163], v[76:79], v[56:59]\n\tds_read_b128 v[72:75], %[pb0] offset:43008\n\t"
        "v_mfma_f32_16x16x32_bf16 v[60:63], v[164:167], v[76:79], v[60:63]\n\ts_waitcnt vmcnt(10) lgkmcnt(3)\n\t"
        "v_mfma_f32_16x16x32_bf16 v[16:19], v[80:83], v[168:171], v[16:19]\n\tds_read_b128 v[76:79], %[pb0] offset:44032\n\t"
        "v_mfma_f32_16x16x32_bf16 v[20:23], v[80:83], v[172:175], v[20:23]\n\ts_waitcnt vmcnt(14)\n\tbuffer_store_dwordx2 %[one], off, %[rs], %[fl] sc1\n\t"
        "v_mfma_f32_16x16x32_bf16 v[24:27], v[84:87], v[168:171], v[24:27]\n\tds_read_b128 v[80:83], %[pb0] offset:45056\n\t"
        "buffer_load_dwordx4 v[216:219], off, %[rt], %[sa] offset:0 sc1\n\tv_mfma_f32_16x16x32_bf16 v[28:31], v[84:87], v[172:175], v[28:31]\n\t"
        "v_mfma_f32_16x16x32_bf16 v[32:35], v[88:91], v[168:171], v[32:35]\n\tds_read_b128 v[84:87], %[pb0] offset:46080\n\t"
        "v_mfma_f32_16x16x32_bf16 v[36:39], v[88:91], v[172:175], v[36:39]\n\tv_mfma_f32_16x16x32_bf16 v[40:43], v[92:95], v[168:171], v[40:43]\n\t"
        "ds_read_b128 v[88:91], %[pb0] offset:47104\n\tv_mfma_f32_16x16x32_bf16 v[44:47], v[92:95], v[172:175], v[44:47]\n\t"
        "buffer_load_dwordx4 v[220:223], off, %[rt], %[sa] offset:1024 sc1\n\ts_add_u32 %[sa], %[sa], 0x2000\n\ts_waitcnt lgkmcnt(3)\n\t"
        "v_mfma_f32_16x16x32_bf16 v[48:51], v[168:171], v[64:67], v[48:51]\n\tds_read_b128 v[92:95], %[pb0] offset:48128\n\t"
        "v_mfma_f32_16x16x32_bf16 v[52:55], v[172:175], v[64:67], v[52:55]\n\tv_mfma_f32_16x16x32_bf16 v[56:59], v[168:171], v[68:71], v[56:59]\n\t"
        "ds_read_b128 v[64:67], %[pb0] offset:49152\n\tv_mfma_f32_16x16x32_bf16 v[60:63], v[172:175], v[68:71], v[60:63]\n\ts_waitcnt vmcnt(11)\n\t"
        "v_mfma_f32_16x16x32_bf16 v[16:19], v[72:75], v[176:179], v[16:19]\n\tds_read_b128 v[68:71], %[pb0] offset:50176\n\t"
        "buffer_load_dwordx4 v[224:227], off, %[rt], %[sa] offset:0 sc1\n\tv_mfma_f32_16x16x32_bf16 v[20:23], v[72:75], v[180:183], v[20:23]\n\t"
        "v_mfma_f32_16x16x32_bf16 v[24:27], v[76:79], v[176:179], v[24:27]\n\tds_read_b128 v[72:75], %[pb0] offset:51200\n\t"
        "v_mfma_f32_16x16x32_bf16 v[28:31], v[76:79], v[180:183], v[28:31]\n\ts_waitcnt lgkmcnt(3)\n\t"
        "v_mfma_f32_16x16x32_bf16 v[32:35], v[80:83], v[176:179], v[32:35]\n\tds_read_b128 v[76:79], %[pb0] offset:52224\n\t"
        "v_mfma_f32_16x16x32_bf16 v[36:39], v[80:83], v[180:183], v[36:39]\n\tbuffer_load_dwordx4 v[228:231], off, %[rt], %[sa] offset:1024 sc1\n\t"
        "s_add_u32 %[sa], %[sa], 0x2000\n\tv_mfma_f32_16x16x32_bf16 v[40:43], v[84:87], v[176:179], v[40:43]\n\tds_read_b128 v[80:83], %[pb0] offset:53248\n\t"
        "v_mfma_f32_16x16x32_bf16 v[44:47], v[84:87], v[180:183], v[44:47]\n\tv_mfma_f32_16x16x32_bf16 v[48:51], v[176:179], v[88:91], v[48:51]\n\t"
        "ds_read_b128 v[84:87], %[pb0] offset:54272\n\tv_mfma_f32_16x16x32_bf16 v[52:55], v[180:183], v[88:91], v[52:55]\n\t"
        "v_mfma_f32_16x16x32_bf16 v[56:59], v[176:179], v[92:95], v[56:59]\n\tds_read_b128 v[88:91], %[pb0] offset:55296\n\t"
        "buffer_load_dwordx4 v[232:235], off, %[rt], %[sa] offset:0 sc1\n\tv_mfma_f32_16x16x32_bf16 v[60:63], v[180:183], v[92:95], v[60:63]\n\t"
        "s_waitcnt vmcnt(12) lgkmcnt(3)\n\tv_mfma_f32_16x16x32_bf16 v[16:19], v[64:67], v[184:187], v[16:19]\n\tds_read_b128 v[92:95], %[pb0] offset:56320\n\t"
        "v_mfma_f32_16x16x32_bf16 v[20:23], v[64:67], v[188:191], v[20:23]\n\tv_mfma_f32_16x16x32_bf16 v[24:27], v[68:71], v[184:187], v[24:27]\n\t"
        "ds_read_b128 v[64:67], %[pb0] offset:57344\n\tv_mfma_f32_16x16x32_bf16 v[28:31], v[68:71], v[188:191], v[28:31]\n\t"
        "buffer_load_dwordx4 v[236:239], off, %[rt], %[sa] offset:1024 sc1\n\ts_add_u32 %[sa], %[sa], 0x2000\n\t"
        "v_mfma_f32_16x16x32_bf16 v[32:35], v[72:75], v[184:187], v[32:35]\n\tds_read_b128 v[68:71], %[pb0] offset:58368\n\t"
        "v_mfma_f32_16x16x32_bf16 v[36:39], v[72:75], v[188:191], v[36:39]\n\tv_mfma_f32_16x16x32_bf16 v[40:43], v[76:79], v[184:187], v[40:43]\n\t"
        "ds_read_b128 v[72:75], %[pb0] offset:59392\n\tv_mfma_f32_16x16x32_bf16 v[44:47], v[76:79], v[188:191], v[44:47]\n\ts_waitcnt lgkmcnt(3)\n\t"
        "v_mfma_f32_16x16x32_bf16 v[48:51], v[184:187], v[80:83], v[48:51]\n\tds_read_b128 v[76:79], %[pb0] offset:60416\n\t"
        "buffer_load_dwordx4 v[240:243], off, %[rt], %[sa] offset:0 sc1\n\tv_mfma_f32_16x16x32_bf16 v[52:55], v[188:191], v[80:83], v[52:55]\n\t"
        "v_mfma_f32_16x16x32_bf16 v[56:59], v[184:187], v[84:87], v[56:59]\n\tds_read_b128 v[80:83], %[pb0] offset:61440\n\t"
        "v_mfma_f32_16x16x32_bf16 v[60:63], v[188:191], v[84:87], v[60:63]\n\ts_waitcnt vmcnt(12)\n\t"
        "v_mfma_f32_16x16x32_bf16 v[16:19], v[88:91], v[192:195], v[16:19]\n\tds_read_b128 v[84:87], %[pb0] offset:62464\n\t"
        "v_mfma_f32_16x16x32_bf16 v[20:23], v[88:91], v[196:199], v[20:23]\n\tbuffer_load_dwordx4 v[244:247], off, %[rt], %[sa] offset:1024 sc1\n\t"
        "s_add_u32 %[sa], %[sa], 0x2000\n\tv_mfma_f32_16x16x32_bf16 v[24:27], v[92:95], v[192:195], v[24:27]\n\tds_read_b128 v[88:91], %[pb0] offset:63488\n\t"
        "v_mfma_f32_16x16x32_bf16 v[28:31], v[92:95], v[196:199], v[28:31]\n\ts_waitcnt lgkmcnt(3)\n\t"
        "v_mfma_f32_16x16x32_bf16 v[32:35], v[64:67], v[192:195], v[32:35]\n\tds_read_b128 v[92:95], %[pb0] offset:64512\n\t"
        "v_mfma_f32_16x16x32_bf16 v[36:39], v[64:67], v[196:199], v[36:39]\n\tv_mfma_f32_16x16x32_bf16 v[40:43], v[68:71], v[192:195], v[40:43]\n\t"
        "ds_read_b128 v[64:67], %[pb1] offset:0\n\tbuffer_load_dwordx4 v[152:155], off, %[rt], %[sa] offset:0 sc1\n\t"
        "v_mfma_f32_16x16x32_bf16 v[44:47], v[68:71], v[196:199], v[44:47]\n\tv_mfma_f32_16x16x32_bf16 v[48:51], v[192:195], v[72:75], v[48:51]\n\t"
        "ds_read_b128 v[68:71], %[pb1] offset:1024\n\tv_mfma_f32_16x16x32_bf16 v[52:55], v[196:199], v[72:75], v[52:55]\n\t"
        "v_mfma_f32_16x16x32_bf16 v[56:59], v[192:195], v[76:79], v[56:59]\n\tds_read_b128 v[72:75], %[pb1] offset:2048\n\t"
        "v_mfma_f32_16x16x32_bf16 v[60:63], v[196:199], v[76:79], v[60:63]\n\tbuffer_load_dwordx4 v[156:159], off, %[rt], %[sa] offset:1024 sc1\n\t"
        "s_add_u32 %[sa], %[sa], 0x2000\n\ts_waitcnt vmcnt(13) lgkmcnt(3)\n\tv_mfma_f32_16x16x32_bf16 v[16:19], v[80:83], v[200:203], v[16:19]\n\t"
        "ds_read_b128 v[76:79], %[pb1] offset:3072\n\tv_mfma_f32_16x16x32_bf16 v[20:23], v[80:83], v[204:207], v[20:23]\n\t"
        "v_mfma_f32_16x16x32_bf16 v[24:27], v[84:87], v[200:203], v[24:27]\n\tds_read_b128 v[80:83], %[pb1] offset:4096\n\t"
        "v_mfma_f32_16x16x32_bf16 v[28:31], v[84:87], v[204:207], v[28:31]\n\tv_mfma_f32_16x16x32_bf16 v[32:35], v[88:91], v[200:203], v[32:35]\n\t"
        "ds_read_b128 v[84:87], %[pb1] offset:5120\n\tbuffer_load_dwordx4 v[160:163], off, %[rt], %[sa] offset:0 sc1\n\t"
        "v_mfma_f32_16x16x32_bf16 v[36:39], v[88:91], v[204:207], v[36:39]\n\tv_mfma_f32_16x16x32_bf16 v[40:43], v[92:95], v[200:203], v[40:43]\n\t"
        "ds_read_b128 v[88:91], %[pb1] offset:6144\n\tv_mfma_f32_16x16x32_bf16 v[44:47], v[92:95], v[204:207], v[44:47]\n\ts_waitcnt lgkmcnt(3)\n\t"
        "v_mfma_f32_16x16x32_bf16 v[48:51], v[200:203], v[64:67], v[48:51]\n\tds_read_b128 v[92:95], %[pb1] offset:7168\n\t"
        "v_mfma_f32_16x16x32_bf16 v[52:55], v[204:207], v[64:67], v[52:55]\n\tbuffer_load_dwordx4 v[164:167], off, %[rt], %[sa] offset:1024 sc1\n\t"
        "s_add_u32 %[sa], %[sa], 0x2000\n\tv_mfma_f32_16x16x32_bf16 v[56:59], v[200:203], v[68:71], v[56:59]\n\ts_barrier\n\t"
        "ds_read_b128 v[64:67], %[pb1] offset:8192\n\tv_mfma_f32_16x16x32_bf16 v[60:63], v[204:207], v[68:71], v[60:63]\n\ts_waitcnt vmcnt(13)\n\t"
        "v_mfma_f32_16x16x32_bf16 v[16:19], v[72:75], v[208:211], v[16:19]\n\tds_read_b128 v[68:71], %[pb1] offset:9216\n\t"
        "v_mfma_f32_16x16x32_bf16 v[20:23], v[72:75], v[212:215], v[20:23]\n\tv_mfma_f32_16x16x32_bf16 v[24:27], v[76:79], v[208:211], v[24:27]\n\t"
        "ds_read_b128 v[72:75], %[pb1] offset:10240\n\tbuffer_load_dwordx4 v[168:171], off, %[rt], %[sa] offset:0 sc1\n\t"
        "v_mfma_f32_16x16x32_bf16 v[28:31], v[76:79], v[212:215], v[28:31]\n\ts_waitcnt lgkmcnt(3)\n\t"
        "v_mfma_f32_16x16x32_bf16 v[32:35], v[80:83], v[208:211], v[32:35]\n\tds_read_b128 v[76:79], %[pb1] offset:11264\n\t"
        "v_mfma_f32_16x16x32_bf16 v[36:39], v[80:83], v[212:215], v[36:39]\n\tv_mfma_f32_16x16x32_bf16 v[40:43], v[84:87], v[208:211], v[40:43]\n\t"
        "ds_read_b128 v[80:83], %[pb1] offset:12288\n\tv_mfma_f32_16x16x32_bf16 v[44:47], v[84:87], v[212:215], v[44:47]\n\t"
        "buffer_load_dwordx4 v[172:175], off, %[rt], %[sa] offset:1024 sc1\n\ts_add_u32 %[sa], %[sa], 0x2000\n\t"
        "v_mfma_f32_16x16x32_bf16 v[48:51], v[208:211], v[88:91], v[48:51]\n\tds_read_b128 v[84:87], %[pb1] offset:13312\n\t"
        "v_mfma_f32_16x16x32_bf16 v[52:55], v[212:215], v[88:91], v[52:55]\n\tv_mfma_f32_16x16x32_bf16 v[56:59], v[208:211], v[92:95], v[56:59]\n\t"
        "ds_read_b128 v[88:91], %[pb1] offset:14336\n\tv_mfma_f32_16x16x32_bf16 v[60:63], v[212:215], v[92:95], v[60:63]\n\ts_waitcnt vmcnt(12) lgkmcnt(3)\n\t"
        "v_mfma_f32_16x16x32_bf16 v[16:19], v[64:67], v[216:219], v[16:19]\n\tds_read_b128 v[92:95], %[pb1] offset:15360\n\t"
        "buffer_load_dwordx4 v[176:179], off, %[rt], %[sa] offset:0 sc1\n\tv_mfma_f32_16x16x32_bf16 v[20:23], v[64:67], v[220:223], v[20:23]\n\t"
        "v_mfma_f32_16x16x32_bf16 v[24:27], v[68:71], v[216:219], v[24:27]\n\tds_read_b128 v[64:67], %[pb1] offset:16384\n\t"
        "v_mfma_f32_16x16x32_bf16 v[28:31], v[68:71], v[220:223], v[28:31]\n\tv_mfma_f32_16x16x32_bf16 v[32:35], v[72:75], v[216:219], v[32:35]\n\t"
        "ds_read_b128 v[68:71], %[pb1] offset:17408\n\tv_mfma_f32_16x16x32_bf16 v[36:39], v[72:75], v[220:223], v[36:39]\n\t"
        "buffer_load_dwordx4 v[180:183], off, %[rt], %[sa] offset:1024 sc1\n\ts_add_u32 %[sa], %[sa], 0x2000\n\t"
        "v_mfma_f32_16x16x32_bf16 v[40:43], v[76:79], v[216:219], v[40:43]\n\tds_read_b128 v[72:75], %[pb1] offset:18432\n\t"
        "v_mfma_f32_16x16x32_bf16 v[44:47], v[76:79], v[220:223], v[44:47]\n\ts_waitcnt lgkmcnt(3)\n\t"
        "v_mfma_f32_16x16x32_bf16 v[48:51], v[216:219], v[80:83], v[48:51]\n\tds_read_b128 v[76:79], %[pb1] offset:19456\n\t"
        "v_mfma_f32_16x16x32_bf16 v[52:55], v[220:223], v[80:83], v[52:55]\n\tv_mfma_f32_16x16x32_bf16 v[56:59], v[216:219], v[84:87], v[56:59]\n\t"
        "ds_read_b128 v[80:83], %[pb1] offset:20480\n\tbuffer_load_dwordx4 v[184:187], off, %[rt], %[sa] offset:0 sc1\n\t"
        "v_mfma_f32_16x16x32_bf16 v[60:63], v[220:223], v[84:87], v[60:63]\n\ts_waitcnt vmcnt(13)\n\t"
        "v_mfma_f32_16x16x32_bf16 v[16:19], v[88:91], v[224:227], v[16:19]\n\tds_read_b128 v[84:87], %[pb1] offset:21504\n\t"
        "v_mfma_f32_16x16x32_bf16 v[20:23], v[88:91], v[228:231], v[20:23]\n\tv_mfma_f32_16x16x32_bf16 v[24:27], v[92:95], v[224:227], v[24:27]\n\t"
        "ds_read_b128 v[88:91], %[pb1] offset:22528\n\tv_mfma_f32_16x16x32_bf16 v[28:31], v[92:95], v[228:231], v[28:31]\n\t"
        "buffer_load_dwordx4 v[188:191], off, %[rt], %[sa] offset:1024 sc1\n\ts_add_u32 %[sa], %[sa], 0x2000\n\ts_waitcnt lgkmcnt(3)\n\t"
        "v_mfma_f32_16x16x32_bf16 v[32:35], v[64:67], v[224:227], v[32:35]\n\tds_read_b128 v[92:95], %[pb1] offset:23552\n\t"
        "v_mfma_f32_16x16x32_bf16 v[36:39], v[64:67], v[228:231], v[36:39]\n\tv_mfma_f32_16x16x32_bf16 v[40:43], v[68:71], v[224:227], v[40:43]\n\t"
        "ds_read_b128 v[64:67], %[pb1] offset:24576\n\tv_mfma_f32_16x16x32_bf16 v[44:47], v[68:71], v[228:231], v[44:47]\n\t"
        "v_mfma_f32_16x16x32_bf16 v[48:51], v[224:227], v[72:75], v[48:51]\n\tds_read_b128 v[68:71], %[pb1] offset:25600\n\t"
        "buffer_load_dwordx4 v[192:195], off, %[rt], %[sa] offset:0 sc1\n\tv_mfma_f32_16x16x32_bf16 v[52:55], v[228:231], v[72:75], v[52:55]\n\t"
        "v_mfma_f32_16x16x32_bf16 v[56:59], v[224:227], v[76:79], v[56:59]\n\tds_read_b128 v[72:75], %[pb1] offset:26624\n\t"
        "v_mfma_f32_16x16x32_bf16 v[60:63], v[228:231], v[76:79], v[60:63]\n\ts_waitcnt vmcnt(13) lgkmcnt(3)\n\t"
        "v_mfma_f32_16x16x32_bf16 v[16:19], v[80:83], v[232:235], v[16:19]\n\tds_read_b128 v[76:79], %[pb1] offset:27648\n\t"
        "v_mfma_f32_16x16x32_bf16 v[20:23], v[80:83], v[236:239], v[20:23]\n\tbuffer_load_dwordx4 v[196:199], off, %[rt], %[sa] offset:1024 sc1\n\t"
        "s_add_u32 %[sa], %[sa], 0x2000\n\tv_mfma_f32_16x16x32_bf16 v[24:27], v[84:87], v[232:235], v[24:27]\n\tds_read_b128 v[80:83], %[pb1] offset:28672\n\t"
        "v_mfma_f32_16x16x32_bf16 v[28:31], v[84:87], v[236:239], v[28:31]\n\tv_mfma_f32_16x16x32_bf16 v[32:35], v[88:91], v[232:235], v[32:35]\n\t"
        "ds_read_b128 v[84:87], %[pb1] offset:29696\n\tv_mfma_f32_16x16x32_bf16 v[36:39], v[88:91], v[236:239], v[36:39]\n\t"
        "v_mfma_f32_16x16x32_bf16 v[40:43], v[92:95], v[232:235], v[40:43]\n\tds_read_b128 v[88:91], %[pb1] offset:30720\n\t"
        "buffer_load_dwordx4 v[200:203], off, %[rt], %[sa] offset:0 sc1\n\tv_mfma_f32_16x16x32_bf16 v[44:47], v[92:95], v[236:239], v[44:47]\n\t"
        "s_waitcnt lgkmcnt(3)\n\tv_mfma_f32_16x16x32_bf16 v[48:51], v[232:235], v[64:67], v[48:51]\n\tds_read_b128 v[92:95], %[pb1] offset:31744\n\t"
        "v_mfma_f32_16x16x32_bf16 v[52:55], v[236:239], v[64:67], v[52:55]\n\tv_mfma_f32_16x16x32_bf16 v[56:59], v[232:235], v[68:71], v[56:59]\n\t"
        "ds_read_b128 v[64:67], %[pb1] offset:32768\n\tv_mfma_f32_16x16x32_bf16 v[60:63], v[236:239], v[68:71], v[60:63]\n\t"
        "buffer_load_dwordx4 v[204:207], off, %[rt], %[sa] offset:1024 sc1\n\ts_add_u32 %[sa], %[sa], 0x2000\n\ts_waitcnt vmcnt(14)\n\t"
        "v_mfma_f32_16x16x32_bf16 v[16:19], v[72:75], v[240:243], v[16:19]\n\tds_read_b128 v[68:71], %[pb1] offset:33792\n\t"
        "v_mfma_f32_16x16x32_bf16 v[20:23], v[72:75], v[244:247], v[20:23]\n\tv_mfma_f32_16x16x32_bf16 v[24:27], v[76:79], v[240:243], v[24:27]\n\t"
        "ds_read_b128 v[72:75], %[pb1] offset:34816\n\tv_mfma_f32_16x16x32_bf16 v[28:31], v[76:79], v[244:247], v[28:31]\n\ts_waitcnt lgkmcnt(3)\n\t"
        "v_mfma_f32_16x16x32_bf16 v[32:35], v[80:83], v[240:243], v[32:35]\n\tds_read_b128 v[76:79], %[pb1] offset:35840\n\t"
        "buffer_load_dwordx4 v[208:211], off, %[rt], %[sa] offset:0 sc1\n\tv_mfma_f32_16x16x32_bf16 v[36:39], v[80:83], v[244:247], v[36:39]\n\t"
        "v_mfma_f32_16x16x32_bf16 v[40:43], v[84:87], v[240:243], v[40:43]\n\tds_read_b128 v[80:83], %[pb1] offset:36864\n\t"
        "v_mfma_f32_16x16x32_bf16 v[44:47], v[84:87], v[244:247], v[44:47]\n\tv_mfma_f32_16x16x32_bf16 v[48:51], v[240:243], v[88:91], v[48:51]\n\t"
        "ds_read_b128 v[84:87], %[pb1] offset:37888\n\tv_mfma_f32_16x16x32_bf16 v[52:55], v[244:247], v[88:91], v[52:55]\n\t"
        "buffer_load_dwordx4 v[212:215], off, %[rt], %[sa] offset:1024 sc1\n\ts_add_u32 %[sa], %[sa], 0xfffd2000\n\t"
        "v_mfma_f32_16x16x32_bf16 v[56:59], v[240:243], v[92:95], v[56:59]\n\tds_read_b128 v[88:91], %[pb1] offset:38912\n\t"
        "v_mfma_f32_16x16x32_bf16 v[60:63], v[244:247], v[92:95], v[60:63]\n\ts_waitcnt vmcnt(14) lgkmcnt(3)\n\t"
        "v_mfma_f32_16x16x32_bf16 v[16:19], v[64:67], v[152:155], v[16:19]\n\tds_read_b128 v[92:95], %[pb1] offset:39936\n\t"
        "v_mfma_f32_16x16x32_bf16 v[20:23], v[64:67], v[156:159], v[20:23]\n\tv_mov_b32 v120, v8\n\t"
        "v_mfma_f32_16x16x32_bf16 v[24:27], v[68:71], v[152:155], v[24:27]\n\tds_read_b128 v[64:67], %[pb1] offset:40960\n\t"
        "buffer_load_dwordx4 v[216:219], off, %[rt], %[sa] offset:0 sc1\n\tv_mfma_f32_16x16x32_bf16 v[28:31], v[68:71], v[156:159], v[28:31]\n\t"
        "v_mov_b32 v121, v8\n\tv_mfma_f32_16x16x32_bf16 v[32:35], v[72:75], v[152:155], v[32:35]\n\tds_read_b128 v[68:71], %[pb1] offset:41984\n\t"
        "v_mfma_f32_16x16x32_bf16 v[36:39], v[72:75], v[156:159], v[36:39]\n\tv_mov_b32 v122, v8\n\t"
        "v_mfma_f32_16x16x32_bf16 v[40:43], v[76:79], v[152:155], v[40:43]\n\tds_read_b128 v[72:75], %[pb1] offset:43008\n\t"
        "v_mfma_f32_16x16x32_bf16 v[44:47], v[76:79], v[156:159], v[44:47]\n\tbuffer_load_dwordx4 v[220:223], off, %[rt], %[sa] offset:1024 sc1\n\t"
        "s_add_u32 %[sa], %[sa], 0x2000\n\tv_mov_b32 v123, v8\n\ts_waitcnt lgkmcnt(3)\n\tv_mfma_f32_16x16x32_bf16 v[48:51], v[152:155], v[80:83], v[48:51]\n\t"
        "ds_read_b128 v[76:79], %[pb1] offset:44032\n\tv_mfma_f32_16x16x32_bf16 v[52:55], v[156:159], v[80:83], v[52:55]\n\tv_mov_b32 v124, v8\n\t"
        ".p2align 12,,12\n\t.Lpg19:\n\ts_cbranch_execz .Lpgret\n\tv_mfma_f32_16x16x32_bf16 v[56:59], v[152:155], v[84:87], v[56:59]\n\t"
        "ds_read_b128 v[80:83], %[pb1] offset:45056\n\tv_mfma_f32_16x16x32_bf16 v[60:63], v[156:159], v[84:87], v[60:63]\n\tv_mov_b32 v125, v8\n\t"
        "s_waitcnt vmcnt(14)\n\tv_mfma_f32_16x16x32_bf16 v[16:19], v[88:91], v[160:163], v[16:19]\n\tds_read_b128 v[84:87], %[pb1] offset:46080\n\t"
        "buffer_load_dwordx4 v[224:227], off, %[rt], %[sa] offset:0 sc1\n\tv_mfma_f32_16x16x32_bf16 v[20:23], v[88:91], v[164:167], v[20:23]\n\t"
        "v_mov_b32 v126, v8\n\tv_mfma_f32_16x16x32_bf16 v[24:27], v[92:95], v[160:163], v[24:27]\n\tds_read_b128 v[88:91], %[pb1] offset:47104\n\t"
        "v_mfma_f32_16x16x32_bf16 v[28:31], v[92:95], v[164:167], v[28:31]\n\tv_mov_b32 v127, v8\n\ts_waitcnt lgkmcnt(3)\n\t"
        "v_mfma_f32_16x16x32_bf16 v[32:35], v[64:67], v[160:163], v[32:35]\n\tds_read_b128 v[92:95], %[pb1] offset:48128\n\t"
        "v_mfma_f32_16x16x32_bf16 v[36:39], v[64:67], v[164:167], v[36:39]\n\tbuffer_load_dwordx4 v[228:231], off, %[rt], %[sa] offset:1024 sc1\n\t"
        "s_add_u32 %[sa], %[sa], 0x2000\n\tv_mov_b32 v128, v8\n\tv_mfma_f32_16x16x32_bf16 v[40:43], v[68:71], v[160:163], v[40:43]\n\t"
        "ds_read_b128 v[64:67], %[pb1] offset:49152\n\tv_mfma_f32_16x16x32_bf16 v[44:47], v[68:71], v[164:167], v[44:47]\n\tv_mov_b32 v129, v8\n\t"
        "v_mfma_f32_16x16x32_bf16 v[48:51], v[160:163], v[72:75], v[48:51]\n\tds_read_b128 v[68:71], %[pb1] offset:50176\n\t"
        "v_mfma_f32_16x16x32_bf16 v[52:55], v[164:167], v[72:75], v[52:55]\n\tv_mov_b32 v130, v8\n\t"
        "v_mfma_f32_16x16x32_bf16 v[56:59], v[160:163], v[76:79], v[56:59]\n\tds_read_b128 v[72:75], %[pb1] offset:51200\n\t"
        "buffer_load_dwordx4 v[232:235], off, %[rt], %[sa] offset:0 sc1\n\tv_mfma_f32_16x16x32_bf16 v[60:63], v[164:167], v[76:79], v[60:63]\n\t"
        "v_mov_b32 v131, v8\n\ts_waitcnt vmcnt(15) lgkmcnt(3)\n\tv_mfma_f32_16x16x32_bf16 v[16:19], v[80:83], v[168:171], v[16:19]\n\t"
        "ds_read_b128 v[76:79], %[pb1] offset:52224\n\tv_mfma_f32_16x16x32_bf16 v[20:23], v[80:83], v[172:175], v[20:23]\n\tv_mov_b32 v132, v8\n\t"
        "v_mfma_f32_16x16x32_bf16 v[24:27], v[84:87], v[168:171], v[24:27]\n\tds_read_b128 v[80:83], %[pb1] offset:53248\n\t"
        "v_mfma_f32_16x16x32_bf16 v[28:31], v[84:87], v[172:175], v[28:31]\n\tbuffer_load_dwordx4 v[236:239], off, %[rt], %[sa] offset:1024 sc1\n\t"
        "s_add_u32 %[sa], %[sa], 0x2000\n\tv_mov_b32 v133, v8\n\tv_mfma_f32_16x16x32_bf16 v[32:35], v[88:91], v[168:171], v[32:35]\n\t"
        "ds_read_b128 v[84:87], %[pb1] offset:54272\n\tv_mfma_f32_16x16x32_bf16 v[36:39], v[88:91], v[172:175], v[36:39]\n\tv_mov_b32 v134, v8\n\t"
        "v_mfma_f32_16x16x32_bf16 v[40:43], v[92:95], v[168:171], v[40:43]\n\tds_read_b128 v[88:91], %[pb1] offset:55296\n\t"
        "v_mfma_f32_16x16x32_bf16 v[44:47], v[92:95], v[172:175], v[44:47]\n\tv_mov_b32 v135, v8\n\ts_waitcnt lgkmcnt(3)\n\t"
        "v_mfma_f32_16x16x32_bf16 v[48:51], v[168:171], v[64:67], v[48:51]\n\tds_read_b128 v[92:95], %[pb1] offset:56320\n\t"
        "buffer_load_dwordx4 v[240:243], off, %[rt], %[sa] offset:0 sc1\n\tv_mfma_f32_16x16x32_bf16 v[52:55], v[172:175], v[64:67], v[52:55]\n\t"
        "v_mov_b32 v136, 0\n\tv_mfma_f32_16x16x32_bf16 v[56:59], v[168:171], v[68:71], v[56:59]\n\tds_read_b128 v[64:67], %[pb1] offset:57344\n\t"
        "v_mfma_f32_16x16x32_bf16 v[60:63], v[172:175], v[68:71], v[60:63]\n\tv_mov_b32 v137, 0\n\ts_waitcnt vmcnt(15)\n\t"
        "v_mfma_f32_16x16x32_bf16 v[16:19], v[72:75], v[176:179], v[16:19]\n\tds_read_b128 v[68:71], %[pb1] offset:58368\n\t"
        "v_mfma_f32_16x16x32_bf16 v[20:23], v[72:75], v[180:183], v[20:23]\n\tbuffer_load_dwordx4 v[244:247], off, %[rt], %[sa] offset:1024 sc1\n\t"
        "s_add_u32 %[sa], %[sa], 0x2000\n\tv_mov_b32 v138, 0\n\tv_mfma_f32_16x16x32_bf16 v[24:27], v[76:79], v[176:179], v[24:27]\n\t"
        "ds_read_b128 v[72:75], %[pb1] offset:59392\n\tv_mfma_f32_16x16x32_bf16 v[28:31], v[76:79], v[180:183], v[28:31]\n\tv_mov_b32 v139, 0\n\t"
        "s_waitcnt lgkmcnt(3)\n\tv_mfma_f32_16x16x32_bf16 v[32:35], v[80:83], v[176:179], v[32:35]\n\tds_read_b128 v[76:79], %[pb1] offset:60416\n\t"
        "v_mfma_f32_16x16x32_bf16 v[36:39], v[80:83], v[180:183], v[36:39]\n\tv_mov_b32 v140, 0\n\t"
        "v_mfma_f32_16x16x32_bf16 v[40:43], v[84:87], v[176:179], v[40:43]\n\tds_read_b128 v[80:83], %[pb1] offset:61440\n\t"
        "buffer_load_dwordx4 v[152:155], off, %[rt], %[sa] offset:0 sc1\n\tv_mfma_f32_16x16x32_bf16 v[44:47], v[84:87], v[180:183], v[44:47]\n\t"
        "v_mov_b32 v141, 0\n\tv_mfma_f32_16x16x32_bf16 v[48:51], v[176:179], v[88:91], v[48:51]\n\tds_read_b128 v[84:87], %[pb1] offset:62464\n\t"
        "v_mfma_f32_16x16x32_bf16 v[52:55], v[180:183], v[88:91], v[52:55]\n\tv_mov_b32 v142, 0\n\t"
        "v_mfma_f32_16x16x32_bf16 v[56:59], v[176:179], v[92:95], v[56:59]\n\tds_read_b128 v[88:91], %[pb1] offset:63488\n\t"
        "v_mfma_f32_16x16x32_bf16 v[60:63], v[180:183], v[92:95], v[60:63]\n\tbuffer_load_dwordx4 v[156:159], off, %[rt], %[sa] offset:1024 sc1\n\t"
        "s_add_u32 %[sa], %[sa], 0x2000\n\tv_mov_b32 v143, 0\n\ts_waitcnt vmcnt(16) lgkmcnt(3)\n\t"
        "v_mfma_f32_16x16x32_bf16 v[16:19], v[64:67], v[184:187], v[16:19]\n\tds_read_b128 v[92:95], %[pb1] offset:64512\n\t"
        "v_mfma_f32_16x16x32_bf16 v[20:23], v[64:67], v[188:191], v[20:23]\n\tv_mov_b32 v144, 0\n\t"
        "v_mfma_f32_16x16x32_bf16 v[24:27], v[68:71], v[184:187], v[24:27]\n\tds_read_b128 v[64:67], %[pb2] offset:0\n\t"
        "v_mfma_f32_16x16x32_bf16 v[28:31], v[68:71], v[188:191], v[28:31]\n\tv_mov_b32 v145, 0\n\t"
        "v_mfma_f32_16x16x32_bf16 v[32:35], v[72:75], v[184:187], v[32:35]\n\tds_read_b128 v[68:71], %[pb2] offset:1024\n\t"
        "buffer_load_dwordx4 v[160:163], off, %[rt], %[sa] offset:0 sc1\n\tv_mfma_f32_16x16x32_bf16 v[36:39], v[72:75], v[188:191], v[36:39]\n\t"
        "v_mov_b32 v146, 0\n\tv_mfma_f32_16x16x32_bf16 v[40:43], v[76:79], v[184:187], v[40:43]\n\tds_read_b128 v[72:75], %[pb2] offset:2048\n\t"
        "v_mfma_f32_16x16x32_bf16 v[44:47], v[76:79], v[188:191], v[44:47]\n\tv_mov_b32 v147, 0\n\ts_waitcnt lgkmcnt(3)\n\t"
        "v_mfma_f32_16x16x32_bf16 v[48:51], v[184:187], v[80:83], v[48:51]\n\tds_read_b128 v[76:79], %[pb2] offset:3072\n\t"
        "v_mfma_f32_16x16x32_bf16 v[52:55], v[188:191], v[80:83], v[52:55]\n\tbuffer_load_dwordx4 v[164:167], off, %[rt], %[sa] offset:1024 sc1\n\t"
        "s_add_u32 %[sa], %[sa], 0x2000\n\tv_mov_b32 v148, 0\n\tv_mfma_f32_16x16x32_bf16 v[56:59], v[184:187], v[84:87], v[56:59]\n\t"
        "ds_read_b128 v[80:83], %[pb2] offset:4096\n\tv_mfma_f32_16x16x32_bf16 v[60:63], v[188:191], v[84:87], v[60:63]\n\tv_mov_b32 v149, 0\n\t"
        "s_waitcnt vmcnt(16)\n\tv_mfma_f32_16x16x32_bf16 v[16:19], v[88:91], v[192:195], v[16:19]\n\tds_read_b128 v[84:87], %[pb2] offset:5120\n\t"
        "v_mfma_f32_16x16x32_bf16 v[20:23], v[88:91], v[196:199], v[20:23]\n\tv_mov_b32 v150, 0\n\t"
        "v_mfma_f32_16x16x32_bf16 v[24:27], v[92:95], v[192:195], v[24:27]\n\tds_read_b128 v[88:91], %[pb2] offset:6144\n\t"
        "buffer_load_dwordx4 v[168:171], off, %[rt], %[sa] offset:0 sc1\n\tv_mfma_f32_16x16x32_bf16 v[28:31], v[92:95], v[196:199], v[28:31]\n\t"
        "v_mov_b32 v151, 0\n\ts_waitcnt lgkmcnt(3)\n\tv_mfma_f32_16x16x32_bf16 v[32:35], v[64:67], v[192:195], v[32:35]\n\t"
        "ds_read_b128 v[92:95], %[pb2] offset:7168\n\tv_mfma_f32_16x16x32_bf16 v[36:39], v[64:67], v[196:199], v[36:39]\n\t"
        "v_mfma_f32_16x16x32_bf16 v[40:43], v[68:71], v[192:195], v[40:43]\n\tds_read_b128 v[64:67], %[pb2] offset:8192\n\t"
        "v_mfma_f32_16x16x32_bf16 v[44:47], v[68:71], v[196:199], v[44:47]\n\tbuffer_load_dwordx4 v[172:175], off, %[rt], %[sa] offset:1024 sc1\n\t"
        "s_add_u32 %[sa], %[sa], 0x2000\n\tv_mfma_f32_16x16x32_bf16 v[48:51], v[192:195], v[72:75], v[48:51]\n\tds_read_b128 v[68:71], %[pb2] offset:9216\n\t"
        "v_mfma_f32_16x16x32_bf16 v[52:55], v[196:199], v[72:75], v[52:55]\n\tv_mfma_f32_16x16x32_bf16 v[56:59], v[192:195], v[76:79], v[56:59]\n\t"
        "ds_read_b128 v[72:75], %[pb2] offset:10240\n\tv_mfma_f32_16x16x32_bf16 v[60:63], v[196:199], v[76:79], v[60:63]\n\ts_waitcnt vmcnt(16) lgkmcnt(3)\n\t"
        "v_mfma_f32_16x16x32_bf16 v[16:19], v[80:83], v[200:203], v[16:19]\n\tds_read_b128 v[76:79], %[pb2] offset:11264\n\t"
        "buffer_load_dwordx4 v[176:179], off, %[rt], %[sa] offset:0 sc1\n\tv_mfma_f32_16x16x32_bf16 v[20:23], v[80:83], v[204:207], v[20:23]\n\t"
        "v_mfma_f32_16x16x32_bf16 v[24:27], v[84:87], v[200:203], v[24:27]\n\tds_read_b128 v[80:83], %[pb2] offset:12288\n\t"
        "v_mfma_f32_16x16x32_bf16 v[28:31], v[84:87], v[204:207], v[28:31]\n\tv_mfma_f32_16x16x32_bf16 v[32:35], v[88:91], v[200:203], v[32:35]\n\t"
        "ds_read_b128 v[84:87], %[pb2] offset:13312\n\tv_mfma_f32_16x16x32_bf16 v[36:39], v[88:91], v[204:207], v[36:39]\n\t"
        "buffer_load_dwordx4 v[180:183], off, %[rt], %[sa] offset:1024 sc1\n\tv_mfma_f32_16x16x32_bf16 v[40:43], v[92:95], v[200:203], v[40:43]\n\t"
        "ds_read_b128 v[88:91], %[pb2] offset:14336\n\tv_mfma_f32_16x16x32_bf16 v[44:47], v[92:95], v[204:207], v[44:47]\n\ts_waitcnt lgkmcnt(3)\n\t"
        "v_mfma_f32_16x16x32_bf16 v[48:51], v[200:203], v[64:67], v[48:51]\n\tds_read_b128 v[92:95], %[pb2] offset:15360\n\t"
        "v_mfma_f32_16x16x32_bf16 v[52:55], v[204:207], v[64:67], v[52:55]\n\tv_mfma_f32_16x16x32_bf16 v[56:59], v[200:203], v[68:71], v[56:59]\n\t"
        "s_barrier\n\tds_read_b128 v[64:67], %[pb0] offset:0\n\tv_mfma_f32_16x16x32_bf16 v[60:63], v[204:207], v[68:71], v[60:63]\n\ts_waitcnt vmcnt(16)\n\t"
        "v_mfma_f32_16x16x32_bf16 v[16:19], v[72:75], v[208:211], v[16:19]\n\tds_read_b128 v[68:71], %[pb0] offset:1024\n\t"
        "v_mfma_f32_16x16x32_bf16 v[20:23], v[72:75], v[212:215], v[20:23]\n\tv_mfma_f32_16x16x32_bf16 v[24:27], v[76:79], v[208:211], v[24:27]\n\t"
        "ds_read_b128 v[72:75], %[pb0] offset:2048\n\tv_mfma_f32_16x16x32_bf16 v[28:31], v[76:79], v[212:215], v[28:31]\n\ts_waitcnt lgkmcnt(3)\n\t"
        "v_mfma_f32_16x16x32_bf16 v[32:35], v[80:83], v[208:211], v[32:35]\n\tds_read_b128 v[76:79], %[pb0] offset:3072\n\t"
        "v_mfma_f32_16x16x32_bf16 v[36:39], v[80:83], v[212:215], v[36:39]\n\tv_mfma_f32_16x16x32_bf16 v[40:43], v[84:87], v[208:211], v[40:43]\n\t"
        "ds_read_b128 v[80:83], %[pb0] offset:4096\n\tv_mfma_f32_16x16x32_bf16 v[44:47], v[84:87], v[212:215], v[44:47]\n\t"
        "v_mfma_f32_16x16x32_bf16 v[48:51], v[208:211], v[88:91], v[48:51]\n\tds_read_b128 v[84:87], %[pb0] offset:5120\n\t"
        "v_mfma_f32_16x16x32_bf16 v[52:55], v[212:215], v[88:91], v[52:55]\n\tv_mfma_f32_16x16x32_bf16 v[56:59], v[208:211], v[92:95], v[56:59]\n\t"
        "ds_read_b128 v[88:91], %[pb0] offset:6144\n\tv_mfma_f32_16x16x32_bf16 v[60:63], v[212:215], v[92:95], v[60:63]\n\ts_waitcnt vmcnt(14) lgkmcnt(3)\n\t"
        "v_mfma_f32_16x16x32_bf16 v[16:19], v[64:67], v[216:219], v[16:19]\n\tds_read_b128 v[92:95], %[pb0] offset:7168\n\t"
        "v_mfma_f32_16x16x32_bf16 v[20:23], v[64:67], v[220:223], v[20:23]\n\tv_mfma_f32_16x16x32_bf16 v[24:27], v[68:71], v[216:219], v[24:27]\n\t"
        "ds_read_b128 v[64:67], %[pb0] offset:8192\n\tv_mfma_f32_16x16x32_bf16 v[28:31], v[68:71], v[220:223], v[28:31]\n\t"
        "v_mfma_f32_16x16x32_bf16 v[32:35], v[72:75], v[216:219], v[32:35]\n\tds_read_b128 v[68:71], %[pb0] offset:9216\n\t"
        "v_mfma_f32_16x16x32_bf16 v[36:39], v[72:75], v[220:223], v[36:39]\n\tv_mfma_f32_16x16x32_bf16 v[40:43], v[76:79], v[216:219], v[40:43]\n\t"
        "ds_read_b128 v[72:75], %[pb0] offset:10240\n\tv_mfma_f32_16x16x32_bf16 v[44:47], v[76:79], v[220:223], v[44:47]\n\ts_waitcnt lgkmcnt(3)\n\t"
        "v_mfma_f32_16x16x32_bf16 v[48:51], v[216:219], v[80:83], v[48:51]\n\tds_read_b128 v[76:79], %[pb0] offset:11264\n\t"
        "v_mfma_f32_16x16x32_bf16 v[52:55], v[220:223], v[80:83], v[52:55]\n\tv_mfma_f32_16x16x32_bf16 v[56:59], v[216:219], v[84:87], v[56:59]\n\t"
        "ds_read_b128 v[80:83], %[pb0] offset:12288\n\tv_mfma_f32_16x16x32_bf16 v[60:63], v[220:223], v[84:87], v[60:63]\n\ts_waitcnt vmcnt(12)\n\t"
        "v_mfma_f32_16x16x32_bf16 v[16:19], v[88:91], v[224:227], v[16:19]\n\tds_read_b128 v[84:87], %[pb0] offset:13312\n\t"
        "v_mfma_f32_16x16x32_bf16 v[20:23], v[88:91], v[228:231], v[20:23]\n\tv_mfma_f32_16x16x32_bf16 v[24:27], v[92:95], v[224:227], v[24:27]\n\t"
        "ds_read_b128 v[88:91], %[pb0] offset:14336\n\tv_mfma_f32_16x16x32_bf16 v[28:31], v[92:95], v[228:231], v[28:31]\n\ts_waitcnt lgkmcnt(3)\n\t"
        "v_mfma_f32_16x16x32_bf16 v[32:35], v[64:67], v[224:227], v[32:35]\n\tds_read_b128 v[92:95], %[pb0] offset:15360\n\t"
        "v_mfma_f32_16x16x32_bf16 v[36:39], v[64:67], v[228:231], v[36:39]\n\tv_mfma_f32_16x16x32_bf16 v[40:43], v[68:71], v[224:227], v[40:43]\n\t"
        "ds_read_b128 v[64:67], %[pb0] offset:16384\n\tv_mfma_f32_16x16x32_bf16 v[44:47], v[68:71], v[228:231], v[44:47]\n\t"
        "v_mfma_f32_16x16x32_bf16 v[48:51], v[224:227], v[72:75], v[48:51]\n\tds_read_b128 v[68:71], %[pb0] offset:17408\n\t"
        "v_mfma_f32_16x16x32_bf16 v[52:55], v[228:231], v[72:75], v[52:55]\n\tv_mfma_f32_16x16x32_bf16 v[56:59], v[224:227], v[76:79], v[56:59]\n\t"
        "ds_read_b128 v[72:75], %[pb0] offset:18432\n\tv_mfma_f32_16x16x32_bf16 v[60:63], v[228:231], v[76:79], v[60:63]\n\ts_waitcnt vmcnt(10) lgkmcnt(3)\n\t"
        "v_mfma_f32_16x16x32_bf16 v[16:19], v[80:83], v[232:235], v[16:19]\n\tds_read_b128 v[76:79], %[pb0] offset:19456\n\t"
        "v_subrev_u32 v226, %[w128], v253\n\tv_mfma_f32_16x16x32_bf16 v[20:23], v[80:83], v[236:239], v[20:23]\n\tds_read_b32 v216, v253 offset:512\n\t"
        "ds_read_b32 v217, v253 offset:576\n\tv_mfma_f32_16x16x32_bf16 v[24:27], v[84:87], v[232:235], v[24:27]\n\t"
        "ds_read_b128 v[80:83], %[pb0] offset:20480\n\tds_read_b32 v218, v253 offset:1536\n\t"
        "v_mfma_f32_16x16x32_bf16 v[28:31], v[84:87], v[236:239], v[28:31]\n\tds_read_b32 v219, v253 offset:1600\n\t"
        "ds_read_b128 v[184:187], v255 offset:2176\n\tv_mfma_f32_16x16x32_bf16 v[32:35], v[88:91], v[232:235], v[32:35]\n\t"
        "ds_read_b128 v[84:87], %[pb0] offset:21504\n\tds_read_b128 v[188:191], v255 offset:2240\n\t"
        "v_mfma_f32_16x16x32_bf16 v[36:39], v[88:91], v[236:239], v[36:39]\n\tds_read_b128 v[192:195], v255 offset:2048\n\t"
        "ds_read_b128 v[196:199], v255 offset:2112\n\tv_mfma_f32_16x16x32_bf16 v[40:43], v[92:95], v[232:235], v[40:43]\n\t"
        "ds_read_b128 v[88:91], %[pb0] offset:22528\n\tds_read_b32 v220, v226 offset:2304\n\t"
        "v_mfma_f32_16x16x32_bf16 v[44:47], v[92:95], v[236:239], v[44:47]\n\tds_read_b32 v221, v226 offset:2368\n\ts_waitcnt lgkmcnt(13)\n\t"
        "v_mfma_f32_16x16x32_bf16 v[48:51], v[232:235], v[64:67], v[48:51]\n\tds_read_b128 v[92:95], %[pb0] offset:23552\n\t"
        "ds_read_b128 v[208:211], v254 offset:1536\n\tv_mfma_f32_16x16x32_bf16 v[52:55], v[236:239], v[64:67], v[52:55]\n\t"
        "ds_read_b128 v[212:215], v254 offset:1600\n\tds_read_b128 v[200:203], v254 offset:512\n\t"
        "v_mfma_f32_16x16x32_bf16 v[56:59], v[232:235], v[68:71], v[56:59]\n\tds_read_b128 v[64:67], %[pb0] offset:24576\n\t"
        "ds_read_b128 v[204:207], v254 offset:576\n\tv_mfma_f32_16x16x32_bf16 v[60:63], v[236:239], v[68:71], v[60:63]\n\ts_waitcnt vmcnt(8)\n\t"
        "v_mfma_f32_16x16x32_bf16 v[16:19], v[72:75], v[240:243], v[16:19]\n\tds_read_b128 v[68:71], %[pb0] offset:25600\n\t"
        "v_mfma_f32_16x16x32_bf16 v[20:23], v[72:75], v[244:247], v[20:23]\n\tv_mfma_f32_16x16x32_bf16 v[24:27], v[76:79], v[240:243], v[24:27]\n\t"
        "ds_read_b128 v[72:75], %[pb0] offset:26624\n\tv_mfma_f32_16x16x32_bf16 v[28:31], v[76:79], v[244:247], v[28:31]\n\ts_waitcnt lgkmcnt(7)\n\t"
        "v_mfma_f32_16x16x32_bf16 v[32:35], v[80:83], v[240:243], v[32:35]\n\tds_read_b128 v[76:79], %[pb0] offset:27648\n\t"
        "v_mfma_f32_16x16x32_bf16 v[36:39], v[80:83], v[244:247], v[36:39]\n\tv_mfma_f32_16x16x32_bf16 v[40:43], v[84:87], v[240:243], v[40:43]\n\t"
        "ds_read_b128 v[80:83], %[pb0] offset:28672\n\tv_mfma_f32_16x16x32_bf16 v[44:47], v[84:87], v[244:247], v[44:47]\n\t"
        "v_mfma_f32_16x16x32_bf16 v[48:51], v[240:243], v[88:91], v[48:51]\n\tds_read_b128 v[84:87], %[pb0] offset:29696\n\t"
        "v_mfma_f32_16x16x32_bf16 v[52:55], v[244:247], v[88:91], v[52:55]\n\tv_mfma_f32_16x16x32_bf16 v[56:59], v[240:243], v[92:95], v[56:59]\n\t"
        "ds_read_b128 v[88:91], %[pb0] offset:30720\n\tv_mfma_f32_16x16x32_bf16 v[60:63], v[244:247], v[92:95], v[60:63]\n\ts_waitcnt vmcnt(6) lgkmcnt(3)\n\t"
        "v_mfma_f32_16x16x32_bf16 v[16:19], v[64:67], v[152:155], v[16:19]\n\tds_read_b128 v[92:95], %[pb0] offset:31744\n\t"
        "v_mul_f32 v222, 0x3e8293ee, v216\n\tv_mfma_f32_16x16x32_bf16 v[20:23], v[64:67], v[156:159], v[20:23]\n\tv_mul_f32 v223, 0x3e8293ee, v217\n\t"
        "v_mul_f32 v224, 0x3e8293ee, v218\n\tv_mfma_f32_16x16x32_bf16 v[24:27], v[68:71], v[152:155], v[24:27]\n\t"
        "ds_read_b128 v[64:67], %[pb0] offset:32768\n\tv_mul_f32 v225, 0x3e8293ee, v219\n\t"
        "v_mfma_f32_16x16x32_bf16 v[28:31], v[68:71], v[156:159], v[28:31]\n\tv_mul_f32 v96, v218, v184\n\tv_mul_f32 v97, v218, v185\n\t"
        "v_mfma_f32_16x16x32_bf16 v[32:35], v[72:75], v[152:155], v[32:35]\n\tds_read_b128 v[68:71], %[pb0] offset:33792\n\tv_mul_f32 v98, v218, v186\n\t"
        "v_mfma_f32_16x16x32_bf16 v[36:39], v[72:75], v[156:159], v[36:39]\n\tv_mul_f32 v99, v218, v187\n\tv_mul_f32 v100, v218, v188\n\t"
        "v_mfma_f32_16x16x32_bf16 v[40:43], v[76:79], v[152:155], v[40:43]\n\tds_read_b128 v[72:75], %[pb0] offset:34816\n\tv_mul_f32 v101, v218, v189\n\t"
        "v_mfma_f32_16x16x32_bf16 v[44:47], v[76:79], v[156:159], v[44:47]\n\tv_mul_f32 v102, v218, v190\n\ts_waitcnt lgkmcnt(3)\n\t"
        "v_mfma_f32_16x16x32_bf16 v[48:51], v[152:155], v[80:83], v[48:51]\n\tds_read_b128 v[76:79], %[pb0] offset:35840\n\tv_mul_f32 v103, v218, v191\n\t"
        "v_mfma_f32_16x16x32_bf16 v[52:55], v[156:159], v[80:83], v[52:55]\n\tv_mul_f32 v184, v219, v184\n\tv_mul_f32 v185, v219, v185\n\t"
        "v_mfma_f32_16x16x32_bf16 v[56:59], v[152:155], v[84:87], v[56:59]\n\tds_read_b128 v[80:83], %[pb0] offset:36864\n\tv_mul_f32 v186, v219, v186\n\t"
        "v_mfma_f32_16x16x32_bf16 v[60:63], v[156:159], v[84:87], v[60:63]\n\tv_mul_f32 v187, v219, v187\n\ts_waitcnt vmcnt(4)\n\t"
        "v_mfma_f32_16x16x32_bf16 v[16:19], v[88:91], v[160:163], v[16:19]\n\tds_read_b128 v[84:87], %[pb0] offset:37888\n\tv_mul_f32 v188, v219, v188\n\t"
        "v_mfma_f32_16x16x32_bf16 v[20:23], v[88:91], v[164:167], v[20:23]\n\tv_mul_f32 v189, v219, v189\n\tv_mul_f32 v190, v219, v190\n\t"
        "v_mfma_f32_16x16x32_bf16 v[24:27], v[92:95], v[160:163], v[24:27]\n\tds_read_b128 v[88:91], %[pb0] offset:38912\n\tv_mul_f32 v191, v219, v191\n\t"
        "v_mfma_f32_16x16x32_bf16 v[28:31], v[92:95], v[164:167], v[28:31]\n\tv_mul_f32 v104, v224, v192\n\ts_waitcnt lgkmcnt(3)\n\t"
        "v_mfma_f32_16x16x32_bf16 v[32:35], v[64:67], v[160:163], v[32:35]\n\tds_read_b128 v[92:95], %[pb0] offset:39936\n\tv_mul_f32 v105, v224, v193\n\t"
        "v_mfma_f32_16x16x32_bf16 v[36:39], v[64:67], v[164:167], v[36:39]\n\tv_mul_f32 v106, v224, v194\n\tv_mul_f32 v107, v224, v195\n\t"
        "v_mfma_f32_16x16x32_bf16 v[40:43], v[68:71], v[160:163], v[40:43]\n\tds_read_b128 v[64:67], %[pb0] offset:40960\n\tv_mul_f32 v108, v224, v196\n\t"
        "v_mfma_f32_16x16x32_bf16 v[44:47], v[68:71], v[164:167], v[44:47]\n\tv_mul_f32 v109, v224, v197\n\tv_mul_f32 v110, v224, v198\n\t"
        "v_mfma_f32_16x16x32_bf16 v[48:51], v[160:163], v[72:75], v[48:51]\n\tds_read_b128 v[68:71], %[pb0] offset:41984\n\tv_mul_f32 v111, v224, v199\n\t"
        "v_mfma_f32_16x16x32_bf16 v[52:55], v[164:167], v[72:75], v[52:55]\n\tv_mul_f32 v192, v225, v192\n\tv_mul_f32 v193, v225, v193\n\t"
        "v_mfma_f32_16x16x32_bf16 v[56:59], v[160:163], v[76:79], v[56:59]\n\tds_read_b128 v[72:75], %[pb0] offset:43008\n\tv_mul_f32 v194, v225, v194\n\t"
        "v_mfma_f32_16x16x32_bf16 v[60:63], v[164:167], v[76:79], v[60:63]\n\tv_mul_f32 v195, v225, v195\n\ts_waitcnt vmcnt(2) lgkmcnt(3)\n\t"
        "v_mfma_f32_16x16x32_bf16 v[16:19], v[80:83], v[168:171], v[16:19]\n\tds_read_b128 v[76:79], %[pb0] offset:44032\n\tv_mul_f32 v196, v225, v196\n\t"
        "v_mfma_f32_16x16x32_bf16 v[20:23], v[80:83], v[172:175], v[20:23]\n\tv_mul_f32 v197, v225, v197\n\tv_mul_f32 v198, v225, v198\n\t"
        "v_mfma_f32_16x16x32_bf16 v[24:27], v[84:87], v[168:171], v[24:27]\n\tds_read_b128 v[80:83], %[pb0] offset:45056\n\tv_mul_f32 v199, v225, v199\n\t"
        "v_mfma_f32_16x16x32_bf16 v[28:31], v[84:87], v[172:175], v[28:31]\n\tv_mul_f32 v112, v220, v208\n\tv_mul_f32 v113, v220, v209\n\t"
        "v_mfma_f32_16x16x32_bf16 v[32:35], v[88:91], v[168:171], v[32:35]\n\tds_read_b128 v[84:87], %[pb0] offset:46080\n\tv_mul_f32 v114, v220, v210\n\t"
        "v_mfma_f32_16x16x32_bf16 v[36:39], v[88:91], v[172:175], v[36:39]\n\tv_mul_f32 v115, v220, v211\n\tv_mul_f32 v116, v220, v212\n\t"
        "v_mfma_f32_16x16x32_bf16 v[40:43], v[92:95], v[168:171], v[40:43]\n\tds_read_b128 v[88:91], %[pb0] offset:47104\n\tv_mul_f32 v117, v220, v213\n\t"
        "v_mfma_f32_16x16x32_bf16 v[44:47], v[92:95], v[172:175], v[44:47]\n\tv_mul_f32 v118, v220, v214\n\ts_waitcnt lgkmcnt(3)\n\t"
        "v_mfma_f32_16x16x32_bf16 v[48:51], v[168:171], v[64:67], v[48:51]\n\tds_read_b128 v[92:95], %[pb0] offset:48128\n\tv_mul_f32 v119, v220, v215\n\t"
        "v_mfma_f32_16x16x32_bf16 v[52:55], v[172:175], v[64:67], v[52:55]\n\tv_mul_f32 v208, v221, v208\n\tv_mul_f32 v209, v221, v209\n\t"
        "v_mfma_f32_16x16x32_bf16 v[56:59], v[168:171], v[68:71], v[56:59]\n\tv_mul_f32 v210, v221, v210\n\tv_mul_f32 v211, v221, v211\n\t"
        "v_mfma_f32_16x16x32_bf16 v[60:63], v[172:175], v[68:71], v[60:63]\n\tv_mul_f32 v212, v221, v212\n\ts_waitcnt vmcnt(0)\n\t"
        "v_mfma_f32_16x16x32_bf16 v[16:19], v[72:75], v[176:179], v[16:19]\n\tv_mul_f32 v213, v221, v213\n\tv_mul_f32 v214, v221, v214\n\t"
        "v_mfma_f32_16x16x32_bf16 v[20:23], v[72:75], v[180:183], v[20:23]\n\tv_mul_f32 v215, v221, v215\n\t"
        "v_mfma_f32_16x16x32_bf16 v[24:27], v[76:79], v[176:179], v[24:27]\n\tv_mfma_f32_16x16x32_bf16 v[28:31], v[76:79], v[180:183], v[28:31]\n\t"
        "s_waitcnt lgkmcnt(0)\n\tv_mfma_f32_16x16x32_bf16 v[32:35], v[80:83], v[176:179], v[32:35]\n\t"
        "v_mfma_f32_16x16x32_bf16 v[36:39], v[80:83], v[180:183], v[36:39]\n\tv_mfma_f32_16x16x32_bf16 v[40:43], v[84:87], v[176:179], v[40:43]\n\t"
        "v_mfma_f32_16x16x32_bf16 v[44:47], v[84:87], v[180:183], v[44:47]\n\tv_mfma_f32_16x16x32_bf16 v[48:51], v[176:179], v[88:91], v[48:51]\n\t"
        "v_mfma_f32_16x16x32_bf16 v[52:55], v[180:183], v[88:91], v[52:55]\n\tv_mfma_f32_16x16x32_bf16 v[56:59], v[176:179], v[92:95], v[56:59]\n\t"
        "v_mfma_f32_16x16x32_bf16 v[60:63], v[180:183], v[92:95], v[60:63]\n\t"
        "v_pk_fma_f32 v[32:33], v[32:33], v[216:217], v[96:97] op_sel:[0,0,0] op_sel_hi:[1,0,1]\n\t"
        "v_pk_fma_f32 v[34:35], v[34:35], v[216:217], v[98:99] op_sel:[0,0,0] op_sel_hi:[1,0,1]\n\t"
        "v_pk_fma_f32 v[40:41], v[40:41], v[216:217], v[100:101] op_sel:[0,0,0] op_sel_hi:[1,0,1]\n\t"
        "v_pk_fma_f32 v[42:43], v[42:43], v[216:217], v[102:103] op_sel:[0,0,0] op_sel_hi:[1,0,1]\n\t"
        "v_pk_fma_f32 v[36:37], v[36:37], v[216:217], v[184:185] op_sel:[0,1,0] op_sel_hi:[1,1,1]\n\t"
        "v_pk_fma_f32 v[38:39], v[38:39], v[216:217], v[186:187] op_sel:[0,1,0] op_sel_hi:[1,1,1]\n\t"
        "v_pk_fma_f32 v[44:45], v[44:45], v[216:217], v[188:189] op_sel:[0,1,0] op_sel_hi:[1,1,1]\n\t"
        "v_pk_fma_f32 v[46:47], v[46:47], v[216:217], v[190:191] op_sel:[0,1,0] op_sel_hi:[1,1,1]\n\tv_cvt_pk_bf16_f32 v32, v32, v33\n\t"
        "v_cvt_pk_bf16_f32 v33, v34, v35\n\tv_cvt_pk_bf16_f32 v34, v40, v41\n\tv_cvt_pk_bf16_f32 v35, v42, v43\n\t"
        "buffer_store_dwordx4 v[32:35], off, %[rt], %[kv] offset:0 sc1\n\tv_cvt_pk_bf16_f32 v36, v36, v37\n\tv_cvt_pk_bf16_f32 v37, v38, v39\n\t"
        "v_cvt_pk_bf16_f32 v38, v44, v45\n\tv_cvt_pk_bf16_f32 v39, v46, v47\n\tbuffer_store_dwordx4 v[36:39], off, %[rt], %[kv] offset:1024 sc1\n\t"
        "v_pk_fma_f32 v[16:17], v[16:17], v[222:223], v[104:105] op_sel:[0,0,0] op_sel_hi:[1,0,1]\n\t"
        "v_pk_fma_f32 v[18:19], v[18:19], v[222:223], v[106:107] op_sel:[0,0,0] op_sel_hi:[1,0,1]\n\t"
        "v_pk_fma_f32 v[24:25], v[24:25], v[222:223], v[108:109] op_sel:[0,0,0] op_sel_hi:[1,0,1]\n\t"
        "v_pk_fma_f32 v[26:27], v[26:27], v[222:223], v[110:111] op_sel:[0,0,0] op_sel_hi:[1,0,1]\n\t"
        "v_pk_fma_f32 v[20:21], v[20:21], v[222:223], v[192:193] op_sel:[0,1,0] op_sel_hi:[1,1,1]\n\t"
        "v_pk_fma_f32 v[22:23], v[22:23], v[222:223], v[194:195] op_sel:[0,1,0] op_sel_hi:[1,1,1]\n\t"
        "v_pk_fma_f32 v[28:29], v[28:29], v[222:223], v[196:197] op_sel:[0,1,0] op_sel_hi:[1,1,1]\n\t"
        "v_pk_fma_f32 v[30:31], v[30:31], v[222:223], v[198:199] op_sel:[0,1,0] op_sel_hi:[1,1,1]\n\t"
        "v_pk_fma_f32 v[48:49], v[48:49], v[200:201], v[112:113]\n\tv_pk_fma_f32 v[50:51], v[50:51], v[202:203], v[114:115]\n\t"
        "v_pk_fma_f32 v[52:53], v[52:53], v[204:205], v[116:117]\n\tv_pk_fma_f32 v[54:55], v[54:55], v[206:207], v[118:119]\n\t"
        "v_pk_fma_f32 v[56:57], v[56:57], v[200:201], v[208:209]\n\tv_pk_fma_f32 v[58:59], v[58:59], v[202:203], v[210:211]\n\t"
        "v_pk_fma_f32 v[60:61], v[60:61], v[204:205], v[212:213]\n\tv_pk_fma_f32 v[62:63], v[62:63], v[206:207], v[214:215]\n\t"
        "v_cvt_pk_bf16_f32 v88, v16, v17\n\tv_cvt_pk_bf16_f32 v89, v18, v19\n\tv_cvt_pk_bf16_f32 v90, v24, v25\n\tv_cvt_pk_bf16_f32 v91, v26, v27\n\t"
        "v_cvt_pk_bf16_f32 v92, v20, v21\n\tv_cvt_pk_bf16_f32 v93, v22, v23\n\tv_cvt_pk_bf16_f32 v94, v28, v29\n\tv_cvt_pk_bf16_f32 v95, v30, v31\n\t"
        "v_cvt_pk_bf16_f32 v48, v48, v49\n\tv_cvt_pk_bf16_f32 v49, v50, v51\n\tv_mfma_f32_16x16x32_bf16 v[40:43], v[32:35], v[88:91], 0\n\t"
        "v_mfma_f32_16x16x32_bf16 v[44:47], v[36:39], v[92:95], 0\n\tv_cvt_pk_bf16_f32 v50, v52, v53\n\tv_cvt_pk_bf16_f32 v51, v54, v55\n\t"
        "buffer_store_dwordx4 v[48:51], off, %[rt], %[kv] offset:2048 sc1\n\tv_cvt_pk_bf16_f32 v56, v56, v57\n\tv_cvt_pk_bf16_f32 v57, v58, v59\n\t"
        "v_cvt_pk_bf16_f32 v58, v60, v61\n\tv_cvt_pk_bf16_f32 v59, v62, v63\n\tbuffer_store_dwordx4 v[56:59], off, %[rt], %[kv] offset:3072 sc1\n\t"
        "v_max3_f32 v40, v40, v41, v42\n\tv_max3_f32 v44, v44, v45, v46\n\tv_max_f32 v40, v40, v43\n\tv_max_f32 v44, v44, v47\n\t"
        "v_permlane16_swap_b32 v88, v92\n\tv_permlane16_swap_b32 v89, v93\n\tv_permlane16_swap_b32 v40, v44\n\tv_max_f32 v40, v40, v44\n\t"
        "v_mov_b32 v44, v40\n\tv_permlane16_swap_b32 v90, v94\n\tv_permlane16_swap_b32 v91, v95\n\tv_permlane32_swap_b32 v40, v44\n\t"
        "v_min_f32_e64 v9, -v40, -v44\n\t.Lmk_q1_e:\n\t"
        : "=&{v[16:31]}"(acc[0]), "=&{v[32:47]}"(acc[1]), "=&{v[48:63]}"(acc[2]), "+{v[152:183]}"(r0), "+{v[184:215]}"(r1), "+{v[216:247]}"(r2), "=&{v[120:135]}"(nega), "=&{v[136:151]}"(oa), "=&{v[88:91]}"(qb0), "=&{v[92:95]}"(qb1), "=&{v9}"(nmb), [sa] "+s"(sa), [sn] "+s"(sn)
        : [pb0] "{v250}"(n.pb0), [pb1] "{v251}"(n.pb1), [pb2] "{v252}"(n.pb2), [rt] "s"(rt4), [rn] "s"(rt4), "{v8}"(nma), [kv] "s"(kvo + 131072), [w128] "s"(n.w128), "{v253}"(n.e1), "{v254}"(n.e2), "{v255}"(n.e3), [rs] "s"(rs4), [one] "{v[12:13]}"(tok), [fl] "s"(fl)
        : "v64", "v65", "v66", "v67", "v68", "v69", "v70", "v71", "v72", "v73", "v74", "v75", "v76", "v77", "v78", "v79", "v80", "v81", "v82", "v83", "v84", "v85", "v86", "v87", "v96", "v97", "v98", "v99", "v100", "v101", "v102", "v103", "v104", "v105", "v106", "v107", "v108", "v109", "v110", "v111", "v112", "v113", "v114", "v115", "v116", "v117", "v118", "v119", "memory", "scc");
  }
}

extern "C" void kernel_launch(void* const* d_in, const int* in_sizes, int n_in, void* d_out, int out_size, void* d_ws, size_t ws_size,
                              hipStream_t stream) {
  char* ws = (char*)d_ws + 1 * ((ws_size - WS_END) & ~(size_t)(2 * MB - 1));
  (void)hipFuncSetAttribute((const void*)main_kernel, hipFuncAttributeMaxDynamicSharedMemorySize, L_TOTAL);
  main_kernel<<<256, 512, L_TOTAL, stream>>>(ws, (const char*)d_in[0], (const char*)d_in[1], (const char*)d_in[2], (const char*)d_in[4], (const char*)d_in[6], (const char*)d_in[5], (const char*)d_in[7], (char*)d_out);
}
```
